```python
import torch

def _softmax(x, axis=-1):
    return torch.softmax(x, dim=axis)
def _gelu(x):
    return torch.nn.functional.gelu(x, approximate="tanh")
def _silu(x):
    return torch.nn.functional.silu(x)
def _sigmoid(x):
    return torch.sigmoid(x)
def _layernorm(x, axis=-1, eps=1e-5):
    m = x.mean(dim=axis, keepdim=True)
    v = x.var(dim=axis, keepdim=True, unbiased=False)
    return (x - m) / torch.sqrt(v + eps)
def _scatter_add(tgt, idx, src, axis=0):
    return tgt.clone().index_add_(axis, idx, src)
def _segment_sum(src, seg, n):
    out = torch.zeros((n,) + src.shape[1:], dtype=src.dtype)
    return out.index_add_(0, seg, src)
def _pad(x, pw):
    flat = tuple(v for pair in reversed(pw) for v in pair)
    return torch.nn.functional.pad(x, flat)

kernel_name = 'interleave_layers__spectral_gfnet__softmax_full_mha__prompt'

def setup_inputs():

    _w = (lambda a: (lambda t: t.to(torch.bfloat16) if t.is_floating_point() else t)(torch.as_tensor(a)))
    def _A_setup():
        B, S, D = 1, 2048, 640
        Df = D // 2 + 1
        return {
            "x": _w((lambda *s: torch.randn(*s, dtype=torch.bfloat16) * 0.05)(B, S, D)),
            "w_r": _w((lambda *s: torch.randn(*s, dtype=torch.bfloat16) * 1.6)(S, Df) * 0.02),
            "w_i": _w((lambda *s: torch.randn(*s, dtype=torch.bfloat16) * 1.6)(S, Df) * 0.02),
        }
    _A_in = _A_setup()

    def _B_setup():
        B, S, D = 1, 2048, 640
        return {
            "x": _w((lambda *s: torch.randn(*s, dtype=torch.bfloat16) * 0.05)(B, S, D)),
            "wqkv": _w((lambda *s: torch.randn(*s, dtype=torch.bfloat16) * 0.05)(D, 3 * D)),
            "wo": _w((lambda *s: torch.randn(*s, dtype=torch.bfloat16) * 0.05)(D, D)),
        }
    _B_in = _B_setup()

    return {
        "A_x": _A_in["x"],
        "A_w_r": _A_in["w_r"],
        "A_w_i": _A_in["w_i"],
        "B_x": _B_in["x"],
        "B_wqkv": _B_in["wqkv"],
        "B_wo": _B_in["wo"],
    }

def reference(A_x, A_w_r, A_w_i, B_x, B_wqkv, B_wo):
    def _A_ref(x, w_r, w_i):
        B, S, D = x.shape
        X = (lambda x, axes=(-2,-1): torch.fft.rfft2(x.float(), dim=axes))(x, axes=(-2, -1))
        W = w_r + 1j * w_i
        X = X * W
        y = (lambda x, s, axes=(-2,-1): torch.fft.irfft2(x, s=s, dim=axes).to(torch.bfloat16))(X, (S, D), axes=(-2, -1))
        return y

    def _B_ref(x, wqkv, wo):
        B, S, D = x.shape
        H = 32; Dh = D // H
        qkv = (lambda x, *d: x.permute(*d))((x @ wqkv).reshape(B, S, 3, H, Dh), 2, 0, 3, 1, 4)
        q, k, v = qkv[0], qkv[1], qkv[2]
        scale = Dh ** -0.5
        scores = torch.einsum("bhqd,bhkd->bhqk", q, k) * scale
        p = _softmax(scores, axis=-1)
        o = torch.einsum("bhqk,bhkd->bhqd", p, v)
        return (lambda x, *d: x.permute(*d))(o, 0, 2, 1, 3).reshape(B, S, D) @ wo

    x = A_x
    x = x + _A_ref(_layernorm(x, axis=-1), A_w_r, A_w_i)
    x = x + _B_ref(_layernorm(x, axis=-1), B_wqkv, B_wo)
    x = x + _A_ref(_layernorm(x, axis=-1), A_w_r, A_w_i)
    x = x + _B_ref(_layernorm(x, axis=-1), B_wqkv, B_wo)
    return x
```

```cpp
#include <hip/hip_runtime.h>
#include <hip/hip_bf16.h>
#include <utility>

typedef unsigned short u16;
typedef __attribute__((ext_vector_type(16))) float f32x16;
typedef __attribute__((ext_vector_type(4))) float f32x4;
typedef __attribute__((ext_vector_type(2))) float f32x2;
typedef __attribute__((ext_vector_type(8))) __bf16 bf16x8;
typedef __attribute__((ext_vector_type(2))) __bf16 bf16x2;
typedef __attribute__((ext_vector_type(4))) short s16x4;
typedef __attribute__((ext_vector_type(4))) unsigned u32x4;
typedef __attribute__((ext_vector_type(2))) unsigned u32x2;
#define DI __device__ __forceinline__
#define LDS_AS __attribute__((address_space(3)))
typedef LDS_AS char* lptr;

constexpr double cx_cos_small(double x) { double x2 = x * x, t = 1, s = 1; for (int i = 1; i < 12; i++) { t *= -x2 / ((2 * i - 1) * (2 * i)); s += t; } return s; }
constexpr double cx_sin_small(double x) { double x2 = x * x, t = x, s = x; for (int i = 1; i < 12; i++) { t *= -x2 / ((2 * i) * (2 * i + 1)); s += t; } return s; }
constexpr double cx_cos640(int m) {
  m %= 640; if (m > 320) m = 640 - m; double sg = 1; if (m > 160) { m = 320 - m; sg = -1; }
  const double w = 6.283185307179586476925 / 640; return sg * (m <= 80 ? cx_cos_small(w * m) : cx_sin_small(w * (160 - m)));
}
constexpr u16 cx_bf16(double d) { float f = (float)d; unsigned u = __builtin_bit_cast(unsigned, f); u += 0x7FFFu + ((u >> 16) & 1u); return (u16)(u >> 16); }
struct Cos640 { u16 v[640]; };
constexpr Cos640 mk_cos() { Cos640 c{}; for (int m = 0; m < 640; m++) c.v[m] = cx_bf16(0.055901699437494742 * cx_cos640(m)); return c; }
constexpr Cos640 COSB = mk_cos();
constexpr u16 fval(int n, int j) {
  int c = j >> 1;
  if (c == 0) { u16 b = cx_bf16(0.039528470752104741); return (j == 0 && (n & 1)) ? (u16)(b | 0x8000) : b; }
  return COSB.v[(c * n + ((j & 1) ? 160 : 0)) % 640];
}
struct FTile { u16 v[20 * 64 * 8]; };
constexpr FTile mk_tile(int jt) {
  FTile t{}; int kk = jt >> 1, T = jt & 1;
  for (int ks = 0; ks < 20; ks++) for (int l = 0; l < 64; l++) {
    int r = l & 15, g = l >> 4, j = 32 * kk + 8 * (r >> 2) + 4 * T + (r & 3);
    for (int e = 0; e < 8; e++) t.v[(ks * 64 + l) * 8 + e] = fval(32 * ks + 8 * g + e, j);
  }
  return t;
}
template <int I> constexpr FTile tile_v = mk_tile(I);
struct FTab { FTile t[40]; };
template <int... I> constexpr FTab mk_tab(std::integer_sequence<int, I...>) { return FTab{{tile_v<I>...}}; }
static __device__ const __attribute__((aligned(1024))) FTab g_ft = mk_tab(std::make_integer_sequence<int, 40>{});

constexpr size_t WS_R = 0;
constexpr size_t WS_RB = WS_R + 640 * 2048 * 4;
constexpr size_t WS_SSQ = WS_RB + 640 * 2048 * 2;
constexpr size_t WS_WF = WS_SSQ + 8 * 2048 * 2;
constexpr size_t WS_WQ = WS_WF + 320 * 8192;
constexpr size_t WS_WOF = WS_WQ + 16 * 10 * 20 * 1024;
constexpr size_t WS_WOP = WS_WOF + 40 * 20 * 1024;
constexpr size_t WS_Q = WS_WOP + 40 * 20 * 1024;
constexpr int KVC = 11264;
constexpr size_t WS_KV = WS_Q + 32 * 64 * 1280;
constexpr size_t WS_O = WS_KV + 32 * 16 * KVC;
constexpr size_t WS_X4 = WS_O + 32 * 64 * 1280;

DI unsigned pk2(float a, float b) { f32x2 v = {a, b}; return __builtin_bit_cast(unsigned, __builtin_convertvector(v, bf16x2)); }
DI float bflo(unsigned u) { return __uint_as_float(u << 16); }
DI float bfhi(unsigned u) { return __uint_as_float(u & 0xFFFF0000u); }
DI int rowof(int p) { return 32 * (p & 63) + (p >> 6); }
DI f32x4 mfma16(u32x4 a, u32x4 b, f32x4 c) {
  return __builtin_amdgcn_mfma_f32_16x16x32_bf16(__builtin_bit_cast(bf16x8, a), __builtin_bit_cast(bf16x8, b), c, 0, 0, 0);
}
DI u32x2 ldtr(lptr p) { return __builtin_bit_cast(u32x2, __builtin_amdgcn_ds_read_tr16_b64_v4i16((LDS_AS s16x4*)p)); }
DI u32x4 ld128(lptr p) { return *(LDS_AS u32x4*)p; }
DI u32x2 ld64(lptr p) { return *(LDS_AS u32x2*)p; }
DI void glds16(unsigned voff, const char* sbase, unsigned lds) {
  unsigned sv;
  asm volatile("s_mov_b32 %0, m0\n\ts_mov_b32 m0, %3\n\ts_nop 0\n\tglobal_load_lds_dwordx4 %1, %2\n\ts_mov_b32 m0, %0"
               : "=&s"(sv) : "v"(voff), "s"(sbase), "s"(lds) : "memory");
}
template <int N> DI void wait_bar() { asm volatile("s_waitcnt vmcnt(%0) lgkmcnt(0)\n\ts_barrier" ::"i"(N) : "memory"); }
DI void lds_bar() { asm volatile("s_waitcnt lgkmcnt(0)\n\ts_barrier" ::: "memory"); }
DI int wave_id() { return __builtin_amdgcn_readfirstlane(threadIdx.x >> 6); }
DI unsigned lds_addr(lptr p) { return (unsigned)(size_t)p; }

DI int kappa_to_i(int kap) {
  int ks = kap >> 5, g = (kap >> 3) & 3, e = kap & 7;
  if (ks < 16) return (2 * ks + (g >> 1)) * 20 + 8 * (e >> 2) + 4 * (g & 1) + (e & 3);
  return (8 * (ks - 16) + 2 * g + (e >> 2)) * 20 + 16 + (e & 3);
}

enum { KMAJOR = 0, KCONTIG = 1, OBLK = 2 };
template <int MT, int NTOT> struct GCfg {
  static constexpr int NP = 2 * (MT + NTOT), SLOT = NP * 1024, NS = (MT == 8) ? 4 : 8, PD = NS - 1;
  static constexpr int RM = 2 * MT / 8, SP = 2 * NTOT, RS = (SP + 7) / 8, XW = SP % 8;
  static constexpr int MW = MT / 4, NT = (NTOT == 8) ? 4 : (NTOT == 10) ? 5 : 3;
  static constexpr int LDS = NS * SLOT;
};
template <int NTOT> DI int stile(int wn, int i) { return NTOT == 5 ? 3 * wn + i : (i < 4 ? 4 * wn + i : 8 + wn); }

template <int MT, int NTOT, int KIND, int ORI, bool SSQ>
DI void gemm_core(lptr lds, const char* mbase, const unsigned* vm, const unsigned* vm_tail, unsigned minc, const char* sbase,
                  f32x4 (&acc)[GCfg<MT, NTOT>::MW][GCfg<MT, NTOT>::NT], float* ssq) {
  using C = GCfg<MT, NTOT>;
  const int lane = threadIdx.x & 63, w = wave_id(), wm = w & 3, wn = w >> 2, g = lane >> 4, r16 = lane & 15;
  const unsigned l0 = lds_addr(lds);
  unsigned vs[C::RS];
#pragma unroll
  for (int r = 0; r < C::RS; r++) { int qs = w + 8 * r; if (qs >= C::SP) qs = 0; vs[r] = ((qs >> 1) * 20 + (qs & 1)) * 1024 + lane * 16; }
  const bool xtra = (C::XW == 0) || (w < C::XW);
  auto issue = [&](int s) {
    const unsigned slot = l0 + (s % C::NS) * C::SLOT + w * 1024;
    if (KIND == OBLK && s >= 8) glds16(vm_tail[0], mbase + (size_t)(s - 8) * (16 * 64 * 1280), slot);
    else {
#pragma unroll
      for (int r = 0; r < C::RM; r++) glds16(vm[r], mbase + (size_t)s * minc, slot + r * 8192);
    }
#pragma unroll
    for (int r = 0; r < C::RS; r++)
      if (r < C::RS - 1 || xtra) glds16(vs[r], sbase + s * 2048, slot + MT * 2048 + r * 8192);
  };
#pragma unroll
  for (int s = 0; s < C::PD; s++) issue(s);
  int mo[C::MW], mo2 = 0;
#pragma unroll
  for (int m = 0; m < C::MW; m++) {
    const int t = C::MW * wm + m;
    if (KIND == KMAJOR) { const int q = r16 >> 2; mo[m] = (8 * g + q) * 256 + ((t ^ (q | ((g & 1) << 2))) * 32) + (lane & 3) * 8; }
    if (KIND == KCONTIG) mo[m] = (16 * t + r16) * 128 + ((g ^ (r16 >> 1)) * 16);
    if (KIND == OBLK) { mo[m] = ((g >> 1) * 2 + (t >> 1)) * 1024 + (g & 1) * 512 + (t & 1) * 256 + r16 * 16; mo2 = (4 * g + (t >> 1)) * 256 + ((t & 1) * 16 + r16) * 8; }
  }
  const int so = MT * 2048 + lane * 16;
  unsigned dcmask = (g == 0) ? 0x0000FFFFu : 0xFFFFFFFFu;
  constexpr int CA = C::RM + C::RS, CB = CA - 1;
#pragma unroll
  for (int s = 0; s < 10; s++) {
    constexpr int dummy = 0; (void)dummy;
    const int rem = (9 - s) < (C::PD - 1) ? (9 - s) : (C::PD - 1);
    if (C::XW == 0 || xtra) {
      switch (rem) { case 0: wait_bar<0>(); break; case 1: wait_bar<CA>(); break; case 2: wait_bar<2 * CA>(); break; case 3: wait_bar<3 * CA>(); break;
                     case 4: wait_bar<4 * CA>(); break; case 5: wait_bar<5 * CA>(); break; default: wait_bar<6 * CA>(); }
    } else {
      switch (rem) { case 0: wait_bar<0>(); break; case 1: wait_bar<CB>(); break; case 2: wait_bar<2 * CB>(); break; case 3: wait_bar<3 * CB>(); break;
                     case 4: wait_bar<4 * CB>(); break; case 5: wait_bar<5 * CB>(); break; default: wait_bar<6 * CB>(); }
    }
    if (s + C::PD < 10) issue(s + C::PD);
    const lptr sl = lds + (s % C::NS) * C::SLOT;
#pragma unroll
    for (int ksl = 0; ksl < 2; ksl++) {
      u32x4 mf[C::MW], sf[C::NT];
#pragma unroll
      for (int m = 0; m < C::MW; m++) {
        if (KIND == KMAJOR) { u32x2 a = ldtr(sl + mo[m] + ksl * 8192), b = ldtr(sl + mo[m] + ksl * 8192 + 1024); mf[m] = u32x4{a[0], a[1], b[0], b[1]}; }
        if (KIND == KCONTIG) mf[m] = ld128(sl + (mo[m] ^ (ksl * 64)));
        if (KIND == OBLK) {
          if (s < 8) mf[m] = ld128(sl + mo[m] + ksl * 4096);
          else { u32x2 a = ld64(sl + mo2 + ksl * 4096), b = ld64(sl + mo2 + ksl * 4096 + 512); mf[m] = u32x4{a[0], a[1], b[0], b[1]}; }
        }
      }
#pragma unroll
      for (int i = 0; i < C::NT; i++)
        if (NTOT != 5 || i < 2 || wn == 0) sf[i] = ld128(sl + so + (stile<NTOT>(wn, i) * 2 + ksl) * 1024);
#pragma unroll
      for (int i = 0; i < C::NT; i++)
        if (NTOT != 5 || i < 2 || wn == 0) {
#pragma unroll
          for (int m = 0; m < C::MW; m++) acc[m][i] = (ORI == 1) ? mfma16(sf[i], mf[m], acc[m][i]) : mfma16(mf[m], sf[i], acc[m][i]);
        }
      if (SSQ && ksl == wn) {
#pragma unroll
        for (int m = 0; m < C::MW; m++) {
          u32x4 f = mf[m];
          if (s == 0 && ksl == 0) f[0] &= dcmask;
#pragma unroll
          for (int e = 0; e < 4; e++) { const unsigned fe = f[e]; const bf16x2 v = __builtin_bit_cast(bf16x2, fe); ssq[m] = __builtin_amdgcn_fdot2_f32_bf16(v, v, ssq[m], false); }
        }
      }
    }
  }
}
DI void vm_kmajor(unsigned* vm, unsigned ld2, int col0) {
  const int lane = threadIdx.x & 63, w = wave_id();
#pragma unroll
  for (int r = 0; r < 2; r++) {
    const int qm = w + 8 * r, k = 4 * qm + (lane >> 4), p16 = lane & 15, f = (k & 3) | (((k >> 3) & 1) << 2);
    vm[r] = k * ld2 + (col0 + (((p16 >> 1) ^ f) * 16) + (p16 & 1) * 8) * 2;
  }
}

__global__ void __launch_bounds__(512) k_prep(const u16* __restrict__ x, const u16* __restrict__ wr, const u16* __restrict__ wi,
                                              const u16* __restrict__ wqkv, const u16* __restrict__ wo, char* __restrict__ ws) {
  extern __shared__ __attribute__((aligned(16))) char smem[];
  const lptr lds = (lptr)smem;
  const int tid = threadIdx.x, lane = tid & 63, w = wave_id(), wm = w & 3, wn = w >> 2, g = lane >> 4, r16 = lane & 15;
  const int xcd = blockIdx.x & 7, bi = blockIdx.x >> 3, nmv = xcd < 4 ? 5 : 4;
  if (bi < 5 * nmv) {
    const int mv = xcd + 8 * (bi / 5), jt5 = bi % 5;
    const char* sbase = (const char*)&g_ft + (size_t)jt5 * 8 * 20 * 1024;
    f32x4 acc[2][4] = {};
    unsigned vm[2];
    if (mv < 16) {
#pragma unroll
      for (int r = 0; r < 2; r++) { const int row = 8 * (w + 8 * r) + (lane >> 3); vm[r] = rowof(128 * mv + row) * 1280 + (((lane & 7) ^ ((row >> 1) & 7)) * 16); }
      gemm_core<8, 8, KCONTIG, 1, false>(lds, (const char*)x, vm, vm, 128, sbase, acc, nullptr);
      float* R = (float*)(ws + WS_R);
      float sq[2] = {0.f, 0.f};
#pragma unroll
      for (int m = 0; m < 2; m++) {
        const int p = 128 * mv + 32 * wm + 16 * m + r16;
#pragma unroll
        for (int i = 0; i < 4; i++) {
          const int j0 = 128 * jt5 + 64 * wn + 32 * (i >> 1) + 8 * g + 4 * (i & 1);
#pragma unroll
          for (int e = 0; e < 4; e++) { const float v = acc[m][i][e]; R[(size_t)(j0 + e) * 2048 + p] = v; if (j0 + e != 1) sq[m] += v * v; }
        }
        sq[m] += __shfl_xor(sq[m], 16); sq[m] += __shfl_xor(sq[m], 32);
      }
      lds_bar();
      LDS_AS float* sl = (LDS_AS float*)lds;
      if (g == 0) { sl[wn * 128 + 32 * wm + r16] = sq[0]; sl[wn * 128 + 32 * wm + 16 + r16] = sq[1]; }
      lds_bar();
      if (tid < 128) ((u16*)(ws + WS_SSQ))[jt5 * 2048 + 128 * mv + tid] = (u16)pk2(sl[tid] + sl[128 + tid], 0.f);
    } else if (mv < 31) {
      const int ct = mv - 16;
      vm_kmajor(vm, 3840, 128 * ct);
      gemm_core<8, 8, KMAJOR, 1, false>(lds, (const char*)wqkv, vm, vm, 64 * 3840, sbase, acc, nullptr);
#pragma unroll
      for (int m = 0; m < 2; m++) {
        const int c = 128 * ct + 32 * wm + 16 * m + r16;
        const int which = c / 640, rem = c - which * 640, head = rem / 20, d = rem - head * 20, grp = d >> 2;
        int T, gs;
        if (grp < 4) { T = (which == 2 ? 2 : 0) + (grp & 1); gs = (which == 1 ? 2 : 0) + (grp >> 1); }
        else { T = (which == 1) ? 3 : 2; gs = (which == 2) ? 3 : 2; }
        const float sc = which == 0 ? 0.22360679774997896f * 1.4426950408889634f : 1.f;
        char* dst = ws + WS_WQ + (size_t)(((head >> 1) * 10 + 4 * (head & 1) + T) * 20) * 1024 + (g * 16 + 4 * gs + (d & 3)) * 16;
#pragma unroll
        for (int ip = 0; ip < 2; ip++) {
          const int ks = 4 * jt5 + 2 * wn + ip;
          f32x4 a = acc[m][2 * ip] * sc, b = acc[m][2 * ip + 1] * sc;
          if (ks == 0 && g == 0) a[1] = 0.f;
          *(u32x4*)(dst + ks * 1024) = u32x4{pk2(a[0], a[1]), pk2(a[2], a[3]), pk2(b[0], b[1]), pk2(b[2], b[3])};
        }
      }
    } else {
      const int it = mv - 31;
#pragma unroll
      for (int r = 0; r < 2; r++) {
        const int row = 8 * (w + 8 * r) + (lane >> 3);
        const int kap = 128 * it + 32 * (row >> 5) + 8 * ((row >> 2) & 3) + 4 * ((row >> 4) & 1) + (row & 3);
        vm[r] = kappa_to_i(kap) * 1280 + (((lane & 7) ^ ((row >> 1) & 7)) * 16);
      }
      gemm_core<8, 8, KCONTIG, 2, false>(lds, (const char*)wo, vm, vm, 128, sbase, acc, nullptr);
#pragma unroll
      for (int i = 0; i < 4; i++) {
        const int j = 128 * jt5 + 64 * wn + 32 * (i >> 1) + 8 * (r16 >> 2) + 4 * (i & 1) + (r16 & 3);
        const f32x4 a = acc[0][i], b = acc[1][i];
        *(u32x4*)(ws + WS_WOF + (size_t)((j >> 4) * 20 + 4 * it + wm) * 1024 + (g * 16 + (j & 15)) * 16) =
            u32x4{pk2(a[0], a[1]), pk2(a[2], a[3]), pk2(b[0], b[1]), pk2(b[2], b[3])};
      }
    }
    return;
  }
  const int mi = (xcd < 4 ? 7 * xcd : 28 + 12 * (xcd - 4)) + bi - 5 * nmv;
  for (int pc = mi * 512 + tid; pc < 640 * 80; pc += 76 * 512) {
    const int n = pc % 640, kq = pc / 640;
    const int u = n / 40, rn = n - 40 * u, hn = rn / 20, dn = rn - 20 * hn, gq = dn >> 2;
    const int T = gq < 4 ? 8 + hn : 4 * hn + 3, gs = gq < 4 ? gq : 3;
    float v[8];
#pragma unroll
    for (int e = 0; e < 4; e++) {
      const int c = 4 * kq + e;
      const float a = (float)((c * n) % 640) * (1.f / 640.f);
      v[2 * e] = 0.055901699437494742f * __builtin_amdgcn_cosf(a);
      v[2 * e + 1] = -0.055901699437494742f * __builtin_amdgcn_sinf(a);
    }
    if (kq == 0) { v[0] = (n & 1) ? -0.039528470752104741f : 0.039528470752104741f; v[1] = 0.039528470752104741f; }
    *(u32x4*)(ws + WS_WQ + (size_t)((u * 10 + T) * 20 + (kq >> 2)) * 1024 + ((kq & 3) * 16 + 4 * gs + (dn & 3)) * 16) =
        u32x4{pk2(v[0], v[1]), pk2(v[2], v[3]), pk2(v[4], v[5]), pk2(v[6], v[7])};
  }
  if (mi < 64) {
    const int m = mi >> 3, oc = mi & 7;
    for (int i = tid; i < 8 * 321; i += 512) {
      const int blk = i / 321, q = i - blk * 321, a = blk >> 2, s2 = 4 * m + (blk & 3);
      const u16* src = (a ? wi : wr) + (size_t)(64 * s2 + 8 * oc) * 321;
      *(LDS_AS u32x4*)(lds + i * 16) = ((const u32x4*)src)[q];
    }
    __syncthreads();
    LDS_AS u16* L = (LDS_AS u16*)lds;
    for (int i = tid; i < 320 * 8; i += 512) {
      const int c = i >> 3, s1 = i & 7, col = c ? c : 320;
      u32x4 o;
#pragma unroll
      for (int e = 0; e < 4; e++) o[e] = (unsigned)L[e * 2568 + s1 * 321 + col] | ((unsigned)L[(4 + e) * 2568 + s1 * 321 + col] << 16);
      *(u32x4*)(ws + WS_WF + (size_t)c * 8192 + (m * 64 + 8 * oc + s1) * 16) = o;
    }
  } else if (mi < 74) {
    const int k0 = 64 * (mi - 64);
    for (int i = tid; i < 64 * 80; i += 512) {
      const int kr = i / 80, q = i - kr * 80;
      *(LDS_AS u32x4*)(lds + kr * 1296 + q * 16) = ((const u32x4*)(wo + (size_t)kappa_to_i(k0 + kr) * 640))[q];
    }
    __syncthreads();
    for (int jb = w; jb < 80; jb += 8) {
      const int ksl = jb / 40, nt = jb - 40 * ksl;
      const lptr a = lds + (32 * ksl + 8 * g + (r16 >> 2)) * 1296 + nt * 32 + (lane & 3) * 8;
      const u32x2 lo = ldtr(a), hi = ldtr(a + 4 * 1296);
      *(u32x4*)(ws + WS_WOP + (size_t)(nt * 20 + (k0 >> 5) + ksl) * 1024 + lane * 16) = u32x4{lo[0], lo[1], hi[0], hi[1]};
    }
  }
}

template <int NPART, bool WRITE_R>
__global__ void __launch_bounds__(512) k_col(char* __restrict__ ws) {
  __shared__ __attribute__((aligned(16))) char smem[8192 + 2 * 16384];
  const lptr lds = (lptr)smem;
  const int tid = threadIdx.x, lane = tid & 63, wv = wave_id(), w = wv & 3, qd = wv >> 2, g = lane >> 4, r16 = lane & 15;
  const int c = 2 * blockIdx.x + qd;
  float* R = (float*)(ws + WS_R) + (size_t)(2 * c) * 2048;
  u32x2 sp[NPART];
#pragma unroll
  for (int i = 0; i < NPART; i++) sp[i] = *(const u32x2*)(ws + WS_SSQ + i * 4096 + tid * 8);
  f32x4 xr[2], xi[2]; u32x4 wf[2];
#pragma unroll
  for (int t = 0; t < 2; t++) {
    const int p0 = 64 * (16 * t + r16) + 16 * w + 4 * g;
    xr[t] = *(const f32x4*)(R + p0); xi[t] = *(const f32x4*)(R + 2048 + p0);
    wf[t] = *(const u32x4*)(ws + WS_WF + (size_t)c * 8192 + ((4 * t + g) * 64 + 16 * w + r16) * 16);
  }
  u32x4 c64[2], s64[2], n64[2], c32[2], s32[2], n32[2];
  float t1c[2][4], t1s[2][4], t2c[2][4], t2s[2][4];
#pragma unroll
  for (int t = 0; t < 2; t++) {
#pragma unroll
    for (int e2 = 0; e2 < 4; e2++) {
      float cc[2], ss[2], dc[2], ds[2];
#pragma unroll
      for (int h = 0; h < 2; h++) {
        const int e = 2 * e2 + h;
        const float a = (float)(((32 * t + 8 * g + e) * (16 * w + r16)) & 63) * (1.f / 64.f);
        cc[h] = __builtin_amdgcn_cosf(a); ss[h] = __builtin_amdgcn_sinf(a);
        const float b = (float)(((16 * t + r16) * (16 * (e >> 2) + 4 * g + (e & 3))) & 31) * (1.f / 32.f);
        dc[h] = __builtin_amdgcn_cosf(b); ds[h] = __builtin_amdgcn_sinf(b);
      }
      c64[t][e2] = pk2(cc[0], cc[1]); s64[t][e2] = pk2(ss[0], ss[1]); n64[t][e2] = s64[t][e2] ^ 0x80008000u;
      c32[t][e2] = pk2(dc[0], dc[1]); s32[t][e2] = pk2(ds[0], ds[1]); n32[t][e2] = s32[t][e2] ^ 0x80008000u;
      const float a1 = (float)((16 * t + 4 * g + e2) * (16 * w + r16)) * (1.f / 2048.f);
      t1c[t][e2] = __builtin_amdgcn_cosf(a1); t1s[t][e2] = __builtin_amdgcn_sinf(a1);
      const float a2 = (float)((16 * t + r16) * (16 * w + 4 * g + e2)) * (1.f / 2048.f);
      t2c[t][e2] = __builtin_amdgcn_cosf(a2) * (1.f / 2048.f); t2s[t][e2] = __builtin_amdgcn_sinf(a2) * (1.f / 2048.f);
    }
  }
  {
    f32x4 s = {0.f, 0.f, 0.f, 0.f};
#pragma unroll
    for (int i = 0; i < NPART; i++) { s[0] += bflo(sp[i][0]); s[1] += bfhi(sp[i][0]); s[2] += bflo(sp[i][1]); s[3] += bfhi(sp[i][1]); }
#pragma unroll
    for (int e = 0; e < 4; e++) s[e] = __builtin_amdgcn_rsqf(s[e] * (1.f / 640.f) + 1e-5f);
    *(LDS_AS f32x4*)(lds + tid * 16) = s;
  }
  __syncthreads();
  const lptr img = lds + 8192 + qd * 16384;
  int wo_[2], ro_[2];
#pragma unroll
  for (int t = 0; t < 2; t++) {
    const int n2 = 16 * t + r16, x = (n2 >> 1) & 7;
    wo_[t] = n2 * 128 + (((2 * w + (g >> 1)) ^ x) * 16) + (g & 1) * 8;
    ro_[t] = n2 * 128 + ((g ^ x) * 16);
    const f32x4 rs = *(LDS_AS f32x4*)(lds + (64 * n2 + 16 * w + 4 * g) * 4);
    const f32x4 a = xr[t] * rs; f32x4 b = xi[t] * rs;
    if (c == 0) b = f32x4{0.f, 0.f, 0.f, 0.f};
    *(LDS_AS u32x2*)(img + wo_[t]) = u32x2{pk2(a[0], a[1]), pk2(a[2], a[3])};
    *(LDS_AS u32x2*)(img + 4096 + wo_[t]) = u32x2{pk2(b[0], b[1]), pk2(b[2], b[3])};
  }
  __syncthreads();
  const f32x4 z4 = {0.f, 0.f, 0.f, 0.f};
  u32x4 br, bi;
  {
#pragma unroll
    for (int t = 0; t < 2; t++) {
      f32x4 ar = z4, ai = z4;
#pragma unroll
      for (int ks = 0; ks < 2; ks++) {
        const u32x4 ur = ld128(img + (ro_[t] ^ (ks * 64))), ui = ld128(img + 4096 + (ro_[t] ^ (ks * 64)));
        ar = mfma16(ur, c64[ks], ar); ai = mfma16(ui, c64[ks], ai);
        ar = mfma16(ui, s64[ks], ar); ai = mfma16(ur, n64[ks], ai);
      }
      float yr[4], yi[4];
#pragma unroll
      for (int e = 0; e < 4; e++) { yr[e] = ar[e] * t1c[t][e] + ai[e] * t1s[t][e]; yi[e] = ai[e] * t1c[t][e] - ar[e] * t1s[t][e]; }
      br[2 * t] = pk2(yr[0], yr[1]); br[2 * t + 1] = pk2(yr[2], yr[3]); bi[2 * t] = pk2(yi[0], yi[1]); bi[2 * t + 1] = pk2(yi[2], yi[3]);
    }
  }
  u32x4 zr, zi;
  {
#pragma unroll
    for (int t = 0; t < 2; t++) {
      f32x4 ar = mfma16(c32[t], br, z4), ai = mfma16(c32[t], bi, z4);
      ar = mfma16(s32[t], bi, ar); ai = mfma16(n32[t], br, ai);
      float yr[4], yi[4];
#pragma unroll
      for (int e = 0; e < 4; e++) { const float a = bflo(wf[t][e]), b = bfhi(wf[t][e]); yr[e] = ar[e] * a - ai[e] * b; yi[e] = ar[e] * b + ai[e] * a; }
      zr[2 * t] = pk2(yr[0], yr[1]); zr[2 * t + 1] = pk2(yr[2], yr[3]); zi[2 * t] = pk2(yi[0], yi[1]); zi[2 * t + 1] = pk2(yi[2], yi[3]);
    }
  }
  {
#pragma unroll
    for (int t = 0; t < 2; t++) {
      f32x4 ar = mfma16(zr, c32[t], z4), ai = mfma16(zi, c32[t], z4);
      ar = mfma16(zi, n32[t], ar); ai = mfma16(zr, s32[t], ai);
      float yr[4], yi[4];
#pragma unroll
      for (int e = 0; e < 4; e++) { yr[e] = ar[e] * t2c[t][e] - ai[e] * t2s[t][e]; yi[e] = ai[e] * t2c[t][e] + ar[e] * t2s[t][e]; }
      *(LDS_AS u32x2*)(img + 8192 + wo_[t]) = u32x2{pk2(yr[0], yr[1]), pk2(yr[2], yr[3])};
      *(LDS_AS u32x2*)(img + 12288 + wo_[t]) = u32x2{pk2(yi[0], yi[1]), pk2(yi[2], yi[3])};
    }
  }
  __syncthreads();
  u16* Rb = (u16*)(ws + WS_RB) + (size_t)(2 * c) * 2048;
#pragma unroll
  for (int t = 0; t < 2; t++) {
    f32x4 ar = xr[t], ai = xi[t];
#pragma unroll
    for (int ks = 0; ks < 2; ks++) {
      const u32x4 ur = ld128(img + 8192 + (ro_[t] ^ (ks * 64))), ui = ld128(img + 12288 + (ro_[t] ^ (ks * 64)));
      ar = mfma16(c64[ks], ur, ar); ai = mfma16(c64[ks], ui, ai);
      ar = mfma16(n64[ks], ui, ar); ai = mfma16(s64[ks], ur, ai);
    }
    if (c == 0) ai = xi[t];
    const int p0 = 64 * (16 * t + r16) + 16 * w + 4 * g;
    if (WRITE_R) { *(f32x4*)(R + p0) = ar; *(f32x4*)(R + 2048 + p0) = ai; }
    *(u32x2*)(Rb + p0) = u32x2{pk2(ar[0], ar[1]), pk2(ar[2], ar[3])};
    *(u32x2*)(Rb + 2048 + p0) = u32x2{pk2(ai[0], ai[1]), pk2(ai[2], ai[3])};
  }
}

template <int NTOT>
__global__ void __launch_bounds__(512) k_qkv(char* __restrict__ ws) {
  extern __shared__ __attribute__((aligned(16))) char smem[];
  const lptr lds = (lptr)smem;
  using C = GCfg<8, NTOT>;
  const int tid = threadIdx.x, lane = tid & 63, w = wave_id(), wm = w & 3, wn = w >> 2, g = lane >> 4, r16 = lane & 15;
  const int xcd = blockIdx.x & 7, bi = blockIdx.x >> 3, u = 8 * (xcd & 1) + (bi & 7), qt = 4 * (xcd >> 1) + (bi >> 3);
  f32x4 acc[2][C::NT] = {};
  float ssq[2] = {0.f, 0.f};
  unsigned vm[2];
  vm_kmajor(vm, 4096, 128 * qt);
  gemm_core<8, NTOT, KMAJOR, 1, true>(lds, ws + WS_RB, vm, vm, 64 * 4096, ws + WS_WQ + (size_t)u * 10 * 20 * 1024, acc, ssq);
#pragma unroll
  for (int m = 0; m < 2; m++) { ssq[m] += __shfl_xor(ssq[m], 16); ssq[m] += __shfl_xor(ssq[m], 32); }
  lds_bar();
  LDS_AS float* sl = (LDS_AS float*)lds;
  if (g == 0) { sl[wn * 128 + 32 * wm + r16] = ssq[0]; sl[wn * 128 + 32 * wm + 16 + r16] = ssq[1]; }
  lds_bar();
  const int head = 2 * u + wn;
  char* qb = ws + WS_Q + (size_t)(head * 64 + 4 * qt + wm) * 1280;
  char* kvb = ws + WS_KV + (size_t)(head * 16 + qt) * KVC;
#pragma unroll
  for (int m = 0; m < 2; m++) {
    const int pl = 32 * wm + 16 * m + r16, pr = 16 * m + r16;
    const float rs = __builtin_amdgcn_rsqf((sl[pl] + sl[128 + pl]) * (1.f / 640.f) + 1e-5f);
    const f32x4 a0 = acc[m][0] * rs, a1 = acc[m][1] * rs, a2 = acc[m][2] * rs, a3 = acc[m][3] * rs;
    char* d0 = ((g >> 1) ? kvb + wm * 1280 : qb) + (g & 1) * 512 + pr * 16;
    *(u32x4*)d0 = u32x4{pk2(a0[0], a0[1]), pk2(a0[2], a0[3]), pk2(a1[0], a1[1]), pk2(a1[2], a1[3])};
    u32x4 v = u32x4{pk2(a2[0], a2[1]), pk2(a2[2], a2[3]), pk2(a3[0], a3[1]), pk2(a3[2], a3[3])};
    if (g == 2) {
      *(u32x2*)(qb + 1024 + pr * 8) = u32x2{v[0], v[1]};
      *(u32x2*)(kvb + wm * 1280 + 1024 + pr * 8) = u32x2{v[2], v[3]};
    } else {
      if (g == 3) { v[2] = 0x3F80u; v[3] = 0u; }
      *(u32x4*)(kvb + 5120 + pl * 48 + (g == 3 ? 32 : g * 16)) = v;
    }
    if (NTOT == 10) {
      float* x4 = (float*)(ws + WS_X4) + (size_t)(128 * qt + pl) * 640 + 40 * u + 20 * wn;
      *(f32x4*)(x4 + 4 * g) = acc[m][C::NT - 1];
      if (g == 3) *(f32x4*)(x4 + 16) = acc[m][3];
    }
  }
}

#define SB() __builtin_amdgcn_sched_barrier(0)
#define PIN(x) asm volatile("" : "+v"(x))
DI f32x16 mfma32(u32x4 a, u32x4 b, f32x16 c) {
  return __builtin_amdgcn_mfma_f32_32x32x16_bf16(__builtin_bit_cast(bf16x8, a), __builtin_bit_cast(bf16x8, b), c, 0, 0, 0);
}
DI f32x16 mfma32k8(u32x2 a, u32x2 b, f32x16 c) {
  return __builtin_amdgcn_mfma_f32_32x32x8bf16_1k(__builtin_bit_cast(s16x4, a), __builtin_bit_cast(s16x4, b), c, 0, 0, 0);
}
DI float max16(const f32x16& s) {
  float m = fmaxf(s[0], s[1]);
#pragma unroll
  for (int i = 2; i < 16; i++) m = fmaxf(m, s[i]);
  return m;
}
constexpr int ANS = 6;
__global__ void __launch_bounds__(256, 2) k_attn(char* __restrict__ ws) {
  extern __shared__ __attribute__((aligned(16))) char smem[];
  const lptr lds = (lptr)smem;
  const int lane = threadIdx.x & 63, w = wave_id(), h = lane >> 5;
  const int xcd = blockIdx.x & 7, bi = blockIdx.x >> 3, head = 4 * xcd + (bi & 3), qt = bi >> 2;
  const char* kvg = ws + WS_KV + (size_t)head * 16 * KVC;
  const unsigned l0 = lds_addr(lds);
  const char* qb = ws + WS_Q + (size_t)(head * 64 + 4 * qt + w) * 1280;
  u32x4 q1 = *(const u32x4*)(qb + lane * 16);
  u32x2 q2 = *(const u32x2*)(qb + 1024 + (lane & 31) * 8);
  if (h) q2 = u32x2{0u, 0u};
  const int p2 = w < 3 ? w + 8 : 10;
  auto issue = [&](int c, int slot) {
    const char* src = kvg + (size_t)c * KVC; const unsigned d = l0 + slot * KVC;
    glds16(lane * 16 + w * 1024, src, d + w * 1024);
    glds16(lane * 16 + (w + 4) * 1024, src, d + (w + 4) * 1024);
    glds16(lane * 16 + p2 * 1024, src, d + p2 * 1024);
  };
  const int ko1 = lane * 16, ko2 = 1024 + (lane & 31) * 8;
  const int vo = 5120 + (4 * h + ((lane & 15) >> 2)) * 48 + ((lane >> 4) & 1) * 32 + (lane & 3) * 8;
  f32x16 o;
  float mtrue = 0.f;
  for (int attempt = 0;; ++attempt) {
    issue(0, 0);
    PIN(q1); PIN(q2);
#pragma unroll
    for (int c = 1; c < ANS - 1; c++) issue(c, c);
    wait_bar<3 * (ANS - 2)>();
    f32x16 sa, sb, ci;
    u32x4 k1; u32x2 k2;
#pragma unroll
    for (int i = 0; i < 16; i++) { o[i] = 0.f; ci[i] = 0.f; }
    sa = mfma32k8(ld64(lds + ko2), q2, mfma32(ld128(lds + ko1), q1, ci));
    {
      float m = mtrue;
      if (attempt == 0) { m = max16(sa); m = fmaxf(m, __shfl_xor(m, 32)); }
#pragma unroll
      for (int i = 0; i < 16; i++) { sa[i] -= m; ci[i] = -m; }
    }
    PIN(ci);
    k1 = ld128(lds + 1280 + ko1); k2 = ld64(lds + 1280 + ko2);
    int cur = 0;
#define TILE(SC, SN, VP, KN)                                                                              \
  {                                                                                                       \
    const u32x2 v0 = ldtr(VP), v1 = ldtr((VP) + 384), v2 = ldtr((VP) + 768), v3 = ldtr((VP) + 1152);     \
    SB();                                                                                                 \
    SN = mfma32(k1, q1, ci);                                                                              \
    _Pragma("unroll") for (int i = 0; i < 4; i++) SC[i] = __builtin_amdgcn_exp2f(SC[i]);                  \
    PIN(SC); SB();                                                                                        \
    SN = mfma32k8(k2, q2, SN);                                                                            \
    _Pragma("unroll") for (int i = 4; i < 8; i++) SC[i] = __builtin_amdgcn_exp2f(SC[i]);                  \
    PIN(SC); SB();                                                                                        \
    k1 = ld128((KN) + ko1); k2 = ld64((KN) + ko2);                                                        \
    u32x4 p0 = u32x4{pk2(SC[0], SC[1]), pk2(SC[2], SC[3]), pk2(SC[4], SC[5]), pk2(SC[6], SC[7])};         \
    PIN(p0);                                                                                              \
    _Pragma("unroll") for (int i = 8; i < 10; i++) SC[i] = __builtin_amdgcn_exp2f(SC[i]);                 \
    PIN(SC); SB();                                                                                        \
    o = mfma32(u32x4{v0[0], v0[1], v1[0], v1[1]}, p0, o);                                                 \
    _Pragma("unroll") for (int i = 10; i < 16; i++) SC[i] = __builtin_amdgcn_exp2f(SC[i]);                \
    u32x4 p1 = u32x4{pk2(SC[8], SC[9]), pk2(SC[10], SC[11]), pk2(SC[12], SC[13]), pk2(SC[14], SC[15])};   \
    PIN(p1); SB();                                                                                        \
    o = mfma32(u32x4{v2[0], v2[1], v3[0], v3[1]}, p1, o);                                                 \
    SB();                                                                                                 \
  }
    for (int c = 0; c < 16; c++) {
      const int nxt = cur == ANS - 1 ? 0 : cur + 1, prv = cur == 0 ? ANS - 1 : cur - 1;
      if (c <= 16 - ANS) { wait_bar<3 * (ANS - 3)>(); issue(c + ANS - 1, prv); }
      else if (c == 16 - ANS + 1) wait_bar<0>();
      const lptr a = lds + cur * KVC, b = lds + nxt * KVC;
      TILE(sa, sb, a + vo, a + 2560)
      TILE(sb, sa, a + vo + 1536, a + 3840)
      TILE(sa, sb, a + vo + 3072, b)
      TILE(sb, sa, a + vo + 4608, b + 1280)
      cur = nxt;
    }
#undef TILE
    if (attempt) break;
    float chk = 0.f;
#pragma unroll
    for (int i = 0; i < 12; i++) chk = fmaf(o[i], 0.f, chk);
    const bool bad = !(chk == 0.f) || (h && !(o[8] < 0x1p60f));
    if (!__syncthreads_or(bad)) break;
    float m = -INFINITY;
    f32x16 z;
#pragma unroll
    for (int i = 0; i < 16; i++) z[i] = 0.f;
    for (int t = 0; t < 64; t++) {
      const char* kb = kvg + (size_t)(t >> 2) * KVC + (t & 3) * 1280;
      const f32x16 s = mfma32k8(*(const u32x2*)(kb + ko2), q2, mfma32(*(const u32x4*)(kb + ko1), q1, z));
      m = fmaxf(m, max16(s));
    }
    mtrue = fmaxf(m, __shfl_xor(m, 32));
  }
  const float l = __shfl(o[8], (lane & 31) + 32);
  const float il = 1.f / l;
  char* ob = ws + WS_O + (size_t)(head * 64 + 4 * qt + w) * 1280;
  *(u32x4*)(ob + lane * 16) = u32x4{pk2(o[0] * il, o[1] * il), pk2(o[2] * il, o[3] * il), pk2(o[4] * il, o[5] * il), pk2(o[6] * il, o[7] * il)};
  if (!h) *(u32x2*)(ob + 1024 + lane * 8) = u32x2{pk2(o[8] * il, o[9] * il), pk2(o[10] * il, o[11] * il)};
}

template <bool FINAL>
__global__ void __launch_bounds__(512) k_oproj(char* __restrict__ ws, u16* __restrict__ out) {
  extern __shared__ __attribute__((aligned(16))) char smem[];
  const lptr lds = (lptr)smem;
  const int tid = threadIdx.x, lane = tid & 63, w = wave_id(), wm = w & 3, wn = w >> 2, g = lane >> 4, r16 = lane & 15;
  const int xcd = blockIdx.x & 7, bi = blockIdx.x >> 3, pt = 8 * (xcd >> 1) + (bi & 7), ct = 4 * (xcd & 1) + (bi >> 3);
  f32x4 acc[1][3] = {}, res[3];
  float* R = (float*)(ws + WS_R);
#pragma unroll
  for (int i = 0; i < 3; i++) {
    const int T = (wn && i == 2) ? 4 : 3 * wn + i;
    if (FINAL) res[i] = *(const f32x4*)((const float*)(ws + WS_X4) + (size_t)(64 * pt + 16 * wm + r16) * 640 + 80 * ct + 16 * T + 4 * g);
    else res[i] = *(const f32x4*)(R + (size_t)(80 * ct + 16 * T + r16) * 2048 + 64 * pt + 16 * wm + 4 * g);
  }
  unsigned vm[1], vt[1];
  vm[0] = ((w >> 1) * 64 + 2 * pt + (w & 1)) * 1280 + lane * 16;
  { const int idx = 4 * w + g; vt[0] = ((idx >> 1) * 64 + 2 * pt + (idx & 1)) * 1280 + 1024 + r16 * 16; }
  gemm_core<4, 5, OBLK, FINAL ? 1 : 2, false>(lds, ws + WS_O, vm, vt, 4 * 64 * 1280, ws + (FINAL ? WS_WOP : WS_WOF) + (size_t)ct * 5 * 20 * 1024, acc, nullptr);
#pragma unroll
  for (int i = 0; i < 3; i++) acc[0][i] += res[i];
  if (FINAL) {
    u16* o = out + (size_t)rowof(64 * pt + 16 * wm + r16) * 640 + 80 * ct + 4 * g;
#pragma unroll
    for (int i = 0; i < 3; i++)
      if (i < 2 || wn == 0) { const f32x4 a = acc[0][i]; *(u32x2*)(o + 16 * (3 * wn + i)) = u32x2{pk2(a[0], a[1]), pk2(a[2], a[3])}; }
  } else {
    f32x4 sq = {0.f, 0.f, 0.f, 0.f};
#pragma unroll
    for (int i = 0; i < 3; i++)
      if (i < 2 || wn == 0) {
        const int j = 80 * ct + 16 * (3 * wn + i) + r16;
        *(f32x4*)(R + (size_t)j * 2048 + 64 * pt + 16 * wm + 4 * g) = acc[0][i];
        if (j != 1) sq += acc[0][i] * acc[0][i];
      }
#pragma unroll
    for (int e = 0; e < 4; e++) {
#pragma unroll
      for (int o = 1; o < 16; o <<= 1) sq[e] += __shfl_xor(sq[e], o);
    }
    lds_bar();
    LDS_AS float* sl = (LDS_AS float*)lds;
    if (r16 == 0) *(LDS_AS f32x4*)(sl + wn * 64 + 16 * wm + 4 * g) = sq;
    lds_bar();
    if (tid < 64) ((u16*)(ws + WS_SSQ))[ct * 2048 + 64 * pt + tid] = (u16)pk2(sl[tid] + sl[64 + tid], 0.f);
  }
}

#define STOPAT(n)
extern "C" void kernel_launch(void* const* d_in, const int* in_sizes, int n_in, void* d_out, int out_size, void* d_ws, size_t ws_size,
                              hipStream_t stream) {
  const u16* x = (const u16*)d_in[0];
  const u16* wr = (const u16*)d_in[1];
  const u16* wi = (const u16*)d_in[2];
  const u16* wqkv = (const u16*)d_in[4];
  const u16* wo = (const u16*)d_in[5];
  char* ws = (char*)d_ws;
  constexpr int L8 = GCfg<8, 8>::LDS, L10 = GCfg<8, 10>::LDS, L5 = GCfg<4, 5>::LDS, LA = ANS * KVC + 64;
  (void)hipFuncSetAttribute((const void*)k_prep, hipFuncAttributeMaxDynamicSharedMemorySize, L8);
  (void)hipFuncSetAttribute((const void*)k_qkv<8>, hipFuncAttributeMaxDynamicSharedMemorySize, L8);
  (void)hipFuncSetAttribute((const void*)k_qkv<10>, hipFuncAttributeMaxDynamicSharedMemorySize, L10);
  (void)hipFuncSetAttribute((const void*)k_attn, hipFuncAttributeMaxDynamicSharedMemorySize, LA);
  (void)hipFuncSetAttribute((const void*)k_oproj<false>, hipFuncAttributeMaxDynamicSharedMemorySize, L5);
  (void)hipFuncSetAttribute((const void*)k_oproj<true>, hipFuncAttributeMaxDynamicSharedMemorySize, L5);
  k_prep<<<256, 512, L8, stream>>>(x, wr, wi, wqkv, wo, ws);
  STOPAT(1)
  k_col<5, true><<<160, 512, 0, stream>>>(ws);
  STOPAT(2)
  k_qkv<8><<<256, 512, L8, stream>>>(ws);
  STOPAT(3)
  k_attn<<<512, 256, LA, stream>>>(ws);
  STOPAT(4)
  k_oproj<false><<<256, 512, L5, stream>>>(ws, (u16*)d_out);
  STOPAT(5)
  k_col<8, false><<<160, 512, 0, stream>>>(ws);
  STOPAT(6)
  k_qkv<10><<<256, 512, L10, stream>>>(ws);
  STOPAT(7)
  k_attn<<<512, 256, LA, stream>>>(ws);
  STOPAT(8)
  k_oproj<true><<<256, 512, L5, stream>>>(ws, (u16*)d_out);
  STOPAT(9)
}
```

```cpp
#include <hip/hip_runtime.h>
#include <hip/hip_bf16.h>
#include <utility>

typedef unsigned short u16;
typedef __attribute__((ext_vector_type(16))) float f32x16;
typedef __attribute__((ext_vector_type(4))) float f32x4;
typedef __attribute__((ext_vector_type(2))) float f32x2;
typedef __attribute__((ext_vector_type(8))) __bf16 bf16x8;
typedef __attribute__((ext_vector_type(2))) __bf16 bf16x2;
typedef __attribute__((ext_vector_type(4))) short s16x4;
typedef __attribute__((ext_vector_type(4))) unsigned u32x4;
typedef __attribute__((ext_vector_type(2))) unsigned u32x2;
#define DI __device__ __forceinline__
#define LDS_AS __attribute__((address_space(3)))
typedef LDS_AS char* lptr;

constexpr double cx_cos_small(double x) { double x2 = x * x, t = 1, s = 1; for (int i = 1; i < 12; i++) { t *= -x2 / ((2 * i - 1) * (2 * i)); s += t; } return s; }
constexpr double cx_sin_small(double x) { double x2 = x * x, t = x, s = x; for (int i = 1; i < 12; i++) { t *= -x2 / ((2 * i) * (2 * i + 1)); s += t; } return s; }
constexpr double cx_cos640(int m) {
  m %= 640; if (m > 320) m = 640 - m; double sg = 1; if (m > 160) { m = 320 - m; sg = -1; }
  const double w = 6.283185307179586476925 / 640; return sg * (m <= 80 ? cx_cos_small(w * m) : cx_sin_small(w * (160 - m)));
}
constexpr u16 cx_bf16(double d) { float f = (float)d; unsigned u = __builtin_bit_cast(unsigned, f); u += 0x7FFFu + ((u >> 16) & 1u); return (u16)(u >> 16); }
struct Cos640 { u16 v[640]; };
constexpr Cos640 mk_cos() { Cos640 c{}; for (int m = 0; m < 640; m++) c.v[m] = cx_bf16(0.055901699437494742 * cx_cos640(m)); return c; }
constexpr Cos640 COSB = mk_cos();
constexpr u16 fval(int n, int j) {
  int c = j >> 1;
  if (c == 0) { u16 b = cx_bf16(0.039528470752104741); return (j == 0 && (n & 1)) ? (u16)(b | 0x8000) : b; }
  return COSB.v[(c * n + ((j & 1) ? 160 : 0)) % 640];
}
struct FTile { u16 v[20 * 64 * 8]; };
constexpr FTile mk_tile(int jt) {
  FTile t{}; int kk = jt >> 1, T = jt & 1;
  for (int ks = 0; ks < 20; ks++) for (int l = 0; l < 64; l++) {
    int r = l & 15, g = l >> 4, j = 32 * kk + 8 * (r >> 2) + 4 * T + (r & 3);
    for (int e = 0; e < 8; e++) t.v[(ks * 64 + l) * 8 + e] = fval(32 * ks + 8 * g + e, j);
  }
  return t;
}
template <int I> constexpr FTile tile_v = mk_tile(I);
struct FTab { FTile t[40]; };
template <int... I> constexpr FTab mk_tab(std::integer_sequence<int, I...>) { return FTab{{tile_v<I>...}}; }
static __device__ const __attribute__((aligned(1024))) FTab g_ft = mk_tab(std::make_integer_sequence<int, 40>{});

struct CTab { u16 v[10 * 512]; };
constexpr CTab mk_ctab() {
  CTab c{}; u16 cs[64] = {};
  for (int m = 0; m < 64; m++) cs[m] = cx_bf16(cx_cos640(10 * m));
  for (int f = 0; f < 10; f++) for (int l = 0; l < 64; l++) for (int e = 0; e < 8; e++) {
    const int r = l & 15, g = l >> 4;
    const int m = f < 8 ? (8 * g + e) * (16 * (f >> 1) + r) : 2 * r * (16 * (e >> 2) + 4 * g + (e & 3));
    c.v[(f * 64 + l) * 8 + e] = cs[(m + (f & 1) * 48) & 63];
  }
  return c;
}
static __device__ const __attribute__((aligned(1024))) CTab g_ct = mk_ctab();

constexpr int RP = 2048 + 64, RBP = 2048 + 128;
constexpr unsigned WS_FL = 0;
constexpr unsigned WS_R = WS_FL + 4 * 256 * 128;
constexpr unsigned WS_RB = WS_R + 640 * RP * 4;
constexpr unsigned WS_SSQ = WS_RB + 640 * RBP * 2;
constexpr unsigned WS_WF = WS_SSQ + 2048 * 16;
constexpr unsigned WS_WQ = WS_WF + 320 * 8192;
constexpr unsigned WS_WOF = WS_WQ + 16 * 10 * 20 * 1024;
constexpr unsigned WS_WOP = WS_WOF + 40 * 20 * 1024;
constexpr int KVC = 11264;
constexpr unsigned WS_KV = WS_WOP + 40 * 20 * 1024;
constexpr unsigned WS_O = WS_KV + 32 * 16 * KVC;
constexpr unsigned WS_X4 = WS_O + 32 * 64 * 1280;
constexpr unsigned WS_END = WS_X4 + 2048 * 640 * 2;
#define KVSC1 true
#define ACQ
#define OAUX 16
#define COLG 160
#define AUXB 16

#define STAMP(k)
#define MARK(i)
#define SB() __builtin_amdgcn_sched_barrier(0)
typedef __amdgpu_buffer_rsrc_t rsrc_t;
DI rsrc_t mk_rsrc(const void* p, unsigned bytes) { return __builtin_amdgcn_make_buffer_rsrc((void*)p, 0, bytes, 0x00020000); }
template <int AUX> DI void st128(rsrc_t r, unsigned off, u32x4 v) { __builtin_amdgcn_raw_buffer_store_b128(v, r, off, 0, AUX); }
template <int AUX> DI void st64(rsrc_t r, unsigned off, u32x2 v) { __builtin_amdgcn_raw_buffer_store_b64(v, r, off, 0, AUX); }
template <int AUX> DI void st32(rsrc_t r, unsigned off, unsigned v) { __builtin_amdgcn_raw_buffer_store_b32(v, r, off, 0, AUX); }
template <int AUX> DI void st16(rsrc_t r, unsigned off, u16 v) { __builtin_amdgcn_raw_buffer_store_b16(v, r, off, 0, AUX); }
template <int AUX> DI u32x4 gl128(rsrc_t r, unsigned off) { return __builtin_amdgcn_raw_buffer_load_b128(r, off, 0, AUX); }
template <int AUX> DI u32x2 gl64(rsrc_t r, unsigned off) { return __builtin_amdgcn_raw_buffer_load_b64(r, off, 0, AUX); }
template <int AUX> DI unsigned gl32(rsrc_t r, unsigned off) { return __builtin_amdgcn_raw_buffer_load_b32(r, off, 0, AUX); }
DI u32x4 asu(f32x4 v) { return __builtin_bit_cast(u32x4, v); }
DI f32x4 asf(u32x4 v) { return __builtin_bit_cast(f32x4, v); }
DI unsigned pk2(float a, float b) { f32x2 v = {a, b}; return __builtin_bit_cast(unsigned, __builtin_convertvector(v, bf16x2)); }
DI u32x2 pk4(f32x4 a) { return u32x2{pk2(a[0], a[1]), pk2(a[2], a[3])}; }
DI u32x4 pk8(f32x4 a, f32x4 b) { return u32x4{pk2(a[0], a[1]), pk2(a[2], a[3]), pk2(b[0], b[1]), pk2(b[2], b[3])}; }
DI float bflo(unsigned u) { return __uint_as_float(u << 16); }
DI float bfhi(unsigned u) { return __uint_as_float(u & 0xFFFF0000u); }
DI float dot2(unsigned a, unsigned b, float c) { return __builtin_amdgcn_fdot2_f32_bf16(__builtin_bit_cast(bf16x2, a), __builtin_bit_cast(bf16x2, b), c, false); }
DI int rowof(int p) { return 32 * (((p >> 4) & 0x3C) + (p & 3)) + ((p >> 6) & 16) + ((p >> 2) & 15); }
DI unsigned ssq_off(int p) { return WS_SSQ + ((p & 3) * 512 + (p >> 2)) * 16; }
template <int CTRL> DI float dpp_add(float v) { return v + __int_as_float(__builtin_amdgcn_update_dpp(0, __float_as_int(v), CTRL, 0xF, 0xF, true)); }
DI float row16_sum(float v) { v = dpp_add<0x128>(v); v = dpp_add<0x124>(v); v = dpp_add<0x122>(v); return dpp_add<0x121>(v); }
DI f32x4 mfma16(u32x4 a, u32x4 b, f32x4 c) {
  return __builtin_amdgcn_mfma_f32_16x16x32_bf16(__builtin_bit_cast(bf16x8, a), __builtin_bit_cast(bf16x8, b), c, 0, 0, 0);
}
DI void mfma16v(f32x4& c, u32x4 a, u32x4 b) { asm volatile("v_mfma_f32_16x16x32_bf16 %0, %1, %2, %0" : "+v"(c) : "v"(a), "v"(b)); }
DI u32x2 ldtr(lptr p) { return __builtin_bit_cast(u32x2, __builtin_amdgcn_ds_read_tr16_b64_v4i16((LDS_AS s16x4*)p)); }
DI u32x4 ld128(lptr p) { return *(LDS_AS u32x4*)p; }
DI u32x2 ld64(lptr p) { return *(LDS_AS u32x2*)p; }
#define GL(v, s, o, x) "global_load_lds_dwordx4 " v ", " s " offset:" #o x "\n\t"
#define M0SET "s_mov_b32 %[sv], m0\n\ts_mov_b32 m0, %[l]\n\ts_nop 2\n\t"
#define M0ADD(x) "s_add_u32 m0, m0, " x "\n\ts_nop 0\n\t"
#define M0RST "s_mov_b32 m0, %[sv]"
DI void dma2(unsigned v, const char* sb, unsigned l) { unsigned sv;
  asm volatile(M0SET GL("%[v]", "%[s]", 0, "") GL("%[v]", "%[s]", 1024, "") M0RST : [sv] "=&s"(sv) : [v] "v"(v), [s] "s"(sb), [l] "s"(l) : "memory"); }
DI void dma4(unsigned v, const char* sb, unsigned l) { unsigned sv;
  asm volatile(M0SET GL("%[v]", "%[s]", 0, "") GL("%[v]", "%[s]", 1024, "") GL("%[v]", "%[s]", 2048, "") GL("%[v]", "%[s]", 3072, "") M0RST
               : [sv] "=&s"(sv) : [v] "v"(v), [s] "s"(sb), [l] "s"(l) : "memory"); }
DI void dma6_sc1(unsigned v, const char* sb, unsigned l) { unsigned sv;
  asm volatile(M0SET GL("%[v]", "%[s]", 0, " sc1") GL("%[v]", "%[s]", 1024, " sc1") GL("%[v]", "%[s]", 2048, " sc1") M0ADD("3072")
               GL("%[w]", "%[s]", 0, " sc1") GL("%[w]", "%[s]", 1024, " sc1") GL("%[w]", "%[s]", 2048, " sc1") M0RST
               : [sv] "=&s"(sv) : [v] "v"(v), [w] "v"(v + 3072), [s] "s"(sb), [l] "s"(l) : "memory", "scc"); }
template <int N> DI void vm_bar() { asm volatile("s_waitcnt vmcnt(%0)\n\ts_barrier" ::"i"(N) : "memory"); }
DI void lds_bar() { asm volatile("s_waitcnt lgkmcnt(0)\n\ts_barrier" ::: "memory"); }
DI void raw_bar() { asm volatile("s_barrier" ::: "memory"); }
DI void drain() { asm volatile("s_waitcnt vmcnt(0)" ::: "memory"); }
DI int wave_id() { return __builtin_amdgcn_readfirstlane(threadIdx.x >> 6); }
DI unsigned lds_addr(lptr p) { return (unsigned)(size_t)p; }

DI int kappa_to_i(int kap) {
  int ks = kap >> 5, g = (kap >> 3) & 3, e = kap & 7;
  if (ks < 16) return (2 * ks + (g >> 1)) * 20 + 8 * (e >> 2) + 4 * (g & 1) + (e & 3);
  return (8 * (ks - 16) + 2 * g + (e >> 2)) * 20 + 16 + (e & 3);
}

DI unsigned wo_frag(int nt, int ks) {
  const int ct = nt / 5, c5 = nt - 5 * ct, kh = ks < 16 ? ks >> 3 : (ks - 16) >> 1, i = ks < 16 ? ks & 7 : 8 + (ks & 1);
  const int a = kh ? (c5 >= 3 ? c5 - 3 : c5 + 2) : c5;
  return ((ct * 2 + kh) * 50 + 5 * i + a) * 1024;
}

enum { KMAJOR = 0, KCONTIG = 1 };
template <int NTOT> struct GCfg { static constexpr int SLOT = (16 + 2 * NTOT) * 1024, NT = NTOT / 2, RS = NTOT / 2, CNT = 4 + RS; };
DI int stile(int wn, int i) { return i < 4 ? 4 * wn + i : 8 + wn; }

template <int NTOT>
DI void gemm_load(lptr lds, const char* mbase, const unsigned* vm, unsigned minc, const char* sbase) {
  using C = GCfg<NTOT>;
  const int lane = threadIdx.x & 63, lw = wave_id() - 8;
  const unsigned l0 = lds_addr(lds) + lw * 4096;
  const unsigned v0 = 2 * lw * 20480 + lane * 16, v1 = v0 + 20480 - 2048, v2 = ((8 + (lw >> 1)) * 20 + (lw & 1)) * 1024 + lane * 16;
  const unsigned x3 = 16384 - lw * 3072;
  mbase -= 3072;
  auto issue = [&](int s) {
    unsigned sv;
    if (NTOT == 8)
      asm volatile(M0SET GL("%[a0]", "%[m]", 0, "") GL("%[a1]", "%[m]", 1024, "") GL("%[a2]", "%[m]", 2048, "") GL("%[a3]", "%[m]", 3072, "") M0ADD("0x4000")
                   GL("%[v0]", "%[s]", 0, "") GL("%[v0]", "%[s]", 1024, "") GL("%[v1]", "%[s]", 2048, "") GL("%[v1]", "%[s]", 3072, "") M0RST
                   : [sv] "=&s"(sv) : [a0] "v"(vm[0]), [a1] "v"(vm[1]), [a2] "v"(vm[2]), [a3] "v"(vm[3]), [v0] "v"(v0), [v1] "v"(v1),
                     [m] "s"(mbase), [s] "s"(sbase), [l] "s"(l0 + (s & 3) * C::SLOT) : "memory", "scc");
    else
      asm volatile(M0SET GL("%[a0]", "%[m]", 0, "") GL("%[a1]", "%[m]", 1024, "") GL("%[a2]", "%[m]", 2048, "") GL("%[a3]", "%[m]", 3072, "") M0ADD("0x4000")
                   GL("%[v0]", "%[s]", 0, "") GL("%[v0]", "%[s]", 1024, "") GL("%[v1]", "%[s]", 2048, "") GL("%[v1]", "%[s]", 3072, "") M0ADD("%[x3]")
                   GL("%[v2]", "%[s]", 0, "") M0RST
                   : [sv] "=&s"(sv) : [a0] "v"(vm[0]), [a1] "v"(vm[1]), [a2] "v"(vm[2]), [a3] "v"(vm[3]), [v0] "v"(v0), [v1] "v"(v1), [v2] "v"(v2),
                     [m] "s"(mbase), [s] "s"(sbase), [l] "s"(l0 + (s & 3) * C::SLOT), [x3] "s"(x3) : "memory", "scc");
    mbase += minc; sbase += 2048;
  };
#pragma unroll 1
  for (int s = 0; s < 10; s++) { if (s >= 3) vm_bar<2 * C::CNT>(); issue(s); }
  vm_bar<2 * C::CNT>();
  vm_bar<C::CNT>();
  vm_bar<0>();
}
template <int NTOT, int KIND, int ORI, bool SSQ>
DI void gemm_mma(lptr lds, f32x4 (&acc)[2][GCfg<NTOT>::NT], f32x4& ssq) {
  using C = GCfg<NTOT>;
  const int lane = threadIdx.x & 63, w = wave_id(), wm = w & 3, wn = w >> 2, g = lane >> 4, r16 = lane & 15;
  int mo[2][2];
#pragma unroll
  for (int m = 0; m < 2; m++) {
    const int t = 2 * wm + (SSQ ? (m ^ wn) : m);
    if (KIND == KMAJOR) { const int q = r16 >> 2; mo[m][0] = (8 * g + q) * 256 + ((t ^ (q | ((g & 1) << 2))) * 32) + (lane & 3) * 8; mo[m][1] = mo[m][0] + 8192; }
    else { mo[m][0] = (16 * t + r16) * 128 + ((g ^ (r16 >> 1)) * 16); mo[m][1] = mo[m][0] ^ 64; }
  }
  const int so = 16384 + lane * 16 + wn * 8192, so5 = 16384 + lane * 16 + (8 + wn) * 2048;
  u32x4 mf[2][2], sf[2][C::NT];
  auto rdm = [&](lptr sl, int k, int m) {
    if (KIND == KMAJOR) { const u32x2 a = ldtr(sl + mo[m][k]), b = ldtr(sl + mo[m][k] + 1024); mf[k][m] = u32x4{a[0], a[1], b[0], b[1]}; }
    else mf[k][m] = ld128(sl + mo[m][k]);
  };
  auto rds = [&](lptr sl, int k, int i) { sf[k][i] = ld128(sl + (i < 4 ? so + i * 2048 : so5) + k * 1024); };
  auto step = [&](int k, lptr nx, bool rd) {
#pragma unroll
    for (int i = 0; i < C::NT; i++) {
      if (rd) {
        if (i == 0) { rdm(nx, k ^ 1, 0); rds(nx, k ^ 1, 0); }
        if (i == 1) { rdm(nx, k ^ 1, 1); rds(nx, k ^ 1, 1); }
        if (i == 2) { rds(nx, k ^ 1, 2); rds(nx, k ^ 1, 3); if (C::NT > 4) rds(nx, k ^ 1, 4); }
      }
      SB();
#pragma unroll
      for (int m = 0; m < 2; m++) { if (ORI == 1) mfma16v(acc[m][i], sf[k][i], mf[k][m]); else mfma16v(acc[m][i], mf[k][m], sf[k][i]); }
      if (SSQ && i == C::NT - 1) mfma16v(ssq, mf[k][0], mf[k][0]);
      SB();
    }
  };
  if (SSQ) asm volatile("" : "+v"(ssq));
#pragma unroll
  for (int i = 0; i < C::NT; i++) asm volatile("" : "+v"(acc[0][i]), "+v"(acc[1][i]));
  raw_bar();
  rdm(lds, 0, 0); rdm(lds, 0, 1);
#pragma unroll
  for (int i = 0; i < C::NT; i++) rds(lds, 0, i);
  for (int s = 0; s < 9; s++) {
    step(0, lds + (s & 3) * C::SLOT, true);
    lds_bar();
    step(1, lds + ((s + 1) & 3) * C::SLOT, true);
  }
  step(0, lds + C::SLOT, true);
  step(1, lds, false);
  asm volatile("s_nop 7\n\ts_nop 7" ::: "memory");
  SB();
}
DI void vm_kmajor(unsigned* vm, unsigned ld2, int col0) {
  const int lane = threadIdx.x & 63, lw = wave_id() - 8;
#pragma unroll
  for (int r = 0; r < 4; r++) {
    const int k = 4 * (4 * lw + r) + (lane >> 4), p16 = lane & 15, f = (k & 3) | (((k >> 3) & 1) << 2);
    vm[r] = k * ld2 + (col0 + (((p16 >> 1) ^ f) * 16) + (p16 & 1) * 8) * 2 + 3072 - 1024 * r;
  }
}

constexpr int LDS_PREP = 4 * GCfg<8>::SLOT + 1024;
__global__ void __launch_bounds__(768) k_prep(const u16* __restrict__ x, const u16* __restrict__ wr, const u16* __restrict__ wi,
                                              const u16* __restrict__ wqkv, const u16* __restrict__ wo, char* __restrict__ ws) {
  STAMP(0);
  extern __shared__ __attribute__((aligned(16))) char smem[];
  const lptr lds = (lptr)smem;
  const rsrc_t rw = mk_rsrc(ws, WS_END);
  const int tid = threadIdx.x, lane = tid & 63, w = wave_id(), wm = w & 3, wn = w >> 2, g = lane >> 4, r16 = lane & 15;
  const int xcd = blockIdx.x & 7, bi = blockIdx.x >> 3, nmv = xcd < 4 ? 5 : 4;
  if (bi < 5 * nmv) {
    const int mv = xcd + 8 * (bi / 5), jt5 = bi % 5;
    if (__builtin_expect(w >= 8, 1)) {
      const char* sbase = (const char*)&g_ft + (size_t)jt5 * 8 * 20 * 1024;
      unsigned vm[4]; const char* mb; unsigned inc = 128;
      if (mv >= 16 && mv < 31) { vm_kmajor(vm, 3840, 128 * (mv - 16)); mb = (const char*)wqkv; inc = 64 * 3840; }
      else {
        mb = (const char*)(mv < 16 ? x : wo);
#pragma unroll
        for (int r = 0; r < 4; r++) {
          const int row = 8 * (4 * (w - 8) + r) + (lane >> 3);
          const int kap = 128 * (mv - 31) + 32 * (row >> 5) + 8 * ((row >> 2) & 3) + 4 * ((row >> 4) & 1) + (row & 3);
          vm[r] = (mv < 16 ? rowof(128 * mv + row) : kappa_to_i(kap)) * 1280 + (((lane & 7) ^ ((row >> 1) & 7)) * 16) + 3072 - 1024 * r;
        }
      }
      gemm_load<8>(lds, mb, vm, inc, sbase);
      return;
    }
    f32x4 acc[2][4] = {};
    if (mv < 16) {
      gemm_mma<8, KCONTIG, 1, false>(lds, acc, acc[0][0]);
      MARK(0);
      float sq[2] = {0.f, 0.f};
#pragma unroll
      for (int m = 0; m < 2; m++) {
        const int p = 128 * mv + 32 * wm + 16 * m + r16;
#pragma unroll
        for (int i = 0; i < 4; i++) {
          const int j0 = 128 * jt5 + 64 * wn + 32 * (i >> 1) + 8 * g + 4 * (i & 1);
#pragma unroll
          for (int e = 0; e < 4; e++) { const float v = acc[m][i][e]; st32<AUXB>(rw, WS_R + ((j0 + e) * RP + p) * 4, __float_as_uint(v)); if (j0 + e != 1) sq[m] += v * v; }
        }
        sq[m] += __shfl_xor(sq[m], 16); sq[m] += __shfl_xor(sq[m], 32);
      }
      LDS_AS float* sl = (LDS_AS float*)(lds + 4 * GCfg<8>::SLOT);
      if (g == 0) { sl[wn * 128 + 32 * wm + r16] = sq[0]; sl[wn * 128 + 32 * wm + 16 + r16] = sq[1]; }
      lds_bar();
      if (tid < 128) {
        const unsigned v = pk2(sl[tid] + sl[128 + tid], 0.f), o = ssq_off(128 * mv + tid) + jt5 * 2;
        if (jt5 < 4) st16<AUXB>(rw, o, (u16)v); else st64<AUXB>(rw, o, u32x2{v, 0u});
      }
    } else if (mv < 31) {
      gemm_mma<8, KMAJOR, 1, false>(lds, acc, acc[0][0]);
      MARK(0);
#pragma unroll
      for (int m = 0; m < 2; m++) {
        const int c = 128 * (mv - 16) + 32 * wm + 16 * m + r16;
        const int which = c / 640, rem = c - which * 640, head = rem / 20, d = rem - head * 20, grp = d >> 2;
        int T, gs;
        if (grp < 4) { T = (which == 2 ? 2 : 0) + (grp & 1); gs = (which == 1 ? 2 : 0) + (grp >> 1); }
        else { T = (which == 1) ? 3 : 2; gs = (which == 2) ? 3 : 2; }
        const float sc = which == 0 ? 0.22360679774997896f * 1.4426950408889634f : 1.f;
        const unsigned dst = WS_WQ + (((head >> 1) * 10 + 4 * (head & 1) + T) * 20) * 1024 + (g * 16 + 4 * gs + (d & 3)) * 16;
#pragma unroll
        for (int ip = 0; ip < 2; ip++) {
          const int ks = 4 * jt5 + 2 * wn + ip;
          f32x4 a = acc[m][2 * ip] * sc, b = acc[m][2 * ip + 1] * sc;
          if (ks == 0 && g == 0) a[1] = 0.f;
          st128<AUXB>(rw, dst + ks * 1024, pk8(a, b));
        }
      }
    } else {
      gemm_mma<8, KCONTIG, 2, false>(lds, acc, acc[0][0]);
      MARK(0);
#pragma unroll
      for (int i = 0; i < 4; i++) {
        const int j = 128 * jt5 + 64 * wn + 32 * (i >> 1) + 8 * (r16 >> 2) + 4 * (i & 1) + (r16 & 3);
        st128<AUXB>(rw, WS_WOF + wo_frag(j >> 4, 4 * (mv - 31) + wm) + (g * 16 + (j & 15)) * 16, pk8(acc[0][i], acc[1][i]));
      }
    }
    return;
  }
  const int mi = (xcd < 4 ? 7 * xcd : 28 + 12 * (xcd - 4)) + bi - 5 * nmv;
  if (mi * 768 + tid < 8192) st128<AUXB>(rw, WS_FL + (mi * 768 + tid) * 16, u32x4{0u, 0u, 0u, 0u});
  {
    const int pc = mi * 768 + tid;
    if (pc < 640 * 80) {
      const int n = pc % 640, kq = pc / 640;
      const int u = n / 40, rn = n - 40 * u, hn = rn / 20, dn = rn - 20 * hn, gq = dn >> 2;
      const int T = gq < 4 ? 8 + hn : 4 * hn + 3, gs = gq < 4 ? gq : 3;
      float v[8];
#pragma unroll
      for (int e = 0; e < 4; e++) {
        const float a = (float)(((4 * kq + e) * n) % 640) * (1.f / 640.f);
        v[2 * e] = 0.055901699437494742f * __builtin_amdgcn_cosf(a);
        v[2 * e + 1] = -0.055901699437494742f * __builtin_amdgcn_sinf(a);
      }
      if (kq == 0) { v[0] = (n & 1) ? -0.039528470752104741f : 0.039528470752104741f; v[1] = 0.039528470752104741f; }
      st128<AUXB>(rw, WS_WQ + ((u * 10 + T) * 20 + (kq >> 2)) * 1024 + ((kq & 3) * 16 + 4 * gs + (dn & 3)) * 16,
                  u32x4{pk2(v[0], v[1]), pk2(v[2], v[3]), pk2(v[4], v[5]), pk2(v[6], v[7])});
    }
  }
  if (mi < 64) {
    const int m = mi >> 3, oc = mi & 7;
#pragma unroll
    for (int it = 0; it < 4; it++) {
      const int i = tid + 768 * it;
      if (i < 8 * 321) {
        const int blk = i / 321, q = i - blk * 321, a = blk >> 2, s2 = 4 * m + (blk & 3);
        const u16* src = (a ? wi : wr) + (size_t)(64 * s2 + 8 * oc) * 321;
        *(LDS_AS u32x4*)(lds + i * 16) = ((const u32x4*)src)[q];
      }
    }
    __syncthreads();
    LDS_AS u16* L = (LDS_AS u16*)lds;
    for (int i = tid; i < 320 * 8; i += 768) {
      const int c = i >> 3, s1 = i & 7, col = c ? c : 320;
      u32x4 o;
#pragma unroll
      for (int e = 0; e < 4; e++) o[e] = (unsigned)L[e * 2568 + s1 * 321 + col] | ((unsigned)L[(4 + e) * 2568 + s1 * 321 + col] << 16);
      st128<AUXB>(rw, WS_WF + c * 8192 + (m * 64 + 8 * oc + s1) * 16, o);
    }
  } else if (mi < 74) {
    const int k0 = 64 * (mi - 64);
#pragma unroll
    for (int it = 0; it < 7; it++) {
      const int i = tid + 768 * it;
      if (i < 64 * 80) {
        const int kr = i / 80, q = i - kr * 80;
        *(LDS_AS u32x4*)(lds + kr * 1296 + q * 16) = ((const u32x4*)(wo + (size_t)kappa_to_i(k0 + kr) * 640))[q];
      }
    }
    __syncthreads();
    for (int jb = w; jb < 80; jb += 12) {
      const int ksl = jb / 40, nt = jb - 40 * ksl;
      const lptr a = lds + (32 * ksl + 8 * g + (r16 >> 2)) * 1296 + nt * 32 + (lane & 3) * 8;
      const u32x2 lo = ldtr(a), hi = ldtr(a + 4 * 1296);
      st128<AUXB>(rw, WS_WOP + wo_frag(nt, (k0 >> 5) + ksl) + lane * 16, u32x4{lo[0], lo[1], hi[0], hi[1]});
    }
  }
}

#define PINF(x) asm volatile("" : "+v"(x))
template <int NM> DI void mix() {
#pragma unroll
  for (int i = 0; i < NM; i++) { __builtin_amdgcn_sched_group_barrier(0x8, 1, 0); __builtin_amdgcn_sched_group_barrier(0x2, NM == 16 ? 2 : 4, 0); }
}
template <bool FIRST>
__global__ void __launch_bounds__(256, 2) k_col(char* __restrict__ ws) {
  STAMP(FIRST ? 1 : 3);
  __shared__ __attribute__((aligned(16))) char smem[32768];
  const lptr img = (lptr)smem;
  const rsrc_t rw = mk_rsrc(ws, WS_END);
  const int tid = threadIdx.x, lane = tid & 63, w = wave_id(), g = lane >> 4, r16 = lane & 15;
  if (blockIdx.x >= 160) {
    constexpr int NT = FIRST ? 8 : 10;
    const int i0 = ((blockIdx.x - 160) >> 3) * 256 + tid;
    unsigned acc = 0;
#pragma unroll
    for (int it = 0; it < 5; it++) {
      const int i = i0 + 3072 * it;
      if (i < NT * 1280) { const int pu = i / (NT * 160), r = i - pu * (NT * 160); acc ^= gl32<0>(rw, WS_WQ + (8 * (blockIdx.x & 1) + pu) * 204800 + r * 128); }
    }
    asm volatile("" ::"v"(acc));
    return;
  }
  const int c0 = 2 * blockIdx.x;
  f32x4 xr[2][2], xi[2][2]; u32x4 wf[2][2], sp[2][4];
#pragma unroll
  for (int t = 0; t < 2; t++) {
#pragma unroll
    for (int e = 0; e < 4; e++) sp[t][e] = gl128<0>(rw, WS_SSQ + (e * 512 + 256 * t + tid) * 16);
#pragma unroll
    for (int q = 0; q < 2; q++) {
      const unsigned o = WS_R + (2 * (c0 + q) * RP + 4 * tid + 1024 * t) * 4;
      xr[q][t] = asf(gl128<0>(rw, o)); xi[q][t] = asf(gl128<0>(rw, o + RP * 4));
    }
  }
  SB();
  f32x4 t1c[2], t1s[2], t2c[2], t2s[2];
  {
    const float f1 = (float)(16 * w + r16) * (1.f / 2048.f), b1 = (float)(4 * g), f2 = (float)(16 * w + 4 * g) * (1.f / 2048.f), b2 = (float)r16;
#pragma unroll
    for (int t = 0; t < 2; t++) {
#pragma unroll
      for (int e = 0; e < 4; e++) {
        const float a1 = (b1 + (float)(16 * t + e)) * f1, a2 = (b2 + (float)(16 * t)) * (f2 + (float)e * (1.f / 2048.f));
        t1c[t][e] = __builtin_amdgcn_cosf(a1); t1s[t][e] = __builtin_amdgcn_sinf(-a1);
        t2c[t][e] = __builtin_amdgcn_cosf(a2); t2s[t][e] = __builtin_amdgcn_sinf(a2);
      }
      PINF(t1c[t]); PINF(t1s[t]); PINF(t2c[t]); PINF(t2s[t]);
    }
  }
  int wo_ = r16 * 128 + (((2 * w + (g >> 1)) ^ (r16 >> 1)) * 16) + (g & 1) * 8, ro_ = r16 * 128 + ((g ^ (r16 >> 1)) * 16), ro2 = ro_ ^ 64;
  PINF(wo_); PINF(ro_); PINF(ro2);
  SB();
  u32x4 c64[2], s64[2], n64[2], c32[2], s32[2], n32[2];
  const u32x4* ct = (const u32x4*)&g_ct + lane;
  c64[0] = ct[(2 * w) * 64]; s64[0] = ct[(2 * w + 1) * 64]; c32[0] = ct[8 * 64]; s32[0] = ct[9 * 64];
#pragma unroll
  for (int q = 0; q < 2; q++)
#pragma unroll
    for (int t = 0; t < 2; t++) wf[q][t] = gl128<0>(rw, WS_WF + (c0 + q) * 8192 + ((4 * t + g) * 64 + 16 * w + r16) * 16);
  SB();
  MARK(0);
#pragma unroll
  for (int t = 0; t < 2; t++) {
    f32x4 rs = {640.f * 1e-5f, 640.f * 1e-5f, 640.f * 1e-5f, 640.f * 1e-5f};
#pragma unroll
    for (int i = 0; i < 4; i++)
#pragma unroll
      for (int e = 0; e < 4; e++) { const unsigned v = sp[t][e][i]; rs[e] = dot2(v, 0x3F803F80u, rs[e]); }
#pragma unroll
    for (int e = 0; e < 4; e++) rs[e] = __builtin_amdgcn_rsqf(rs[e] * (2048.f * 2048.f / 640.f));
#pragma unroll
    for (int q = 0; q < 2; q++) {
      u32x2 pb = pk4(xi[q][t] * rs);
      if (q == 0 && c0 == 0) pb = u32x2{0u, 0u};
      *(LDS_AS u32x2*)(img + q * 16384 + wo_ + t * 2048) = pk4(xr[q][t] * rs);
      *(LDS_AS u32x2*)(img + q * 16384 + 4096 + wo_ + t * 2048) = pb;
    }
  }
  lds_bar();
  MARK(1);
  u32x4 ur[2][2][2], ui[2][2][2];
  auto rd_in = [&](int off) {
#pragma unroll
    for (int q = 0; q < 2; q++)
#pragma unroll
      for (int ks = 0; ks < 2; ks++)
#pragma unroll
        for (int t = 0; t < 2; t++) { const lptr p = img + off + q * 16384 + (ks ? ro2 : ro_) + t * 2048; ur[q][t][ks] = ld128(p); ui[q][t][ks] = ld128(p + 4096); }
  };
  rd_in(0);
  {
    const unsigned par = (lane & 1) ? 0x80008000u : 0u;
    c64[1] = c64[0] ^ par; s64[1] = s64[0] ^ par; n64[0] = s64[0] ^ 0x80008000u; n64[1] = s64[1] ^ 0x80008000u;
    PINF(c64[1]); PINF(s64[1]); PINF(n64[0]); PINF(n64[1]);
  }
  SB();
  const f32x4 z4 = {0.f, 0.f, 0.f, 0.f};
  f32x4 ar[2][2], ai[2][2];
  u32x4 br[2], bi[2];
  auto pin_acc = [&](int q) { PINF(ar[q][0]); PINF(ai[q][0]); PINF(ar[q][1]); PINF(ai[q][1]); };
  auto st1 = [&](int q) {
#pragma unroll
    for (int ks = 0; ks < 2; ks++) {
#pragma unroll
      for (int t = 0; t < 2; t++) { ar[q][t] = mfma16(ur[q][t][ks], c64[ks], ks ? ar[q][t] : z4); ai[q][t] = mfma16(ui[q][t][ks], c64[ks], ks ? ai[q][t] : z4); }
#pragma unroll
      for (int t = 0; t < 2; t++) { ar[q][t] = mfma16(ui[q][t][ks], s64[ks], ar[q][t]); ai[q][t] = mfma16(ur[q][t][ks], n64[ks], ai[q][t]); }
    }
  };
  auto st2 = [&](int q) {
#pragma unroll
    for (int t = 0; t < 2; t++) { ar[q][t] = mfma16(c32[t], br[q], z4); ai[q][t] = mfma16(c32[t], bi[q], z4); }
#pragma unroll
    for (int t = 0; t < 2; t++) { ar[q][t] = mfma16(s32[t], bi[q], ar[q][t]); ai[q][t] = mfma16(n32[t], br[q], ai[q][t]); }
  };
  auto st3 = [&](int q) {
#pragma unroll
    for (int t = 0; t < 2; t++) { ar[q][t] = mfma16(br[q], c32[t], z4); ai[q][t] = mfma16(bi[q], c32[t], z4); }
#pragma unroll
    for (int t = 0; t < 2; t++) { ar[q][t] = mfma16(bi[q], n32[t], ar[q][t]); ai[q][t] = mfma16(br[q], s32[t], ai[q][t]); }
  };
  auto cmul = [&](int q, const f32x4* c, const f32x4* sn) {
    br[q] = pk8(ar[q][0] * c[0] - ai[q][0] * sn[0], ar[q][1] * c[1] - ai[q][1] * sn[1]);
    bi[q] = pk8(ai[q][0] * c[0] + ar[q][0] * sn[0], ai[q][1] * c[1] + ar[q][1] * sn[1]);
    PINF(br[q]); PINF(bi[q]);
  };
  auto filt = [&](int q) {
    const f32x4 a[2] = {asf(wf[q][0] << 16), asf(wf[q][1] << 16)}, b[2] = {asf(wf[q][0] & 0xFFFF0000u), asf(wf[q][1] & 0xFFFF0000u)};
    cmul(q, a, b);
  };
  auto put = [&](int q) {
#pragma unroll
    for (int t = 0; t < 2; t++) {
      *(LDS_AS u32x2*)(img + q * 16384 + 8192 + wo_ + t * 2048) = u32x2{br[q][2 * t], br[q][2 * t + 1]};
      *(LDS_AS u32x2*)(img + q * 16384 + 12288 + wo_ + t * 2048) = u32x2{bi[q][2 * t], bi[q][2 * t + 1]};
    }
  };
  st1(0);
  c32[1] = c32[0] ^ 0x80000000u; s32[1] = s32[0] ^ 0x80000000u; n32[0] = s32[0] ^ 0x80008000u; n32[1] = s32[0] ^ 0x00008000u;
  PINF(c32[1]); PINF(s32[1]); PINF(n32[0]); PINF(n32[1]);
  mix<16>(); pin_acc(0); SB();
  MARK(4);
  st1(1); cmul(0, t1c, t1s); mix<16>(); pin_acc(1); SB();
  st2(0); cmul(1, t1c, t1s); mix<8>(); pin_acc(0); SB();
  MARK(5);
  st2(1); filt(0); mix<8>(); pin_acc(1); SB();
  MARK(6);
  st3(0); filt(1); mix<8>(); pin_acc(0); SB();
  st3(1); cmul(0, t2c, t2s); put(0); mix<8>(); pin_acc(1); SB();
  cmul(1, t2c, t2s); put(1);
  MARK(7);
  lds_bar();
  MARK(2);
  rd_in(8192);
  SB();
  auto st4 = [&](int q) {
#pragma unroll
    for (int ks = 0; ks < 2; ks++) {
#pragma unroll
      for (int t = 0; t < 2; t++) { ar[q][t] = mfma16(c64[ks], ur[q][t][ks], ks ? ar[q][t] : xr[q][t]); ai[q][t] = mfma16(c64[ks], ui[q][t][ks], ks ? ai[q][t] : xi[q][t]); }
#pragma unroll
      for (int t = 0; t < 2; t++) { ar[q][t] = mfma16(n64[ks], ui[q][t][ks], ar[q][t]); ai[q][t] = mfma16(s64[ks], ur[q][t][ks], ai[q][t]); }
    }
  };
  auto outp = [&](int q) {
#pragma unroll
    for (int t = 0; t < 2; t++) {
      const bool dc = (q == 0 && c0 == 0);
      const unsigned ob = WS_RB + (2 * (c0 + q) * RBP + 4 * tid + 1024 * t) * 2, o = WS_R + (2 * (c0 + q) * RP + 4 * tid + 1024 * t) * 4;
      st64<AUXB>(rw, ob, pk4(ar[q][t]));
      st64<AUXB>(rw, ob + RBP * 2, dc ? u32x2{0u, 0u} : pk4(ai[q][t]));
      if (FIRST) { st128<AUXB>(rw, o, asu(ar[q][t])); st128<AUXB>(rw, dc ? WS_END : o + RP * 4, asu(ai[q][t])); }
    }
  };
  st4(0); pin_acc(0); SB();
  st4(1); outp(0); pin_acc(1); SB();
  outp(1);
  MARK(3);
}

DI f32x16 mfma32(u32x4 a, u32x4 b, f32x16 c) {
  return __builtin_amdgcn_mfma_f32_32x32x16_bf16(__builtin_bit_cast(bf16x8, a), __builtin_bit_cast(bf16x8, b), c, 0, 0, 0);
}
DI f32x16 mfma32k8(u32x2 a, u32x2 b, f32x16 c) {
  return __builtin_amdgcn_mfma_f32_32x32x8bf16_1k(__builtin_bit_cast(s16x4, a), __builtin_bit_cast(s16x4, b), c, 0, 0, 0);
}
DI float max16(const f32x16& s) {
  float m = fmaxf(s[0], s[1]);
#pragma unroll
  for (int i = 2; i < 16; i++) m = fmaxf(m, s[i]);
  return m;
}
#define STR_(x) #x
#define STR(x) STR_(x)
#define KOF(t) ((((t)&7)>>1)*11264+((t)&1)*1280)
#define VOF(t) ((((t)&7)>>1)*11264+5120+((t)&1)*1536)
#define EXP(d, s) "v_exp_f32 v" #d ", v" #s "\n\t"
#define CVT(d, s) "v_cvt_pk_bf16_f32 v" #d ", v" #s ", v[" #s "+1]\n\t"
#define STEPA(t) \
  "v_mfma_f32_32x32x16_bf16 v[48:63], v[80:83], v[136:139], v[120:135]\n\t" \
  "ds_read_b128 v[80:83], v100 offset:" STR(KOF(t+1)) "\n\t" \
  EXP(64,32) EXP(65,33) CVT(78,68) CVT(79,70) EXP(66,34) EXP(67,35) \
  "v_mfma_f32_32x32x16_bf16 v[104:119], v[90:93], v[76:79], v[104:119]\n\t" \
  "ds_read_b64_tr_b16 v[90:91], v102 offset:" STR(VOF(t)+768) "\n\t" \
  "ds_read_b64_tr_b16 v[92:93], v102 offset:" STR(VOF(t)+1152) "\n\t" \
  EXP(68,36) EXP(69,37) CVT(72,64) CVT(73,66) EXP(70,38) EXP(71,39) \
  "v_mfma_f32_32x32x8_bf16 v[48:63], v[84:85], v[140:141], v[48:63]\n\t" \
  "ds_read_b64 v[84:85], v101 offset:" STR(KOF(t+1)) "\n\t" \
  EXP(64,40) EXP(65,41) CVT(74,68) CVT(75,70) EXP(66,42) EXP(67,43) \
  "s_waitcnt lgkmcnt(4)\n\t" \
  "v_mfma_f32_32x32x16_bf16 v[0:15], v[86:89], v[72:75], v[0:15]\n\t" \
  EXP(68,44) EXP(69,45) CVT(76,64) CVT(77,66) EXP(70,46) EXP(71,47)
#define STEPB(t) \
  "s_waitcnt lgkmcnt(0)\n\t" \
  "v_mfma_f32_32x32x16_bf16 v[32:47], v[80:83], v[94:97], v[16:31]\n\t" \
  EXP(64,48) EXP(65,49) CVT(78,68) CVT(79,70) EXP(66,50) EXP(67,51) \
  "v_mfma_f32_32x32x16_bf16 v[0:15], v[90:93], v[76:79], v[0:15]\n\t" \
  EXP(68,52) EXP(69,53) CVT(72,64) CVT(73,66) EXP(70,54) EXP(71,55) \
  "v_mfma_f32_32x32x8_bf16 v[32:47], v[84:85], v[98:99], v[32:47]\n\t" \
  EXP(64,56) EXP(65,57) CVT(74,68) CVT(75,70) EXP(66,58) EXP(67,59) \
  "v_mfma_f32_32x32x16_bf16 v[104:119], v[86:89], v[72:75], v[104:119]\n\t" \
  "ds_read_b64_tr_b16 v[86:87], v102 offset:" STR(VOF(t+1)) "\n\t" \
  "ds_read_b64_tr_b16 v[88:89], v102 offset:" STR(VOF(t+1)+384) "\n\t" \
  EXP(68,60) EXP(69,61) CVT(76,64) CVT(77,66) EXP(70,62) EXP(71,63)
#define PR(n) "s_setprio " #n "\n\t"
#define CHUNK(i) PR(1) STEPA(2*(i)) PR(0) STEPB(2*(i)) "s_barrier\n\t" PR(2) STEPA(2*(i)+1) STEPB(2*(i)+1)
#define Z4(a) "v_mov_b32 v[" #a "], 0\n\tv_mov_b32 v[" #a "+1], 0\n\tv_mov_b32 v[" #a "+2], 0\n\tv_mov_b32 v[" #a "+3], 0\n\t"

constexpr int HR = 4 * KVC;
constexpr int L_A = 2 * HR;
constexpr int L_EX = 15360;
constexpr int L_MISC = L_A + 70 * 1024;
constexpr int LDS_FUSED = L_MISC + 2048;

DI bool poll16(rsrc_t rw, unsigned off, unsigned need) {
  const int lane = threadIdx.x & 63;
#pragma unroll 1
  for (int spins = 0; spins < (1 << 20); spins++) {
    const unsigned v = gl32<16>(rw, off + (lane & 15) * 4);
    const unsigned got = (unsigned)__builtin_amdgcn_ballot_w64(v != 0) & 0xFFFFu;
    if ((got & need) == need) return true;
    __builtin_amdgcn_s_sleep(1);
  }
  return false;
}

template <bool FINAL>
__global__ void __launch_bounds__(768) k_fused(char* __restrict__ ws, u16* __restrict__ out) {
  STAMP(FINAL ? 4 : 2);
  extern __shared__ __attribute__((aligned(16))) char smem[];
  constexpr int NTOT = FINAL ? 10 : 8;
  using C = GCfg<NTOT>;
  const lptr lds = (lptr)smem;
  const rsrc_t rw = mk_rsrc(ws, WS_END);
  const int lane = threadIdx.x & 63, w = wave_id(), g = lane >> 4, r16 = lane & 15;
  const int xcd = blockIdx.x & 7, bi = blockIdx.x >> 3, u = 8 * (xcd & 1) + (bi & 7), qt = 4 * (xcd >> 1) + (bi >> 3);
  const int hf = u & 1, ct = u >> 1;
  constexpr unsigned FKV = WS_FL + (FINAL ? 2 : 0) * 32768, FO = FKV + 32768;
  LDS_AS unsigned* misc = (LDS_AS unsigned*)(lds + L_MISC + 1024);

  if (__builtin_expect(w >= 8, 1)) {
    const int lw = w - 8;
    __builtin_amdgcn_s_setprio(3);
    if (lane < 2 && lw == 0) misc[lane] = 0u;
    unsigned vm[4];
    vm_kmajor(vm, RBP * 2, 128 * qt);
    gemm_load<NTOT>(lds, ws + WS_RB, vm, 64 * RBP * 2, ws + WS_WQ + (size_t)u * 10 * 20 * 1024);
    MARK(0);
    const int hh = lw >> 1, hp = lw & 1;
    const unsigned kvv = WS_KV + (2 * u + hh) * 16 * KVC + hp * 5120 + lane * 16;
    const unsigned lkv = lds_addr(lds) + hh * HR + hp * 5120;
    const unsigned wv = (FINAL ? WS_WOP : WS_WOF) + (ct * 2 + hh) * 51200 + lane * 16;
    const unsigned myfl = FKV + (u * 16 + qt) * 128;
    lds_bar();
    raw_bar();
    MARK(1);
    {
      u32x4 v[6];
#pragma unroll
      for (int i = 0; i < 6; i++) v[i] = ld128(lds + hh * HR + hp * 5120 + lane * 16 + i * 1024);
#pragma unroll
      for (int i = 0; i < 6; i++) st128<16>(rw, kvv + qt * KVC + i * 1024, v[i]);
    }
    drain();
    unsigned cnt = 0;
    if (lane == 0) cnt = __hip_atomic_fetch_add(misc, 1u, __ATOMIC_RELAXED, __HIP_MEMORY_SCOPE_WORKGROUP);
    if (__builtin_amdgcn_readfirstlane(cnt) == 3 && lane < 16) st32<16>(rw, FKV + (u * 16 + lane) * 128 + qt * 4, 1u);
    MARK(2);
    auto ldkv = [&](int seq) {
      const int ch = seq == 0 ? qt : seq < 13 ? ((qt & 12) + 3 + seq) & 15 : (qt & 12) | ((qt + seq) & 3);
      dma6_sc1(kvv + ch * KVC, ws, lkv + (seq & 3) * KVC);
    };
    poll16(rw, myfl, 7u << (((qt & 12) + 4) & 15));
    MARK(3);
#pragma unroll 1
    for (int attempt = 0;; attempt++) {
      if (attempt) { ldkv(0); vm_bar<0>(); }
#pragma unroll 1
      for (int c = -2; c < 13; c++) {
        if (c == 1 && !attempt) poll16(rw, myfl, 0xFFFFu);
        if (c >= 2) { const int e = min(4 * (c - 2) + 2 * hp, 33); dma2(wv + e * 1024, ws, lds_addr(lds) + L_A + (hh * 35 + e) * 1024); }
        ldkv(c + 3);
        if (c == 0) { vm_bar<12>(); MARK(4); }
        if (c >= 2) vm_bar<8>(); else if (c >= 0) vm_bar<6>();
      }
      vm_bar<0>();
      dma4(wv + (35 + 7 * hp) * 1024, ws, lds_addr(lds) + hh * HR + 7 * hp * 1024);
      dma4(wv + (39 + 7 * hp) * 1024, ws, lds_addr(lds) + hh * HR + (4 + 7 * hp) * 1024);
      raw_bar();
      vm_bar<0>();
      if (attempt || !*(volatile LDS_AS unsigned*)(misc + 1)) break;
      raw_bar();
    }
    MARK(5);
    raw_bar();
    if (lw == 0) {
      if (lane < 16) st32<16>(rw, FO + (lane * 16 + qt) * 128 + u * 4, 1u);
      poll16(rw, FO + (u * 16 + qt) * 128, 0xFFFFu);
    }
    MARK(6);
    raw_bar();
    return;
  }

  const int wm = w & 3, wn = w >> 2, h = lane >> 5;
  {
    f32x4 acc[2][C::NT] = {};
    f32x4 gram = {0.f, 0.f, 0.f, 0.f};
    gemm_mma<NTOT, KMAJOR, 1, true>(lds, acc, gram);
    MARK(0);
    LDS_AS float* sl = (LDS_AS float*)(lds + L_MISC);
    { const float d = (r16 & 2) ? ((r16 & 1) ? gram[3] : gram[2]) : ((r16 & 1) ? gram[1] : gram[0]);
      if (g == (r16 >> 2)) sl[32 * wm + 16 * wn + r16] = __builtin_amdgcn_rsqf(d * (1.f / 640.f) + 1e-5f); }
    lds_bar();
    const lptr qb = lds + L_A + (wn * 4 + wm) * 1280, kvb = lds + wn * HR;
#pragma unroll
    for (int m = 0; m < 2; m++) {
      const int pr = 16 * (m ^ wn) + r16, pl = 32 * wm + pr;
      const float rs = sl[pl];
      *(LDS_AS u32x4*)(((g >> 1) ? kvb + wm * 1280 : qb) + (g & 1) * 512 + pr * 16) = pk8(acc[m][0] * rs, acc[m][1] * rs);
      const u32x2 lo = pk4(acc[m][2] * rs); u32x2 hi = pk4(acc[m][3] * rs);
      if (g == 3) hi = u32x2{0x3F80u, 0u};
      const lptr vr = kvb + 5120 + pl * 48 + (g == 3 ? 32 : g * 16);
      *(LDS_AS u32x2*)(g == 2 ? qb + 1024 + pr * 8 : vr) = lo;
      *(LDS_AS u32x2*)(g == 2 ? kvb + wm * 1280 + 1024 + pr * 8 : vr + 8) = hi;
      if (FINAL) {
        const unsigned x4 = WS_X4 + ((128 * qt + pl) * 640 + 40 * u + 20 * wn) * 2;
        st64<16>(rw, x4 + 8 * g, pk4(acc[m][C::NT - 1]));
        if (g == 3) st64<16>(rw, x4 + 32, pk4(acc[m][3]));
      }
    }
    lds_bar();
  }
  MARK(1);
  const int head = 2 * u + wn, qh = w & 1, kh = (w >> 1) & 1;
  const lptr ring = lds + wn * HR;
  f32x16 o;
  {
    const lptr qpa = lds + L_A + (wn * 4 + 2 * qh + kh) * 1280, qpb = lds + L_A + (wn * 4 + 2 * qh + (kh ^ 1)) * 1280;
    const u32x4 qa1 = ld128(qpa + lane * 16), qb1 = ld128(qpb + lane * 16);
    u32x2 qa2 = ld64(qpa + 1024 + (lane & 31) * 8), qb2 = ld64(qpb + 1024 + (lane & 31) * 8);
    if (h) { qa2 = u32x2{0u, 0u}; qb2 = qa2; }
    const unsigned ka = lds_addr(ring) + kh * 2560 + lane * 16, kb = lds_addr(ring) + kh * 2560 + 1024 + (lane & 31) * 8;
    const unsigned va = lds_addr(ring) + kh * 3072 + (4 * h + (r16 >> 2)) * 48 + ((g & 1) ? ((lane & 3) ? 40 : 32) : (lane & 3) * 8);
    f32x16 z;
#pragma unroll
    for (int i = 0; i < 16; i++) z[i] = 0.f;
    float ma, mb;
    {
      const u32x4 k1 = ld128(ring + lane * 16); const u32x2 k2 = ld64(ring + 1024 + (lane & 31) * 8);
      ma = max16(mfma32k8(k2, qa2, mfma32(k1, qa1, z))); mb = max16(mfma32k8(k2, qb2, mfma32(k1, qb1, z)));
    }
    for (int attempt = 0;; attempt++) {
      ma = fmaxf(ma, __shfl_xor(ma, 32)); mb = fmaxf(mb, __shfl_xor(mb, 32));
      f32x16 cia, cib, oa, ob;
#pragma unroll
      for (int i = 0; i < 16; i++) { cia[i] = -ma; cib[i] = -mb; }
      int cnt = 4;
      MARK(2);
      asm volatile(
          "ds_read_b128 v[80:83], v100\n\t"
          "ds_read_b64 v[84:85], v101\n\t"
          "ds_read_b64_tr_b16 v[86:87], v102 offset:5120\n\t"
          "ds_read_b64_tr_b16 v[88:89], v102 offset:5504\n\t"
          Z4(0) Z4(68) Z4(76) Z4(90)
          "s_waitcnt lgkmcnt(0)\n\t"
          "v_mfma_f32_32x32x16_bf16 v[32:47], v[80:83], v[94:97], v[16:31]\n\t"
          "v_mfma_f32_32x32x8_bf16 v[32:47], v[84:85], v[98:99], v[32:47]\n\t"
          Z4(4) Z4(8) Z4(12) Z4(104) Z4(108) Z4(112) Z4(116)
          "L_attn_%=:\n\t"
          CHUNK(0) CHUNK(1) CHUNK(2) CHUNK(3)
          "s_sub_u32 %[cnt], %[cnt], 1\n\ts_cmp_lg_u32 %[cnt], 0\n\ts_cbranch_scc1 L_attn_%=\n\t"
          "s_setprio 0\n\t" CVT(78,68) CVT(79,70) "s_waitcnt lgkmcnt(0)\n\ts_nop 1\n\t"
          "v_mfma_f32_32x32x16_bf16 v[104:119], v[90:93], v[76:79], v[104:119]\n\ts_nop 15\n\t"
          : "={v[0:15]}"(oa), "={v[104:119]}"(ob), [cnt] "+s"(cnt)
          : "{v[16:31]}"(cia), "{v[120:135]}"(cib), "{v[94:97]}"(qa1), "{v[98:99]}"(qa2), "{v[136:139]}"(qb1), "{v[140:141]}"(qb2),
            "{v100}"(ka), "{v101}"(kb), "{v102}"(va)
          : "memory", "scc", "v32", "v33", "v34", "v35", "v36", "v37", "v38", "v39", "v40", "v41", "v42", "v43", "v44", "v45", "v46", "v47",
            "v48", "v49", "v50", "v51", "v52", "v53", "v54", "v55", "v56", "v57", "v58", "v59", "v60", "v61", "v62", "v63", "v64", "v65",
            "v66", "v67", "v68", "v69", "v70", "v71", "v72", "v73", "v74", "v75", "v76", "v77", "v78", "v79", "v80", "v81", "v82", "v83",
            "v84", "v85", "v86", "v87", "v88", "v89", "v90", "v91", "v92", "v93");
      MARK(3);
      LDS_AS f32x4* ex = (LDS_AS f32x4*)(ring + L_EX + wm * 3072) + lane;
#pragma unroll
      for (int i = 0; i < 3; i++) ex[i * 64] = f32x4{ob[4 * i], ob[4 * i + 1], ob[4 * i + 2], ob[4 * i + 3]};
      o = oa;
      f32x4 cs = {0.f, 0.f, 0.f, 0.f};
#pragma unroll
      for (int i = 0; i < 3; i++) cs += f32x4{oa[4 * i], oa[4 * i + 1], oa[4 * i + 2], oa[4 * i + 3]} + f32x4{ob[4 * i], ob[4 * i + 1], ob[4 * i + 2], ob[4 * i + 3]};
      if (!(fabsf(cs[0] + cs[1] + cs[2] + cs[3]) < INFINITY) || (h && !(oa[8] + ob[8] < 0x1p60f))) misc[1] = 1u;
      lds_bar();
      if (attempt || !*(volatile LDS_AS unsigned*)(misc + 1)) break;
      raw_bar();
      ma = -INFINITY; mb = -INFINITY;
      for (int t = 0; t < 64; t++) {
        const unsigned kb_ = WS_KV + (head * 16 + (t >> 2)) * KVC + (t & 3) * 1280;
        const u32x4 k1 = gl128<16>(rw, kb_ + lane * 16); const u32x2 k2 = gl64<16>(rw, kb_ + 1024 + (lane & 31) * 8);
        ma = fmaxf(ma, max16(mfma32k8(k2, qa2, mfma32(k1, qa1, z)))); mb = fmaxf(mb, max16(mfma32k8(k2, qb2, mfma32(k1, qb1, z))));
      }
      raw_bar();
    }
  }
  {
    LDS_AS f32x4* ey = (LDS_AS f32x4*)(ring + L_EX + (wm ^ 2) * 3072) + lane;
    f32x4 r[3];
#pragma unroll
    for (int i = 0; i < 3; i++) r[i] = ey[i * 64] + f32x4{o[4 * i], o[4 * i + 1], o[4 * i + 2], o[4 * i + 3]};
    const float il = __builtin_amdgcn_rcpf(__shfl(r[2][0], (lane & 31) + 32));
    const unsigned ob = WS_O + (head * 64 + 4 * qt + 2 * qh + kh) * 1280;
    st128<16>(rw, ob + lane * 16, pk8(r[0] * il, r[1] * il));
    if (!h) st64<16>(rw, ob + 1024 + lane * 8, pk4(r[2] * il));
  }
  const int prow = 128 * qt + 64 * hf + 16 * wm;
  drain();
  lds_bar();
  MARK(4);
  f32x4 res[3]; u32x2 x4v[3]; float dc = 0.f;
  if (!FINAL) {
#pragma unroll
    for (int i = 0; i < 3; i++) res[i] = asf(gl128<0>(rw, WS_R + ((80 * ct + 48 * wn + 16 * (wn ? (i & 1) : i) + r16) * RP + prow + 4 * g) * 4));
  } else dc = __uint_as_float(gl32<0>(rw, WS_R + (RP + prow + r16) * 4));
  const lptr wa = lds + L_A + wn * 35 * 1024 + lane * 16, wb = ring + lane * 16;
  constexpr int NPRE = 20;
  u32x4 bf[50];
#pragma unroll
  for (int j = 0; j < NPRE; j++) bf[j] = ld128(wa + j * 1024);
  const unsigned obase = WS_O + ((16 * wn) * 64 + (prow >> 5)) * 1280;
  unsigned o1 = obase + (g >> 1) * 81920 + (g & 1) * 512 + (wm & 1) * 256 + r16 * 16, o2 = obase + 2 * g * 81920 + 1024 + (wm & 1) * 128 + r16 * 8;
  unsigned o3 = WS_X4 + ((prow + r16) * 640 + 80 * ct + 48 * wn + 4 * g) * 2;
  f32x4 acc[5] = {};
#pragma unroll
  for (int j = 0; j < 5; j++) asm volatile("" : "+v"(acc[j]));
  asm volatile("" : "+v"(o1), "+v"(o2), "+v"(o3));
  SB();
  lds_bar();
  MARK(5);
  u32x4 af[10];
#pragma unroll
  for (int i = 0; i < 8; i++) af[i] = __builtin_amdgcn_raw_buffer_load_b128(rw, o1, i * 2 * 81920, 16);
#pragma unroll
  for (int i = 0; i < 2; i++) {
    const u32x2 a = __builtin_amdgcn_raw_buffer_load_b64(rw, o2, i * 8 * 81920, 16), b = __builtin_amdgcn_raw_buffer_load_b64(rw, o2, (i * 8 + 1) * 81920, 16);
    af[8 + i] = u32x4{a[0], a[1], b[0], b[1]};
  }
  if (FINAL) {
#pragma unroll
    for (int i = 0; i < 3; i++) x4v[i] = gl64<16>(rw, o3 + 32 * ((i == 2 && wn) ? 0 : i));
  }
  SB();
#pragma unroll
  for (int i = 0; i < 10; i++) {
#pragma unroll
    for (int a = 0; a < 5; a++) {
      if (FINAL) mfma16v(acc[a], bf[5 * i + a], af[i]); else mfma16v(acc[a], af[i], bf[5 * i + a]);
      const int j = 5 * i + a + NPRE;
      if (j < 50) bf[j] = ld128(j < 35 ? wa + j * 1024 : wb + (j - 35) * 1024);
      SB();
    }
  }
  asm volatile("s_nop 7\n\ts_nop 1" ::: "memory");
  MARK(6);
  {
    LDS_AS f32x4* ex = (LDS_AS f32x4*)(ring + L_EX + wm * 3072) + lane;
    LDS_AS f32x4* ey = (LDS_AS f32x4*)(lds + (wn ^ 1) * HR + L_EX + wm * 3072) + lane + 64 * wn;
    ex[0] = acc[2]; ex[64] = acc[3]; ex[128] = acc[4];
    if (FINAL) {
      dc *= 0.039528470752104741f;
#pragma unroll
      for (int i = 0; i < 3; i++) res[i] = f32x4{bflo(x4v[i][0]), bfhi(x4v[i][0]), bflo(x4v[i][1]), bfhi(x4v[i][1])} + dc;
    }
#pragma unroll
    for (int i = 0; i < 3; i++) acc[i] += res[i];
    lds_bar();
#pragma unroll
    for (int i = 0; i < 3; i++) acc[i] += ey[64 * i];
  }
  if (FINAL) {
    u16* op = out + (size_t)rowof(prow + r16) * 640 + 80 * ct + 48 * wn + 4 * g;
#pragma unroll
    for (int i = 0; i < 3; i++)
      if (i < 2 || wn == 0) *(u32x2*)(op + 16 * i) = pk4(acc[i]);
  } else {
    f32x4 sq = {0.f, 0.f, 0.f, 0.f};
#pragma unroll
    for (int i = 0; i < 3; i++)
      if (i < 2 || wn == 0) {
        const int j = 80 * ct + 48 * wn + 16 * i + r16;
        st128<AUXB>(rw, WS_R + (j * RP + prow + 4 * g) * 4, asu(acc[i]));
        if (j != 1) sq += acc[i] * acc[i];
      }
#pragma unroll
    for (int e = 0; e < 4; e++) sq[e] = row16_sum(sq[e]);
    LDS_AS float* sl = (LDS_AS float*)(lds + L_MISC);
    if (r16 == 0) *(LDS_AS f32x4*)(sl + wn * 64 + 16 * wm + 4 * g) = sq;
    lds_bar();
    if (w == 0) st16<AUXB>(rw, ssq_off(128 * qt + 64 * hf + lane) + ct * 2, (u16)pk2(sl[lane] + sl[64 + lane], 0.f));
  }
  MARK(7);
}

#define STOPAT(n)
extern "C" void kernel_launch(void* const* d_in, const int* in_sizes, int n_in, void* d_out, int out_size, void* d_ws, size_t ws_size,
                              hipStream_t stream) {
  const u16* x = (const u16*)d_in[0];
  const u16* wr = (const u16*)d_in[1];
  const u16* wi = (const u16*)d_in[2];
  const u16* wqkv = (const u16*)d_in[4];
  const u16* wo = (const u16*)d_in[5];
  char* ws = (char*)d_ws;
  (void)hipFuncSetAttribute((const void*)k_prep, hipFuncAttributeMaxDynamicSharedMemorySize, LDS_PREP);
  (void)hipFuncSetAttribute((const void*)k_fused<false>, hipFuncAttributeMaxDynamicSharedMemorySize, LDS_FUSED);
  (void)hipFuncSetAttribute((const void*)k_fused<true>, hipFuncAttributeMaxDynamicSharedMemorySize, LDS_FUSED);
  k_prep<<<256, 768, LDS_PREP, stream>>>(x, wr, wi, wqkv, wo, ws);
  STOPAT(1)
  k_col<true><<<COLG, 256, 0, stream>>>(ws);
  STOPAT(2)
  k_fused<false><<<256, 768, LDS_FUSED, stream>>>(ws, (u16*)d_out);
  STOPAT(3)
  k_col<false><<<COLG, 256, 0, stream>>>(ws);
  STOPAT(4)
  k_fused<true><<<256, 768, LDS_FUSED, stream>>>(ws, (u16*)d_out);
}
```

```cpp
#include <hip/hip_runtime.h>
#include <hip/hip_bf16.h>
#include <utility>
#include <type_traits>

typedef unsigned short u16;
typedef __attribute__((ext_vector_type(16))) float f32x16;
typedef __attribute__((ext_vector_type(4))) float f32x4;
typedef __attribute__((ext_vector_type(2))) float f32x2;
typedef __attribute__((ext_vector_type(8))) __bf16 bf16x8;
typedef __attribute__((ext_vector_type(2))) __bf16 bf16x2;
typedef __attribute__((ext_vector_type(4))) short s16x4;
typedef __attribute__((ext_vector_type(4))) unsigned u32x4;
typedef __attribute__((ext_vector_type(2))) unsigned u32x2;
#define DI __device__ __forceinline__
#define LDS_AS __attribute__((address_space(3)))
typedef LDS_AS char* lptr;

constexpr double cx_cos_small(double x) { double x2 = x * x, t = 1, s = 1; for (int i = 1; i < 12; i++) { t *= -x2 / ((2 * i - 1) * (2 * i)); s += t; } return s; }
constexpr double cx_sin_small(double x) { double x2 = x * x, t = x, s = x; for (int i = 1; i < 12; i++) { t *= -x2 / ((2 * i) * (2 * i + 1)); s += t; } return s; }
constexpr double cx_cos640(int m) {
  m %= 640; if (m > 320) m = 640 - m; double sg = 1; if (m > 160) { m = 320 - m; sg = -1; }
  const double w = 6.283185307179586476925 / 640; return sg * (m <= 80 ? cx_cos_small(w * m) : cx_sin_small(w * (160 - m)));
}
constexpr u16 cx_bf16(double d) { float f = (float)d; unsigned u = __builtin_bit_cast(unsigned, f); u += 0x7FFFu + ((u >> 16) & 1u); return (u16)(u >> 16); }
struct Cos640 { double v[640]; };
constexpr Cos640 mk_cos() { Cos640 c{}; for (int m = 0; m < 640; m++) c.v[m] = cx_cos640(m); return c; }
constexpr Cos640 COS = mk_cos();
__host__ __device__ constexpr int kf_of(int G, int i) { return G ? G + 40 * i : (i == 0 ? 320 : i < 8 ? 40 * i : 40 * i - 300); }
__host__ __device__ constexpr int freq_of(int G, int i) { return kf_of(G, i) > 320 ? 640 - kf_of(G, i) : kf_of(G, i); }
constexpr double a1val(int out, int a) {
  if (out >= 40 || a >= 40) return 0;
  const int G = out >> 1, part = out & 1;
  if (G == 0) return (part && (a & 1)) ? -1 : 1;
  return COS.v[(16 * a * G + 160 * part) % 640];
}
constexpr double m2val(int G, int o, int k) {
  const int i = o >> 1, po = o & 1, part = k >> 4, b = k & 15, kf = kf_of(G, i);
  const double c = COS.v[(b * kf) % 640], sn = COS.v[(b * kf + 480) % 640];
  if (G == 0) {
    if (part != (i >= 8)) return 0;
    if (i == 0) return 0.039528470752104741 * (po ? 1.0 : c);
    return 0.055901699437494742 * (po ? -sn : c);
  }
  const double v = po ? (part ? c : -sn) : (part ? sn : c);
  return 0.055901699437494742 * ((po && kf > 320) ? -v : v);
}
struct DTab { u16 a1[3][64][8]; u16 a1b[3][64][4]; u16 pad[256]; u16 m2[20][2][64][8]; };
constexpr DTab mk_dtab() {
  DTab t{};
  for (int l = 0; l < 64; l++) {
    const int r = l & 15, g = l >> 4;
    for (int T = 0; T < 3; T++) {
      for (int e = 0; e < 8; e++) t.a1[T][l][e] = cx_bf16(a1val(16 * T + r, 16 * (e >> 2) + 4 * g + (e & 3)));
      for (int e = 0; e < 4; e++) t.a1b[T][l][e] = cx_bf16(a1val(16 * T + r, 32 + 4 * g + e));
    }
    for (int G = 0; G < 20; G++) for (int T = 0; T < 2; T++) for (int e = 0; e < 8; e++)
      t.m2[G][T][l][e] = cx_bf16(m2val(G, 8 * (r >> 2) + 4 * T + (r & 3), 8 * g + e));
  }
  return t;
}
static __device__ const __attribute__((aligned(1024))) DTab g_dt = mk_dtab();

struct CTab { u16 v[10 * 512]; };
constexpr CTab mk_ctab() {
  CTab c{}; u16 cs[64] = {};
  for (int m = 0; m < 64; m++) cs[m] = cx_bf16(cx_cos640(10 * m));
  for (int f = 0; f < 10; f++) for (int l = 0; l < 64; l++) for (int e = 0; e < 8; e++) {
    const int r = l & 15, g = l >> 4;
    const int m = f < 8 ? (8 * g + e) * (16 * (f >> 1) + r) : 2 * r * (16 * (e >> 2) + 4 * g + (e & 3));
    c.v[(f * 64 + l) * 8 + e] = cs[(m + (f & 1) * 48) & 63];
  }
  return c;
}
static __device__ const __attribute__((aligned(1024))) CTab g_ct = mk_ctab();

constexpr int RP = 2048 + 64, RBP = 2048 + 128;
constexpr unsigned WS_FL = 0;
constexpr unsigned WS_R = WS_FL + 4 * 256 * 128;
constexpr unsigned WS_RB = WS_R + 640 * RP * 4;
constexpr unsigned WS_SSQ = WS_RB + 640 * RBP * 2;
constexpr unsigned WS_WF = WS_SSQ + 2048 * 16;
constexpr unsigned WS_WQ = WS_WF + 320 * 8192;
constexpr unsigned WS_WOF = WS_WQ + 16 * 10 * 20 * 1024;
constexpr unsigned WS_WOP = WS_WOF + 40 * 20 * 1024;
constexpr int KVC = 11264;
constexpr unsigned WS_KV = WS_WOP + 40 * 20 * 1024;
constexpr unsigned WS_O = WS_KV + 32 * 16 * KVC;
constexpr unsigned LAYER2 = 32 * 16 * KVC + 32 * 64 * 1280;
constexpr unsigned WS_X4 = WS_KV + 2 * LAYER2;
constexpr unsigned EMPTY = 0xFFFFFFFFu;
constexpr unsigned WS_END = WS_X4 + 2048 * 640 * 2;
#define KVSC1 true
#define ACQ
#define OAUX 16
#define COLG 256
#define RSLEEP 2
#define AUXB 16

#define STW 0
#define STAMP(k)
#define MARK(i)
#define SB() __builtin_amdgcn_sched_barrier(0)
typedef __amdgpu_buffer_rsrc_t rsrc_t;
DI rsrc_t mk_rsrc(const void* p, unsigned bytes) { return __builtin_amdgcn_make_buffer_rsrc((void*)p, 0, bytes, 0x00020000); }
template <int AUX> DI void st128(rsrc_t r, unsigned off, u32x4 v) { __builtin_amdgcn_raw_buffer_store_b128(v, r, off, 0, AUX); }
template <int AUX> DI void st64(rsrc_t r, unsigned off, u32x2 v) { __builtin_amdgcn_raw_buffer_store_b64(v, r, off, 0, AUX); }
template <int AUX> DI void st32(rsrc_t r, unsigned off, unsigned v) { __builtin_amdgcn_raw_buffer_store_b32(v, r, off, 0, AUX); }
template <int AUX> DI void st16(rsrc_t r, unsigned off, u16 v) { __builtin_amdgcn_raw_buffer_store_b16(v, r, off, 0, AUX); }
template <int AUX> DI u32x4 gl128(rsrc_t r, unsigned off) { return __builtin_amdgcn_raw_buffer_load_b128(r, off, 0, AUX); }
template <int AUX> DI u32x2 gl64(rsrc_t r, unsigned off) { return __builtin_amdgcn_raw_buffer_load_b64(r, off, 0, AUX); }
template <int AUX> DI unsigned gl32(rsrc_t r, unsigned off) { return __builtin_amdgcn_raw_buffer_load_b32(r, off, 0, AUX); }
DI u32x4 asu(f32x4 v) { return __builtin_bit_cast(u32x4, v); }
DI f32x4 asf(u32x4 v) { return __builtin_bit_cast(f32x4, v); }
DI unsigned pk2(float a, float b) { f32x2 v = {a, b}; return __builtin_bit_cast(unsigned, __builtin_convertvector(v, bf16x2)); }
DI u32x2 pk4(f32x4 a) { return u32x2{pk2(a[0], a[1]), pk2(a[2], a[3])}; }
DI u32x4 pk8(f32x4 a, f32x4 b) { return u32x4{pk2(a[0], a[1]), pk2(a[2], a[3]), pk2(b[0], b[1]), pk2(b[2], b[3])}; }
DI u32x4 clean(u32x4 v) { return __builtin_elementwise_min(v, u32x4{EMPTY - 1, EMPTY - 1, EMPTY - 1, EMPTY - 1}); }
DI unsigned umax4(unsigned m, u32x4 v) { return max(max(max(m, v[0]), v[1]), max(v[2], v[3])); }
DI float bflo(unsigned u) { return __uint_as_float(u << 16); }
DI float bfhi(unsigned u) { return __uint_as_float(u & 0xFFFF0000u); }
DI float dot2(unsigned a, unsigned b, float c) { return __builtin_amdgcn_fdot2_f32_bf16(__builtin_bit_cast(bf16x2, a), __builtin_bit_cast(bf16x2, b), c, false); }
DI int rowof(int p) { return 32 * (((p >> 5) & 0x3C) + (p & 3)) + ((p << 2) & 16) + ((p >> 3) & 15); }
DI int rpos(int p) { return ((p << 8) & 1024) + ((p >> 3) << 2) + (p & 3); }
DI int trow(int T, int r) { return 8 * (r >> 2) + 4 * T + (r & 3); }
DI unsigned ssq_off(int p) { return WS_SSQ + ((p & 7) * 256 + (p >> 3)) * 16; }
template <int CTRL> DI float dpp_add(float v) { return v + __int_as_float(__builtin_amdgcn_update_dpp(0, __float_as_int(v), CTRL, 0xF, 0xF, true)); }
DI float row16_sum(float v) { v = dpp_add<0x128>(v); v = dpp_add<0x124>(v); v = dpp_add<0x122>(v); return dpp_add<0x121>(v); }
DI f32x4 mfma16(u32x4 a, u32x4 b, f32x4 c) {
  return __builtin_amdgcn_mfma_f32_16x16x32_bf16(__builtin_bit_cast(bf16x8, a), __builtin_bit_cast(bf16x8, b), c, 0, 0, 0);
}
DI void mfma16v(f32x4& c, u32x4 a, u32x4 b) { asm volatile("v_mfma_f32_16x16x32_bf16 %0, %1, %2, %0" : "+v"(c) : "v"(a), "v"(b)); }
DI u32x2 ldtr(lptr p) { return __builtin_bit_cast(u32x2, __builtin_amdgcn_ds_read_tr16_b64_v4i16((LDS_AS s16x4*)p)); }
DI u32x4 ld128(lptr p) { return *(LDS_AS u32x4*)p; }
DI u32x2 ld64(lptr p) { return *(LDS_AS u32x2*)p; }
#define GL(v, s, o, x) "global_load_lds_dwordx4 " v ", " s " offset:" #o x "\n\t"
#define M0SET "s_mov_b32 %[sv], m0\n\ts_mov_b32 m0, %[l]\n\ts_nop 2\n\t"
#define M0ADD(x) "s_add_u32 m0, m0, " x "\n\ts_nop 0\n\t"
#define M0RST "s_mov_b32 m0, %[sv]"
DI void dma2(unsigned v, const char* sb, unsigned l) { unsigned sv;
  asm volatile(M0SET GL("%[v]", "%[s]", 0, "") GL("%[v]", "%[s]", 1024, "") M0RST : [sv] "=&s"(sv) : [v] "v"(v), [s] "s"(sb), [l] "s"(l) : "memory"); }
DI void dma4(unsigned v, const char* sb, unsigned l) { unsigned sv;
  asm volatile(M0SET GL("%[v]", "%[s]", 0, "") GL("%[v]", "%[s]", 1024, "") GL("%[v]", "%[s]", 2048, "") GL("%[v]", "%[s]", 3072, "") M0RST
               : [sv] "=&s"(sv) : [v] "v"(v), [s] "s"(sb), [l] "s"(l) : "memory"); }
DI void dma6_sc1(unsigned v, const char* sb, unsigned l) { unsigned sv;
  asm volatile(M0SET GL("%[v]", "%[s]", 0, " sc1") GL("%[v]", "%[s]", 1024, " sc1") GL("%[v]", "%[s]", 2048, " sc1") M0ADD("3072")
               GL("%[w]", "%[s]", 0, " sc1") GL("%[w]", "%[s]", 1024, " sc1") GL("%[w]", "%[s]", 2048, " sc1") M0RST
               : [sv] "=&s"(sv) : [v] "v"(v), [w] "v"(v + 3072), [s] "s"(sb), [l] "s"(l) : "memory", "scc"); }
template <int N> DI void vm_bar() { asm volatile("s_waitcnt vmcnt(%0)\n\ts_barrier" ::"i"(N) : "memory"); }
DI void lds_bar() { asm volatile("s_waitcnt lgkmcnt(0)\n\ts_barrier" ::: "memory"); }
DI void raw_bar() { asm volatile("s_barrier" ::: "memory"); }
DI void drain() { asm volatile("s_waitcnt vmcnt(0)" ::: "memory"); }
#define WARM_CODE(sym, lines) do { unsigned lo_, hi_; \
    asm volatile("s_getpc_b64 vcc\n\ts_add_u32 %0, vcc_lo, " sym "@rel32@lo+4\n\ts_addc_u32 %1, vcc_hi, " sym "@rel32@hi+12" : "=s"(lo_), "=s"(hi_) :: "vcc", "scc"); \
    if ((int)(threadIdx.x & 255) < (lines)) { const unsigned v_ = *(const unsigned*)((((unsigned long long)hi_ << 32) | lo_) + (threadIdx.x & 255) * 128); asm volatile("" ::"v"(v_)); } } while (0)
#define SYM_PREP "_Z6k_prepPKtS0_S0_S0_S0_Pc"
#define SYM_COL1 "_Z5k_colILb1EEvPc"
#define SYM_COL2 "_Z5k_colILb0EEvPc"
#define SYM_FUS1 "_Z7k_fusedILb0EEvPcPt"
#define SYM_FUS2 "_Z7k_fusedILb1EEvPcPt"
DI int wave_id() { return __builtin_amdgcn_readfirstlane(threadIdx.x >> 6); }
DI unsigned lds_addr(lptr p) { return (unsigned)(size_t)p; }

DI int kappa_to_i(int kap) {
  int ks = kap >> 5, g = (kap >> 3) & 3, e = kap & 7;
  if (ks < 16) return (2 * ks + (g >> 1)) * 20 + 8 * (e >> 2) + 4 * (g & 1) + (e & 3);
  return (8 * (ks - 16) + 2 * g + (e >> 2)) * 20 + 16 + (e & 3);
}

DI unsigned wo_frag(int nt, int ks) {
  const int ct = nt / 5, c5 = nt - 5 * ct, kh = ks < 16 ? ks >> 3 : (ks - 16) >> 1, i = ks < 16 ? ks & 7 : 8 + (ks & 1);
  const int a = kh ? (c5 >= 3 ? c5 - 3 : c5 + 2) : c5;
  return ((ct * 2 + kh) * 50 + 5 * i + a) * 1024;
}

constexpr int SLOT32 = 36864;
DI void dma_stage(const unsigned* vm, unsigned v0, unsigned v1, const char* mb, const char* sb, unsigned l) {
  unsigned sv;
  asm volatile(M0SET GL("%[a0]", "%[m]", 0, "") GL("%[a1]", "%[m]", 1024, "") GL("%[a2]", "%[m]", 2048, "") GL("%[a3]", "%[m]", 3072, "") M0ADD("0x4000")
               GL("%[v0]", "%[s]", 0, "") GL("%[v0]", "%[s]", 1024, "") GL("%[v1]", "%[s]", 2048, "") GL("%[v1]", "%[s]", 3072, "") M0RST
               : [sv] "=&s"(sv) : [a0] "v"(vm[0]), [a1] "v"(vm[1]), [a2] "v"(vm[2]), [a3] "v"(vm[3]), [v0] "v"(v0), [v1] "v"(v1),
                 [m] "s"(mb), [s] "s"(sb), [l] "s"(l) : "memory", "scc");
}
DI void dma1(unsigned v, const char* sb, unsigned l) { unsigned sv;
  asm volatile(M0SET GL("%[v]", "%[s]", 0, "") M0RST : [sv] "=&s"(sv) : [v] "v"(v), [s] "s"(sb), [l] "s"(l) : "memory"); }

constexpr int L_I2 = 40 * 1056, LDS_PREP = L_I2 + 20 * 2064;
__global__ void __launch_bounds__(512, 2) k_prep(const u16* __restrict__ x, const u16* __restrict__ wr, const u16* __restrict__ wi,
                                                 const u16* __restrict__ wqkv, const u16* __restrict__ wo, char* __restrict__ ws) {
  STAMP(0);
  extern __shared__ __attribute__((aligned(16))) char smem[];
  const lptr lds = (lptr)smem;
  const rsrc_t rw = mk_rsrc(ws, WS_END);
  const int tid = threadIdx.x, lane = tid & 63, w = wave_id(), g = lane >> 4, r16 = lane & 15;
  const int job = blockIdx.x;
  if (__builtin_expect(job < 144, 1)) {
    const bool cols = job >= 64 && job < 124;
    if (cols) {
#pragma unroll
      for (int i = 0; i < 5; i++) dma1((16 * (5 * w + i) + (lane >> 2)) * 3840 + (job - 64) * 64 + (lane & 3) * 16, (const char*)wqkv, lds_addr(lds) + (5 * w + i) * 1056);
    } else {
      const char* src = (const char*)(job < 64 ? x : wo);
      const int sl = 8 * (w & 3) + 4 * (w >> 2);
      src += (job < 64 ? rowof(32 * job + sl) : kappa_to_i(32 * (job - 124) + sl)) * 1280;
      const int st = (job < 64 ? 31 : 0) * 1280;
#pragma unroll
      for (int i = 0; i < 5; i++) dma1(1024 * i + lane * 16 + ((i == 4 || lane < 16 * i) ? i - 1 : i) * st, src, lds_addr(lds) + (5 * w + i) * 1024);
    }
    u32x4 a1[3], m2[3][2]; u32x2 a1b[3];
#pragma unroll
    for (int T = 0; T < 3; T++) { a1[T] = *(const u32x4*)g_dt.a1[T][lane]; a1b[T] = *(const u32x2*)g_dt.a1b[T][lane]; }
#pragma unroll
    for (int i = 0; i < 3; i++)
#pragma unroll
      for (int T = 0; T < 2; T++) m2[i][T] = *(const u32x4*)g_dt.m2[(i < 2 || w < 4) ? w + 8 * i : 0][T][lane];
    const f32x4 z4 = {0.f, 0.f, 0.f, 0.f};
    const lptr i2 = lds + L_I2;
    asm volatile("s_waitcnt vmcnt(0)" : "+v"(a1[0]), "+v"(a1[1]), "+v"(a1[2]), "+v"(a1b[0]), "+v"(a1b[1]), "+v"(a1b[2]) :: "memory");
    MARK(0);
    f32x4 t1[4][3];
    if (cols) {
      raw_bar();
      const lptr src = lds + (4 * g + (r16 >> 2)) * 1056 + (w >> 1) * 256 + (w & 1) * 32 + (lane & 3) * 8, srb = src + (32 - 8 * (g >> 1)) * 1056;
#pragma unroll
      for (int i = 0; i < 4; i++) {
        const u32x2 f0 = ldtr(src + i * 64), f1 = ldtr(src + 16 * 1056 + i * 64), f2 = ldtr(srb + i * 64);
#pragma unroll
        for (int T = 0; T < 3; T++) t1[i][T] = mfma16(u32x4{a1b[T][0], a1b[T][1], 0u, 0u}, u32x4{f2[0], f2[1], 0u, 0u}, mfma16(a1[T], u32x4{f0[0], f0[1], f1[0], f1[1]}, z4));
      }
      const int xo = ((w >> 1) * 8) ^ (((r16 >> 1) & 3) * 16);
      const lptr dst = i2 + 2 * g * 2064 + (16 * (w & 1) + r16) * 64;
#pragma unroll
      for (int T = 0; T < 3; T++)
#pragma unroll
        for (int e = 0; e < 4; e++)
          if (T < 2 || g < 2) *(LDS_AS u32x2*)(dst + (8 * T + (e >> 1)) * 2064 + (xo ^ ((e & 1) * 32))) = u32x2{pk2(t1[0][T][e], t1[1][T][e]), pk2(t1[2][T][e], t1[3][T][e])};
    } else {
      const lptr src = lds + w * 5120 + lane * 8, srb = lds + w * 5120 + 1024 + (lane & 31) * 8;
#pragma unroll
      for (int i = 0; i < 4; i++) {
        const u32x2 f0 = ldtr(src + i * 1280), f1 = ldtr(src + i * 1280 + 512), f2 = ldtr(srb + i * 1280);
#pragma unroll
        for (int T = 0; T < 3; T++) t1[i][T] = mfma16(u32x4{f2[0], f2[1], 0u, 0u}, u32x4{a1b[T][0], a1b[T][1], 0u, 0u}, mfma16(u32x4{f0[0], f0[1], f1[0], f1[1]}, a1[T], z4));
      }
      const int xo = ((r16 & 1) * 32 + g * 8) ^ ((w & 1) * 32);
      const lptr dst = i2 + (r16 >> 1) * 2064 + w * 256;
#pragma unroll
      for (int i = 0; i < 4; i++)
#pragma unroll
        for (int T = 0; T < 3; T++)
          if (T < 2 || r16 < 8) *(LDS_AS u32x2*)(dst + T * 8 * 2064 + i * 64 + (xo ^ ((i >> 1) * 16))) = pk4(t1[i][T]);
    }
    lds_bar();
    MARK(1);
    f32x4 sq[2] = {z4, z4};
    unsigned dq[2]; float sc[2];
    if (cols) {
#pragma unroll
      for (int m = 0; m < 2; m++) {
        const int c = 32 * (job - 64) + 16 * m + r16;
        const int which = c / 640, rem = c - which * 640, head = rem / 20, d = rem - head * 20, grp = d >> 2;
        int T, gs;
        if (grp < 4) { T = (which == 2 ? 2 : 0) + (grp & 1); gs = (which == 1 ? 2 : 0) + (grp >> 1); }
        else { T = (which == 1) ? 3 : 2; gs = (which == 2) ? 3 : 2; }
        sc[m] = which == 0 ? 0.22360679774997896f * 1.4426950408889634f : 1.f;
        dq[m] = WS_WQ + (((head >> 1) * 10 + 4 * (head & 1) + T) * 20) * 1024 + (g * 16 + 4 * gs + (d & 3)) * 16;
      }
    }
#pragma unroll
    for (int i = 0; i < 3; i++)
      if (i < 2 || w < 4) {
        const int G = w + 8 * i;
        const lptr fp = i2 + G * 2064 + r16 * 64 + ((g ^ ((r16 >> 1) & 3)) * 16);
        const u32x4 f[2] = {ld128(fp), ld128(fp + 1024)};
        if (cols) {
#pragma unroll
          for (int m = 0; m < 2; m++) st128<AUXB>(rw, dq[m] + G * 1024, pk8(mfma16(m2[i][0], f[m], z4) * sc[m], mfma16(m2[i][1], f[m], z4) * sc[m]));
        } else {
#pragma unroll
          for (int T = 0; T < 2; T++) {
            const int j = 32 * G + trow(T, r16);
            const f32x4 d0 = mfma16(f[0], m2[i][T], z4), d1 = mfma16(f[1], m2[i][T], z4);
            st128<AUXB>(rw, job < 64 ? WS_RB + (j * RBP + 32 * job + 8 * g) * 2 : WS_WOF + wo_frag(j >> 4, job - 124) + (g * 16 + (j & 15)) * 16, pk8(d0, d1));
            if (j != 1) { sq[0] += d0 * d0; sq[1] += d1 * d1; }
          }
        }
      }
    MARK(2);
    if (job < 64) {
      LDS_AS float* sl = (LDS_AS float*)lds;
#pragma unroll
      for (int m = 0; m < 2; m++) {
#pragma unroll
        for (int e = 0; e < 4; e++) sq[m][e] = row16_sum(sq[m][e]);
        if (r16 == 0) *(LDS_AS f32x4*)(sl + w * 32 + 8 * g + 4 * m) = sq[m];
      }
      lds_bar();
      if (tid < 32) {
        float v = 0.f;
#pragma unroll
        for (int i = 0; i < 8; i++) v += sl[i * 32 + tid];
        st128<AUXB>(rw, ssq_off(32 * job + tid), u32x4{pk2(v, v - bflo(pk2(v, 0.f))), 0u, 0u, 0u});
      }
    }
    if (job >= 124) {
#pragma unroll
      for (int i = 0; i < 5; i++) {
        const lptr a = lds + (4 * g + (r16 >> 2)) * 1280 + (5 * w + i) * 32 + (lane & 3) * 8;
        const u32x2 lo = ldtr(a), hi = ldtr(a + 16 * 1280);
        st128<AUXB>(rw, WS_WOP + wo_frag(5 * w + i, job - 124) + lane * 16, u32x4{lo[0], lo[1], hi[0], hi[1]});
      }
    }
    return;
  }
  const int mi = job - 144;
  if (mi < 64) {
    const int m = mi >> 3, oc = mi & 7;
    const char* src = (const char*)((w >> 2) ? wi : wr) + (64 * (4 * m + (w & 3)) + 8 * oc) * 642;
    dma4(lane * 16, src, lds_addr(lds) + w * 5152);
    dma1(4096 + lane * 16, src, lds_addr(lds) + w * 5152 + 4096);
    if (lane == 0) dma1(5120, src, lds_addr(lds) + w * 5152 + 5120);
    vm_bar<0>();
    MARK(3);
    LDS_AS u16* L = (LDS_AS u16*)lds + (tid & 7) * 321;
#pragma unroll
    for (int it = 0; it < 5; it++) {
      const int c = (tid >> 3) + 64 * it, col = freq_of(c >> 4, c & 15);
      u32x4 o;
#pragma unroll
      for (int e = 0; e < 4; e++) o[e] = (unsigned)L[e * 2576 + col] | ((unsigned)L[(4 + e) * 2576 + col] << 16);
      st128<AUXB>(rw, WS_WF + c * 8192 + (m * 64 + 8 * oc + (tid & 7)) * 16, o);
    }
    MARK(4);
  } else {
    const int i0 = (mi - 64) * 512 + tid;
    if (mi < 72) WARM_CODE(SYM_PREP, 64);
    else if (mi < 80) { WARM_CODE(SYM_COL1, 48); if (tid >= 256 && tid < 336) { const unsigned v_ = ((const unsigned*)&g_ct)[(tid - 256) * 32]; asm volatile("" ::"v"(v_)); } }
    else if (mi < 88) WARM_CODE(SYM_FUS1, 96);
    if (i0 < 4096) st128<AUXB>(rw, WS_FL + i0 * 16, u32x4{0u, 0u, 0u, 0u});
#pragma unroll
    for (int it = 0; it < 7; it++) { const int i = i0 + it * 24576; if (i < 163840) st128<AUXB>(rw, WS_O + i * 16, u32x4{EMPTY, EMPTY, EMPTY, EMPTY}); }
  }
}

#define PINF(x) asm volatile("" : "+v"(x))
template <int NM> DI void mix() {
#pragma unroll
  for (int i = 0; i < NM; i++) { __builtin_amdgcn_sched_group_barrier(0x8, 1, 0); __builtin_amdgcn_sched_group_barrier(0x2, NM == 16 ? 2 : 4, 0); }
}
template <bool FIRST>
__global__ void __launch_bounds__(256, 2) k_col(char* __restrict__ ws) {
  STAMP(FIRST ? 1 : 3);
  __shared__ __attribute__((aligned(16))) char smem[32768];
  const lptr img = (lptr)smem;
  const rsrc_t rw = mk_rsrc(ws, WS_END);
  const int tid = threadIdx.x, lane = tid & 63, w = wave_id(), g = lane >> 4, r16 = lane & 15;
  if (blockIdx.x >= 160) {
    const int i0 = (blockIdx.x - 160) * 256 + tid;
    if (blockIdx.x < 168) { if (FIRST) WARM_CODE(SYM_FUS1, 96); else WARM_CODE(SYM_FUS2, 96); }
    if (FIRST) {
#pragma unroll
      for (int it = 0; it < 8; it++) { const int k = i0 + it * 24576, b = k / 1408; if (k < 128 * 1408) st128<AUXB>(rw, WS_KV + b * 4 * KVC + (k - b * 1408) * 16, u32x4{EMPTY, EMPTY, EMPTY, EMPTY}); }
    } else {
      for (int pc = i0; pc < 640 * 80; pc += 24576) {
      const int n = pc % 640, kq = pc / 640;
      const int u = n / 40, rn = n - 40 * u, hn = rn / 20, dn = rn - 20 * hn, gq = dn >> 2;
      const int T = gq < 4 ? 8 + hn : 4 * hn + 3, gs = gq < 4 ? gq : 3;
      float v[8];
#pragma unroll
      for (int e = 0; e < 4; e++) {
        const float a = (float)((freq_of(kq >> 2, 4 * (kq & 3) + e) * n) % 640) * (1.f / 640.f);
        v[2 * e] = 0.055901699437494742f * __builtin_amdgcn_cosf(a);
        v[2 * e + 1] = -0.055901699437494742f * __builtin_amdgcn_sinf(a);
      }
      if (kq == 0) { v[0] = (n & 1) ? -0.039528470752104741f : 0.039528470752104741f; v[1] = 0.039528470752104741f; }
      st128<AUXB>(rw, WS_WQ + ((u * 10 + T) * 20 + (kq >> 2)) * 1024 + ((kq & 3) * 16 + 4 * gs + (dn & 3)) * 16,
                  u32x4{pk2(v[0], v[1]), pk2(v[2], v[3]), pk2(v[4], v[5]), pk2(v[6], v[7])});
      }
    }
    return;
  }
  const int c0 = 2 * blockIdx.x;
  f32x4 xr[2][2], xi[2][2]; u32x4 wf[2][2], sp[2][4], xb[2][2];
#pragma unroll
  for (int t = 0; t < 2; t++) {
#pragma unroll
    for (int e = 0; e < 4; e++) sp[t][e] = gl128<0>(rw, WS_SSQ + ((4 * t + e) * 256 + tid) * 16);
    if (!FIRST) {
#pragma unroll
      for (int q = 0; q < 2; q++) {
        const unsigned o = WS_R + (2 * (c0 + q) * RP + 4 * tid + 1024 * t) * 4;
        xr[q][t] = asf(gl128<0>(rw, o)); xi[q][t] = asf(gl128<0>(rw, o + RP * 4));
      }
    } else { xb[t][0] = gl128<0>(rw, WS_RB + (2 * (c0 + t) * RBP + 8 * tid) * 2); xb[t][1] = gl128<0>(rw, WS_RB + ((2 * (c0 + t) + 1) * RBP + 8 * tid) * 2); }
  }
  SB();
  f32x4 t1c[2], t1s[2], t2c[2], t2s[2];
  {
    const float f1 = (float)(16 * w + r16) * (1.f / 2048.f), b1 = (float)(4 * g), f2 = (float)(16 * w + 4 * g) * (1.f / 2048.f), b2 = (float)r16;
#pragma unroll
    for (int t = 0; t < 2; t++) {
#pragma unroll
      for (int e = 0; e < 4; e++) {
        const float a1 = (b1 + (float)(16 * t + e)) * f1, a2 = (b2 + (float)(16 * t)) * (f2 + (float)e * (1.f / 2048.f));
        t1c[t][e] = __builtin_amdgcn_cosf(a1); t1s[t][e] = __builtin_amdgcn_sinf(-a1);
        t2c[t][e] = __builtin_amdgcn_cosf(a2); t2s[t][e] = __builtin_amdgcn_sinf(a2);
      }
      PINF(t1c[t]); PINF(t1s[t]); PINF(t2c[t]); PINF(t2s[t]);
    }
  }
  int wo_ = r16 * 128 + (((2 * w + (g >> 1)) ^ (r16 >> 1)) * 16) + (g & 1) * 8, ro_ = r16 * 128 + ((g ^ (r16 >> 1)) * 16), ro2 = ro_ ^ 64;
  PINF(wo_); PINF(ro_); PINF(ro2);
  SB();
  u32x4 c64[2], s64[2], n64[2], c32[2], s32[2], n32[2];
  const u32x4* ct = (const u32x4*)&g_ct + lane;
  c64[0] = ct[(2 * w) * 64]; s64[0] = ct[(2 * w + 1) * 64]; c32[0] = ct[8 * 64]; s32[0] = ct[9 * 64];
#pragma unroll
  for (int q = 0; q < 2; q++)
#pragma unroll
    for (int t = 0; t < 2; t++) wf[q][t] = gl128<0>(rw, WS_WF + (c0 + q) * 8192 + ((4 * t + g) * 64 + 16 * w + r16) * 16);
  SB();
  MARK(0);
  if (FIRST) {
#pragma unroll
    for (int q = 0; q < 2; q++)
#pragma unroll
      for (int t = 0; t < 2; t++) {
        xr[q][t] = f32x4{bflo(xb[q][0][2 * t]), bfhi(xb[q][0][2 * t]), bflo(xb[q][0][2 * t + 1]), bfhi(xb[q][0][2 * t + 1])};
        xi[q][t] = f32x4{bflo(xb[q][1][2 * t]), bfhi(xb[q][1][2 * t]), bflo(xb[q][1][2 * t + 1]), bfhi(xb[q][1][2 * t + 1])};
      }
    if (c0 == 0) { st128<AUXB>(rw, WS_R + (RP + 4 * tid) * 4, asu(xi[0][0])); st128<AUXB>(rw, WS_R + (RP + 4 * tid + 1024) * 4, asu(xi[0][1])); }
  }
#pragma unroll
  for (int t = 0; t < 2; t++) {
    f32x4 rs = {640.f * 1e-5f, 640.f * 1e-5f, 640.f * 1e-5f, 640.f * 1e-5f};
#pragma unroll
    for (int i = 0; i < 4; i++)
#pragma unroll
      for (int e = 0; e < 4; e++) { const unsigned v = sp[t][e][i]; rs[e] = dot2(v, 0x3F803F80u, rs[e]); }
#pragma unroll
    for (int e = 0; e < 4; e++) rs[e] = __builtin_amdgcn_rsqf(rs[e] * (2048.f * 2048.f / 640.f));
#pragma unroll
    for (int q = 0; q < 2; q++) {
      u32x2 pb = pk4(xi[q][t] * rs);
      if (q == 0 && c0 == 0) pb = u32x2{0u, 0u};
      *(LDS_AS u32x2*)(img + q * 16384 + wo_ + t * 2048) = pk4(xr[q][t] * rs);
      *(LDS_AS u32x2*)(img + q * 16384 + 4096 + wo_ + t * 2048) = pb;
    }
  }
  lds_bar();
  MARK(1);
  u32x4 ur[2][2][2], ui[2][2][2];
  auto rd_in = [&](int off) {
#pragma unroll
    for (int q = 0; q < 2; q++)
#pragma unroll
      for (int ks = 0; ks < 2; ks++)
#pragma unroll
        for (int t = 0; t < 2; t++) { const lptr p = img + off + q * 16384 + (ks ? ro2 : ro_) + t * 2048; ur[q][t][ks] = ld128(p); ui[q][t][ks] = ld128(p + 4096); }
  };
  rd_in(0);
  {
    const unsigned par = (lane & 1) ? 0x80008000u : 0u;
    c64[1] = c64[0] ^ par; s64[1] = s64[0] ^ par; n64[0] = s64[0] ^ 0x80008000u; n64[1] = s64[1] ^ 0x80008000u;
    PINF(c64[1]); PINF(s64[1]); PINF(n64[0]); PINF(n64[1]);
  }
  SB();
  const f32x4 z4 = {0.f, 0.f, 0.f, 0.f};
  f32x4 ar[2][2], ai[2][2];
  u32x4 br[2], bi[2];
  auto pin_acc = [&](int q) { PINF(ar[q][0]); PINF(ai[q][0]); PINF(ar[q][1]); PINF(ai[q][1]); };
  auto st1 = [&](int q) {
#pragma unroll
    for (int ks = 0; ks < 2; ks++) {
#pragma unroll
      for (int t = 0; t < 2; t++) { ar[q][t] = mfma16(ur[q][t][ks], c64[ks], ks ? ar[q][t] : z4); ai[q][t] = mfma16(ui[q][t][ks], c64[ks], ks ? ai[q][t] : z4); }
#pragma unroll
      for (int t = 0; t < 2; t++) { ar[q][t] = mfma16(ui[q][t][ks], s64[ks], ar[q][t]); ai[q][t] = mfma16(ur[q][t][ks], n64[ks], ai[q][t]); }
    }
  };
  auto st2 = [&](int q) {
#pragma unroll
    for (int t = 0; t < 2; t++) { ar[q][t] = mfma16(c32[t], br[q], z4); ai[q][t] = mfma16(c32[t], bi[q], z4); }
#pragma unroll
    for (int t = 0; t < 2; t++) { ar[q][t] = mfma16(s32[t], bi[q], ar[q][t]); ai[q][t] = mfma16(n32[t], br[q], ai[q][t]); }
  };
  auto st3 = [&](int q) {
#pragma unroll
    for (int t = 0; t < 2; t++) { ar[q][t] = mfma16(br[q], c32[t], z4); ai[q][t] = mfma16(bi[q], c32[t], z4); }
#pragma unroll
    for (int t = 0; t < 2; t++) { ar[q][t] = mfma16(bi[q], n32[t], ar[q][t]); ai[q][t] = mfma16(br[q], s32[t], ai[q][t]); }
  };
  auto cmul = [&](int q, const f32x4* c, const f32x4* sn) {
    br[q] = pk8(ar[q][0] * c[0] - ai[q][0] * sn[0], ar[q][1] * c[1] - ai[q][1] * sn[1]);
    bi[q] = pk8(ai[q][0] * c[0] + ar[q][0] * sn[0], ai[q][1] * c[1] + ar[q][1] * sn[1]);
    PINF(br[q]); PINF(bi[q]);
  };
  auto filt = [&](int q) {
    const f32x4 a[2] = {asf(wf[q][0] << 16), asf(wf[q][1] << 16)}, b[2] = {asf(wf[q][0] & 0xFFFF0000u), asf(wf[q][1] & 0xFFFF0000u)};
    cmul(q, a, b);
  };
  auto put = [&](int q) {
#pragma unroll
    for (int t = 0; t < 2; t++) {
      *(LDS_AS u32x2*)(img + q * 16384 + 8192 + wo_ + t * 2048) = u32x2{br[q][2 * t], br[q][2 * t + 1]};
      *(LDS_AS u32x2*)(img + q * 16384 + 12288 + wo_ + t * 2048) = u32x2{bi[q][2 * t], bi[q][2 * t + 1]};
    }
  };
  st1(0);
  c32[1] = c32[0] ^ 0x80000000u; s32[1] = s32[0] ^ 0x80000000u; n32[0] = s32[0] ^ 0x80008000u; n32[1] = s32[0] ^ 0x00008000u;
  PINF(c32[1]); PINF(s32[1]); PINF(n32[0]); PINF(n32[1]);
  mix<16>(); pin_acc(0); SB();
  MARK(4);
  st1(1); cmul(0, t1c, t1s); mix<16>(); pin_acc(1); SB();
  st2(0); cmul(1, t1c, t1s); mix<8>(); pin_acc(0); SB();
  MARK(5);
  st2(1); filt(0); mix<8>(); pin_acc(1); SB();
  MARK(6);
  st3(0); filt(1); mix<8>(); pin_acc(0); SB();
  st3(1); cmul(0, t2c, t2s); put(0); mix<8>(); pin_acc(1); SB();
  cmul(1, t2c, t2s); put(1);
  MARK(7);
  lds_bar();
  MARK(2);
  rd_in(8192);
  SB();
  auto st4 = [&](int q) {
#pragma unroll
    for (int ks = 0; ks < 2; ks++) {
#pragma unroll
      for (int t = 0; t < 2; t++) { ar[q][t] = mfma16(c64[ks], ur[q][t][ks], ks ? ar[q][t] : xr[q][t]); ai[q][t] = mfma16(c64[ks], ui[q][t][ks], ks ? ai[q][t] : xi[q][t]); }
#pragma unroll
      for (int t = 0; t < 2; t++) { ar[q][t] = mfma16(n64[ks], ui[q][t][ks], ar[q][t]); ai[q][t] = mfma16(s64[ks], ur[q][t][ks], ai[q][t]); }
    }
  };
  auto outp = [&](int q) {
    const bool dc = (q == 0 && c0 == 0);
    const unsigned ob = WS_RB + (2 * (c0 + q) * RBP + 8 * tid) * 2;
    st128<AUXB>(rw, ob, pk8(ar[q][0], ar[q][1]));
    st128<AUXB>(rw, ob + RBP * 2, dc ? u32x4{0u, 0u, 0u, 0u} : pk8(ai[q][0], ai[q][1]));
    if (FIRST) {
#pragma unroll
      for (int t = 0; t < 2; t++) {
        const unsigned o = WS_R + (2 * (c0 + q) * RP + 4 * tid + 1024 * t) * 4;
        st128<AUXB>(rw, o, asu(ar[q][t])); st128<AUXB>(rw, dc ? WS_END : o + RP * 4, asu(ai[q][t]));
      }
    }
  };
  st4(0); pin_acc(0); SB();
  st4(1); outp(0); pin_acc(1); SB();
  outp(1);
  MARK(3);
}

DI f32x16 mfma32(u32x4 a, u32x4 b, f32x16 c) {
  return __builtin_amdgcn_mfma_f32_32x32x16_bf16(__builtin_bit_cast(bf16x8, a), __builtin_bit_cast(bf16x8, b), c, 0, 0, 0);
}
DI f32x16 mfma32k8(u32x2 a, u32x2 b, f32x16 c) {
  return __builtin_amdgcn_mfma_f32_32x32x8bf16_1k(__builtin_bit_cast(s16x4, a), __builtin_bit_cast(s16x4, b), c, 0, 0, 0);
}
DI f32x16 scores(u32x4 k1, u32x2 k2, u32x4 q1, u32x2 q2) {
  f32x16 z;
#pragma unroll
  for (int i = 0; i < 16; i++) z[i] = 0.f;
  f32x16 s = mfma32(k1, q1, z);
  asm volatile("s_nop 11" : "+v"(s));
  return mfma32k8(k2, q2, s);
}
DI float max16(const f32x16& s) {
  float m = fmaxf(s[0], s[1]);
#pragma unroll
  for (int i = 2; i < 16; i++) m = fmaxf(m, s[i]);
  return m;
}
#define STR_(x) #x
#define STR(x) STR_(x)
#define KOF(t) ((((t)&7)>>1)*11264+((t)&1)*1280)
#define VOF(t) ((((t)&7)>>1)*11264+5120+((t)&1)*1536)
#define EXP(d, s) "v_exp_f32 v" #d ", v" #s "\n\t"
#define CVT(d, s) "v_cvt_pk_bf16_f32 v" #d ", v" #s ", v[" #s "+1]\n\t"
#define STEPA(t) \
  "v_mfma_f32_32x32x16_bf16 v[48:63], v[80:83], v[136:139], v[120:135]\n\t" \
  "ds_read_b128 v[80:83], v100 offset:" STR(KOF(t+1)) "\n\t" \
  EXP(64,32) EXP(65,33) CVT(78,68) CVT(79,70) EXP(66,34) EXP(67,35) \
  "v_mfma_f32_32x32x16_bf16 v[104:119], v[90:93], v[76:79], v[104:119]\n\t" \
  "ds_read_b64_tr_b16 v[90:91], v102 offset:" STR(VOF(t)+768) "\n\t" \
  "ds_read_b64_tr_b16 v[92:93], v102 offset:" STR(VOF(t)+1152) "\n\t" \
  EXP(68,36) EXP(69,37) CVT(72,64) CVT(73,66) EXP(70,38) EXP(71,39) \
  "v_mfma_f32_32x32x8_bf16 v[48:63], v[84:85], v[140:141], v[48:63]\n\t" \
  "ds_read_b64 v[84:85], v101 offset:" STR(KOF(t+1)) "\n\t" \
  EXP(64,40) EXP(65,41) CVT(74,68) CVT(75,70) EXP(66,42) EXP(67,43) \
  "s_waitcnt lgkmcnt(4)\n\t" \
  "v_mfma_f32_32x32x16_bf16 v[0:15], v[86:89], v[72:75], v[0:15]\n\t" \
  EXP(68,44) EXP(69,45) CVT(76,64) CVT(77,66) EXP(70,46) EXP(71,47)
#define STEPB(t) \
  "s_waitcnt lgkmcnt(0)\n\t" \
  "v_mfma_f32_32x32x16_bf16 v[32:47], v[80:83], v[94:97], v[16:31]\n\t" \
  EXP(64,48) EXP(65,49) CVT(78,68) CVT(79,70) EXP(66,50) EXP(67,51) \
  "v_mfma_f32_32x32x16_bf16 v[0:15], v[90:93], v[76:79], v[0:15]\n\t" \
  EXP(68,52) EXP(69,53) CVT(72,64) CVT(73,66) EXP(70,54) EXP(71,55) \
  "v_mfma_f32_32x32x8_bf16 v[32:47], v[84:85], v[98:99], v[32:47]\n\t" \
  EXP(64,56) EXP(65,57) CVT(74,68) CVT(75,70) EXP(66,58) EXP(67,59) \
  "v_mfma_f32_32x32x16_bf16 v[104:119], v[86:89], v[72:75], v[104:119]\n\t" \
  "ds_read_b64_tr_b16 v[86:87], v102 offset:" STR(VOF(t+1)) "\n\t" \
  "ds_read_b64_tr_b16 v[88:89], v102 offset:" STR(VOF(t+1)+384) "\n\t" \
  EXP(68,60) EXP(69,61) CVT(76,64) CVT(77,66) EXP(70,62) EXP(71,63)
#define PR(n) "s_setprio " #n "\n\t"
#define CHUNK(i) PR(1) STEPA(2*(i)) PR(0) STEPB(2*(i)) "s_barrier\n\t" PR(2) STEPA(2*(i)+1) STEPB(2*(i)+1)
#define Z4(a) "v_mov_b64 v[" #a ":" #a "+1], 0\n\tv_mov_b64 v[" #a "+2:" #a "+3], 0\n\t"

constexpr int HR = 4 * KVC;
constexpr int L_A = 2 * HR;
constexpr int L_EX = 15360;
constexpr int L_MISC = L_A + 70 * 1024;
constexpr int LDS_FUSED = L_MISC + 2048;

DI bool poll16(rsrc_t rw, unsigned off, unsigned need) {
  const int lane = threadIdx.x & 63;
#pragma unroll 1
  for (int spins = 0; spins < (1 << 20); spins++) {
    const unsigned v = gl32<16>(rw, off + (lane & 15) * 4);
    const unsigned got = (unsigned)__builtin_amdgcn_ballot_w64(v != 0) & 0xFFFFu;
    if ((got & need) == need) return true;
    __builtin_amdgcn_s_sleep(1);
  }
  return false;
}

template <bool FINAL>
__global__ void __launch_bounds__(768) k_fused(char* __restrict__ ws, u16* __restrict__ out) {
  STAMP(FINAL ? 4 : 2);
  extern __shared__ __attribute__((aligned(16))) char smem[];
  const lptr lds = (lptr)smem;
  const rsrc_t rw = mk_rsrc(ws, WS_END);
  const int lane = threadIdx.x & 63, w = wave_id(), g = lane >> 4, r16 = lane & 15;
  const int xcd = blockIdx.x & 7, bi = blockIdx.x >> 3, u = 8 * (xcd & 1) + (bi & 7), qt = 4 * (xcd >> 1) + (bi >> 3);
  const int hf = u & 1, ct = u >> 1;
  constexpr unsigned FKV = WS_FL + (FINAL ? 32768 : 0), KV = WS_KV + (FINAL ? LAYER2 : 0), OB = WS_O + (FINAL ? LAYER2 : 0);
  LDS_AS unsigned* misc = (LDS_AS unsigned*)(lds + L_MISC + 1024);

  if (__builtin_expect(w >= 8, 1)) {
    const int lw = w - 8;
    __builtin_amdgcn_s_setprio(3);
    if (lane < 2 && lw == 0) misc[lane] = 0u;
    {
      unsigned vm[4];
#pragma unroll
      for (int r = 0; r < 4; r++) vm[r] = (4 * (4 * lw + r) + g) * (RBP * 2) + 256 * qt + (((r16 >> 1) ^ (g | ((r & 2) << 1))) * 32) + (r16 & 1) * 16 + 3072 - 1024 * r;
      const unsigned l0 = lds_addr(lds) + lw * 4096, v0 = 2 * lw * 20480 + lane * 16, v2 = ((8 + (lw >> 1)) * 20 + (lw & 1)) * 1024 + lane * 16;
      const char* mb = ws + WS_RB - 3072; const char* sb = ws + WS_WQ + u * 204800;
      constexpr int CNT = FINAL ? 9 : 8;
#pragma unroll 1
      for (int s = 0; s < 10; s++) {
        if (s == 2) vm_bar<CNT>(); else if (s > 3) vm_bar<2 * CNT>();
        dma_stage(vm, v0, v0 + 20480 - 2048, mb, sb, l0 + (s & 3) * SLOT32);
        if (FINAL) dma1(v2, sb, l0 + (s & 3) * SLOT32 + 32768 - lw * 3072);
        mb += 64 * RBP * 2; sb += 2048;
      }
      vm_bar<2 * CNT>(); vm_bar<CNT>(); vm_bar<0>();
    }
    MARK(0);
    const int hh = lw >> 1, hp = lw & 1;
    const unsigned kvv = KV + (2 * u + hh) * 16 * KVC + hp * 5120 + lane * 16;
    const unsigned lkv = lds_addr(lds) + hh * HR + hp * 5120;
    const unsigned wv = (FINAL ? WS_WOP : WS_WOF) + (ct * 2 + hh) * 51200 + lane * 16;
    const unsigned myfl = FKV + (u * 16 + qt) * 128;
    lds_bar();
    raw_bar();
    MARK(1);
    {
      u32x4 v[6];
#pragma unroll
      for (int i = 0; i < 6; i++) v[i] = ld128(lds + hh * HR + hp * 5120 + lane * 16 + i * 1024);
#pragma unroll
      for (int i = 0; i < 6; i++) st128<16>(rw, kvv + qt * KVC + i * 1024, clean(v[i]));
    }
    drain();
    unsigned cnt = 0;
    if (lane == 0) cnt = __hip_atomic_fetch_add(misc, 1u, __ATOMIC_RELAXED, __HIP_MEMORY_SCOPE_WORKGROUP);
    if (__builtin_amdgcn_readfirstlane(cnt) == 3 && lane < 16) st32<16>(rw, FKV + (u * 16 + lane) * 128 + qt * 4, 1u);
    MARK(2);
    auto ldkv = [&](int seq) {
      const int ch = seq == 0 ? qt : seq < 13 ? ((qt & 12) + 3 + seq) & 15 : (qt & 12) | ((qt + seq) & 3);
      dma6_sc1(kvv + ch * KVC, ws, lkv + (seq & 3) * KVC);
    };
    auto fetch = [&](int seq) {
      const unsigned src = kvv + ((((qt & 12) + 3 + seq) & 15)) * KVC;
      u32x4 v[6];
#pragma unroll 1
      for (int spins = 0; spins < (1 << 18); spins++) {
#pragma unroll
        for (int i = 0; i < 6; i++) v[i] = gl128<16>(rw, src + i * 1024);
        unsigned m = 0;
#pragma unroll
        for (int i = 0; i < 6; i++) m = umax4(m, v[i]);
        if (!__builtin_amdgcn_ballot_w64(m == EMPTY)) break;
        __builtin_amdgcn_s_sleep(2);
      }
#pragma unroll
      for (int i = 0; i < 6; i++) *(LDS_AS u32x4*)(lds + hh * HR + hp * 5120 + seq * KVC + lane * 16 + i * 1024) = v[i];
    };
#pragma unroll 1
    for (int attempt = 0;; attempt++) {
      int c = -2;
      if (attempt) { ldkv(0); vm_bar<0>(); }
      else {
        fetch(1); MARK(3); lds_bar();
        MARK(4);
        fetch(2); poll16(rw, myfl, 12u << (((qt & 12) + 4) & 15)); ldkv(3); MARK(5); lds_bar();
        c = 1;
      }
#pragma unroll 1
      for (; c < 13; c++) {
        if (c == 2 && !attempt) poll16(rw, myfl, 0xFFFFu);
        if (c >= 2) { const int e = min(4 * (c - 2) + 2 * hp, 33); dma2(wv + e * 1024, ws, lds_addr(lds) + L_A + (hh * 35 + e) * 1024); }
        ldkv(c + 3);
        if (c == 0) vm_bar<12>();
        if (c >= 2) vm_bar<8>(); else if (c >= 0) vm_bar<6>();
      }
      vm_bar<0>();
      dma4(wv + (35 + 7 * hp) * 1024, ws, lds_addr(lds) + hh * HR + 7 * hp * 1024);
      dma4(wv + (39 + 7 * hp) * 1024, ws, lds_addr(lds) + hh * HR + (4 + 7 * hp) * 1024);
      if (!FINAL && bi == 0 && lw == 0) {
        unsigned sv;
        asm volatile("s_getpc_b64 vcc\n\ts_add_u32 vcc_lo, vcc_lo, " SYM_COL2 "@rel32@lo+4\n\ts_addc_u32 vcc_hi, vcc_hi, " SYM_COL2 "@rel32@hi+12\n\t"
                     M0SET "global_load_lds_dword %[v], vcc\n\t" M0RST : [sv] "=&s"(sv) : [v] "v"(min(lane, 47) * 128), [l] "s"(lds_addr(lds) + L_MISC + 1536) : "memory", "vcc", "scc");
      }
      if (!FINAL) {
        const u32x4 e = {EMPTY, EMPTY, EMPTY, EMPTY};
#pragma unroll
        for (int i = 0; i < 3; i++) {
          st128<16>(rw, OB + LAYER2 + ((u * 16 + qt) * 2 + hh) * 5120 + hp * 2048 + lane * 16 + i * 1024, e);
          st128<16>(rw, WS_X4 + ((u * 16 + qt) * 2 + hh) * 5120 + hp * 2048 + lane * 16 + i * 1024, e);
        }
        if ((qt & 3) < 2) {
#pragma unroll
          for (int i = 0; i < 6; i++) st128<16>(rw, kvv + LAYER2 + qt * KVC + i * 1024, e);
        }
      }
      raw_bar();
      vm_bar<0>();
      if (attempt || !*(volatile LDS_AS unsigned*)(misc + 1)) break;
      raw_bar();
    }
    return;
  }

  const int wm = w & 3, wn = w >> 2, h = lane >> 5;
  {
    constexpr int NT = FINAL ? 5 : 4;
    f32x4 acc[2][NT] = {}, gram = {0.f, 0.f, 0.f, 0.f};
    int mo[2]; unsigned a0[2], a1[2], a2[2], a_r[2];
#pragma unroll
    for (int m = 0; m < 2; m++) {
      const int q = r16 >> 2, pr = 16 * (m ^ wn) + r16;
      mo[m] = (8 * g + q) * 256 + (((2 * wm + (m ^ wn)) ^ (q | ((g & 1) << 2))) * 32) + (lane & 3) * 8;
      const unsigned qb = L_A + (wn * 4 + wm) * 1280, kb = wn * HR + wm * 1280, vr = wn * HR + 5120 + (32 * wm + pr) * 48 + (g == 3 ? 32 : g * 16);
      a0[m] = ((g >> 1) ? kb : qb) + (g & 1) * 512 + pr * 16;
      a1[m] = g == 2 ? qb + 1024 + pr * 8 : vr; a2[m] = g == 2 ? kb + 1024 + pr * 8 : vr + 8;
      a_r[m] = L_MISC + (32 * wm + pr) * 4;
      asm volatile("" : "+v"(a0[m]), "+v"(a1[m]), "+v"(a2[m]), "+v"(a_r[m]));
    }
    unsigned a_w = a_r[0] + (g == (r16 >> 2) ? 0 : 512);
    const int so = 16384 + lane * 16 + wn * 8192, so5 = 16384 + lane * 16 + (8 + wn) * 2048;
    u32x4 mf[2][2], sf[2][NT];
    auto rdm = [&](lptr sl, int k, int m) { const u32x2 x = ldtr(sl + mo[m] + k * 8192), y = ldtr(sl + mo[m] + k * 8192 + 1024); mf[k][m] = u32x4{x[0], x[1], y[0], y[1]}; };
    auto rds = [&](lptr sl, int k, int i) { sf[k][i] = ld128(sl + (i < 4 ? so + i * 2048 : so5) + k * 1024); };
    auto step = [&](int k, lptr nx) {
#pragma unroll
      for (int i = 0; i < NT; i++) {
        if (i < 2) { rdm(nx, k ^ 1, i); rds(nx, k ^ 1, i); }
        if (i == 2) { rds(nx, k ^ 1, 2); rds(nx, k ^ 1, 3); if (NT > 4) rds(nx, k ^ 1, 4); }
        SB();
        mfma16v(acc[0][i], sf[k][i], mf[k][0]); mfma16v(acc[1][i], sf[k][i], mf[k][1]);
        if (i == NT - 1) mfma16v(gram, mf[k][0], mf[k][0]);
        SB();
      }
    };
    asm volatile("" : "+v"(gram), "+v"(a_w));
#pragma unroll
    for (int i = 0; i < NT; i++) asm volatile("" : "+v"(acc[0][i]), "+v"(acc[1][i]));
    raw_bar();
    rdm(lds, 0, 0); rdm(lds, 0, 1);
#pragma unroll
    for (int i = 0; i < NT; i++) rds(lds, 0, i);
    for (int s = 0; s < 9; s++) {
      step(0, lds + (s & 3) * SLOT32);
      lds_bar();
      step(1, lds + ((s + 1) & 3) * SLOT32);
    }
    step(0, lds + SLOT32);
    mfma16v(gram, mf[1][0], mf[1][0]);
#pragma unroll
    for (int i = 0; i < 2; i++) { mfma16v(acc[0][i], sf[1][i], mf[1][0]); mfma16v(acc[1][i], sf[1][i], mf[1][1]); }
    asm volatile("s_nop 4" : "+v"(gram));
    { const float g0 = gram[0], g1 = gram[1], g2 = gram[2], g3 = gram[3], d0 = (lane & 1) ? g1 : g0, d1 = (lane & 1) ? g3 : g2, d = (lane & 2) ? d1 : d0;
      *(LDS_AS float*)(lds + a_w) = __builtin_amdgcn_rsqf(d * (1.f / 640.f) + 1e-5f); }
    SB();
    lds_bar();
    const float rs[2] = {*(LDS_AS float*)(lds + a_r[0]), *(LDS_AS float*)(lds + a_r[1])};
    SB();
#pragma unroll
    for (int i = 2; i < NT; i++) { mfma16v(acc[0][i], sf[1][i], mf[1][0]); mfma16v(acc[1][i], sf[1][i], mf[1][1]); }
    SB();
    MARK(0);
#pragma unroll
    for (int m = 0; m < 2; m++) *(LDS_AS u32x4*)(lds + a0[m]) = pk8(acc[m][0] * rs[m], acc[m][1] * rs[m]);
    asm volatile("s_nop 1" : "+v"(acc[0][2]), "+v"(acc[1][2]), "+v"(acc[0][3]), "+v"(acc[1][3]), "+v"(acc[0][NT - 1]), "+v"(acc[1][NT - 1]) :: "memory");
#pragma unroll
    for (int m = 0; m < 2; m++) {
      u32x2 hi = pk4(acc[m][3] * rs[m]);
      if (g == 3) hi = u32x2{0x3F80u, 0u};
      *(LDS_AS u32x2*)(lds + a1[m]) = pk4(acc[m][2] * rs[m]);
      *(LDS_AS u32x2*)(lds + a2[m]) = hi;
    }
    lds_bar();
    if (FINAL) {
      const unsigned x4 = WS_X4 + (((u * 16 + qt) * 4 + wm) * 2 + wn) * 1280;
      const u32x2 p0 = pk4(acc[0][NT - 1]), p1 = pk4(acc[1][NT - 1]), q0 = pk4(acc[0][3]), q1 = pk4(acc[1][3]);
      st128<16>(rw, x4 + lane * 16, clean(u32x4{p0[0], p0[1], p1[0], p1[1]}));
      if (g == 3) st128<16>(rw, x4 + 1024 + r16 * 16, clean(u32x4{q0[0], q0[1], q1[0], q1[1]}));
    }
  }
  MARK(1);
  const int head = 2 * u + wn, kh = w & 1;
  const lptr ring = lds + wn * HR;
  f32x16 o;
  {
    const lptr qpa = lds + L_A + (wn * 4 + wm) * 1280, qpb = lds + L_A + (wn * 4 + (wm ^ 1)) * 1280;
    const u32x4 qa1 = ld128(qpa + lane * 16), qb1 = ld128(qpb + lane * 16);
    u32x2 qa2 = ld64(qpa + 1024 + (lane & 31) * 8), qb2 = ld64(qpb + 1024 + (lane & 31) * 8);
    if (h) { qa2 = u32x2{0u, 0u}; qb2 = qa2; }
    const unsigned ka = lds_addr(ring) + kh * 2560 + lane * 16, kb = lds_addr(ring) + kh * 2560 + 1024 + (lane & 31) * 8;
    const unsigned va = lds_addr(ring) + kh * 3072 + (4 * h + (r16 >> 2)) * 48 + ((g & 1) ? ((lane & 3) ? 40 : 32) : (lane & 3) * 8);
    float ma, mb;
    {
      const u32x4 k1 = ld128(ring + lane * 16); const u32x2 k2 = ld64(ring + 1024 + (lane & 31) * 8);
      ma = max16(scores(k1, k2, qa1, qa2)); mb = max16(scores(k1, k2, qb1, qb2));
    }
    for (int attempt = 0;; attempt++) {
      ma = fmaxf(ma, __shfl_xor(ma, 32)); mb = fmaxf(mb, __shfl_xor(mb, 32));
      f32x16 cia, cib, oa, ob;
#pragma unroll
      for (int i = 0; i < 16; i++) { cia[i] = -ma; cib[i] = -mb; }
      int cnt = 4;
      MARK(2);
      asm volatile(
          "ds_read_b128 v[80:83], v100\n\t"
          "ds_read_b64 v[84:85], v101\n\t"
          "ds_read_b64_tr_b16 v[86:87], v102 offset:5120\n\t"
          "ds_read_b64_tr_b16 v[88:89], v102 offset:5504\n\t"
          Z4(0) Z4(68) Z4(76) Z4(90)
          "s_waitcnt lgkmcnt(0)\n\t"
          "v_mfma_f32_32x32x16_bf16 v[32:47], v[80:83], v[94:97], v[16:31]\n\t"
          Z4(4) Z4(8) Z4(12) Z4(104) Z4(108) Z4(112) Z4(116)
          "v_mfma_f32_32x32x8_bf16 v[32:47], v[84:85], v[98:99], v[32:47]\n\ts_nop 9\n\t"
          "L_attn_%=:\n\t"
          CHUNK(0) CHUNK(1) CHUNK(2) CHUNK(3)
          "s_sub_u32 %[cnt], %[cnt], 1\n\ts_cmp_lg_u32 %[cnt], 0\n\ts_cbranch_scc1 L_attn_%=\n\t"
          "s_setprio 0\n\t" CVT(78,68) CVT(79,70) "s_waitcnt lgkmcnt(0)\n\ts_nop 1\n\t"
          "v_mfma_f32_32x32x16_bf16 v[104:119], v[90:93], v[76:79], v[104:119]\n\ts_nop 15\n\t"
          : "={v[0:15]}"(oa), "={v[104:119]}"(ob), [cnt] "+s"(cnt)
          : "{v[16:31]}"(cia), "{v[120:135]}"(cib), "{v[94:97]}"(qa1), "{v[98:99]}"(qa2), "{v[136:139]}"(qb1), "{v[140:141]}"(qb2),
            "{v100}"(ka), "{v101}"(kb), "{v102}"(va)
          : "memory", "scc", "v32", "v33", "v34", "v35", "v36", "v37", "v38", "v39", "v40", "v41", "v42", "v43", "v44", "v45", "v46", "v47",
            "v48", "v49", "v50", "v51", "v52", "v53", "v54", "v55", "v56", "v57", "v58", "v59", "v60", "v61", "v62", "v63", "v64", "v65",
            "v66", "v67", "v68", "v69", "v70", "v71", "v72", "v73", "v74", "v75", "v76", "v77", "v78", "v79", "v80", "v81", "v82", "v83",
            "v84", "v85", "v86", "v87", "v88", "v89", "v90", "v91", "v92", "v93");
      MARK(3);
      LDS_AS f32x4* ex = (LDS_AS f32x4*)(ring + L_EX + wm * 3072) + lane;
#pragma unroll
      for (int i = 0; i < 3; i++) ex[i * 64] = f32x4{ob[4 * i], ob[4 * i + 1], ob[4 * i + 2], ob[4 * i + 3]};
      o = oa;
      f32x4 cs = {0.f, 0.f, 0.f, 0.f};
#pragma unroll
      for (int i = 0; i < 3; i++) cs += f32x4{oa[4 * i], oa[4 * i + 1], oa[4 * i + 2], oa[4 * i + 3]} + f32x4{ob[4 * i], ob[4 * i + 1], ob[4 * i + 2], ob[4 * i + 3]};
      if (!(fabsf(cs[0] + cs[1] + cs[2] + cs[3]) < INFINITY) || (h && !(oa[8] + ob[8] < 0x1p60f))) misc[1] = 1u;
      lds_bar();
      if (attempt || !*(volatile LDS_AS unsigned*)(misc + 1)) break;
      raw_bar();
      ma = -INFINITY; mb = -INFINITY;
      for (int t = 0; t < 64; t++) {
        const unsigned kb_ = KV + (head * 16 + (t >> 2)) * KVC + (t & 3) * 1280;
        const u32x4 k1 = gl128<16>(rw, kb_ + lane * 16); const u32x2 k2 = gl64<16>(rw, kb_ + 1024 + (lane & 31) * 8);
        ma = fmaxf(ma, max16(scores(k1, k2, qa1, qa2))); mb = fmaxf(mb, max16(scores(k1, k2, qb1, qb2)));
      }
      raw_bar();
    }
  }
  {
    LDS_AS f32x4* ey = (LDS_AS f32x4*)(ring + L_EX + (wm ^ 1) * 3072) + lane;
    f32x4 r[3];
#pragma unroll
    for (int i = 0; i < 3; i++) r[i] = ey[i * 64] + f32x4{o[4 * i], o[4 * i + 1], o[4 * i + 2], o[4 * i + 3]};
    const float il = __builtin_amdgcn_rcpf(__shfl(r[2][0], (lane & 31) + 32));
    const unsigned ob = OB + ((4 * qt + wm) * 32 + head) * 1280;
    const int sl_ = ((lane << 2) & 16) + ((lane >> 1) & 12) + (lane & 3);
    st128<16>(rw, ob + h * 512 + sl_ * 16, clean(pk8(r[0] * il, r[1] * il)));
    const u32x4 t = clean(pk8(r[2] * il, r[2] * il));
    if (!h) st64<16>(rw, ob + 1024 + sl_ * 8, u32x2{t[0], t[1]});
  }
  const int prow = 128 * qt + 64 * hf + 16 * wm, pblk = prow & ~31;
  const int rp0 = 1024 * (wm & 1) + (pblk >> 1);
  MARK(4);
  f32x4 res[3]; u32x2 x4v[3]; float dc = 0.f;
  const lptr wa = lds + L_A + wn * 35 * 1024 + lane * 16, wb = ring + lane * 16;
  const unsigned obase = OB + ((prow >> 5) * 32 + 16 * wn) * 1280;
  unsigned o1 = obase + (g >> 1) * 1280 + (g & 1) * 512 + (wm & 1) * 256 + r16 * 16, o2 = obase + 2 * g * 1280 + 1024 + (wm & 1) * 128 + r16 * 8;
  constexpr int NPRE = 15;
  u32x4 bf[50];
  f32x4 acc[5] = {};
  if (!FINAL) {
#pragma unroll
    for (int i = 0; i < 3; i++) res[i] = asf(gl128<0>(rw, WS_R + ((80 * ct + 48 * wn + 16 * (wn ? (i & 1) : i) + r16) * RP + rp0 + 4 * g) * 4));
  } else {
    dc = __uint_as_float(gl32<0>(rw, WS_R + (RP + rp0 + r16) * 4));
    unsigned o3[3];
#pragma unroll
    for (int i = 0; i < 3; i++) {
      const int cg = 12 * wn + 4 * ((i == 2 && wn) ? 0 : i) + g, pw = cg / 5, gq = cg - 5 * pw;
      o3[i] = WS_X4 + ((((2 * ct + (pw >> 1)) * 16 + qt) * 4 + 2 * hf + (wm >> 1)) * 2 + (pw & 1)) * 1280 + (gq * 16 + (trow(wm & 1, r16) & 15)) * 16 + ((((r16 >> 3) ^ pw) & 1) * 8);
    }
#pragma unroll 1
    for (int spins = 0; spins < (1 << 18); spins++) {
#pragma unroll
      for (int i = 0; i < 3; i++) x4v[i] = gl64<16>(rw, o3[i]);
      if (!__builtin_amdgcn_ballot_w64(umax4(max(x4v[0][0], x4v[0][1]), u32x4{x4v[1][0], x4v[1][1], x4v[2][0], x4v[2][1]}) == EMPTY)) break;
      __builtin_amdgcn_s_sleep(8);
    }
  }
#pragma unroll
  for (int j = 0; j < NPRE; j++) bf[j] = ld128(wa + j * 1024);
#pragma unroll
  for (int j = 0; j < 5; j++) asm volatile("" : "+v"(acc[j]));
  asm volatile("s_waitcnt vmcnt(0)" : "+v"(o1), "+v"(o2) :: "memory");
  MARK(5);
  {
    u32x4 af[10];
    auto ldf = [&](int i) {
      if (i < 8) af[i] = __builtin_amdgcn_raw_buffer_load_b128(rw, o1, i * 2 * 1280, 16);
      else { const u32x2 a = __builtin_amdgcn_raw_buffer_load_b64(rw, o2, (i - 8) * 8 * 1280, 16), b = __builtin_amdgcn_raw_buffer_load_b64(rw, o2, ((i - 8) * 8 + 1) * 1280, 16);
             af[i] = u32x4{a[0], a[1], b[0], b[1]}; }
    };
#pragma unroll
    for (int i = 0; i < 10; i++) ldf(i);
#pragma unroll 1
    for (int spins = 0; spins < (1 << 18); spins++) {
      unsigned m = 0;
#pragma unroll
      for (int i = 0; i < 10; i++) m = umax4(m, af[i]);
      if (!__builtin_amdgcn_ballot_w64(m == EMPTY)) break;
      __builtin_amdgcn_s_sleep(RSLEEP);
#pragma unroll
      for (int i = 0; i < 10; i++) if (__builtin_amdgcn_ballot_w64(umax4(0, af[i]) == EMPTY)) ldf(i);
    }
    MARK(8);
#pragma unroll
    for (int i = 0; i < 10; i++) asm volatile("" : "+v"(af[i]));
    asm volatile("s_nop 1" : "+v"(acc[0]), "+v"(acc[1]), "+v"(acc[2]), "+v"(acc[3]), "+v"(acc[4]));
    SB();
#pragma unroll
    for (int i = 0; i < 10; i++) {
#pragma unroll
      for (int a = 0; a < 5; a++) {
        if (FINAL) mfma16v(acc[a], bf[5 * i + a], af[i]); else mfma16v(acc[a], af[i], bf[5 * i + a]);
        const int j = 5 * i + a + NPRE; if (j < 50) bf[j] = ld128(j < 35 ? wa + j * 1024 : wb + (j - 35) * 1024);
        SB();
      }
    }
  }
  asm volatile("s_nop 7\n\ts_nop 1" : "+v"(acc[0]), "+v"(acc[1]), "+v"(acc[2]), "+v"(acc[3]), "+v"(acc[4]) :: "memory");
  MARK(6);
  {
    LDS_AS f32x4* ex = (LDS_AS f32x4*)(ring + L_EX + 12288 + wm * 3072) + lane;
    LDS_AS f32x4* ey = (LDS_AS f32x4*)(lds + (wn ^ 1) * HR + L_EX + 12288 + wm * 3072) + lane + 64 * wn;
    ex[0] = acc[2]; ex[64] = acc[3]; ex[128] = acc[4];
    if (FINAL) {
      dc *= 0.039528470752104741f;
#pragma unroll
      for (int i = 0; i < 3; i++) res[i] = f32x4{bflo(x4v[i][0]), bfhi(x4v[i][0]), bflo(x4v[i][1]), bfhi(x4v[i][1])} + dc;
    }
#pragma unroll
    for (int i = 0; i < 3; i++) acc[i] += res[i];
    lds_bar();
#pragma unroll
    for (int i = 0; i < 3; i++) acc[i] += ey[64 * i];
  }
  if (FINAL) {
    u16* op = out + (size_t)rowof(pblk + trow(wm & 1, r16)) * 640 + 80 * ct + 48 * wn + 4 * g;
#pragma unroll
    for (int i = 0; i < 3; i++)
      if (i < 2 || wn == 0) *(u32x2*)(op + 16 * i) = pk4(acc[i]);
  } else {
    f32x4 sq = {0.f, 0.f, 0.f, 0.f};
#pragma unroll
    for (int i = 0; i < 3; i++)
      if (i < 2 || wn == 0) {
        const int j = 80 * ct + 48 * wn + 16 * i + r16;
        st128<AUXB>(rw, WS_R + (j * RP + rp0 + 4 * g) * 4, asu(acc[i]));
        if (j != 1) sq += acc[i] * acc[i];
      }
#pragma unroll
    for (int e = 0; e < 4; e++) sq[e] = row16_sum(sq[e]);
    LDS_AS float* sl = (LDS_AS float*)(lds + L_MISC);
    if (r16 == 0) *(LDS_AS f32x4*)(sl + wn * 64 + 32 * (wm >> 1) + 8 * g + 4 * (wm & 1)) = sq;
    lds_bar();
    if (w == 0) st16<AUXB>(rw, ssq_off(128 * qt + 64 * hf + lane) + ct * 2, (u16)pk2(sl[lane] + sl[64 + lane], 0.f));
  }
  MARK(7);
}

#define STOPAT(n)
extern "C" void kernel_launch(void* const* d_in, const int* in_sizes, int n_in, void* d_out, int out_size, void* d_ws, size_t ws_size,
                              hipStream_t stream) {
  const u16* x = (const u16*)d_in[0];
  const u16* wr = (const u16*)d_in[1];
  const u16* wi = (const u16*)d_in[2];
  const u16* wqkv = (const u16*)d_in[4];
  const u16* wo = (const u16*)d_in[5];
  char* ws = (char*)d_ws;
  (void)hipFuncSetAttribute((const void*)k_prep, hipFuncAttributeMaxDynamicSharedMemorySize, LDS_PREP);
  (void)hipFuncSetAttribute((const void*)k_fused<false>, hipFuncAttributeMaxDynamicSharedMemorySize, LDS_FUSED);
  (void)hipFuncSetAttribute((const void*)k_fused<true>, hipFuncAttributeMaxDynamicSharedMemorySize, LDS_FUSED);
  k_prep<<<256, 512, LDS_PREP, stream>>>(x, wr, wi, wqkv, wo, ws);
  STOPAT(1)
  k_col<true><<<COLG, 256, 0, stream>>>(ws);
  STOPAT(2)
  k_fused<false><<<256, 768, LDS_FUSED, stream>>>(ws, (u16*)d_out);
  STOPAT(3)
  k_col<false><<<COLG, 256, 0, stream>>>(ws);
  STOPAT(4)
  k_fused<true><<<256, 768, LDS_FUSED, stream>>>(ws, (u16*)d_out);
}
```

```cpp
#include <hip/hip_runtime.h>
#include <hip/hip_bf16.h>
#include <utility>
#include <type_traits>

typedef unsigned short u16;
typedef __attribute__((ext_vector_type(16))) float f32x16;
typedef __attribute__((ext_vector_type(4))) float f32x4;
typedef __attribute__((ext_vector_type(2))) float f32x2;
typedef __attribute__((ext_vector_type(8))) __bf16 bf16x8;
typedef __attribute__((ext_vector_type(2))) __bf16 bf16x2;
typedef __attribute__((ext_vector_type(4))) short s16x4;
typedef __attribute__((ext_vector_type(4))) unsigned u32x4;
typedef __attribute__((ext_vector_type(2))) unsigned u32x2;
#define DI __device__ __forceinline__
#define LDS_AS __attribute__((address_space(3)))
typedef LDS_AS char* lptr;

constexpr double cx_cos_small(double x) { double x2 = x * x, t = 1, s = 1; for (int i = 1; i < 12; i++) { t *= -x2 / ((2 * i - 1) * (2 * i)); s += t; } return s; }
constexpr double cx_sin_small(double x) { double x2 = x * x, t = x, s = x; for (int i = 1; i < 12; i++) { t *= -x2 / ((2 * i) * (2 * i + 1)); s += t; } return s; }
constexpr double cx_cos640(int m) {
  m %= 640; if (m > 320) m = 640 - m; double sg = 1; if (m > 160) { m = 320 - m; sg = -1; }
  const double w = 6.283185307179586476925 / 640; return sg * (m <= 80 ? cx_cos_small(w * m) : cx_sin_small(w * (160 - m)));
}
constexpr u16 cx_bf16(double d) { float f = (float)d; unsigned u = __builtin_bit_cast(unsigned, f); u += 0x7FFFu + ((u >> 16) & 1u); return (u16)(u >> 16); }
struct Cos640 { double v[640]; };
constexpr Cos640 mk_cos() { Cos640 c{}; for (int m = 0; m < 640; m++) c.v[m] = cx_cos640(m); return c; }
constexpr Cos640 COS = mk_cos();
__host__ __device__ constexpr int kf_of(int G, int i) { return G ? G + 40 * i : (i == 0 ? 320 : i < 8 ? 40 * i : 40 * i - 300); }
__host__ __device__ constexpr int freq_of(int G, int i) { return kf_of(G, i) > 320 ? 640 - kf_of(G, i) : kf_of(G, i); }
constexpr double a1val(int out, int a) {
  if (out >= 40 || a >= 40) return 0;
  const int G = out >> 1, part = out & 1;
  if (G == 0) return (part && (a & 1)) ? -1 : 1;
  return COS.v[(16 * a * G + 160 * part) % 640];
}
constexpr double m2val(int G, int o, int k) {
  const int i = o >> 1, po = o & 1, part = k >> 4, b = k & 15, kf = kf_of(G, i);
  const double c = COS.v[(b * kf) % 640], sn = COS.v[(b * kf + 480) % 640];
  if (G == 0) {
    if (part != (i >= 8)) return 0;
    if (i == 0) return 0.039528470752104741 * (po ? 1.0 : c);
    return 0.055901699437494742 * (po ? -sn : c);
  }
  const double v = po ? (part ? c : -sn) : (part ? sn : c);
  return 0.055901699437494742 * ((po && kf > 320) ? -v : v);
}
struct DTab { u16 a1[3][64][8]; u16 a1b[3][64][4]; u16 pad[256]; u16 m2[20][2][64][8]; };
constexpr DTab mk_dtab() {
  DTab t{};
  for (int l = 0; l < 64; l++) {
    const int r = l & 15, g = l >> 4;
    for (int T = 0; T < 3; T++) {
      for (int e = 0; e < 8; e++) t.a1[T][l][e] = cx_bf16(a1val(16 * T + r, 16 * (e >> 2) + 4 * g + (e & 3)));
      for (int e = 0; e < 4; e++) t.a1b[T][l][e] = cx_bf16(a1val(16 * T + r, 32 + 4 * g + e));
    }
    for (int G = 0; G < 20; G++) for (int T = 0; T < 2; T++) for (int e = 0; e < 8; e++)
      t.m2[G][T][l][e] = cx_bf16(m2val(G, 8 * (r >> 2) + 4 * T + (r & 3), 8 * g + e));
  }
  return t;
}
static __device__ const __attribute__((aligned(1024))) DTab g_dt = mk_dtab();

struct CTab { u16 v[10 * 512]; };
constexpr CTab mk_ctab() {
  CTab c{}; u16 cs[64] = {};
  for (int m = 0; m < 64; m++) cs[m] = cx_bf16(cx_cos640(10 * m));
  for (int f = 0; f < 10; f++) for (int l = 0; l < 64; l++) for (int e = 0; e < 8; e++) {
    const int r = l & 15, g = l >> 4;
    const int m = f < 8 ? (8 * g + e) * (16 * (f >> 1) + r) : 2 * r * (16 * (e >> 2) + 4 * g + (e & 3));
    c.v[(f * 64 + l) * 8 + e] = cs[(m + (f & 1) * 48) & 63];
  }
  return c;
}
static __device__ const __attribute__((aligned(1024))) CTab g_ct = mk_ctab();

constexpr int RP = 2048 + 64, RBP = 2048 + 128;
constexpr unsigned RBSZ = 640 * RBP * 2;
constexpr unsigned WS_FL = 0;
constexpr unsigned WS_AUX = WS_FL + 3 * 32768;
constexpr unsigned WS_RS1 = WS_AUX + 2048;
constexpr unsigned WS_RB0 = WS_RS1 + 8192;
constexpr unsigned WS_RB1 = WS_RB0 + RBSZ;
constexpr unsigned WS_WF = WS_RB1 + RBSZ;
constexpr unsigned WS_WQ = WS_WF + 320 * 8192;
constexpr unsigned WS_WOF = WS_WQ + 16 * 10 * 20 * 1024;
constexpr unsigned WS_WOP = WS_WOF + 40 * 20 * 1024;
constexpr int KVC = 11264;
constexpr unsigned WS_KV = WS_WOP + 40 * 20 * 1024;
constexpr unsigned WS_O = WS_KV + 32 * 16 * KVC;
constexpr unsigned LAYER2 = 32 * 16 * KVC + 32 * 64 * 1280;
constexpr unsigned WS_X4 = WS_KV + 2 * LAYER2;
constexpr unsigned WS_R3 = WS_X4 + 2048 * 640 * 2;
constexpr unsigned WS_RB2 = WS_R3 + 640 * RP * 4;
constexpr unsigned WS_SSQ = WS_RB2 + RBSZ;
constexpr unsigned EMPTY = 0xFFFFFFFFu;
constexpr unsigned WS_END = WS_SSQ + 2048 * 16;
#define FILLDELAY 30
#define PFDELAY 40
#define YSLEEP 4
#define XSLEEP 4
#define RSLEEP 2
#define AUXB 16

#define STW 0
#define STAMP(k)
#define MARK(i)
#define SARG
#define SPASS
#define SB() __builtin_amdgcn_sched_barrier(0)
typedef __amdgpu_buffer_rsrc_t rsrc_t;
DI rsrc_t mk_rsrc(const void* p, unsigned bytes) { return __builtin_amdgcn_make_buffer_rsrc((void*)p, 0, bytes, 0x00020000); }
template <int AUX> DI void st128(rsrc_t r, unsigned off, u32x4 v) { __builtin_amdgcn_raw_buffer_store_b128(v, r, off, 0, AUX); }
template <int AUX> DI void st64(rsrc_t r, unsigned off, u32x2 v) { __builtin_amdgcn_raw_buffer_store_b64(v, r, off, 0, AUX); }
template <int AUX> DI void st32(rsrc_t r, unsigned off, unsigned v) { __builtin_amdgcn_raw_buffer_store_b32(v, r, off, 0, AUX); }
template <int AUX> DI void st16(rsrc_t r, unsigned off, u16 v) { __builtin_amdgcn_raw_buffer_store_b16(v, r, off, 0, AUX); }
template <int AUX> DI u32x4 gl128(rsrc_t r, unsigned off) { return __builtin_amdgcn_raw_buffer_load_b128(r, off, 0, AUX); }
template <int AUX> DI u32x2 gl64(rsrc_t r, unsigned off) { return __builtin_amdgcn_raw_buffer_load_b64(r, off, 0, AUX); }
template <int AUX> DI unsigned gl32(rsrc_t r, unsigned off) { return __builtin_amdgcn_raw_buffer_load_b32(r, off, 0, AUX); }
DI u32x4 asu(f32x4 v) { return __builtin_bit_cast(u32x4, v); }
DI f32x4 asf(u32x4 v) { return __builtin_bit_cast(f32x4, v); }
DI unsigned pk2(float a, float b) { f32x2 v = {a, b}; return __builtin_bit_cast(unsigned, __builtin_convertvector(v, bf16x2)); }
DI u32x2 pk4(f32x4 a) { return u32x2{pk2(a[0], a[1]), pk2(a[2], a[3])}; }
DI u32x4 pk8(f32x4 a, f32x4 b) { return u32x4{pk2(a[0], a[1]), pk2(a[2], a[3]), pk2(b[0], b[1]), pk2(b[2], b[3])}; }
DI u32x4 clean(u32x4 v) { return __builtin_elementwise_min(v, u32x4{EMPTY - 1, EMPTY - 1, EMPTY - 1, EMPTY - 1}); }
DI unsigned umax4(unsigned m, u32x4 v) { return max(max(max(m, v[0]), v[1]), max(v[2], v[3])); }
DI float bflo(unsigned u) { return __uint_as_float(u << 16); }
DI float bfhi(unsigned u) { return __uint_as_float(u & 0xFFFF0000u); }
DI float dot2(unsigned a, unsigned b, float c) { return __builtin_amdgcn_fdot2_f32_bf16(__builtin_bit_cast(bf16x2, a), __builtin_bit_cast(bf16x2, b), c, false); }
DI int rowof(int p) { return 32 * (((p >> 5) & 0x3C) + (p & 3)) + ((p << 2) & 16) + ((p >> 3) & 15); }
DI int rpos(int p) { return ((p << 8) & 1024) + ((p >> 3) << 2) + (p & 3); }
DI int trow(int T, int r) { return 8 * (r >> 2) + 4 * T + (r & 3); }
template <int CTRL> DI float dpp_add(float v) { return v + __int_as_float(__builtin_amdgcn_update_dpp(0, __float_as_int(v), CTRL, 0xF, 0xF, true)); }
DI float row16_sum(float v) { v = dpp_add<0x128>(v); v = dpp_add<0x124>(v); v = dpp_add<0x122>(v); return dpp_add<0x121>(v); }
DI f32x4 mfma16(u32x4 a, u32x4 b, f32x4 c) {
  return __builtin_amdgcn_mfma_f32_16x16x32_bf16(__builtin_bit_cast(bf16x8, a), __builtin_bit_cast(bf16x8, b), c, 0, 0, 0);
}
DI void mfma16v(f32x4& c, u32x4 a, u32x4 b) { asm volatile("v_mfma_f32_16x16x32_bf16 %0, %1, %2, %0" : "+v"(c) : "v"(a), "v"(b)); }
DI u32x2 ldtr(lptr p) { return __builtin_bit_cast(u32x2, __builtin_amdgcn_ds_read_tr16_b64_v4i16((LDS_AS s16x4*)p)); }
DI u32x4 ld128(lptr p) { return *(LDS_AS u32x4*)p; }
DI u32x2 ld64(lptr p) { return *(LDS_AS u32x2*)p; }
#define GL(v, s, o, x) "global_load_lds_dwordx4 " v ", " s " offset:" #o x "\n\t"
#define M0SET "s_mov_b32 %[sv], m0\n\ts_mov_b32 m0, %[l]\n\ts_nop 2\n\t"
#define M0ADD(x) "s_add_u32 m0, m0, " x "\n\ts_nop 0\n\t"
#define M0RST "s_mov_b32 m0, %[sv]"
DI void dma2(unsigned v, const char* sb, unsigned l) { unsigned sv;
  asm volatile(M0SET GL("%[v]", "%[s]", 0, "") GL("%[v]", "%[s]", 1024, "") M0RST : [sv] "=&s"(sv) : [v] "v"(v), [s] "s"(sb), [l] "s"(l) : "memory"); }
DI void dma4(unsigned v, const char* sb, unsigned l) { unsigned sv;
  asm volatile(M0SET GL("%[v]", "%[s]", 0, "") GL("%[v]", "%[s]", 1024, "") GL("%[v]", "%[s]", 2048, "") GL("%[v]", "%[s]", 3072, "") M0RST
               : [sv] "=&s"(sv) : [v] "v"(v), [s] "s"(sb), [l] "s"(l) : "memory"); }
DI void dma6_sc1(unsigned v, const char* sb, unsigned l) { unsigned sv;
  asm volatile(M0SET GL("%[v]", "%[s]", 0, " sc1") GL("%[v]", "%[s]", 1024, " sc1") GL("%[v]", "%[s]", 2048, " sc1") M0ADD("3072")
               GL("%[w]", "%[s]", 0, " sc1") GL("%[w]", "%[s]", 1024, " sc1") GL("%[w]", "%[s]", 2048, " sc1") M0RST
               : [sv] "=&s"(sv) : [v] "v"(v), [w] "v"(v + 3072), [s] "s"(sb), [l] "s"(l) : "memory", "scc"); }
template <int N> DI void vm_bar() { asm volatile("s_waitcnt vmcnt(%0)\n\ts_barrier" ::"i"(N) : "memory"); }
DI void lds_bar() { asm volatile("s_waitcnt lgkmcnt(0)\n\ts_barrier" ::: "memory"); }
DI void raw_bar() { asm volatile("s_barrier" ::: "memory"); }
template <int N> DI void nap() { __builtin_amdgcn_s_sleep(N); asm volatile("" ::: "memory"); }
DI void drain() { asm volatile("s_waitcnt vmcnt(0)" ::: "memory"); }
#define WARM_CODE(sym, lines) do { unsigned lo_, hi_; \
    asm volatile("s_getpc_b64 vcc\n\ts_add_u32 %0, vcc_lo, " sym "@rel32@lo+4\n\ts_addc_u32 %1, vcc_hi, " sym "@rel32@hi+12" : "=s"(lo_), "=s"(hi_) :: "vcc", "scc"); \
    if ((int)(threadIdx.x & 255) < (lines)) { const unsigned v_ = *(const unsigned*)((((unsigned long long)hi_ << 32) | lo_) + (threadIdx.x & 255) * 128); asm volatile("" ::"v"(v_)); } } while (0)
#define SYM_PREP "_Z6k_prepPKtS0_S0_S0_S0_Pc"
#define SYM_MAIN "_Z6k_mainPcPt"
DI int wave_id() { return __builtin_amdgcn_readfirstlane(threadIdx.x >> 6); }
DI unsigned lds_addr(lptr p) { return (unsigned)(size_t)p; }

DI int kappa_to_i(int kap) {
  int ks = kap >> 5, g = (kap >> 3) & 3, e = kap & 7;
  if (ks < 16) return (2 * ks + (g >> 1)) * 20 + 8 * (e >> 2) + 4 * (g & 1) + (e & 3);
  return (8 * (ks - 16) + 2 * g + (e >> 2)) * 20 + 16 + (e & 3);
}

DI unsigned wo_frag(int nt, int ks) {
  const int ct = nt / 5, c5 = nt - 5 * ct, kh = ks < 16 ? ks >> 3 : (ks - 16) >> 1, i = ks < 16 ? ks & 7 : 8 + (ks & 1);
  const int a = kh ? (c5 >= 3 ? c5 - 3 : c5 + 2) : c5;
  return ((ct * 2 + kh) * 50 + 5 * i + a) * 1024;
}

constexpr int SLOT32 = 36864;
DI void dma_a(const unsigned* vm, const char* mb, unsigned l) { unsigned sv;
  asm volatile(M0SET GL("%[a0]", "%[m]", 0, " sc1") GL("%[a1]", "%[m]", 1024, " sc1") GL("%[a2]", "%[m]", 2048, " sc1") GL("%[a3]", "%[m]", 3072, " sc1") M0RST
               : [sv] "=&s"(sv) : [a0] "v"(vm[0]), [a1] "v"(vm[1]), [a2] "v"(vm[2]), [a3] "v"(vm[3]), [m] "s"(mb), [l] "s"(l) : "memory"); }
DI void dma_s(unsigned v0, unsigned v1, const char* sb, unsigned l) { unsigned sv;
  asm volatile(M0SET GL("%[v0]", "%[s]", 0, "") GL("%[v0]", "%[s]", 1024, "") GL("%[v1]", "%[s]", 2048, "") GL("%[v1]", "%[s]", 3072, "") M0RST
               : [sv] "=&s"(sv) : [v0] "v"(v0), [v1] "v"(v1), [s] "s"(sb), [l] "s"(l) : "memory"); }
DI void dma1(unsigned v, const char* sb, unsigned l) { unsigned sv;
  asm volatile(M0SET GL("%[v]", "%[s]", 0, "") M0RST : [sv] "=&s"(sv) : [v] "v"(v), [s] "s"(sb), [l] "s"(l) : "memory"); }

constexpr int L_I2 = 40 * 1056, LDS_PREP = L_I2 + 20 * 2064;
__global__ void __launch_bounds__(512, 2) k_prep(const u16* __restrict__ x, const u16* __restrict__ wr, const u16* __restrict__ wi,
                                                 const u16* __restrict__ wqkv, const u16* __restrict__ wo, char* __restrict__ ws) {
  STAMP(0);
  extern __shared__ __attribute__((aligned(16))) char smem[];
  const lptr lds = (lptr)smem;
  const rsrc_t rw = mk_rsrc(ws, WS_END);
  const int tid = threadIdx.x, lane = tid & 63, w = wave_id(), g = lane >> 4, r16 = lane & 15;
  const int job = blockIdx.x;
  if (__builtin_expect(job < 144, 1)) {
    const bool cols = job >= 64 && job < 124;
    if (cols) {
#pragma unroll
      for (int i = 0; i < 5; i++) dma1((16 * (5 * w + i) + (lane >> 2)) * 3840 + (job - 64) * 64 + (lane & 3) * 16, (const char*)wqkv, lds_addr(lds) + (5 * w + i) * 1056);
    } else {
      const char* src = (const char*)(job < 64 ? x : wo);
      const int sl = 8 * (w & 3) + 4 * (w >> 2);
      src += (job < 64 ? rowof(32 * job + sl) : kappa_to_i(32 * (job - 124) + sl)) * 1280;
      const int st = (job < 64 ? 31 : 0) * 1280;
#pragma unroll
      for (int i = 0; i < 5; i++) dma1(1024 * i + lane * 16 + ((i == 4 || lane < 16 * i) ? i - 1 : i) * st, src, lds_addr(lds) + (5 * w + i) * 1024);
    }
    u32x4 a1[3], m2[3][2]; u32x2 a1b[3];
#pragma unroll
    for (int T = 0; T < 3; T++) { a1[T] = *(const u32x4*)g_dt.a1[T][lane]; a1b[T] = *(const u32x2*)g_dt.a1b[T][lane]; }
#pragma unroll
    for (int i = 0; i < 3; i++)
#pragma unroll
      for (int T = 0; T < 2; T++) m2[i][T] = *(const u32x4*)g_dt.m2[(i < 2 || w < 4) ? w + 8 * i : 0][T][lane];
    const f32x4 z4 = {0.f, 0.f, 0.f, 0.f};
    const lptr i2 = lds + L_I2;
    asm volatile("s_waitcnt vmcnt(0)" : "+v"(a1[0]), "+v"(a1[1]), "+v"(a1[2]), "+v"(a1b[0]), "+v"(a1b[1]), "+v"(a1b[2]) :: "memory");
    MARK(0);
    f32x4 t1[4][3];
    if (cols) {
      raw_bar();
      const lptr src = lds + (4 * g + (r16 >> 2)) * 1056 + (w >> 1) * 256 + (w & 1) * 32 + (lane & 3) * 8, srb = src + (32 - 8 * (g >> 1)) * 1056;
#pragma unroll
      for (int i = 0; i < 4; i++) {
        const u32x2 f0 = ldtr(src + i * 64), f1 = ldtr(src + 16 * 1056 + i * 64), f2 = ldtr(srb + i * 64);
#pragma unroll
        for (int T = 0; T < 3; T++) t1[i][T] = mfma16(u32x4{a1b[T][0], a1b[T][1], 0u, 0u}, u32x4{f2[0], f2[1], 0u, 0u}, mfma16(a1[T], u32x4{f0[0], f0[1], f1[0], f1[1]}, z4));
      }
      const int xo = ((w >> 1) * 8) ^ (((r16 >> 1) & 3) * 16);
      const lptr dst = i2 + 2 * g * 2064 + (16 * (w & 1) + r16) * 64;
#pragma unroll
      for (int T = 0; T < 3; T++)
#pragma unroll
        for (int e = 0; e < 4; e++)
          if (T < 2 || g < 2) *(LDS_AS u32x2*)(dst + (8 * T + (e >> 1)) * 2064 + (xo ^ ((e & 1) * 32))) = u32x2{pk2(t1[0][T][e], t1[1][T][e]), pk2(t1[2][T][e], t1[3][T][e])};
    } else {
      const lptr src = lds + w * 5120 + lane * 8, srb = lds + w * 5120 + 1024 + (lane & 31) * 8;
#pragma unroll
      for (int i = 0; i < 4; i++) {
        const u32x2 f0 = ldtr(src + i * 1280), f1 = ldtr(src + i * 1280 + 512), f2 = ldtr(srb + i * 1280);
#pragma unroll
        for (int T = 0; T < 3; T++) t1[i][T] = mfma16(u32x4{f2[0], f2[1], 0u, 0u}, u32x4{a1b[T][0], a1b[T][1], 0u, 0u}, mfma16(u32x4{f0[0], f0[1], f1[0], f1[1]}, a1[T], z4));
      }
      const int xo = ((r16 & 1) * 32 + g * 8) ^ ((w & 1) * 32);
      const lptr dst = i2 + (r16 >> 1) * 2064 + w * 256;
#pragma unroll
      for (int i = 0; i < 4; i++)
#pragma unroll
        for (int T = 0; T < 3; T++)
          if (T < 2 || r16 < 8) *(LDS_AS u32x2*)(dst + T * 8 * 2064 + i * 64 + (xo ^ ((i >> 1) * 16))) = pk4(t1[i][T]);
    }
    lds_bar();
    MARK(1);
    f32x4 sq[2] = {z4, z4};
    unsigned dq[2]; float sc[2];
    if (cols) {
#pragma unroll
      for (int m = 0; m < 2; m++) {
        const int c = 32 * (job - 64) + 16 * m + r16;
        const int which = c / 640, rem = c - which * 640, head = rem / 20, d = rem - head * 20, grp = d >> 2;
        int T, gs;
        if (grp < 4) { T = (which == 2 ? 2 : 0) + (grp & 1); gs = (which == 1 ? 2 : 0) + (grp >> 1); }
        else { T = (which == 1) ? 3 : 2; gs = (which == 2) ? 3 : 2; }
        sc[m] = which == 0 ? 0.22360679774997896f * 1.4426950408889634f : 1.f;
        dq[m] = WS_WQ + (((head >> 1) * 10 + 4 * (head & 1) + T) * 20) * 1024 + (g * 16 + 4 * gs + (d & 3)) * 16;
      }
    }
#pragma unroll
    for (int i = 0; i < 3; i++)
      if (i < 2 || w < 4) {
        const int G = w + 8 * i;
        const lptr fp = i2 + G * 2064 + r16 * 64 + ((g ^ ((r16 >> 1) & 3)) * 16);
        const u32x4 f[2] = {ld128(fp), ld128(fp + 1024)};
        if (cols) {
#pragma unroll
          for (int m = 0; m < 2; m++) st128<AUXB>(rw, dq[m] + G * 1024, pk8(mfma16(m2[i][0], f[m], z4) * sc[m], mfma16(m2[i][1], f[m], z4) * sc[m]));
        } else {
#pragma unroll
          for (int T = 0; T < 2; T++) {
            const int j = 32 * G + trow(T, r16);
            const f32x4 d0 = mfma16(f[0], m2[i][T], z4), d1 = mfma16(f[1], m2[i][T], z4);
            st128<AUXB>(rw, job < 64 ? WS_RB0 + (j * RBP + 32 * job + 8 * g) * 2 : WS_WOF + wo_frag(j >> 4, job - 124) + (g * 16 + (j & 15)) * 16, pk8(d0, d1));
            if (j != 1) { sq[0] += d0 * d0; sq[1] += d1 * d1; }
          }
        }
      }
    MARK(2);
    if (job < 64) {
      LDS_AS float* sl = (LDS_AS float*)lds;
#pragma unroll
      for (int m = 0; m < 2; m++) {
#pragma unroll
        for (int e = 0; e < 4; e++) sq[m][e] = row16_sum(sq[m][e]);
        if (r16 == 0) *(LDS_AS f32x4*)(sl + w * 32 + 8 * g + 4 * m) = sq[m];
      }
      lds_bar();
      if (tid < 32) {
        float v = 0.f;
#pragma unroll
        for (int i = 0; i < 8; i++) v += sl[i * 32 + tid];
        st32<AUXB>(rw, WS_RS1 + rpos(32 * job + tid) * 4, __float_as_uint(__builtin_amdgcn_rsqf((v + 640.f * 1e-5f) * (2048.f * 2048.f / 640.f))));
      }
    }
    if (job >= 124) {
#pragma unroll
      for (int i = 0; i < 5; i++) {
        const lptr a = lds + (4 * g + (r16 >> 2)) * 1280 + (5 * w + i) * 32 + (lane & 3) * 8;
        const u32x2 lo = ldtr(a), hi = ldtr(a + 16 * 1280);
        st128<AUXB>(rw, WS_WOP + wo_frag(5 * w + i, job - 124) + lane * 16, u32x4{lo[0], lo[1], hi[0], hi[1]});
      }
    }
    return;
  }
  const int mi = job - 144;
  if (mi < 64) {
    const int m = mi >> 3, oc = mi & 7;
    const char* src = (const char*)((w >> 2) ? wi : wr) + (64 * (4 * m + (w & 3)) + 8 * oc) * 642;
    dma4(lane * 16, src, lds_addr(lds) + w * 5152);
    dma1(4096 + lane * 16, src, lds_addr(lds) + w * 5152 + 4096);
    if (lane == 0) dma1(5120, src, lds_addr(lds) + w * 5152 + 5120);
    vm_bar<0>();
    MARK(3);
    LDS_AS u16* L = (LDS_AS u16*)lds + (tid & 7) * 321;
#pragma unroll
    for (int it = 0; it < 5; it++) {
      const int c = (tid >> 3) + 64 * it, col = freq_of(c >> 4, c & 15);
      u32x4 o;
#pragma unroll
      for (int e = 0; e < 4; e++) o[e] = (unsigned)L[e * 2576 + col] | ((unsigned)L[(4 + e) * 2576 + col] << 16);
      st128<AUXB>(rw, WS_WF + c * 8192 + (m * 64 + 8 * oc + (tid & 7)) * 16, o);
    }
    MARK(4);
  } else {
    const int i0 = (mi - 64) * 512 + tid;
    if (mi < 72) WARM_CODE(SYM_PREP, 64);
    else if (mi < 80) { WARM_CODE(SYM_MAIN, 248); if (tid >= 256 && tid < 336) { const unsigned v_ = ((const unsigned*)&g_ct)[(tid - 256) * 32]; asm volatile("" ::"v"(v_)); } }
    if (i0 < 6272) st128<AUXB>(rw, WS_FL + i0 * 16, u32x4{0u, 0u, 0u, 0u});
#pragma unroll
    for (int it = 0; it < 8; it++) { const int i = i0 + it * 24576; if (i < (int)RBSZ / 16) st128<AUXB>(rw, WS_RB1 + i * 16, u32x4{EMPTY, EMPTY, EMPTY, EMPTY}); }
    for (int pc = i0; pc < 640 * 80; pc += 24576) {
      const int n = pc % 640, kq = pc / 640;
      const int u = n / 40, rn = n - 40 * u, hn = rn / 20, dn = rn - 20 * hn, gq = dn >> 2;
      const int T = gq < 4 ? 8 + hn : 4 * hn + 3, gs = gq < 4 ? gq : 3;
      float v[8];
#pragma unroll
      for (int e = 0; e < 4; e++) {
        const float a = (float)((freq_of(kq >> 2, 4 * (kq & 3) + e) * n) % 640) * (1.f / 640.f);
        v[2 * e] = 0.055901699437494742f * __builtin_amdgcn_cosf(a);
        v[2 * e + 1] = -0.055901699437494742f * __builtin_amdgcn_sinf(a);
      }
      if (kq == 0) { v[0] = (n & 1) ? -0.039528470752104741f : 0.039528470752104741f; v[1] = 0.039528470752104741f; }
      st128<AUXB>(rw, WS_WQ + ((u * 10 + T) * 20 + (kq >> 2)) * 1024 + ((kq & 3) * 16 + 4 * gs + (dn & 3)) * 16,
                  u32x4{pk2(v[0], v[1]), pk2(v[2], v[3]), pk2(v[4], v[5]), pk2(v[6], v[7])});
    }
  }
}

#define PINF(x) asm volatile("" : "+v"(x))
template <bool FIRST>
DI void col_phase(rsrc_t rw, lptr lds, int c0 SARG) {
  int lane = threadIdx.x & 63; PINF(lane);
  const int wv = wave_id(), w = wv & 3, q = wv >> 2, tid = w * 64 + lane, g = lane >> 4, r16 = lane & 15;
  const int c = c0 + q;
  const lptr img = lds + q * SLOT32;
  f32x4 xr[2], xi[2], rs[2]; u32x4 wf[2], xb[2];
  if (FIRST) {
#pragma unroll
    for (int t = 0; t < 2; t++) { xb[t] = gl128<0>(rw, WS_RB0 + ((2 * c + t) * RBP + 8 * tid) * 2); rs[t] = asf(gl128<0>(rw, WS_RS1 + (1024 * t + 4 * tid) * 4)); }
  }
  u32x4 c64[2], s64[2], n64[2], c32[2], s32[2], n32[2];
  const u32x4* ct = (const u32x4*)&g_ct + lane;
  c64[0] = ct[(2 * w) * 64]; s64[0] = ct[(2 * w + 1) * 64]; c32[0] = ct[8 * 64]; s32[0] = ct[9 * 64];
#pragma unroll
  for (int t = 0; t < 2; t++) wf[t] = gl128<0>(rw, WS_WF + c * 8192 + ((4 * t + g) * 64 + 16 * w + r16) * 16);
  SB();
  f32x4 t1c[2], t1s[2], t2c[2], t2s[2];
  {
    const float f1 = (float)(16 * w + r16) * (1.f / 2048.f), b1 = (float)(4 * g), f2 = (float)(16 * w + 4 * g) * (1.f / 2048.f), b2 = (float)r16;
#pragma unroll
    for (int t = 0; t < 2; t++) {
#pragma unroll
      for (int e = 0; e < 4; e++) {
        const float a1 = (b1 + (float)(16 * t + e)) * f1, a2 = (b2 + (float)(16 * t)) * (f2 + (float)e * (1.f / 2048.f));
        t1c[t][e] = __builtin_amdgcn_cosf(a1); t1s[t][e] = __builtin_amdgcn_sinf(-a1);
        t2c[t][e] = __builtin_amdgcn_cosf(a2); t2s[t][e] = __builtin_amdgcn_sinf(a2);
      }
      PINF(t1c[t]); PINF(t1s[t]); PINF(t2c[t]); PINF(t2s[t]);
    }
  }
  int wo_ = r16 * 128 + (((2 * w + (g >> 1)) ^ (r16 >> 1)) * 16) + (g & 1) * 8, ro_ = r16 * 128 + ((g ^ (r16 >> 1)) * 16), ro2 = ro_ ^ 64;
  PINF(wo_); PINF(ro_); PINF(ro2);
  {
    const unsigned par = (lane & 1) ? 0x80008000u : 0u;
    c64[1] = c64[0] ^ par; s64[1] = s64[0] ^ par; n64[0] = s64[0] ^ 0x80008000u; n64[1] = s64[1] ^ 0x80008000u;
    c32[1] = c32[0] ^ 0x80000000u; s32[1] = s32[0] ^ 0x80000000u; n32[0] = s32[0] ^ 0x80008000u; n32[1] = s32[0] ^ 0x00008000u;
    PINF(c64[1]); PINF(s64[1]); PINF(n64[0]); PINF(n64[1]); PINF(c32[1]); PINF(s32[1]); PINF(n32[0]); PINF(n32[1]);
  }
  SB();
  if (FIRST) {
#pragma unroll
    for (int t = 0; t < 2; t++) {
      xr[t] = f32x4{bflo(xb[0][2 * t]), bfhi(xb[0][2 * t]), bflo(xb[0][2 * t + 1]), bfhi(xb[0][2 * t + 1])};
      xi[t] = f32x4{bflo(xb[1][2 * t]), bfhi(xb[1][2 * t]), bflo(xb[1][2 * t + 1]), bfhi(xb[1][2 * t + 1])};
    }
  } else {
    drain();
    u32x2 sp[8];
    const unsigned so = WS_SSQ + (1024 * q + 4 * tid) * 2, xo = WS_R3 + (2 * c * RP + 4 * tid) * 4;
    bool bs = true, bx = true;
#pragma unroll 1
    for (int spins = 0; spins < (1 << 18); spins++) {
      if (bx) {
#pragma unroll
        for (int t = 0; t < 2; t++) { xr[t] = asf(gl128<16>(rw, xo + 4096 * t)); xi[t] = asf(gl128<16>(rw, xo + RP * 4 + 4096 * t)); }
      }
      if (bs) {
#pragma unroll
        for (int i = 0; i < 8; i++) sp[i] = gl64<16>(rw, so + i * 4096);
      }
      unsigned m = 0;
#pragma unroll
      for (int t = 0; t < 2; t++) m = umax4(umax4(m, asu(xr[t])), asu(xi[t]));
      bx = __builtin_amdgcn_ballot_w64(m == EMPTY) != 0;
      m = 0;
#pragma unroll
      for (int i = 0; i < 8; i++) m = max(max(m, sp[i][0]), sp[i][1]);
      bs = __builtin_amdgcn_ballot_w64(m == EMPTY) != 0;
      if (!bs && !bx) break;
      nap<YSLEEP>();
    }
    f32x4 r = {640.f * 1e-5f, 640.f * 1e-5f, 640.f * 1e-5f, 640.f * 1e-5f};
#pragma unroll
    for (int i = 0; i < 8; i++) r += f32x4{bflo(sp[i][0]), bfhi(sp[i][0]), bflo(sp[i][1]), bfhi(sp[i][1])};
#pragma unroll
    for (int e = 0; e < 4; e++) r[e] = __builtin_amdgcn_rsqf(r[e] * (2048.f * 2048.f / 640.f));
    LDS_AS f32x4* ex = (LDS_AS f32x4*)(lds + 2 * SLOT32) + tid;
    ex[q * 256] = r;
    lds_bar();
    rs[0] = ex[0]; rs[1] = ex[256];
  }
  MARK(FIRST ? 9 : 21);
#pragma unroll
  for (int t = 0; t < 2; t++) {
    u32x2 pb = pk4(xi[t] * rs[t]);
    if (c == 0) pb = u32x2{0u, 0u};
    *(LDS_AS u32x2*)(img + wo_ + t * 2048) = pk4(xr[t] * rs[t]);
    *(LDS_AS u32x2*)(img + 4096 + wo_ + t * 2048) = pb;
  }
  lds_bar();
  u32x4 ur[2][2], ui[2][2], br, bi;
  f32x4 ar[2], ai[2];
  const f32x4 z4 = {0.f, 0.f, 0.f, 0.f};
  auto rd_in = [&](int off) {
#pragma unroll
    for (int ks = 0; ks < 2; ks++)
#pragma unroll
      for (int t = 0; t < 2; t++) { const lptr p = img + off + (ks ? ro2 : ro_) + t * 2048; ur[t][ks] = ld128(p); ui[t][ks] = ld128(p + 4096); }
    SB();
  };
  auto cmul = [&](const f32x4* cc, const f32x4* sn) {
    br = pk8(ar[0] * cc[0] - ai[0] * sn[0], ar[1] * cc[1] - ai[1] * sn[1]);
    bi = pk8(ai[0] * cc[0] + ar[0] * sn[0], ai[1] * cc[1] + ar[1] * sn[1]);
  };
  rd_in(0);
#pragma unroll
  for (int ks = 0; ks < 2; ks++) {
#pragma unroll
    for (int t = 0; t < 2; t++) { ar[t] = mfma16(ur[t][ks], c64[ks], ks ? ar[t] : z4); ai[t] = mfma16(ui[t][ks], c64[ks], ks ? ai[t] : z4); }
#pragma unroll
    for (int t = 0; t < 2; t++) { ar[t] = mfma16(ui[t][ks], s64[ks], ar[t]); ai[t] = mfma16(ur[t][ks], n64[ks], ai[t]); }
  }
  const f32x4 fa[2] = {asf(wf[0] << 16), asf(wf[1] << 16)}, fb[2] = {asf(wf[0] & 0xFFFF0000u), asf(wf[1] & 0xFFFF0000u)};
  cmul(t1c, t1s);
#pragma unroll
  for (int t = 0; t < 2; t++) { ar[t] = mfma16(c32[t], br, z4); ai[t] = mfma16(c32[t], bi, z4); }
#pragma unroll
  for (int t = 0; t < 2; t++) { ar[t] = mfma16(s32[t], bi, ar[t]); ai[t] = mfma16(n32[t], br, ai[t]); }
  cmul(fa, fb);
#pragma unroll
  for (int t = 0; t < 2; t++) { ar[t] = mfma16(br, c32[t], z4); ai[t] = mfma16(bi, c32[t], z4); }
#pragma unroll
  for (int t = 0; t < 2; t++) { ar[t] = mfma16(bi, n32[t], ar[t]); ai[t] = mfma16(br, s32[t], ai[t]); }
  cmul(t2c, t2s);
#pragma unroll
  for (int t = 0; t < 2; t++) {
    *(LDS_AS u32x2*)(img + 8192 + wo_ + t * 2048) = u32x2{br[2 * t], br[2 * t + 1]};
    *(LDS_AS u32x2*)(img + 12288 + wo_ + t * 2048) = u32x2{bi[2 * t], bi[2 * t + 1]};
  }
  lds_bar();
  rd_in(8192);
#pragma unroll
  for (int ks = 0; ks < 2; ks++) {
#pragma unroll
    for (int t = 0; t < 2; t++) { ar[t] = mfma16(c64[ks], ur[t][ks], ks ? ar[t] : xr[t]); ai[t] = mfma16(c64[ks], ui[t][ks], ks ? ai[t] : xi[t]); }
#pragma unroll
    for (int t = 0; t < 2; t++) { ar[t] = mfma16(n64[ks], ui[t][ks], ar[t]); ai[t] = mfma16(s64[ks], ur[t][ks], ai[t]); }
  }
  const unsigned ob = (FIRST ? WS_RB1 : WS_RB2) + (2 * c * RBP + 8 * tid) * 2;
  st128<16>(rw, ob, clean(pk8(ar[0], ar[1])));
  st128<16>(rw, ob + RBP * 2, c == 0 ? u32x4{0u, 0u, 0u, 0u} : clean(pk8(ai[0], ai[1])));
}

DI f32x16 mfma32(u32x4 a, u32x4 b, f32x16 c) {
  return __builtin_amdgcn_mfma_f32_32x32x16_bf16(__builtin_bit_cast(bf16x8, a), __builtin_bit_cast(bf16x8, b), c, 0, 0, 0);
}
DI f32x16 mfma32k8(u32x2 a, u32x2 b, f32x16 c) {
  return __builtin_amdgcn_mfma_f32_32x32x8bf16_1k(__builtin_bit_cast(s16x4, a), __builtin_bit_cast(s16x4, b), c, 0, 0, 0);
}
DI f32x16 scores(u32x4 k1, u32x2 k2, u32x4 q1, u32x2 q2) {
  f32x16 z;
#pragma unroll
  for (int i = 0; i < 16; i++) z[i] = 0.f;
  f32x16 s = mfma32(k1, q1, z);
  asm volatile("s_nop 11" : "+v"(s));
  return mfma32k8(k2, q2, s);
}
DI float max16(const f32x16& s) {
  float m = fmaxf(s[0], s[1]);
#pragma unroll
  for (int i = 2; i < 16; i++) m = fmaxf(m, s[i]);
  return m;
}
#define STR_(x) #x
#define STR(x) STR_(x)
#define KOF(t) ((((t)&7)>>1)*11264+((t)&1)*1280)
#define VOF(t) ((((t)&7)>>1)*11264+5120+((t)&1)*1536)
#define EXP(d, s) "v_exp_f32 v" #d ", v" #s "\n\t"
#define CVT(d, s) "v_cvt_pk_bf16_f32 v" #d ", v" #s ", v[" #s "+1]\n\t"
#define STEPA(t) \
  "v_mfma_f32_32x32x16_bf16 v[48:63], v[80:83], v[136:139], v[120:135]\n\t" \
  "ds_read_b128 v[80:83], v100 offset:" STR(KOF(t+1)) "\n\t" \
  EXP(64,32) EXP(65,33) CVT(78,68) CVT(79,70) EXP(66,34) EXP(67,35) \
  "v_mfma_f32_32x32x16_bf16 v[104:119], v[90:93], v[76:79], v[104:119]\n\t" \
  "ds_read_b64_tr_b16 v[90:91], v102 offset:" STR(VOF(t)+768) "\n\t" \
  "ds_read_b64_tr_b16 v[92:93], v102 offset:" STR(VOF(t)+1152) "\n\t" \
  EXP(68,36) EXP(69,37) CVT(72,64) CVT(73,66) EXP(70,38) EXP(71,39) \
  "v_mfma_f32_32x32x8_bf16 v[48:63], v[84:85], v[140:141], v[48:63]\n\t" \
  "ds_read_b64 v[84:85], v101 offset:" STR(KOF(t+1)) "\n\t" \
  EXP(64,40) EXP(65,41) CVT(74,68) CVT(75,70) EXP(66,42) EXP(67,43) \
  "s_waitcnt lgkmcnt(4)\n\t" \
  "v_mfma_f32_32x32x16_bf16 v[0:15], v[86:89], v[72:75], v[0:15]\n\t" \
  EXP(68,44) EXP(69,45) CVT(76,64) CVT(77,66) EXP(70,46) EXP(71,47)
#define STEPB(t) \
  "s_waitcnt lgkmcnt(0)\n\t" \
  "v_mfma_f32_32x32x16_bf16 v[32:47], v[80:83], v[94:97], v[16:31]\n\t" \
  EXP(64,48) EXP(65,49) CVT(78,68) CVT(79,70) EXP(66,50) EXP(67,51) \
  "v_mfma_f32_32x32x16_bf16 v[0:15], v[90:93], v[76:79], v[0:15]\n\t" \
  EXP(68,52) EXP(69,53) CVT(72,64) CVT(73,66) EXP(70,54) EXP(71,55) \
  "v_mfma_f32_32x32x8_bf16 v[32:47], v[84:85], v[98:99], v[32:47]\n\t" \
  EXP(64,56) EXP(65,57) CVT(74,68) CVT(75,70) EXP(66,58) EXP(67,59) \
  "v_mfma_f32_32x32x16_bf16 v[104:119], v[86:89], v[72:75], v[104:119]\n\t" \
  "ds_read_b64_tr_b16 v[86:87], v102 offset:" STR(VOF(t+1)) "\n\t" \
  "ds_read_b64_tr_b16 v[88:89], v102 offset:" STR(VOF(t+1)+384) "\n\t" \
  EXP(68,60) EXP(69,61) CVT(76,64) CVT(77,66) EXP(70,62) EXP(71,63)
#define PR(n) "s_setprio " #n "\n\t"
#define CHUNK(i) PR(1) STEPA(2*(i)) PR(0) STEPB(2*(i)) "s_barrier\n\t" PR(2) STEPA(2*(i)+1) STEPB(2*(i)+1)
#define Z4(a) "v_mov_b64 v[" #a ":" #a "+1], 0\n\tv_mov_b64 v[" #a "+2:" #a "+3], 0\n\t"

#define R4(b) "v[" STR(b) ":" STR(b) "+3]"
#define MM(d, a, b) "v_mfma_f32_16x16x32_bf16 " R4(d) ", " R4(a) ", " R4(b) ", " R4(d) "\n\t"
#define GM(B, m, i) MM(20*m+4*i, B+8+4*i, B+4*m)
#define RT(B, m, hf, k) "ds_read_b64_tr_b16 v[" STR(B+4*m+2*hf) ":" STR(B+4*m+2*hf+1) "], v10" #m " offset:" STR(k*8192+hf*1024) "\n\t"
#define RS(B, i, k) "ds_read_b128 " R4(B+8+4*i) ", v102 offset:" STR(i*2048+k*1024) "\n\t"
#define IF5_4(x) ""
#define IF5_5(x) x
#define GSTEP(N, B, C, k) GM(B,0,0) RT(C,0,0,k) RT(C,0,1,k) GM(B,1,0) RS(C,0,k) RT(C,1,0,k) GM(B,0,1) RT(C,1,1,k) RS(C,1,k) GM(B,1,1) RS(C,2,k) \
  GM(B,0,2) RS(C,3,k) GM(B,1,2) IF5_##N(RS(C,4,k)) GM(B,0,3) GM(B,1,3) IF5_##N(GM(B,0,4) GM(B,1,4)) MM(40,B,B)
#define GEMM_ASM(N) \
  Z4(0) Z4(4) Z4(8) Z4(12) Z4(16) Z4(20) Z4(24) Z4(28) Z4(32) Z4(36) Z4(40) "s_mov_b32 %[c], 0\n\ts_barrier\n\t" \
  RT(44,0,0,0) RT(44,0,1,0) RS(44,0,0) RT(44,1,0,0) RT(44,1,1,0) RS(44,1,0) RS(44,2,0) RS(44,3,0) IF5_##N(RS(44,4,0)) \
  "L_gemm_%=:\n\ts_add_u32 %[c], %[c], 1\n\ts_and_b32 %[t], %[c], 3\n\ts_cselect_b32 %[t], 1, -3\n\ts_mul_i32 %[t], %[t], 36864\n\ts_waitcnt lgkmcnt(0)\n\t" \
  GSTEP(N,44,72,1) \
  "v_add_u32 v100, %[t], v100\n\tv_add_u32 v101, %[t], v101\n\tv_add_u32 v102, %[t], v102\n\ts_waitcnt lgkmcnt(0)\n\ts_barrier\n\t" \
  GSTEP(N,72,44,0) \
  "s_cmp_lg_u32 %[c], 9\n\ts_cbranch_scc1 L_gemm_%=\n\ts_waitcnt lgkmcnt(0)\n\t" \
  GSTEP(N,44,72,1) "s_waitcnt lgkmcnt(0)\n\t" \
  MM(40,72,72) GM(72,0,0) GM(72,1,0) GM(72,0,1) GM(72,1,1) "s_nop 4\n\t" \
  "v_cndmask_b32 v44, v40, v41, %[m1]\n\tv_cndmask_b32 v45, v42, v43, %[m1]\n\tv_mov_b32 v46, 0x3727c5ac\n\tv_cndmask_b32 v44, v44, v45, %[m2]\n\t" \
  "v_fmac_f32 v46, 0x3acccccd, v44\n\tv_cmp_u_f32 vcc, v44, v44\n\tv_rsq_f32 v46, v46\n\ts_cbranch_vccz L_gok_%=\n\tds_write_b32 v106, v106\n\t" \
  "L_gok_%=:\n\tds_write_b32 v103, v46\n\ts_waitcnt lgkmcnt(0)\n\ts_barrier\n\t" \
  "ds_read_b32 v44, v104\n\tds_read_b32 v45, v105\n\tds_read_b32 v46, v106\n\t" \
  GM(72,0,2) GM(72,1,2) GM(72,0,3) GM(72,1,3) IF5_##N(GM(72,0,4) GM(72,1,4)) "s_waitcnt lgkmcnt(0)"
#define GEMM_OPS \
  : "={v[0:3]}"(acc[0][0]), "={v[4:7]}"(acc[0][1]), "={v[8:11]}"(acc[0][2]), "={v[12:15]}"(acc[0][3]), "={v[16:19]}"(acc[0][4]), \
    "={v[20:23]}"(acc[1][0]), "={v[24:27]}"(acc[1][1]), "={v[28:31]}"(acc[1][2]), "={v[32:35]}"(acc[1][3]), "={v[36:39]}"(acc[1][4]), \
    "={v44}"(rs[0]), "={v45}"(rs[1]), "={v46}"(vote), "+{v100}"(am0), "+{v101}"(am1), "+{v102}"(as), [c] "=&s"(c_), [t] "=&s"(t_) \
  : "{v103}"(a_w), "{v104}"(a_r[0]), "{v105}"(a_r[1]), "{v106}"(a_v), [m1] "s"(0xAAAAAAAAAAAAAAAAull), [m2] "s"(0xCCCCCCCCCCCCCCCCull) \
  : "memory", "scc", "vcc", "v40", "v41", "v42", "v43", "v47", "v48", "v49", "v50", "v51", "v52", "v53", "v54", "v55", "v56", "v57", "v58", "v59", "v60", \
    "v61", "v62", "v63", "v64", "v65", "v66", "v67", "v68", "v69", "v70", "v71", "v72", "v73", "v74", "v75", "v76", "v77", "v78", "v79", "v80", "v81", \
    "v82", "v83", "v84", "v85", "v86", "v87", "v88", "v89", "v90", "v91", "v92", "v93", "v94", "v95", "v96", "v97", "v98", "v99"

constexpr int HR = 4 * KVC;
constexpr int L_A = 2 * HR;
constexpr int L_EX = 15360;
constexpr int L_MISC = L_A + 70 * 1024;
constexpr int LDS_FUSED = L_MISC + 2048;

DI bool poll16(rsrc_t rw, unsigned off, unsigned need) {
  const int lane = threadIdx.x & 63;
#pragma unroll 1
  for (int spins = 0; spins < (1 << 20); spins++) {
    const unsigned v = gl32<16>(rw, off + (lane & 15) * 4);
    const unsigned got = (unsigned)__builtin_amdgcn_ballot_w64(v != 0) & 0xFFFFu;
    if ((got & need) == need) return true;
    nap<1>();
  }
  return false;
}

DI void poll_aux(rsrc_t rw, unsigned off) {
#pragma unroll 1
  for (int spins = 0; spins < (1 << 20); spins++) {
    const u32x4 v = gl128<16>(rw, off + (threadIdx.x & 63) * 16);
    if (!__builtin_amdgcn_ballot_w64(min(min(v[0], v[1]), min(v[2], v[3])) == 0u)) break;
    nap<4>();
  }
}
#define LAYER_VARS \
  extern __shared__ __attribute__((aligned(16))) char smem[]; \
  const lptr lds = (lptr)smem; \
  const rsrc_t rw = mk_rsrc(ws, WS_END); \
  int lane = threadIdx.x & 63; asm volatile("" : "+v"(lane)); \
  const int w = wave_id(), g = lane >> 4, r16 = lane & 15; \
  const int xcd = blockIdx.x & 7, bi = blockIdx.x >> 3, u = 8 * (xcd & 1) + (bi & 7), qt = 4 * (xcd >> 1) + (bi >> 3); \
  const int hf = u & 1, ct = u >> 1;     \
  constexpr unsigned FKV = WS_FL + (FINAL ? 32768 : 0), KV = WS_KV + (FINAL ? LAYER2 : 0), OB = WS_O + (FINAL ? LAYER2 : 0); \
  constexpr unsigned AUX = WS_AUX + (FINAL ? 1024 : 0), RB = FINAL ? WS_RB2 : WS_RB1; \
  constexpr int MK = FINAL ? 12 : 0; (void)MK; (void)hf; (void)AUX; (void)RB; (void)FKV; \
  LDS_AS unsigned* misc = (LDS_AS unsigned*)(lds + L_MISC + 1024) + (FINAL ? 8 : 0);

template <bool FINAL>
DI void loader(char* ws SARG) {
  LAYER_VARS
  {
    const int lw = w - 8;
    {
      unsigned vm[4];
#pragma unroll
      for (int r = 0; r < 4; r++) vm[r] = (4 * (4 * lw + r) + g) * (RBP * 2) + 256 * qt + (((r16 >> 1) ^ (g | ((r & 2) << 1))) * 32) + (r16 & 1) * 16 + 3072 - 1024 * r;
      const unsigned l0 = lds_addr(lds) + lw * 4096, v0 = 2 * lw * 20480 + lane * 16, v2 = ((8 + (lw >> 1)) * 20 + (lw & 1)) * 1024 + lane * 16;
      constexpr int CNT = FINAL ? 9 : 8;
      auto lds_w = [&](int s) {
        const char* sb = ws + WS_WQ + u * 204800 + s * 2048;
        dma_s(v0, v0 + 20480 - 2048, sb, l0 + (s & 3) * SLOT32 + 16384 + (lw >> 1) * 2048);
        if (FINAL) dma1(v2, sb, lds_addr(lds) + (s & 3) * SLOT32 + 24576 + (lw >> 1) * 10240 + (lw & 1) * 1024);
      };
      auto lds_a = [&](int s) {
        dma_a(vm, ws + RB - 3072 + s * (64 * RBP * 2), l0 + (s & 3) * SLOT32); };
#pragma unroll 1
      for (int att = 0;; att++) {
        const bool cb = att == 0 && blockIdx.x < 160;
        if (!FINAL && att == 0) { if (cb) raw_bar(); else nap<PFDELAY>(); }
        lds_w(0); lds_w(1);
        if (!FINAL && cb) raw_bar();
        lds_w(2); lds_w(3);
        if (att == 0) {
          if (FINAL && cb) { raw_bar(); raw_bar(); raw_bar(); }
          raw_bar();
          MARK(MK + 7);
          const int pl = min(lane, 39);
          const unsigned po = RB + ((64 * (pl >> 2) + 16 * lw + 4 * (pl & 3) + 1) * RBP + 128 * qt + 126) * 2;
#pragma unroll 1
          for (int spins = 0; spins < (1 << 20); spins++) {
            const unsigned x0 = gl32<16>(rw, po), x1 = gl32<16>(rw, po + 2 * RBP * 2);
            if (!__builtin_amdgcn_ballot_w64(max(x0, x1) == EMPTY)) break;
            nap<XSLEEP>();
          }
        }
        drain();
        MARK(MK + 8);
#pragma unroll 1
        for (int s = 0; s < 4; s++) lds_a(s);
        vm_bar<12>(); vm_bar<8>();
        lds_a(4); lds_w(4);
        vm_bar<4 + CNT>();
#pragma unroll 1
        for (int s = 5; s < 10; s++) { lds_a(s); lds_w(s); vm_bar<2 * CNT>(); }
        vm_bar<CNT>();
        poll_aux(rw, AUX);
        vm_bar<0>();
        raw_bar();
        if (att || !*(volatile LDS_AS unsigned*)(misc + 2)) break;
      }
    }
    MARK(MK + 0);
    const int hh = lw >> 1, hp = lw & 1;
    const unsigned kvv = KV + (2 * u + hh) * 16 * KVC + hp * 5120 + lane * 16;
    const unsigned lkv = lds_addr(lds) + hh * HR + hp * 5120;
    const unsigned wv = (FINAL ? WS_WOP : WS_WOF) + (ct * 2 + hh) * 51200 + lane * 16;
    const unsigned myfl = FKV + (u * 16 + qt) * 128;
    raw_bar();
    MARK(MK + 1);
    {
      u32x4 v[6];
#pragma unroll
      for (int i = 0; i < 6; i++) v[i] = ld128(lds + hh * HR + hp * 5120 + lane * 16 + i * 1024);
#pragma unroll
      for (int i = 0; i < 6; i++) st128<16>(rw, kvv + qt * KVC + i * 1024, clean(v[i]));
    }
    drain();
    unsigned cnt = 0;
    if (lane == 0) cnt = __hip_atomic_fetch_add(misc, 1u, __ATOMIC_RELAXED, __HIP_MEMORY_SCOPE_WORKGROUP);
    if (__builtin_amdgcn_readfirstlane(cnt) == 3 && lane < 16) st32<16>(rw, FKV + (u * 16 + lane) * 128 + qt * 4, 1u);
    MARK(MK + 2);
    auto ldkv = [&](int seq) {
      const int ch = seq == 0 ? qt : seq < 13 ? ((qt & 12) + 3 + seq) & 15 : (qt & 12) | ((qt + seq) & 3);
      dma6_sc1(kvv + ch * KVC, ws, lkv + (seq & 3) * KVC);
    };
    auto fetch = [&](int seq) {
      const unsigned src = kvv + ((((qt & 12) + 3 + seq) & 15)) * KVC;
      u32x4 v[6];
#pragma unroll 1
      for (int spins = 0; spins < (1 << 18); spins++) {
#pragma unroll
        for (int i = 0; i < 6; i++) v[i] = gl128<16>(rw, src + i * 1024);
        unsigned m = 0;
#pragma unroll
        for (int i = 0; i < 6; i++) m = umax4(m, v[i]);
        if (!__builtin_amdgcn_ballot_w64(m == EMPTY)) break;
        nap<2>();
      }
#pragma unroll
      for (int i = 0; i < 6; i++) *(LDS_AS u32x4*)(lds + hh * HR + hp * 5120 + seq * KVC + lane * 16 + i * 1024) = v[i];
    };
#pragma unroll 1
    for (int attempt = 0;; attempt++) {
      int c = -2;
      if (attempt) { ldkv(0); vm_bar<0>(); }
      else {
        fetch(1); MARK(MK + 3); lds_bar();
        MARK(MK + 4);
        fetch(2); poll16(rw, myfl, 12u << (((qt & 12) + 4) & 15)); ldkv(3); MARK(MK + 5); lds_bar();
        c = 1;
      }
#pragma unroll 1
      for (; c < 13; c++) {
        if (c == 2 && !attempt) poll16(rw, myfl, 0xFFFFu);
        if (!FINAL && !attempt) {
          if (c == 10) {
            unsigned n = 0;
            if (lane == 0) n = __hip_atomic_fetch_add(misc + 3, 1u, __ATOMIC_RELAXED, __HIP_MEMORY_SCOPE_WORKGROUP);
            if (__builtin_amdgcn_readfirstlane(n) == 3 && lane < 32) st32<16>(rw, WS_FL + 65536 + (lane < 16 ? (u * 16 + lane) * 128 + qt * 4 : (r16 * 16 + qt) * 128 + 64 + u * 4), 1u);
          }
        }
        if (c >= 2) { const int e = min(4 * (c - 2) + 2 * hp, 33); dma2(wv + e * 1024, ws, lds_addr(lds) + L_A + (hh * 35 + e) * 1024); }
        ldkv(c + 3);
        if (c == 0) vm_bar<12>();
        if (!FINAL && !attempt && c >= 2 && c < 9) {
          const u32x4 e = {EMPTY, EMPTY, EMPTY, EMPTY};
          const unsigned o = OB + LAYER2 + (((c - 2) * 256 + blockIdx.x) * 4 + lw) * 2048 + lane * 16;
          st128<16>(rw, o, e); st128<16>(rw, o + 1024, e);
          st128<16>(rw, ((qt & 3) < 2 && c < 8) ? kvv + LAYER2 + qt * KVC + (c - 2) * 1024 : WS_END, e);
          st128<16>(rw, c < 5 ? OB + ((4 * qt + lw) * 32 + 2 * u) * 1280 + min((c - 2) * 1024, 1536) + lane * 16 : WS_END, e);
          if (c == 2) vm_bar<12>(); else vm_bar<16>();
        } else if (c >= 2) vm_bar<8>(); else if (c >= 0) vm_bar<6>();
      }
      if (!FINAL && !attempt) poll16(rw, myfl + 65536 + hp * 64, 0xFFFFu);
      vm_bar<0>();
      dma4(wv + (35 + 7 * hp) * 1024, ws, lds_addr(lds) + hh * HR + 7 * hp * 1024);
      dma4(wv + (39 + 7 * hp) * 1024, ws, lds_addr(lds) + hh * HR + (4 + 7 * hp) * 1024);
      raw_bar();
      vm_bar<0>();
      if (attempt || !*(volatile LDS_AS unsigned*)(misc + 1)) break;
      raw_bar();
    }
    if (!FINAL) { raw_bar(); raw_bar(); }
  }
}

template <bool FINAL>
DI void compute(char* ws, u16* out SARG) {
  LAYER_VARS
  const int wm = w & 3, wn = w >> 2, h = lane >> 5;
  if (blockIdx.x < 160) col_phase<!FINAL>(rw, lds, 2 * blockIdx.x SPASS);
  else if (!FINAL) {
    nap<FILLDELAY>();
#pragma unroll
    for (int it = 0; it < 4; it++) {
      const int k = (it * 96 + blockIdx.x - 160) * 512 + threadIdx.x, b = k / 1408;
      st128<16>(rw, k < 128 * 1408 ? WS_KV + b * 4 * KVC + (k - b * 1408) * 16 : WS_END, u32x4{EMPTY, EMPTY, EMPTY, EMPTY});
    }
  }
  MARK(MK + 10);
  lds_bar();
  {
    drain();
    unsigned n = 0;
    if (lane == 0) n = __hip_atomic_fetch_add(misc + 4, 1u, __ATOMIC_RELAXED, __HIP_MEMORY_SCOPE_WORKGROUP);
    if (n == 7) st32<16>(rw, AUX + blockIdx.x * 4, 1u);
  }
  MARK(MK + 11);
  {
    constexpr int NT = FINAL ? 5 : 4;
    f32x4 acc[2][5]; float rs[2];
    unsigned mo[2], a0[2], a1[2], a2[2], a_r[2];
#pragma unroll
    for (int m = 0; m < 2; m++) {
      const int q = r16 >> 2, pr = 16 * (m ^ wn) + r16;
      mo[m] = lds_addr(lds) + (8 * g + q) * 256 + (((2 * wm + (m ^ wn)) ^ (q | ((g & 1) << 2))) * 32) + (lane & 3) * 8;
      const unsigned qb = L_A + (wn * 4 + wm) * 1280, kb = wn * HR + wm * 1280, vr = wn * HR + 5120 + (32 * wm + pr) * 48 + (g == 3 ? 32 : g * 16);
      a0[m] = ((g >> 1) ? kb : qb) + (g & 1) * 512 + pr * 16;
      a1[m] = g == 2 ? qb + 1024 + pr * 8 : vr; a2[m] = g == 2 ? kb + 1024 + pr * 8 : vr + 8;
      a_r[m] = lds_addr(lds) + L_MISC + (32 * wm + pr) * 4;
      asm volatile("" : "+v"(a0[m]), "+v"(a1[m]), "+v"(a2[m]));
    }
    const unsigned a_w = a_r[0] + (g == (r16 >> 2) ? 0 : 512), a_v = lds_addr((lptr)(misc + 2));
#pragma unroll 1
    for (int att = 0;; att++) {
      unsigned am0 = mo[0], am1 = mo[1], as = lds_addr(lds) + 16384 + wn * 10240 + lane * 16, vote; int c_, t_;
      if (FINAL) asm volatile(GEMM_ASM(5) GEMM_OPS); else asm volatile(GEMM_ASM(4) GEMM_OPS);
      if (att || !__builtin_amdgcn_readfirstlane(vote)) break;
    }
    SB();
    MARK(MK + 0);
#pragma unroll
    for (int m = 0; m < 2; m++) *(LDS_AS u32x4*)(lds + a0[m]) = pk8(acc[m][0] * rs[m], acc[m][1] * rs[m]);
    asm volatile("s_nop 1" : "+v"(acc[0][2]), "+v"(acc[1][2]), "+v"(acc[0][3]), "+v"(acc[1][3]), "+v"(acc[0][NT - 1]), "+v"(acc[1][NT - 1]) :: "memory");
#pragma unroll
    for (int m = 0; m < 2; m++) {
      u32x2 hi = pk4(acc[m][3] * rs[m]);
      if (g == 3) hi = u32x2{0x3F80u, 0u};
      *(LDS_AS u32x2*)(lds + a1[m]) = pk4(acc[m][2] * rs[m]);
      *(LDS_AS u32x2*)(lds + a2[m]) = hi;
    }
    lds_bar();
    if (FINAL) {
      const unsigned x4 = WS_X4 + (((u * 16 + qt) * 4 + wm) * 2 + wn) * 1280;
      const u32x2 p0 = pk4(acc[0][NT - 1]), p1 = pk4(acc[1][NT - 1]), q0 = pk4(acc[0][3]), q1 = pk4(acc[1][3]);
      st128<16>(rw, x4 + lane * 16, clean(u32x4{p0[0], p0[1], p1[0], p1[1]}));
      if (g == 3) st128<16>(rw, x4 + 1024 + r16 * 16, clean(u32x4{q0[0], q0[1], q1[0], q1[1]}));
    }
  }
  MARK(MK + 1);
  const int head = 2 * u + wn, kh = w & 1;
  const lptr ring = lds + wn * HR;
  f32x16 o;
  {
    const lptr qpa = lds + L_A + (wn * 4 + wm) * 1280, qpb = lds + L_A + (wn * 4 + (wm ^ 1)) * 1280;
    const u32x4 qa1 = ld128(qpa + lane * 16), qb1 = ld128(qpb + lane * 16);
    u32x2 qa2 = ld64(qpa + 1024 + (lane & 31) * 8), qb2 = ld64(qpb + 1024 + (lane & 31) * 8);
    if (h) { qa2 = u32x2{0u, 0u}; qb2 = qa2; }
    const unsigned ka = lds_addr(ring) + kh * 2560 + lane * 16, kb = lds_addr(ring) + kh * 2560 + 1024 + (lane & 31) * 8;
    const unsigned va = lds_addr(ring) + kh * 3072 + (4 * h + (r16 >> 2)) * 48 + ((g & 1) ? ((lane & 3) ? 40 : 32) : (lane & 3) * 8);
    float ma, mb;
    {
      const u32x4 k1 = ld128(ring + lane * 16); const u32x2 k2 = ld64(ring + 1024 + (lane & 31) * 8);
      ma = max16(scores(k1, k2, qa1, qa2)); mb = max16(scores(k1, k2, qb1, qb2));
    }
    for (int attempt = 0;; attempt++) {
      ma = fmaxf(ma, __shfl_xor(ma, 32)); mb = fmaxf(mb, __shfl_xor(mb, 32));
      f32x16 cia, cib, oa, ob;
#pragma unroll
      for (int i = 0; i < 16; i++) { cia[i] = -ma; cib[i] = -mb; }
      int cnt = 4;
      MARK(MK + 2);
      asm volatile(
          "ds_read_b128 v[80:83], v100\n\t"
          "ds_read_b64 v[84:85], v101\n\t"
          "ds_read_b64_tr_b16 v[86:87], v102 offset:5120\n\t"
          "ds_read_b64_tr_b16 v[88:89], v102 offset:5504\n\t"
          Z4(0) Z4(68) Z4(76) Z4(90)
          "s_waitcnt lgkmcnt(0)\n\t"
          "v_mfma_f32_32x32x16_bf16 v[32:47], v[80:83], v[94:97], v[16:31]\n\t"
          Z4(4) Z4(8) Z4(12) Z4(104) Z4(108) Z4(112) Z4(116)
          "v_mfma_f32_32x32x8_bf16 v[32:47], v[84:85], v[98:99], v[32:47]\n\ts_nop 9\n\t"
          "L_attn_%=:\n\t"
          CHUNK(0) CHUNK(1) CHUNK(2) CHUNK(3)
          "s_sub_u32 %[cnt], %[cnt], 1\n\ts_cmp_lg_u32 %[cnt], 0\n\ts_cbranch_scc1 L_attn_%=\n\t"
          "s_setprio 0\n\t" CVT(78,68) CVT(79,70) "s_waitcnt lgkmcnt(0)\n\ts_nop 1\n\t"
          "v_mfma_f32_32x32x16_bf16 v[104:119], v[90:93], v[76:79], v[104:119]\n\ts_nop 15\n\t"
          : "={v[0:15]}"(oa), "={v[104:119]}"(ob), [cnt] "+s"(cnt)
          : "{v[16:31]}"(cia), "{v[120:135]}"(cib), "{v[94:97]}"(qa1), "{v[98:99]}"(qa2), "{v[136:139]}"(qb1), "{v[140:141]}"(qb2),
            "{v100}"(ka), "{v101}"(kb), "{v102}"(va)
          : "memory", "scc", "v32", "v33", "v34", "v35", "v36", "v37", "v38", "v39", "v40", "v41", "v42", "v43", "v44", "v45", "v46", "v47",
            "v48", "v49", "v50", "v51", "v52", "v53", "v54", "v55", "v56", "v57", "v58", "v59", "v60", "v61", "v62", "v63", "v64", "v65",
            "v66", "v67", "v68", "v69", "v70", "v71", "v72", "v73", "v74", "v75", "v76", "v77", "v78", "v79", "v80", "v81", "v82", "v83",
            "v84", "v85", "v86", "v87", "v88", "v89", "v90", "v91", "v92", "v93");
      MARK(MK + 3);
      LDS_AS f32x4* ex = (LDS_AS f32x4*)(ring + L_EX + wm * 3072) + lane;
#pragma unroll
      for (int i = 0; i < 3; i++) ex[i * 64] = f32x4{ob[4 * i], ob[4 * i + 1], ob[4 * i + 2], ob[4 * i + 3]};
      o = oa;
      f32x4 cs = {0.f, 0.f, 0.f, 0.f};
#pragma unroll
      for (int i = 0; i < 3; i++) cs += f32x4{oa[4 * i], oa[4 * i + 1], oa[4 * i + 2], oa[4 * i + 3]} + f32x4{ob[4 * i], ob[4 * i + 1], ob[4 * i + 2], ob[4 * i + 3]};
      if (!(fabsf(cs[0] + cs[1] + cs[2] + cs[3]) < INFINITY) || (h && !(oa[8] + ob[8] < 0x1p60f))) misc[1] = 1u;
      lds_bar();
      if (attempt || !*(volatile LDS_AS unsigned*)(misc + 1)) break;
      raw_bar();
      ma = -INFINITY; mb = -INFINITY;
      for (int t = 0; t < 64; t++) {
        const unsigned kb_ = KV + (head * 16 + (t >> 2)) * KVC + (t & 3) * 1280;
        const u32x4 k1 = gl128<16>(rw, kb_ + lane * 16); const u32x2 k2 = gl64<16>(rw, kb_ + 1024 + (lane & 31) * 8);
        ma = fmaxf(ma, max16(scores(k1, k2, qa1, qa2))); mb = fmaxf(mb, max16(scores(k1, k2, qb1, qb2)));
      }
      raw_bar();
    }
  }
  {
    LDS_AS f32x4* ey = (LDS_AS f32x4*)(ring + L_EX + (wm ^ 1) * 3072) + lane;
    f32x4 r[3];
#pragma unroll
    for (int i = 0; i < 3; i++) r[i] = ey[i * 64] + f32x4{o[4 * i], o[4 * i + 1], o[4 * i + 2], o[4 * i + 3]};
    const float il = __builtin_amdgcn_rcpf(__shfl(r[2][0], (lane & 31) + 32));
    const unsigned ob = OB + ((4 * qt + wm) * 32 + head) * 1280;
    const int sl_ = ((lane << 2) & 16) + ((lane >> 1) & 12) + (lane & 3);
    st128<16>(rw, ob + h * 512 + sl_ * 16, clean(pk8(r[0] * il, r[1] * il)));
    const u32x4 t = clean(pk8(r[2] * il, r[2] * il));
    if (!h) st64<16>(rw, ob + 1024 + sl_ * 8, u32x2{t[0], t[1]});
  }
  const int prow = 128 * qt + 64 * hf + 16 * wm, pblk = prow & ~31;
  const int rp0 = 1024 * (wm & 1) + (pblk >> 1);
  MARK(MK + 4);
  u32x2 x4v[3]; float dc = 0.f;
  const lptr wa = lds + L_A + wn * 35 * 1024 + lane * 16, wb = ring + lane * 16;
  const unsigned obase = OB + ((prow >> 5) * 32 + 16 * wn) * 1280;
  unsigned o1 = obase + (g >> 1) * 1280 + (g & 1) * 512 + (wm & 1) * 256 + r16 * 16, o2 = obase + 2 * g * 1280 + 1024 + (wm & 1) * 128 + r16 * 8;
  constexpr int NPRE = 15;
  u32x4 bf[50];
  f32x4 acc[5] = {};
  if (!FINAL) {
#pragma unroll
    for (int i = 0; i < 3; i++) {
      const int j = 80 * ct + 48 * wn + 16 * (wn ? (i & 1) : i) + r16;
      x4v[i] = gl64<16>(rw, (j == 1 ? WS_RB0 : WS_RB1) + (j * RBP + pblk + 8 * g + 4 * (wm & 1)) * 2);
    }
  } else {
    dc = __uint_as_float(gl32<16>(rw, WS_R3 + (RP + rp0 + r16) * 4));
    unsigned o3[3];
#pragma unroll
    for (int i = 0; i < 3; i++) {
      const int cg = 12 * wn + 4 * ((i == 2 && wn) ? 0 : i) + g, pw = cg / 5, gq = cg - 5 * pw;
      o3[i] = WS_X4 + ((((2 * ct + (pw >> 1)) * 16 + qt) * 4 + 2 * hf + (wm >> 1)) * 2 + (pw & 1)) * 1280 + (gq * 16 + (trow(wm & 1, r16) & 15)) * 16 + ((((r16 >> 3) ^ pw) & 1) * 8);
    }
#pragma unroll 1
    for (int spins = 0; spins < (1 << 18); spins++) {
#pragma unroll
      for (int i = 0; i < 3; i++) x4v[i] = gl64<16>(rw, o3[i]);
      if (!__builtin_amdgcn_ballot_w64(umax4(max(x4v[0][0], x4v[0][1]), u32x4{x4v[1][0], x4v[1][1], x4v[2][0], x4v[2][1]}) == EMPTY)) break;
      nap<8>();
    }
  }
#pragma unroll
  for (int j = 0; j < NPRE; j++) bf[j] = ld128(wa + j * 1024);
#pragma unroll
  for (int j = 0; j < 5; j++) asm volatile("" : "+v"(acc[j]));
  asm volatile("s_waitcnt vmcnt(0)" : "+v"(o1), "+v"(o2) :: "memory");
  MARK(MK + 5);
  {
    u32x4 af[10];
    auto ldf = [&](int i) {
      if (i < 8) af[i] = __builtin_amdgcn_raw_buffer_load_b128(rw, o1, i * 2 * 1280, 16);
      else { const u32x2 a = __builtin_amdgcn_raw_buffer_load_b64(rw, o2, (i - 8) * 8 * 1280, 16), b = __builtin_amdgcn_raw_buffer_load_b64(rw, o2, ((i - 8) * 8 + 1) * 1280, 16);
             af[i] = u32x4{a[0], a[1], b[0], b[1]}; }
    };
#pragma unroll
    for (int i = 0; i < 10; i++) ldf(i);
#pragma unroll 1
    for (int spins = 0; spins < (1 << 18); spins++) {
      unsigned m = 0;
#pragma unroll
      for (int i = 0; i < 10; i++) m = umax4(m, af[i]);
      if (!__builtin_amdgcn_ballot_w64(m == EMPTY)) break;
      nap<RSLEEP>();
#pragma unroll
      for (int i = 0; i < 10; i++) if (__builtin_amdgcn_ballot_w64(umax4(0, af[i]) == EMPTY)) ldf(i);
    }
    MARK(MK + 8);
#pragma unroll
    for (int i = 0; i < 10; i++) asm volatile("" : "+v"(af[i]));
    asm volatile("s_nop 1" : "+v"(acc[0]), "+v"(acc[1]), "+v"(acc[2]), "+v"(acc[3]), "+v"(acc[4]));
    SB();
#pragma unroll
    for (int i = 0; i < 10; i++) {
#pragma unroll
      for (int a = 0; a < 5; a++) {
        if (FINAL) mfma16v(acc[a], bf[5 * i + a], af[i]); else mfma16v(acc[a], af[i], bf[5 * i + a]);
        const int j = 5 * i + a + NPRE; if (j < 50) bf[j] = ld128(j < 35 ? wa + j * 1024 : wb + (j - 35) * 1024);
        SB();
      }
    }
  }
  asm volatile("s_nop 7\n\ts_nop 1" : "+v"(acc[0]), "+v"(acc[1]), "+v"(acc[2]), "+v"(acc[3]), "+v"(acc[4]) :: "memory");
  MARK(MK + 6);
  {
    LDS_AS f32x4* ex = (LDS_AS f32x4*)(ring + L_EX + 12288 + wm * 3072) + lane;
    LDS_AS f32x4* ey = (LDS_AS f32x4*)(lds + (wn ^ 1) * HR + L_EX + 12288 + wm * 3072) + lane + 64 * wn;
    ex[0] = acc[2]; ex[64] = acc[3]; ex[128] = acc[4];
    dc *= 0.039528470752104741f;
#pragma unroll
    for (int i = 0; i < 3; i++) { acc[i] += f32x4{bflo(x4v[i][0]), bfhi(x4v[i][0]), bflo(x4v[i][1]), bfhi(x4v[i][1])}; if (FINAL) acc[i] += dc; }
    lds_bar();
#pragma unroll
    for (int i = 0; i < 3; i++) acc[i] += ey[64 * i];
  }
  if (FINAL) {
    u16* op = out + (size_t)rowof(pblk + trow(wm & 1, r16)) * 640 + 80 * ct + 48 * wn + 4 * g;
#pragma unroll
    for (int i = 0; i < 3; i++)
      if (i < 2 || wn == 0) *(u32x2*)(op + 16 * i) = pk4(acc[i]);
  } else {
    f32x4 sq = {0.f, 0.f, 0.f, 0.f};
#pragma unroll
    for (int i = 0; i < 3; i++)
      if (i < 2 || wn == 0) {
        const int j = 80 * ct + 48 * wn + 16 * i + r16;
        st128<16>(rw, WS_R3 + (j * RP + rp0 + 4 * g) * 4, clean(asu(acc[i])));
        if (j != 1) sq += acc[i] * acc[i];
      }
#pragma unroll
    for (int e = 0; e < 4; e++) sq[e] = row16_sum(sq[e]);
    LDS_AS float* sl = (LDS_AS float*)(lds + L_MISC);
    if (r16 == 0) *(LDS_AS f32x4*)(sl + wn * 64 + 32 * (wm & 1) + 16 * (wm >> 1) + 4 * g) = sq;
    lds_bar();
    if (w == 0 && lane < 8) {
      LDS_AS f32x4* s4 = (LDS_AS f32x4*)sl + 2 * lane;
      st128<16>(rw, WS_SSQ + (ct * 2048 + 1024 * (lane >> 2) + 64 * qt + 32 * hf + 8 * (lane & 3)) * 2, clean(pk8(s4[0] + s4[16], s4[1] + s4[17])));
    }
  }
  MARK(MK + 7);
}

__global__ void __launch_bounds__(768) k_main(char* ws, u16* out) {
  STAMP(1);
  if (__builtin_expect(wave_id() >= 8, 1)) {
    extern __shared__ __attribute__((aligned(16))) char smem[];
    __builtin_amdgcn_s_setprio(3);
    if (threadIdx.x < 528) ((LDS_AS unsigned*)((lptr)smem + L_MISC + 1024))[threadIdx.x - 512] = 0u;
    asm volatile("s_waitcnt lgkmcnt(0)" ::: "memory");
    loader<false>(ws SPASS);
    loader<true>(ws SPASS);
  } else {
    compute<false>(ws, out SPASS);
    compute<true>(ws, out SPASS);
  }
}

#define STOPAT(n)
extern "C" void kernel_launch(void* const* d_in, const int* in_sizes, int n_in, void* d_out, int out_size, void* d_ws, size_t ws_size,
                              hipStream_t stream) {
  const u16* x = (const u16*)d_in[0];
  const u16* wr = (const u16*)d_in[1];
  const u16* wi = (const u16*)d_in[2];
  const u16* wqkv = (const u16*)d_in[4];
  const u16* wo = (const u16*)d_in[5];
  char* ws = (char*)d_ws;
  (void)hipFuncSetAttribute((const void*)k_prep, hipFuncAttributeMaxDynamicSharedMemorySize, LDS_PREP);
  (void)hipFuncSetAttribute((const void*)k_main, hipFuncAttributeMaxDynamicSharedMemorySize, LDS_FUSED);
  k_prep<<<256, 512, LDS_PREP, stream>>>(x, wr, wi, wqkv, wo, ws);
  STOPAT(1)
  k_main<<<256, 768, LDS_FUSED, stream>>>(ws, (u16*)d_out);
}
```

```cpp
#include <hip/hip_runtime.h>
#include <hip/hip_bf16.h>
#include <utility>
#include <type_traits>

typedef unsigned short u16;
typedef __attribute__((ext_vector_type(16))) float f32x16;
typedef __attribute__((ext_vector_type(4))) float f32x4;
typedef __attribute__((ext_vector_type(2))) float f32x2;
typedef __attribute__((ext_vector_type(8))) __bf16 bf16x8;
typedef __attribute__((ext_vector_type(2))) __bf16 bf16x2;
typedef __attribute__((ext_vector_type(4))) short s16x4;
typedef __attribute__((ext_vector_type(4))) unsigned u32x4;
typedef __attribute__((ext_vector_type(2))) unsigned u32x2;
#define DI __device__ __forceinline__
#define LDS_AS __attribute__((address_space(3)))
typedef LDS_AS char* lptr;

constexpr double cx_cos_small(double x) { double x2 = x * x, t = 1, s = 1; for (int i = 1; i < 12; i++) { t *= -x2 / ((2 * i - 1) * (2 * i)); s += t; } return s; }
constexpr double cx_sin_small(double x) { double x2 = x * x, t = x, s = x; for (int i = 1; i < 12; i++) { t *= -x2 / ((2 * i) * (2 * i + 1)); s += t; } return s; }
constexpr double cx_cos640(int m) {
  m %= 640; if (m > 320) m = 640 - m; double sg = 1; if (m > 160) { m = 320 - m; sg = -1; }
  const double w = 6.283185307179586476925 / 640; return sg * (m <= 80 ? cx_cos_small(w * m) : cx_sin_small(w * (160 - m)));
}
constexpr u16 cx_bf16(double d) { float f = (float)d; unsigned u = __builtin_bit_cast(unsigned, f); u += 0x7FFFu + ((u >> 16) & 1u); return (u16)(u >> 16); }
struct Cos640 { double v[640]; };
constexpr Cos640 mk_cos() { Cos640 c{}; for (int m = 0; m < 640; m++) c.v[m] = cx_cos640(m); return c; }
constexpr Cos640 COS = mk_cos();
__host__ __device__ constexpr int kf_of(int G, int i) { return G ? G + 40 * i : (i == 0 ? 320 : i < 8 ? 40 * i : 40 * i - 300); }
__host__ __device__ constexpr int freq_of(int G, int i) { return kf_of(G, i) > 320 ? 640 - kf_of(G, i) : kf_of(G, i); }
constexpr double a1val(int out, int a) {
  if (out >= 40 || a >= 40) return 0;
  const int G = out >> 1, part = out & 1;
  if (G == 0) return (part && (a & 1)) ? -1 : 1;
  return COS.v[(16 * a * G + 160 * part) % 640];
}
constexpr double m2val(int G, int o, int k) {
  const int i = o >> 1, po = o & 1, part = k >> 4, b = k & 15, kf = kf_of(G, i);
  const double c = COS.v[(b * kf) % 640], sn = COS.v[(b * kf + 480) % 640];
  if (G == 0) {
    if (part != (i >= 8)) return 0;
    if (i == 0) return 0.039528470752104741 * (po ? 1.0 : c);
    return 0.055901699437494742 * (po ? -sn : c);
  }
  const double v = po ? (part ? c : -sn) : (part ? sn : c);
  return 0.055901699437494742 * ((po && kf > 320) ? -v : v);
}
struct DTab { u16 a1[3][64][8]; u16 a1b[3][64][4]; u16 pad[256]; u16 m2[20][2][64][8]; };
constexpr DTab mk_dtab() {
  DTab t{};
  for (int l = 0; l < 64; l++) {
    const int r = l & 15, g = l >> 4;
    for (int T = 0; T < 3; T++) {
      for (int e = 0; e < 8; e++) t.a1[T][l][e] = cx_bf16(a1val(16 * T + r, 16 * (e >> 2) + 4 * g + (e & 3)));
      for (int e = 0; e < 4; e++) t.a1b[T][l][e] = cx_bf16(a1val(16 * T + r, 32 + 4 * g + e));
    }
    for (int G = 0; G < 20; G++) for (int T = 0; T < 2; T++) for (int e = 0; e < 8; e++)
      t.m2[G][T][l][e] = cx_bf16(m2val(G, 8 * (r >> 2) + 4 * T + (r & 3), 8 * g + e));
  }
  return t;
}
static __device__ const __attribute__((aligned(1024))) DTab g_dt = mk_dtab();

struct CTab { u16 v[10 * 512]; };
constexpr CTab mk_ctab() {
  CTab c{}; u16 cs[64] = {};
  for (int m = 0; m < 64; m++) cs[m] = cx_bf16(cx_cos640(10 * m));
  for (int f = 0; f < 10; f++) for (int l = 0; l < 64; l++) for (int e = 0; e < 8; e++) {
    const int r = l & 15, g = l >> 4;
    const int m = f < 8 ? (8 * g + e) * (16 * (f >> 1) + r) : 2 * r * (16 * (e >> 2) + 4 * g + (e & 3));
    c.v[(f * 64 + l) * 8 + e] = cs[(m + (f & 1) * 48) & 63];
  }
  return c;
}
static __device__ const __attribute__((aligned(1024))) CTab g_ct = mk_ctab();

constexpr int RP = 2048 + 64, RBP = 2048 + 128;
constexpr unsigned RBSZ = 640 * RBP * 2;
constexpr unsigned WS_FL = 0;
constexpr unsigned WS_AUX = WS_FL + 3 * 32768;
constexpr unsigned WS_RS1 = WS_AUX + 2048;
constexpr unsigned WS_RB0 = WS_RS1 + 8192;
constexpr unsigned WS_RB1 = WS_RB0 + RBSZ;
constexpr unsigned WS_WF = WS_RB1 + RBSZ;
constexpr unsigned WS_WQ = WS_WF + 320 * 8192;
constexpr unsigned WS_WOF = WS_WQ + 16 * 10 * 20 * 1024;
constexpr unsigned WS_WOP = WS_WOF + 40 * 20 * 1024;
constexpr int KVC = 11264;
constexpr unsigned WS_KV = WS_WOP + 40 * 20 * 1024;
constexpr unsigned WS_O = WS_KV + 32 * 16 * KVC;
constexpr unsigned LAYER2 = 32 * 16 * KVC + 32 * 64 * 1280;
constexpr unsigned WS_X4 = WS_KV + 2 * LAYER2;
constexpr unsigned WS_R3 = WS_X4 + 2048 * 640 * 2;
constexpr unsigned WS_RB2 = WS_R3 + 640 * RP * 4;
constexpr unsigned WS_SSQ = WS_RB2 + RBSZ;
constexpr unsigned EMPTY = 0xFFFFFFFFu;
constexpr unsigned WS_END = WS_SSQ + 2048 * 16;
#define FILLDELAY 10
#define OUTAUX 0
#define XDELAY 14
#define SW1 50
#define SW2 46
#define XNL 0
#define K1DELAY 40
#define PFDELAY 40
#define YSLEEP 4
#define XSLEEP 1
#define RSLEEP 2
#define AUXB 16

#define STW 0
#define STAMP(k)
#define MARK(i)
#define SARG
#define SPASS
#define SB() __builtin_amdgcn_sched_barrier(0)
typedef __amdgpu_buffer_rsrc_t rsrc_t;
DI rsrc_t mk_rsrc(const void* p, unsigned bytes) { return __builtin_amdgcn_make_buffer_rsrc((void*)p, 0, bytes, 0x00020000); }
template <int AUX> DI void st128(rsrc_t r, unsigned off, u32x4 v) { __builtin_amdgcn_raw_buffer_store_b128(v, r, off, 0, AUX); }
template <int AUX> DI void st64(rsrc_t r, unsigned off, u32x2 v) { __builtin_amdgcn_raw_buffer_store_b64(v, r, off, 0, AUX); }
template <int AUX> DI void st32(rsrc_t r, unsigned off, unsigned v) { __builtin_amdgcn_raw_buffer_store_b32(v, r, off, 0, AUX); }
template <int AUX> DI void st16(rsrc_t r, unsigned off, u16 v) { __builtin_amdgcn_raw_buffer_store_b16(v, r, off, 0, AUX); }
template <int AUX> DI u32x4 gl128(rsrc_t r, unsigned off) { return __builtin_amdgcn_raw_buffer_load_b128(r, off, 0, AUX); }
template <int AUX> DI u32x2 gl64(rsrc_t r, unsigned off) { return __builtin_amdgcn_raw_buffer_load_b64(r, off, 0, AUX); }
template <int AUX> DI unsigned gl32(rsrc_t r, unsigned off) { return __builtin_amdgcn_raw_buffer_load_b32(r, off, 0, AUX); }
DI u32x4 asu(f32x4 v) { return __builtin_bit_cast(u32x4, v); }
DI f32x4 asf(u32x4 v) { return __builtin_bit_cast(f32x4, v); }
DI unsigned pk2(float a, float b) { f32x2 v = {a, b}; return __builtin_bit_cast(unsigned, __builtin_convertvector(v, bf16x2)); }
DI u32x2 pk4(f32x4 a) { return u32x2{pk2(a[0], a[1]), pk2(a[2], a[3])}; }
DI u32x4 pk8(f32x4 a, f32x4 b) { return u32x4{pk2(a[0], a[1]), pk2(a[2], a[3]), pk2(b[0], b[1]), pk2(b[2], b[3])}; }
DI u32x4 clean(u32x4 v) { return __builtin_elementwise_min(v, u32x4{EMPTY - 1, EMPTY - 1, EMPTY - 1, EMPTY - 1}); }
DI unsigned umax4(unsigned m, u32x4 v) { return max(max(max(m, v[0]), v[1]), max(v[2], v[3])); }
DI float bflo(unsigned u) { return __uint_as_float(u << 16); }
DI float bfhi(unsigned u) { return __uint_as_float(u & 0xFFFF0000u); }
DI float dot2(unsigned a, unsigned b, float c) { return __builtin_amdgcn_fdot2_f32_bf16(__builtin_bit_cast(bf16x2, a), __builtin_bit_cast(bf16x2, b), c, false); }
DI int rowof(int p) { return 32 * (((p >> 5) & 0x3C) + (p & 3)) + ((p << 2) & 16) + ((p >> 3) & 15); }
DI int rpos(int p) { return ((p << 8) & 1024) + ((p >> 3) << 2) + (p & 3); }
DI int trow(int T, int r) { return 8 * (r >> 2) + 4 * T + (r & 3); }
template <int CTRL> DI float dpp_add(float v) { return v + __int_as_float(__builtin_amdgcn_update_dpp(0, __float_as_int(v), CTRL, 0xF, 0xF, true)); }
DI float row16_sum(float v) { v = dpp_add<0x128>(v); v = dpp_add<0x124>(v); v = dpp_add<0x122>(v); return dpp_add<0x121>(v); }
DI f32x4 mfma16(u32x4 a, u32x4 b, f32x4 c) {
  return __builtin_amdgcn_mfma_f32_16x16x32_bf16(__builtin_bit_cast(bf16x8, a), __builtin_bit_cast(bf16x8, b), c, 0, 0, 0);
}
DI void mfma16v(f32x4& c, u32x4 a, u32x4 b) { asm volatile("v_mfma_f32_16x16x32_bf16 %0, %1, %2, %0" : "+v"(c) : "v"(a), "v"(b)); }
DI u32x2 ldtr(lptr p) { return __builtin_bit_cast(u32x2, __builtin_amdgcn_ds_read_tr16_b64_v4i16((LDS_AS s16x4*)p)); }
DI u32x4 ld128(lptr p) { return *(LDS_AS u32x4*)p; }
DI u32x2 ld64(lptr p) { return *(LDS_AS u32x2*)p; }
#define GL(v, s, o, x) "global_load_lds_dwordx4 " v ", " s " offset:" #o x "\n\t"
#define M0SET "s_mov_b32 %[sv], m0\n\ts_mov_b32 m0, %[l]\n\ts_nop 2\n\t"
#define M0ADD(x) "s_add_u32 m0, m0, " x "\n\ts_nop 0\n\t"
#define M0RST "s_mov_b32 m0, %[sv]"
DI void dma2(unsigned v, const char* sb, unsigned l) { unsigned sv;
  asm volatile(M0SET GL("%[v]", "%[s]", 0, "") GL("%[v]", "%[s]", 1024, "") M0RST : [sv] "=&s"(sv) : [v] "v"(v), [s] "s"(sb), [l] "s"(l) : "memory"); }
DI void dma4(unsigned v, const char* sb, unsigned l) { unsigned sv;
  asm volatile(M0SET GL("%[v]", "%[s]", 0, "") GL("%[v]", "%[s]", 1024, "") GL("%[v]", "%[s]", 2048, "") GL("%[v]", "%[s]", 3072, "") M0RST
               : [sv] "=&s"(sv) : [v] "v"(v), [s] "s"(sb), [l] "s"(l) : "memory"); }
DI void dma6_sc1(unsigned v, const char* sb, unsigned l) { unsigned sv;
  asm volatile(M0SET GL("%[v]", "%[s]", 0, " sc1") GL("%[v]", "%[s]", 1024, " sc1") GL("%[v]", "%[s]", 2048, " sc1") M0ADD("3072")
               GL("%[w]", "%[s]", 0, " sc1") GL("%[w]", "%[s]", 1024, " sc1") GL("%[w]", "%[s]", 2048, " sc1") M0RST
               : [sv] "=&s"(sv) : [v] "v"(v), [w] "v"(v + 3072), [s] "s"(sb), [l] "s"(l) : "memory", "scc"); }
template <int N> DI void vm_bar() { asm volatile("s_waitcnt vmcnt(%0)\n\ts_barrier" ::"i"(N) : "memory"); }
DI void lds_bar() { asm volatile("s_waitcnt lgkmcnt(0)\n\ts_barrier" ::: "memory"); }
DI void raw_bar() { asm volatile("s_barrier" ::: "memory"); }
template <int N> DI void nap() { __builtin_amdgcn_s_sleep(N); asm volatile("" ::: "memory"); }
DI void drain() { asm volatile("s_waitcnt vmcnt(0)" ::: "memory"); }
#define CODE_ADDR(sym) ({ unsigned lo_, hi_; \
    asm volatile("s_getpc_b64 vcc\n\ts_add_u32 %0, vcc_lo, " sym "@rel32@lo+4\n\ts_addc_u32 %1, vcc_hi, " sym "@rel32@hi+12" : "=s"(lo_), "=s"(hi_) :: "vcc", "scc"); \
    (const unsigned*)(((unsigned long long)hi_ << 32) | lo_); })
#define SYM_PREP "_Z6k_prepPKtS0_S0_S0_S0_Pc"
#define SYM_MAIN "_Z6k_mainPcPt"
DI int wave_id() { return __builtin_amdgcn_readfirstlane(threadIdx.x >> 6); }
DI unsigned lds_addr(lptr p) { return (unsigned)(size_t)p; }

DI int kappa_to_i(int kap) {
  int ks = kap >> 5, g = (kap >> 3) & 3, e = kap & 7;
  if (ks < 16) return (2 * ks + (g >> 1)) * 20 + 8 * (e >> 2) + 4 * (g & 1) + (e & 3);
  return (8 * (ks - 16) + 2 * g + (e >> 2)) * 20 + 16 + (e & 3);
}

DI unsigned wo_frag(int nt, int ks) {
  const int ct = nt / 5, c5 = nt - 5 * ct, kh = ks < 16 ? ks >> 3 : (ks - 16) >> 1, i = ks < 16 ? ks & 7 : 8 + (ks & 1);
  const int a = kh ? (c5 >= 3 ? c5 - 3 : c5 + 2) : c5;
  return ((ct * 2 + kh) * 50 + 5 * i + a) * 1024;
}

constexpr int SLOT32 = 36864;
DI void dma_a(const unsigned* vm, const char* mb, unsigned l) { unsigned sv;
  asm volatile(M0SET GL("%[a0]", "%[m]", 0, " sc1") GL("%[a1]", "%[m]", 1024, " sc1") GL("%[a2]", "%[m]", 2048, " sc1") GL("%[a3]", "%[m]", 3072, " sc1") M0RST
               : [sv] "=&s"(sv) : [a0] "v"(vm[0]), [a1] "v"(vm[1]), [a2] "v"(vm[2]), [a3] "v"(vm[3]), [m] "s"(mb), [l] "s"(l) : "memory"); }
DI void dma_s(unsigned v0, unsigned v1, const char* sb, unsigned l) { unsigned sv;
  asm volatile(M0SET GL("%[v0]", "%[s]", 0, " sc1") GL("%[v0]", "%[s]", 1024, " sc1") GL("%[v1]", "%[s]", 2048, " sc1") GL("%[v1]", "%[s]", 3072, " sc1") M0RST
               : [sv] "=&s"(sv) : [v0] "v"(v0), [v1] "v"(v1), [s] "s"(sb), [l] "s"(l) : "memory"); }
DI void dma1_sc1(unsigned v, const char* sb, unsigned l) { unsigned sv;
  asm volatile(M0SET GL("%[v]", "%[s]", 0, " sc1") M0RST : [sv] "=&s"(sv) : [v] "v"(v), [s] "s"(sb), [l] "s"(l) : "memory"); }
DI void dma1(unsigned v, const char* sb, unsigned l) { unsigned sv;
  asm volatile(M0SET GL("%[v]", "%[s]", 0, "") M0RST : [sv] "=&s"(sv) : [v] "v"(v), [s] "s"(sb), [l] "s"(l) : "memory"); }

constexpr int L_I2 = 40 * 1056, LDS_PREP = L_I2 + 20 * 2064 + 1024;
__global__ void __launch_bounds__(512, 2) k_prep(const u16* __restrict__ x, const u16* __restrict__ wr, const u16* __restrict__ wi,
                                                 const u16* __restrict__ wqkv, const u16* __restrict__ wo, char* __restrict__ ws) {
  STAMP(0);
  extern __shared__ __attribute__((aligned(16))) char smem[];
  const lptr lds = (lptr)smem;
  const int tid = threadIdx.x, lane = tid & 63, w = wave_id(), g = lane >> 4, r16 = lane & 15;
  const int job = blockIdx.x;
  if (__builtin_expect(job < 144, 1)) {
    const bool cols = job >= 64 && job < 124;
    const int cj = job - 64, cb = cj < 48 ? (cj & 48) + 2 * (cj & 7) + ((cj >> 3) & 1) : cj;
    u32x4 a1[3], m2[3][2]; u32x2 a1b[3];
#pragma unroll
    for (int T = 0; T < 3; T++) { a1[T] = *(const u32x4*)g_dt.a1[T][lane]; a1b[T] = *(const u32x2*)g_dt.a1b[T][lane]; }
#pragma unroll
    for (int i = 0; i < 3; i++)
#pragma unroll
      for (int T = 0; T < 2; T++) m2[i][T] = *(const u32x4*)g_dt.m2[(i < 2 || w < 4) ? w + 8 * i : 0][T][lane];
    SB();
    if (cols) {
#pragma unroll
      for (int i = 0; i < 5; i++) dma1((16 * (5 * w + i) + (lane >> 2)) * 3840 + cb * 64 + (lane & 3) * 16, (const char*)wqkv, lds_addr(lds) + (5 * w + i) * 1056);
    } else {
      const char* src = (const char*)(job < 64 ? x : wo);
      const int sl = 8 * (w & 3) + 4 * (w >> 2);
      src += (job < 64 ? rowof(32 * job + sl) : kappa_to_i(32 * (job - 124) + sl)) * 1280;
      const int st = (job < 64 ? 31 : 0) * 1280;
#pragma unroll
      for (int i = 0; i < 5; i++) dma1(1024 * i + lane * 16 + ((i == 4 || lane < 16 * i) ? i - 1 : i) * st, src, lds_addr(lds) + (5 * w + i) * 1024);
    }
    unsigned so[3][2]; float sc[2] = {1.f, 1.f};
    if (cols) {
#pragma unroll
      for (int m = 0; m < 2; m++) {
        const int c = 32 * cb + 16 * m + r16;
        const int which = c / 640, rem = c - which * 640, head = rem / 20, d = rem - head * 20, grp = d >> 2;
        const int T = grp < 4 ? (which == 2 ? 2 : 0) + (grp & 1) : (which == 1 ? 3 : 2), gs = grp < 4 ? (which == 1 ? 2 : 0) + (grp >> 1) : (which == 2 ? 3 : 2);
        if (which == 0) sc[m] = 0.22360679774997896f * 1.4426950408889634f;
#pragma unroll
        for (int i = 0; i < 3; i++) so[i][m] = WS_WQ + (((head >> 1) * 10 + 4 * (head & 1) + T) * 20 + w + 8 * i) * 1024 + (g * 16 + 4 * gs + (d & 3)) * 16;
      }
    } else {
#pragma unroll
      for (int i = 0; i < 3; i++) {
        const int G = w + 8 * i;
        const unsigned f0 = wo_frag(2 * G, job - 124), f1 = wo_frag(2 * G + 1, job - 124);
#pragma unroll
        for (int T = 0; T < 2; T++)
          so[i][T] = job < 64 ? WS_RB0 + (job * 641 + 32 * G + trow(T, r16)) * 64 + (job & 1) * 64 + g * 16 : WS_WOF + (r16 < 8 ? f0 : f1) + (g * 16 + (trow(T, r16) & 15)) * 16;
      }
    }
    if (w >= 4) { so[2][0] = WS_END; so[2][1] = WS_END; }
    asm volatile("" : "+v"(so[0][0]), "+v"(so[0][1]), "+v"(so[1][0]), "+v"(so[1][1]), "+v"(so[2][0]), "+v"(so[2][1]), "+v"(sc[0]), "+v"(sc[1]));
    const f32x4 z4 = {0.f, 0.f, 0.f, 0.f};
    const lptr i2 = lds + L_I2;
    asm volatile("s_waitcnt vmcnt(0)" : "+v"(a1[0]), "+v"(a1[1]), "+v"(a1[2]), "+v"(a1b[0]), "+v"(a1b[1]), "+v"(a1b[2]) :: "memory");
    MARK(0);
    f32x4 t1[4][3];
    if (cols) {
      raw_bar();
      const lptr src = lds + (4 * g + (r16 >> 2)) * 1056 + (w >> 1) * 256 + (w & 1) * 32 + (lane & 3) * 8, srb = src + (32 - 8 * (g >> 1)) * 1056;
#pragma unroll
      for (int i = 0; i < 4; i++) {
        const u32x2 f0 = ldtr(src + i * 64), f1 = ldtr(src + 16 * 1056 + i * 64), f2 = ldtr(srb + i * 64);
#pragma unroll
        for (int T = 0; T < 3; T++) t1[i][T] = mfma16(u32x4{a1b[T][0], a1b[T][1], 0u, 0u}, u32x4{f2[0], f2[1], 0u, 0u}, mfma16(a1[T], u32x4{f0[0], f0[1], f1[0], f1[1]}, z4));
      }
      const int xo = ((w >> 1) * 8) ^ (((r16 >> 1) & 3) * 16);
      const lptr dst = i2 + 2 * g * 2064 + (16 * (w & 1) + r16) * 64;
#pragma unroll
      for (int T = 0; T < 3; T++)
#pragma unroll
        for (int e = 0; e < 4; e++)
          if (T < 2 || g < 2) *(LDS_AS u32x2*)(dst + (8 * T + (e >> 1)) * 2064 + (xo ^ ((e & 1) * 32))) = u32x2{pk2(t1[0][T][e], t1[1][T][e]), pk2(t1[2][T][e], t1[3][T][e])};
    } else {
      const lptr src = lds + w * 5120 + lane * 8, srb = lds + w * 5120 + 1024 + (lane & 31) * 8;
#pragma unroll
      for (int i = 0; i < 4; i++) {
        const u32x2 f0 = ldtr(src + i * 1280), f1 = ldtr(src + i * 1280 + 512), f2 = ldtr(srb + i * 1280);
#pragma unroll
        for (int T = 0; T < 3; T++) t1[i][T] = mfma16(u32x4{f2[0], f2[1], 0u, 0u}, u32x4{a1b[T][0], a1b[T][1], 0u, 0u}, mfma16(u32x4{f0[0], f0[1], f1[0], f1[1]}, a1[T], z4));
      }
      const int xo = ((r16 & 1) * 32 + g * 8) ^ ((w & 1) * 32);
      const lptr dst = i2 + (r16 >> 1) * 2064 + w * 256;
#pragma unroll
      for (int i = 0; i < 4; i++)
#pragma unroll
        for (int T = 0; T < 3; T++)
          if (T < 2 || r16 < 8) *(LDS_AS u32x2*)(dst + T * 8 * 2064 + i * 64 + (xo ^ ((i >> 1) * 16))) = pk4(t1[i][T]);
    }
    lds_bar();
    MARK(1);
    const rsrc_t rw = mk_rsrc(ws, WS_END);
    f32x4 sq[2] = {z4, z4};
    u32x4 f[3][2];
#pragma unroll
    for (int i = 0; i < 3; i++) {
      const lptr fp = i2 + ((i < 2 || w < 4) ? w + 8 * i : 0) * 2064 + r16 * 64 + ((g ^ ((r16 >> 1) & 3)) * 16);
      f[i][0] = ld128(fp); f[i][1] = ld128(fp + 1024);
    }
    if (cols) {
#pragma unroll
      for (int i = 0; i < 3; i++)
#pragma unroll
        for (int m = 0; m < 2; m++) st128<AUXB>(rw, so[i][m], pk8(mfma16(m2[i][0], f[i][m], z4) * sc[m], mfma16(m2[i][1], f[i][m], z4) * sc[m]));
    } else {
#pragma unroll
      for (int i = 0; i < 3; i++)
#pragma unroll
        for (int T = 0; T < 2; T++) {
          const f32x4 d0 = mfma16(f[i][0], m2[i][T], z4), d1 = mfma16(f[i][1], m2[i][T], z4);
          st128<AUXB>(rw, so[i][T], pk8(d0, d1));
          const float k = ((i == 2 && w >= 4) || (i == 0 && T == 0 && tid < 64 && r16 == 1)) ? 0.f : 1.f;
          sq[0] += d0 * d0 * k; sq[1] += d1 * d1 * k;
        }
    }
    LDS_AS float* sl = (LDS_AS float*)(lds + LDS_PREP - 1024);
    if (job < 64) {
#pragma unroll
      for (int m = 0; m < 2; m++) {
#pragma unroll
        for (int e = 0; e < 4; e++) sq[m][e] = row16_sum(sq[m][e]);
        if (r16 == 0) *(LDS_AS f32x4*)(sl + w * 32 + 8 * g + 4 * m) = sq[m];
      }
    }
    MARK(2);
    if (job < 64) {
      lds_bar();
      if (tid < 32) {
        float v = 0.f;
#pragma unroll
        for (int i = 0; i < 8; i++) v += sl[i * 32 + tid];
        st32<AUXB>(rw, WS_RS1 + rpos(32 * job + tid) * 4, __float_as_uint(__builtin_amdgcn_rsqf((v + 640.f * 1e-5f) * (2048.f * 2048.f / 640.f))));
      }
    }
    return;
  }
  const int mi = job - 144;
  const rsrc_t rw = mk_rsrc(ws, WS_END);
  if (mi < 64) {
    const int m = mi >> 3, oc = mi & 7;
    const char* src = (const char*)((w >> 2) ? wi : wr) + (64 * (4 * m + (w & 3)) + 8 * oc) * 642;
    dma4(lane * 16, src, lds_addr(lds) + w * 5152);
    dma1(4096 + lane * 16, src, lds_addr(lds) + w * 5152 + 4096);
    if (lane == 0) dma1(5120, src, lds_addr(lds) + w * 5152 + 5120);
    vm_bar<0>();
    MARK(3);
    LDS_AS u16* L = (LDS_AS u16*)lds + (tid & 7) * 321;
#pragma unroll
    for (int it = 0; it < 5; it++) {
      const int c = (tid >> 3) + 64 * it, col = freq_of(c >> 4, c & 15);
      u32x4 o;
#pragma unroll
      for (int e = 0; e < 4; e++) o[e] = (unsigned)L[e * 2576 + col] | ((unsigned)L[(4 + e) * 2576 + col] << 16);
      st128<AUXB>(rw, WS_WF + c * 8192 + (m * 64 + 8 * oc + (tid & 7)) * 16, o);
    }
    MARK(4);
#pragma unroll
    for (int it = 0; it < 3; it++) st128<AUXB>(rw, WS_RB1 + ((it * 64 + mi) * 512 + tid) * 16, u32x4{EMPTY, EMPTY, EMPTY, EMPTY});
  } else if (mi < 84) {
    const char* src = (const char*)wo + kappa_to_i(32 * (mi - 64) + 8 * (w & 3) + 4 * (w >> 2)) * 1280;
    dma4(lane * 16, src, lds_addr(lds) + w * 5152);
    dma1(4096 + lane * 16, src, lds_addr(lds) + w * 5152 + 4096);
    vm_bar<0>();
#pragma unroll
    for (int i = 0; i < 5; i++) {
      const lptr a = lds + g * 5152 + (r16 >> 2) * 1280 + (5 * w + i) * 32 + (lane & 3) * 8;
      const u32x2 lo = ldtr(a), hi = ldtr(a + 4 * 5152);
      st128<AUXB>(rw, WS_WOP + wo_frag(5 * w + i, mi - 64) + lane * 16, u32x4{lo[0], lo[1], hi[0], hi[1]});
    }
  } else {
    const int i0 = (mi - 84) * 512 + tid;
    unsigned pf0, pf1;
    {
      const unsigned* cp = CODE_ADDR(SYM_PREP); const unsigned* cm = CODE_ADDR(SYM_MAIN);
      const unsigned* p = (const unsigned*)wqkv + min(14336 + i0 - 4096, 19199) * 32;
      if (mi < 92) p = tid < 72 ? cp + tid * 32 : tid < 152 ? (const unsigned*)&g_ct + (tid - 72) * 32 : cm + min(tid - 152, 259) * 32;
      asm volatile("global_load_dword %0, %1, off" : "=v"(pf0) : "v"(p) : "memory");
      asm volatile("global_load_dword %0, %1, off" : "=v"(pf1) : "v"((const unsigned*)wqkv + i0 * 32) : "memory");
    }
    nap<K1DELAY>();
    if (i0 < 6272) st128<AUXB>(rw, WS_FL + i0 * 16, u32x4{0u, 0u, 0u, 0u});
#pragma unroll
    for (int it = 0; it < 6; it++) { const int i = 98304 + i0 + it * 14336; if (i < (int)RBSZ / 16) st128<AUXB>(rw, WS_RB1 + i * 16, u32x4{EMPTY, EMPTY, EMPTY, EMPTY}); }
    asm volatile("s_waitcnt vmcnt(0)" : "+v"(pf0), "+v"(pf1));
  }
}

#define PINF(x) asm volatile("" : "+v"(x))
template <bool FIRST>
DI void col_phase(rsrc_t rw, lptr lds, int c0 SARG) {
  int lane = threadIdx.x & 63; PINF(lane);
  const int wv = wave_id(), w = wv & 3, q = wv >> 2, tid = w * 64 + lane, g = lane >> 4, r16 = lane & 15;
  const int c = c0 + q;
  const lptr img = lds + q * SLOT32;
  f32x4 xr[2], xi[2], rs[2]; u32x4 wf[2], xb[2];
  if (FIRST) {
#pragma unroll
    for (int t = 0; t < 2; t++) { xb[t] = gl128<0>(rw, WS_RB0 + ((tid >> 2) * 641 + ((tid >> 2) & 1) + 2 * c + t) * 64 + (tid & 3) * 16); rs[t] = asf(gl128<0>(rw, WS_RS1 + (1024 * t + 4 * tid) * 4)); }
  }
  u32x4 c64[2], s64[2], n64[2], c32[2], s32[2], n32[2];
  const u32x4* ct = (const u32x4*)&g_ct + lane;
  c64[0] = ct[(2 * w) * 64]; s64[0] = ct[(2 * w + 1) * 64]; c32[0] = ct[8 * 64]; s32[0] = ct[9 * 64];
#pragma unroll
  for (int t = 0; t < 2; t++) wf[t] = gl128<0>(rw, WS_WF + c * 8192 + ((4 * t + g) * 64 + 16 * w + r16) * 16);
  SB();
  f32x4 t1c[2], t1s[2], t2c[2], t2s[2];
  {
    const float f1 = (float)(16 * w + r16) * (1.f / 2048.f), b1 = (float)(4 * g), f2 = (float)(16 * w + 4 * g) * (1.f / 2048.f), b2 = (float)r16;
#pragma unroll
    for (int t = 0; t < 2; t++) {
#pragma unroll
      for (int e = 0; e < 4; e++) {
        const float a1 = (b1 + (float)(16 * t + e)) * f1, a2 = (b2 + (float)(16 * t)) * (f2 + (float)e * (1.f / 2048.f));
        t1c[t][e] = __builtin_amdgcn_cosf(a1); t1s[t][e] = __builtin_amdgcn_sinf(-a1);
        t2c[t][e] = __builtin_amdgcn_cosf(a2); t2s[t][e] = __builtin_amdgcn_sinf(a2);
      }
      PINF(t1c[t]); PINF(t1s[t]); PINF(t2c[t]); PINF(t2s[t]);
    }
  }
  int wo_ = r16 * 128 + (((2 * w + (g >> 1)) ^ (r16 >> 1)) * 16) + (g & 1) * 8, ro_ = r16 * 128 + ((g ^ (r16 >> 1)) * 16), ro2 = ro_ ^ 64;
  PINF(wo_); PINF(ro_); PINF(ro2);
  {
    const unsigned par = (lane & 1) ? 0x80008000u : 0u;
    c64[1] = c64[0] ^ par; s64[1] = s64[0] ^ par; n64[0] = s64[0] ^ 0x80008000u; n64[1] = s64[1] ^ 0x80008000u;
    c32[1] = c32[0] ^ 0x80000000u; s32[1] = s32[0] ^ 0x80000000u; n32[0] = s32[0] ^ 0x80008000u; n32[1] = s32[0] ^ 0x00008000u;
    PINF(c64[1]); PINF(s64[1]); PINF(n64[0]); PINF(n64[1]); PINF(c32[1]); PINF(s32[1]); PINF(n32[0]); PINF(n32[1]);
  }
  SB();
  if (FIRST) {
#pragma unroll
    for (int t = 0; t < 2; t++) {
      xr[t] = f32x4{bflo(xb[0][2 * t]), bfhi(xb[0][2 * t]), bflo(xb[0][2 * t + 1]), bfhi(xb[0][2 * t + 1])};
      xi[t] = f32x4{bflo(xb[1][2 * t]), bfhi(xb[1][2 * t]), bflo(xb[1][2 * t + 1]), bfhi(xb[1][2 * t + 1])};
    }
  } else {
    drain();
    u32x2 sp[8];
    const unsigned so = WS_SSQ + (1024 * q + 4 * tid) * 2, xo = WS_R3 + (2 * c * RP + 4 * tid) * 4;
    bool bs = true, bx = true;
#pragma unroll 1
    for (int spins = 0; spins < (1 << 18); spins++) {
      if (bx) {
#pragma unroll
        for (int t = 0; t < 2; t++) { xr[t] = asf(gl128<16>(rw, xo + 4096 * t)); xi[t] = asf(gl128<16>(rw, xo + RP * 4 + 4096 * t)); }
      }
      if (bs) {
#pragma unroll
        for (int i = 0; i < 8; i++) sp[i] = gl64<16>(rw, so + i * 4096);
      }
      unsigned m = 0;
#pragma unroll
      for (int t = 0; t < 2; t++) m = umax4(umax4(m, asu(xr[t])), asu(xi[t]));
      bx = __builtin_amdgcn_ballot_w64(m == EMPTY) != 0;
      m = 0;
#pragma unroll
      for (int i = 0; i < 8; i++) m = max(max(m, sp[i][0]), sp[i][1]);
      bs = __builtin_amdgcn_ballot_w64(m == EMPTY) != 0;
      if (!bs && !bx) break;
      nap<YSLEEP>();
    }
    f32x4 r = {640.f * 1e-5f, 640.f * 1e-5f, 640.f * 1e-5f, 640.f * 1e-5f};
#pragma unroll
    for (int i = 0; i < 8; i++) r += f32x4{bflo(sp[i][0]), bfhi(sp[i][0]), bflo(sp[i][1]), bfhi(sp[i][1])};
#pragma unroll
    for (int e = 0; e < 4; e++) r[e] = __builtin_amdgcn_rsqf(r[e] * (2048.f * 2048.f / 640.f));
    LDS_AS f32x4* ex = (LDS_AS f32x4*)(lds + 2 * SLOT32) + tid;
    ex[q * 256] = r;
    lds_bar();
    rs[0] = ex[0]; rs[1] = ex[256];
  }
  MARK(FIRST ? 9 : 21);
#pragma unroll
  for (int t = 0; t < 2; t++) {
    u32x2 pb = pk4(xi[t] * rs[t]);
    if (c == 0) pb = u32x2{0u, 0u};
    *(LDS_AS u32x2*)(img + wo_ + t * 2048) = pk4(xr[t] * rs[t]);
    *(LDS_AS u32x2*)(img + 4096 + wo_ + t * 2048) = pb;
  }
  lds_bar();
  u32x4 ur[2][2], ui[2][2], br, bi;
  f32x4 ar[2], ai[2];
  const f32x4 z4 = {0.f, 0.f, 0.f, 0.f};
  auto rd_in = [&](int off) {
#pragma unroll
    for (int ks = 0; ks < 2; ks++)
#pragma unroll
      for (int t = 0; t < 2; t++) { const lptr p = img + off + (ks ? ro2 : ro_) + t * 2048; ur[t][ks] = ld128(p); ui[t][ks] = ld128(p + 4096); }
    SB();
  };
  auto cmul = [&](const f32x4* cc, const f32x4* sn) {
    br = pk8(ar[0] * cc[0] - ai[0] * sn[0], ar[1] * cc[1] - ai[1] * sn[1]);
    bi = pk8(ai[0] * cc[0] + ar[0] * sn[0], ai[1] * cc[1] + ar[1] * sn[1]);
  };
  rd_in(0);
#pragma unroll
  for (int ks = 0; ks < 2; ks++) {
#pragma unroll
    for (int t = 0; t < 2; t++) { ar[t] = mfma16(ur[t][ks], c64[ks], ks ? ar[t] : z4); ai[t] = mfma16(ui[t][ks], c64[ks], ks ? ai[t] : z4); }
#pragma unroll
    for (int t = 0; t < 2; t++) { ar[t] = mfma16(ui[t][ks], s64[ks], ar[t]); ai[t] = mfma16(ur[t][ks], n64[ks], ai[t]); }
  }
  const f32x4 fa[2] = {asf(wf[0] << 16), asf(wf[1] << 16)}, fb[2] = {asf(wf[0] & 0xFFFF0000u), asf(wf[1] & 0xFFFF0000u)};
  cmul(t1c, t1s);
#pragma unroll
  for (int t = 0; t < 2; t++) { ar[t] = mfma16(c32[t], br, z4); ai[t] = mfma16(c32[t], bi, z4); }
#pragma unroll
  for (int t = 0; t < 2; t++) { ar[t] = mfma16(s32[t], bi, ar[t]); ai[t] = mfma16(n32[t], br, ai[t]); }
  cmul(fa, fb);
#pragma unroll
  for (int t = 0; t < 2; t++) { ar[t] = mfma16(br, c32[t], z4); ai[t] = mfma16(bi, c32[t], z4); }
#pragma unroll
  for (int t = 0; t < 2; t++) { ar[t] = mfma16(bi, n32[t], ar[t]); ai[t] = mfma16(br, s32[t], ai[t]); }
  cmul(t2c, t2s);
#pragma unroll
  for (int t = 0; t < 2; t++) {
    *(LDS_AS u32x2*)(img + 8192 + wo_ + t * 2048) = u32x2{br[2 * t], br[2 * t + 1]};
    *(LDS_AS u32x2*)(img + 12288 + wo_ + t * 2048) = u32x2{bi[2 * t], bi[2 * t + 1]};
  }
  lds_bar();
  rd_in(8192);
#pragma unroll
  for (int ks = 0; ks < 2; ks++) {
#pragma unroll
    for (int t = 0; t < 2; t++) { ar[t] = mfma16(c64[ks], ur[t][ks], ks ? ar[t] : xr[t]); ai[t] = mfma16(c64[ks], ui[t][ks], ks ? ai[t] : xi[t]); }
#pragma unroll
    for (int t = 0; t < 2; t++) { ar[t] = mfma16(n64[ks], ui[t][ks], ar[t]); ai[t] = mfma16(s64[ks], ur[t][ks], ai[t]); }
  }
  const unsigned ob = (FIRST ? WS_RB1 : WS_RB2) + (2 * c * RBP + 8 * tid) * 2;
  st128<16>(rw, ob, clean(pk8(ar[0], ar[1])));
  st128<16>(rw, ob + RBP * 2, c == 0 ? u32x4{0u, 0u, 0u, 0u} : clean(pk8(ai[0], ai[1])));
}

DI f32x16 mfma32(u32x4 a, u32x4 b, f32x16 c) {
  return __builtin_amdgcn_mfma_f32_32x32x16_bf16(__builtin_bit_cast(bf16x8, a), __builtin_bit_cast(bf16x8, b), c, 0, 0, 0);
}
DI f32x16 mfma32k8(u32x2 a, u32x2 b, f32x16 c) {
  return __builtin_amdgcn_mfma_f32_32x32x8bf16_1k(__builtin_bit_cast(s16x4, a), __builtin_bit_cast(s16x4, b), c, 0, 0, 0);
}
DI f32x16 scores(u32x4 k1, u32x2 k2, u32x4 q1, u32x2 q2) {
  f32x16 z;
#pragma unroll
  for (int i = 0; i < 16; i++) z[i] = 0.f;
  f32x16 s = mfma32(k1, q1, z);
  asm volatile("s_nop 11" : "+v"(s));
  return mfma32k8(k2, q2, s);
}
DI float max16(const f32x16& s) {
  float m = fmaxf(s[0], s[1]);
#pragma unroll
  for (int i = 2; i < 16; i++) m = fmaxf(m, s[i]);
  return m;
}
#define STR_(x) #x
#define STR(x) STR_(x)
#define KOF(t) ((((t)&7)>>1)*11264+((t)&1)*1280)
#define VOF(t) ((((t)&7)>>1)*11264+5120+((t)&1)*1536)
#define EXP(d, s) "v_exp_f32 v" #d ", v" #s "\n\t"
#define CVT(d, s) "v_cvt_pk_bf16_f32 v" #d ", v" #s ", v[" #s "+1]\n\t"
#define STEPA(t) \
  "v_mfma_f32_32x32x16_bf16 v[48:63], v[80:83], v[136:139], v[120:135]\n\t" \
  "ds_read_b128 v[80:83], v100 offset:" STR(KOF(t+1)) "\n\t" \
  EXP(64,32) EXP(65,33) CVT(78,68) CVT(79,70) EXP(66,34) EXP(67,35) \
  "v_mfma_f32_32x32x16_bf16 v[104:119], v[90:93], v[76:79], v[104:119]\n\t" \
  "ds_read_b64_tr_b16 v[90:91], v102 offset:" STR(VOF(t)+768) "\n\t" \
  "ds_read_b64_tr_b16 v[92:93], v102 offset:" STR(VOF(t)+1152) "\n\t" \
  EXP(68,36) EXP(69,37) CVT(72,64) CVT(73,66) EXP(70,38) EXP(71,39) \
  "v_mfma_f32_32x32x8_bf16 v[48:63], v[84:85], v[140:141], v[48:63]\n\t" \
  "ds_read_b64 v[84:85], v101 offset:" STR(KOF(t+1)) "\n\t" \
  EXP(64,40) EXP(65,41) CVT(74,68) CVT(75,70) EXP(66,42) EXP(67,43) \
  "s_waitcnt lgkmcnt(4)\n\t" \
  "v_mfma_f32_32x32x16_bf16 v[0:15], v[86:89], v[72:75], v[0:15]\n\t" \
  EXP(68,44) EXP(69,45) CVT(76,64) CVT(77,66) EXP(70,46) EXP(71,47)
#define STEPB(t) \
  "s_waitcnt lgkmcnt(0)\n\t" \
  "v_mfma_f32_32x32x16_bf16 v[32:47], v[80:83], v[94:97], v[16:31]\n\t" \
  EXP(64,48) EXP(65,49) CVT(78,68) CVT(79,70) EXP(66,50) EXP(67,51) \
  "v_mfma_f32_32x32x16_bf16 v[0:15], v[90:93], v[76:79], v[0:15]\n\t" \
  EXP(68,52) EXP(69,53) CVT(72,64) CVT(73,66) EXP(70,54) EXP(71,55) \
  "v_mfma_f32_32x32x8_bf16 v[32:47], v[84:85], v[98:99], v[32:47]\n\t" \
  EXP(64,56) EXP(65,57) CVT(74,68) CVT(75,70) EXP(66,58) EXP(67,59) \
  "v_mfma_f32_32x32x16_bf16 v[104:119], v[86:89], v[72:75], v[104:119]\n\t" \
  "ds_read_b64_tr_b16 v[86:87], v102 offset:" STR(VOF(t+1)) "\n\t" \
  "ds_read_b64_tr_b16 v[88:89], v102 offset:" STR(VOF(t+1)+384) "\n\t" \
  EXP(68,60) EXP(69,61) CVT(76,64) CVT(77,66) EXP(70,62) EXP(71,63)
#define PR(n) "s_setprio " #n "\n\t"
#define CHUNK(i) PR(1) STEPA(2*(i)) PR(0) STEPB(2*(i)) "s_barrier\n\t" PR(2) STEPA(2*(i)+1) STEPB(2*(i)+1)
#define Z4(a) "v_mov_b64 v[" #a ":" #a "+1], 0\n\tv_mov_b64 v[" #a "+2:" #a "+3], 0\n\t"

#define R4(b) "v[" STR(b) ":" STR(b) "+3]"
#define MM(d, a, b) "v_mfma_f32_16x16x32_bf16 " R4(d) ", " R4(a) ", " R4(b) ", " R4(d) "\n\t"
#define GM(B, m, i) MM(20*m+4*i, B+8+4*i, B+4*m)
#define RT(B, m, hf, k) "ds_read_b64_tr_b16 v[" STR(B+4*m+2*hf) ":" STR(B+4*m+2*hf+1) "], v10" #m " offset:" STR(k*8192+hf*1024) "\n\t"
#define RS(B, i, k) "ds_read_b128 " R4(B+8+4*i) ", v102 offset:" STR(i*2048+k*1024) "\n\t"
#define IF5_4(x) ""
#define IF5_5(x) x
#define GSTEP(N, B, C, k) GM(B,0,0) RT(C,0,0,k) RT(C,0,1,k) GM(B,1,0) RS(C,0,k) RT(C,1,0,k) GM(B,0,1) RT(C,1,1,k) RS(C,1,k) GM(B,1,1) RS(C,2,k) \
  GM(B,0,2) RS(C,3,k) GM(B,1,2) IF5_##N(RS(C,4,k)) GM(B,0,3) GM(B,1,3) IF5_##N(GM(B,0,4) GM(B,1,4)) MM(40,B,B)
#define GEMM_ASM(N) \
  Z4(0) Z4(4) Z4(8) Z4(12) Z4(16) Z4(20) Z4(24) Z4(28) Z4(32) Z4(36) Z4(40) "s_mov_b32 %[c], 0\n\ts_barrier\n\t" \
  RT(44,0,0,0) RT(44,0,1,0) RS(44,0,0) RT(44,1,0,0) RT(44,1,1,0) RS(44,1,0) RS(44,2,0) RS(44,3,0) IF5_##N(RS(44,4,0)) \
  "L_gemm_%=:\n\ts_add_u32 %[c], %[c], 1\n\ts_and_b32 %[t], %[c], 3\n\ts_cselect_b32 %[t], 1, -3\n\ts_mul_i32 %[t], %[t], 36864\n\ts_waitcnt lgkmcnt(0)\n\t" \
  GSTEP(N,44,72,1) \
  "v_add_u32 v100, %[t], v100\n\tv_add_u32 v101, %[t], v101\n\tv_add_u32 v102, %[t], v102\n\ts_waitcnt lgkmcnt(0)\n\ts_barrier\n\t" \
  GSTEP(N,72,44,0) \
  "s_cmp_lg_u32 %[c], 9\n\ts_cbranch_scc1 L_gemm_%=\n\ts_waitcnt lgkmcnt(0)\n\t" \
  GSTEP(N,44,72,1) "s_waitcnt lgkmcnt(0)\n\t" \
  MM(40,72,72) GM(72,0,0) GM(72,1,0) GM(72,0,1) GM(72,1,1) "s_nop 4\n\t" \
  "v_cndmask_b32 v44, v40, v41, %[m1]\n\tv_cndmask_b32 v45, v42, v43, %[m1]\n\tv_mov_b32 v46, 0x3727c5ac\n\tv_cndmask_b32 v44, v44, v45, %[m2]\n\t" \
  "v_fmac_f32 v46, 0x3acccccd, v44\n\tv_cmp_u_f32 vcc, v44, v44\n\tv_rsq_f32 v46, v46\n\ts_cbranch_vccz L_gok_%=\n\tds_write_b32 v106, v106\n\t" \
  "L_gok_%=:\n\tds_write_b32 v103, v46\n\ts_waitcnt lgkmcnt(0)\n\ts_barrier\n\t" \
  "ds_read_b32 v44, v104\n\tds_read_b32 v45, v105\n\tds_read_b32 v46, v106\n\t" \
  GM(72,0,2) GM(72,1,2) GM(72,0,3) GM(72,1,3) IF5_##N(GM(72,0,4) GM(72,1,4)) "s_waitcnt lgkmcnt(0)"
#define GEMM_OPS \
  : "={v[0:3]}"(acc[0][0]), "={v[4:7]}"(acc[0][1]), "={v[8:11]}"(acc[0][2]), "={v[12:15]}"(acc[0][3]), "={v[16:19]}"(acc[0][4]), \
    "={v[20:23]}"(acc[1][0]), "={v[24:27]}"(acc[1][1]), "={v[28:31]}"(acc[1][2]), "={v[32:35]}"(acc[1][3]), "={v[36:39]}"(acc[1][4]), \
    "={v44}"(rs[0]), "={v45}"(rs[1]), "={v46}"(vote), "+{v100}"(am0), "+{v101}"(am1), "+{v102}"(as), [c] "=&s"(c_), [t] "=&s"(t_) \
  : "{v103}"(a_w), "{v104}"(a_r[0]), "{v105}"(a_r[1]), "{v106}"(a_v), [m1] "s"(0xAAAAAAAAAAAAAAAAull), [m2] "s"(0xCCCCCCCCCCCCCCCCull) \
  : "memory", "scc", "vcc", "v40", "v41", "v42", "v43", "v47", "v48", "v49", "v50", "v51", "v52", "v53", "v54", "v55", "v56", "v57", "v58", "v59", "v60", \
    "v61", "v62", "v63", "v64", "v65", "v66", "v67", "v68", "v69", "v70", "v71", "v72", "v73", "v74", "v75", "v76", "v77", "v78", "v79", "v80", "v81", \
    "v82", "v83", "v84", "v85", "v86", "v87", "v88", "v89", "v90", "v91", "v92", "v93", "v94", "v95", "v96", "v97", "v98", "v99"

constexpr int HR = 4 * KVC;
constexpr int L_A = 2 * HR;
constexpr int L_EX = 15360;
constexpr int L_MISC = L_A + 70 * 1024;
constexpr int LDS_FUSED = L_MISC + 2048;

DI bool poll16(rsrc_t rw, unsigned off, unsigned need) {
  const int lane = threadIdx.x & 63;
#pragma unroll 1
  for (int spins = 0; spins < (1 << 20); spins++) {
    const unsigned v = gl32<16>(rw, off + (lane & 15) * 4);
    const unsigned got = (unsigned)__builtin_amdgcn_ballot_w64(v != 0) & 0xFFFFu;
    if ((got & need) == need) return true;
    nap<1>();
  }
  return false;
}

DI void poll_aux(rsrc_t rw, unsigned off) {
#pragma unroll 1
  for (int spins = 0; spins < (1 << 20); spins++) {
    const u32x4 v = gl128<16>(rw, off + (threadIdx.x & 63) * 16);
    if (!__builtin_amdgcn_ballot_w64(min(min(v[0], v[1]), min(v[2], v[3])) == 0u)) break;
    nap<4>();
  }
}
#define LAYER_VARS \
  extern __shared__ __attribute__((aligned(16))) char smem[]; \
  const lptr lds = (lptr)smem; \
  const rsrc_t rw = mk_rsrc(ws, WS_END); \
  int lane = threadIdx.x & 63; asm volatile("" : "+v"(lane)); \
  const int w = wave_id(), g = lane >> 4, r16 = lane & 15; \
  const int xcd = blockIdx.x & 7, bi = blockIdx.x >> 3, u = 8 * (xcd & 1) + (bi & 7), qt = 4 * (xcd >> 1) + (bi >> 3); \
  const int hf = u & 1, ct = u >> 1;     \
  constexpr unsigned FKV = WS_FL + (FINAL ? 32768 : 0), KV = WS_KV + (FINAL ? LAYER2 : 0), OB = WS_O + (FINAL ? LAYER2 : 0); \
  constexpr unsigned AUX = WS_AUX + (FINAL ? 1024 : 0), RB = FINAL ? WS_RB2 : WS_RB1; \
  constexpr int MK = FINAL ? 12 : 0; (void)MK; (void)hf; (void)AUX; (void)RB; (void)FKV; \
  LDS_AS unsigned* misc = (LDS_AS unsigned*)(lds + L_MISC + 1024) + (FINAL ? 8 : 0);

template <bool FINAL>
DI void loader(char* ws SARG) {
  LAYER_VARS
  {
    const int lw = w - 8;
    {
      unsigned vm[4];
#pragma unroll
      for (int r = 0; r < 4; r++) vm[r] = (4 * (4 * lw + r) + g) * (RBP * 2) + 256 * qt + (((r16 >> 1) ^ (g | ((r & 2) << 1))) * 32) + (r16 & 1) * 16 + 3072 - 1024 * r;
      const unsigned l0 = lds_addr(lds) + lw * 4096, v0 = 2 * lw * 20480 + lane * 16, v2 = ((8 + (lw >> 1)) * 20 + (lw & 1)) * 1024 + lane * 16;
      constexpr int CNT = FINAL ? 9 : 8;
      auto lds_w = [&](int s) {
        const char* sb = ws + WS_WQ + u * 204800 + s * 2048;
        dma_s(v0, v0 + 20480 - 2048, sb, l0 + (s & 3) * SLOT32 + 16384 + (lw >> 1) * 2048);
        if (FINAL) dma1_sc1(v2, sb, lds_addr(lds) + (s & 3) * SLOT32 + 24576 + (lw >> 1) * 10240 + (lw & 1) * 1024);
      };
      auto lds_a = [&](int s) {
        dma_a(vm, ws + RB - 3072 + s * (64 * RBP * 2), l0 + (s & 3) * SLOT32); };
#pragma unroll 1
      for (int att = 0;; att++) {
        const bool cb = att == 0 && blockIdx.x < 160;
        if (att == 0) { if (cb) raw_bar(); else nap<FINAL ? 100 : PFDELAY>(); }
        lds_w(0); lds_w(1);
        if (cb) raw_bar();
        lds_w(2); lds_w(3);
        if (att == 0) {
          if (FINAL && cb) raw_bar();
          raw_bar();
          if (cb) nap<XDELAY>(); else nap<FINAL ? SW2 : SW1>();
          MARK(MK + 7);
          const int pl = min(lane, 39);
          const unsigned po = RB + ((64 * (pl >> 2) + 16 * lw + 4 * (pl & 3) + 1) * RBP + 128 * qt + 126) * 2;
#pragma unroll 1
          for (int spins = 0; spins < (1 << 20); spins++) {
            const unsigned x0 = gl32<16>(rw, po), x1 = gl32<16>(rw, po + 2 * RBP * 2);
            if (!__builtin_amdgcn_ballot_w64(max(x0, x1) == EMPTY)) break;
            nap<XSLEEP>();
          }
        }
        drain();
        MARK(MK + 8);
#pragma unroll 1
        for (int s = 0; s < 4; s++) lds_a(s);
        vm_bar<12>(); vm_bar<8>();
        lds_a(4); lds_w(4);
        vm_bar<4 + CNT>();
#pragma unroll 1
        for (int s = 5; s < 10; s++) { lds_a(s); lds_w(s); vm_bar<2 * CNT>(); }
        vm_bar<CNT>();
        poll_aux(rw, AUX);
        vm_bar<0>();
        raw_bar();
        if (XNL || att || !*(volatile LDS_AS unsigned*)(misc + 2)) break;
      }
    }
    MARK(MK + 0);
    const int hh = lw >> 1, hp = lw & 1;
    const unsigned kvv = KV + (2 * u + hh) * 16 * KVC + hp * 5120 + lane * 16;
    const unsigned lkv = lds_addr(lds) + hh * HR + hp * 5120;
    const unsigned wv = (FINAL ? WS_WOP : WS_WOF) + (ct * 2 + hh) * 51200 + lane * 16;
    const unsigned myfl = FKV + (u * 16 + qt) * 128;
    raw_bar();
    MARK(MK + 1);
    {
      u32x4 v[6];
#pragma unroll
      for (int i = 0; i < 6; i++) v[i] = ld128(lds + hh * HR + hp * 5120 + lane * 16 + i * 1024);
#pragma unroll
      for (int i = 0; i < 6; i++) st128<16>(rw, kvv + qt * KVC + i * 1024, clean(v[i]));
    }
    drain();
    unsigned cnt = 0;
    if (lane == 0) cnt = __hip_atomic_fetch_add(misc, 1u, __ATOMIC_RELAXED, __HIP_MEMORY_SCOPE_WORKGROUP);
    if (__builtin_amdgcn_readfirstlane(cnt) == 3 && lane < 16) st32<16>(rw, FKV + (u * 16 + lane) * 128 + qt * 4, 1u);
    MARK(MK + 2);
    auto ldkv = [&](int seq) {
      const int ch = seq == 0 ? qt : seq < 13 ? ((qt & 12) + 3 + seq) & 15 : (qt & 12) | ((qt + seq) & 3);
      dma6_sc1(kvv + ch * KVC, ws, lkv + (seq & 3) * KVC);
    };
    auto fetch = [&](int seq) {
      const unsigned src = kvv + ((((qt & 12) + 3 + seq) & 15)) * KVC;
      u32x4 v[6];
#pragma unroll 1
      for (int spins = 0; spins < (1 << 18); spins++) {
#pragma unroll
        for (int i = 0; i < 6; i++) v[i] = gl128<16>(rw, src + i * 1024);
        unsigned m = 0;
#pragma unroll
        for (int i = 0; i < 6; i++) m = umax4(m, v[i]);
        if (!__builtin_amdgcn_ballot_w64(m == EMPTY)) break;
        nap<2>();
      }
#pragma unroll
      for (int i = 0; i < 6; i++) *(LDS_AS u32x4*)(lds + hh * HR + hp * 5120 + seq * KVC + lane * 16 + i * 1024) = v[i];
    };
#pragma unroll 1
    for (int attempt = 0;; attempt++) {
      int c = -2;
      if (attempt) { ldkv(0); vm_bar<0>(); }
      else {
        fetch(1); MARK(MK + 3); lds_bar();
        MARK(MK + 4);
        fetch(2); poll16(rw, myfl, 12u << (((qt & 12) + 4) & 15)); ldkv(3); MARK(MK + 5); lds_bar();
        c = 1;
      }
#pragma unroll 1
      for (; c < 13; c++) {
        if (c == 2 && !attempt) poll16(rw, myfl, 0xFFFFu);
        if (!FINAL && !attempt) {
          if (c == 10) {
            unsigned n = 0;
            if (lane == 0) n = __hip_atomic_fetch_add(misc + 3, 1u, __ATOMIC_RELAXED, __HIP_MEMORY_SCOPE_WORKGROUP);
            if (__builtin_amdgcn_readfirstlane(n) == 3 && lane < 32) st32<16>(rw, WS_FL + 65536 + (lane < 16 ? (u * 16 + lane) * 128 + qt * 4 : (r16 * 16 + qt) * 128 + 64 + u * 4), 1u);
          }
        }
        if (c >= 2) { const int e = min(4 * (c - 2) + 2 * hp, 33); dma2(wv + e * 1024, ws, lds_addr(lds) + L_A + (hh * 35 + e) * 1024); }
        ldkv(c + 3);
        if (c == 0) vm_bar<12>();
        if (!FINAL && !attempt && c >= 2 && c < 9) {
          const u32x4 e = {EMPTY, EMPTY, EMPTY, EMPTY};
          const unsigned o = OB + LAYER2 + (((c - 2) * 256 + blockIdx.x) * 4 + lw) * 2048 + lane * 16;
          st128<16>(rw, o, e); st128<16>(rw, o + 1024, e);
          st128<16>(rw, ((qt & 3) < 2 && c < 8) ? kvv + LAYER2 + qt * KVC + (c - 2) * 1024 : WS_END, e);
          st128<16>(rw, c < 5 ? OB + ((4 * qt + lw) * 32 + 2 * u) * 1280 + min((c - 2) * 1024, 1536) + lane * 16 : WS_END, e);
          if (c == 2) vm_bar<12>(); else vm_bar<16>();
        } else if (c >= 2) vm_bar<8>(); else if (c >= 0) vm_bar<6>();
      }
      if (!FINAL && !attempt) poll16(rw, myfl + 65536 + hp * 64, 0xFFFFu);
      vm_bar<0>();
      dma4(wv + (35 + 7 * hp) * 1024, ws, lds_addr(lds) + hh * HR + 7 * hp * 1024);
      dma4(wv + (39 + 7 * hp) * 1024, ws, lds_addr(lds) + hh * HR + (4 + 7 * hp) * 1024);
      raw_bar();
      vm_bar<0>();
      if (attempt || !*(volatile LDS_AS unsigned*)(misc + 1)) break;
      raw_bar();
    }
    if (!FINAL) { raw_bar(); raw_bar(); }
  }
}

template <bool FINAL>
DI void compute(char* ws, u16* out SARG) {
  LAYER_VARS
  const int wm = w & 3, wn = w >> 2, h = lane >> 5;
  if (blockIdx.x < 160) col_phase<!FINAL>(rw, lds, 2 * blockIdx.x SPASS);
  else if (!FINAL) {
    for (int pc = (blockIdx.x - 160) * 512 + threadIdx.x; pc < 640 * 80; pc += 96 * 512) {
      const int n = pc % 640, kq = pc / 640;
      const int u_ = n / 40, rn = n - 40 * u_, hn = rn / 20, dn = rn - 20 * hn, gq = dn >> 2;
      const int T = gq < 4 ? 8 + hn : 4 * hn + 3, gs = gq < 4 ? gq : 3;
      float v[8];
#pragma unroll
      for (int e = 0; e < 4; e++) {
        const float a = (float)((freq_of(kq >> 2, 4 * (kq & 3) + e) * n) % 640) * (1.f / 640.f);
        v[2 * e] = 0.055901699437494742f * __builtin_amdgcn_cosf(a);
        v[2 * e + 1] = -0.055901699437494742f * __builtin_amdgcn_sinf(a);
      }
      if (kq == 0) { v[0] = (n & 1) ? -0.039528470752104741f : 0.039528470752104741f; v[1] = 0.039528470752104741f; }
      st128<16>(rw, WS_WQ + ((u_ * 10 + T) * 20 + (kq >> 2)) * 1024 + ((kq & 3) * 16 + 4 * gs + (dn & 3)) * 16,
                  u32x4{pk2(v[0], v[1]), pk2(v[2], v[3]), pk2(v[4], v[5]), pk2(v[6], v[7])});
    }
    nap<FILLDELAY>();
#pragma unroll
    for (int it = 0; it < 4; it++) {
      const int k = (it * 96 + blockIdx.x - 160) * 512 + threadIdx.x, b = k / 1408;
      st128<16>(rw, k < 128 * 1408 ? WS_KV + b * 4 * KVC + (k - b * 1408) * 16 : WS_END, u32x4{EMPTY, EMPTY, EMPTY, EMPTY});
    }
  }
  MARK(MK + 10);
  lds_bar();
  {
    drain();
    unsigned n = 0;
    if (lane == 0) n = __hip_atomic_fetch_add(misc + 4, 1u, __ATOMIC_RELAXED, __HIP_MEMORY_SCOPE_WORKGROUP);
    if (n == 7) st32<16>(rw, AUX + blockIdx.x * 4, 1u);
  }
  MARK(MK + 11);
  {
    constexpr int NT = FINAL ? 5 : 4;
    f32x4 acc[2][5]; float rs[2];
    unsigned mo[2], a0[2], a1[2], a2[2], a_r[2];
#pragma unroll
    for (int m = 0; m < 2; m++) {
      const int q = r16 >> 2, pr = 16 * (m ^ wn) + r16;
      mo[m] = lds_addr(lds) + (8 * g + q) * 256 + (((2 * wm + (m ^ wn)) ^ (q | ((g & 1) << 2))) * 32) + (lane & 3) * 8;
      const unsigned qb = L_A + (wn * 4 + wm) * 1280, kb = wn * HR + wm * 1280, vr = wn * HR + 5120 + (32 * wm + pr) * 48 + (g == 3 ? 32 : g * 16);
      a0[m] = ((g >> 1) ? kb : qb) + (g & 1) * 512 + pr * 16;
      a1[m] = g == 2 ? qb + 1024 + pr * 8 : vr; a2[m] = g == 2 ? kb + 1024 + pr * 8 : vr + 8;
      a_r[m] = lds_addr(lds) + L_MISC + (32 * wm + pr) * 4;
      asm volatile("" : "+v"(a0[m]), "+v"(a1[m]), "+v"(a2[m]));
    }
    const unsigned a_w = a_r[0] + (g == (r16 >> 2) ? 0 : 512), a_v = lds_addr((lptr)(misc + 2));
#pragma unroll 1
    for (int att = 0;; att++) {
      unsigned am0 = mo[0], am1 = mo[1], as = lds_addr(lds) + 16384 + wn * 10240 + lane * 16, vote; int c_, t_;
      if (FINAL) asm volatile(GEMM_ASM(5) GEMM_OPS); else asm volatile(GEMM_ASM(4) GEMM_OPS);
      if (XNL || att || !__builtin_amdgcn_readfirstlane(vote)) break;
    }
    SB();
    MARK(MK + 0);
#pragma unroll
    for (int m = 0; m < 2; m++) *(LDS_AS u32x4*)(lds + a0[m]) = pk8(acc[m][0] * rs[m], acc[m][1] * rs[m]);
    asm volatile("s_nop 1" : "+v"(acc[0][2]), "+v"(acc[1][2]), "+v"(acc[0][3]), "+v"(acc[1][3]), "+v"(acc[0][NT - 1]), "+v"(acc[1][NT - 1]) :: "memory");
#pragma unroll
    for (int m = 0; m < 2; m++) {
      u32x2 hi = pk4(acc[m][3] * rs[m]);
      if (g == 3) hi = u32x2{0x3F80u, 0u};
      *(LDS_AS u32x2*)(lds + a1[m]) = pk4(acc[m][2] * rs[m]);
      *(LDS_AS u32x2*)(lds + a2[m]) = hi;
    }
    lds_bar();
    if (FINAL) {
      const unsigned x4 = WS_X4 + (((u * 16 + qt) * 4 + wm) * 2 + wn) * 1280;
      const u32x2 p0 = pk4(acc[0][NT - 1]), p1 = pk4(acc[1][NT - 1]), q0 = pk4(acc[0][3]), q1 = pk4(acc[1][3]);
      st128<16>(rw, x4 + lane * 16, clean(u32x4{p0[0], p0[1], p1[0], p1[1]}));
      if (g == 3) st128<16>(rw, x4 + 1024 + r16 * 16, clean(u32x4{q0[0], q0[1], q1[0], q1[1]}));
    }
  }
  MARK(MK + 1);
  const int head = 2 * u + wn, kh = w & 1;
  const lptr ring = lds + wn * HR;
  f32x16 o;
  {
    const lptr qpa = lds + L_A + (wn * 4 + wm) * 1280, qpb = lds + L_A + (wn * 4 + (wm ^ 1)) * 1280;
    const u32x4 qa1 = ld128(qpa + lane * 16), qb1 = ld128(qpb + lane * 16);
    u32x2 qa2 = ld64(qpa + 1024 + (lane & 31) * 8), qb2 = ld64(qpb + 1024 + (lane & 31) * 8);
    if (h) { qa2 = u32x2{0u, 0u}; qb2 = qa2; }
    const unsigned ka = lds_addr(ring) + kh * 2560 + lane * 16, kb = lds_addr(ring) + kh * 2560 + 1024 + (lane & 31) * 8;
    const unsigned va = lds_addr(ring) + kh * 3072 + (4 * h + (r16 >> 2)) * 48 + ((g & 1) ? ((lane & 3) ? 40 : 32) : (lane & 3) * 8);
    float ma, mb;
    {
      const u32x4 k1 = ld128(ring + lane * 16); const u32x2 k2 = ld64(ring + 1024 + (lane & 31) * 8);
      ma = max16(scores(k1, k2, qa1, qa2)); mb = max16(scores(k1, k2, qb1, qb2));
    }
    for (int attempt = 0;; attempt++) {
      ma = fmaxf(ma, __shfl_xor(ma, 32)); mb = fmaxf(mb, __shfl_xor(mb, 32));
      f32x16 cia, cib, oa, ob;
#pragma unroll
      for (int i = 0; i < 16; i++) { cia[i] = -ma; cib[i] = -mb; }
      int cnt = 4;
      MARK(MK + 2);
      asm volatile(
          "ds_read_b128 v[80:83], v100\n\t"
          "ds_read_b64 v[84:85], v101\n\t"
          "ds_read_b64_tr_b16 v[86:87], v102 offset:5120\n\t"
          "ds_read_b64_tr_b16 v[88:89], v102 offset:5504\n\t"
          Z4(0) Z4(68) Z4(76) Z4(90)
          "s_waitcnt lgkmcnt(0)\n\t"
          "v_mfma_f32_32x32x16_bf16 v[32:47], v[80:83], v[94:97], v[16:31]\n\t"
          Z4(4) Z4(8) Z4(12) Z4(104) Z4(108) Z4(112) Z4(116)
          "v_mfma_f32_32x32x8_bf16 v[32:47], v[84:85], v[98:99], v[32:47]\n\ts_nop 9\n\t"
          "L_attn_%=:\n\t"
          CHUNK(0) CHUNK(1) CHUNK(2) CHUNK(3)
          "s_sub_u32 %[cnt], %[cnt], 1\n\ts_cmp_lg_u32 %[cnt], 0\n\ts_cbranch_scc1 L_attn_%=\n\t"
          "s_setprio 0\n\t" CVT(78,68) CVT(79,70) "s_waitcnt lgkmcnt(0)\n\ts_nop 1\n\t"
          "v_mfma_f32_32x32x16_bf16 v[104:119], v[90:93], v[76:79], v[104:119]\n\ts_nop 15\n\t"
          : "={v[0:15]}"(oa), "={v[104:119]}"(ob), [cnt] "+s"(cnt)
          : "{v[16:31]}"(cia), "{v[120:135]}"(cib), "{v[94:97]}"(qa1), "{v[98:99]}"(qa2), "{v[136:139]}"(qb1), "{v[140:141]}"(qb2),
            "{v100}"(ka), "{v101}"(kb), "{v102}"(va)
          : "memory", "scc", "v32", "v33", "v34", "v35", "v36", "v37", "v38", "v39", "v40", "v41", "v42", "v43", "v44", "v45", "v46", "v47",
            "v48", "v49", "v50", "v51", "v52", "v53", "v54", "v55", "v56", "v57", "v58", "v59", "v60", "v61", "v62", "v63", "v64", "v65",
            "v66", "v67", "v68", "v69", "v70", "v71", "v72", "v73", "v74", "v75", "v76", "v77", "v78", "v79", "v80", "v81", "v82", "v83",
            "v84", "v85", "v86", "v87", "v88", "v89", "v90", "v91", "v92", "v93");
      MARK(MK + 3);
      LDS_AS f32x4* ex = (LDS_AS f32x4*)(ring + L_EX + wm * 3072) + lane;
#pragma unroll
      for (int i = 0; i < 3; i++) ex[i * 64] = f32x4{ob[4 * i], ob[4 * i + 1], ob[4 * i + 2], ob[4 * i + 3]};
      o = oa;
      f32x4 cs = {0.f, 0.f, 0.f, 0.f};
#pragma unroll
      for (int i = 0; i < 3; i++) cs += f32x4{oa[4 * i], oa[4 * i + 1], oa[4 * i + 2], oa[4 * i + 3]} + f32x4{ob[4 * i], ob[4 * i + 1], ob[4 * i + 2], ob[4 * i + 3]};
      if (!(fabsf(cs[0] + cs[1] + cs[2] + cs[3]) < INFINITY) || (h && !(oa[8] + ob[8] < 0x1p60f))) misc[1] = 1u;
      lds_bar();
      if (attempt || !*(volatile LDS_AS unsigned*)(misc + 1)) break;
      raw_bar();
      ma = -INFINITY; mb = -INFINITY;
      for (int t = 0; t < 64; t++) {
        const unsigned kb_ = KV + (head * 16 + (t >> 2)) * KVC + (t & 3) * 1280;
        const u32x4 k1 = gl128<16>(rw, kb_ + lane * 16); const u32x2 k2 = gl64<16>(rw, kb_ + 1024 + (lane & 31) * 8);
        ma = fmaxf(ma, max16(scores(k1, k2, qa1, qa2))); mb = fmaxf(mb, max16(scores(k1, k2, qb1, qb2)));
      }
      raw_bar();
    }
  }
  {
    LDS_AS f32x4* ey = (LDS_AS f32x4*)(ring + L_EX + (wm ^ 1) * 3072) + lane;
    f32x4 r[3];
#pragma unroll
    for (int i = 0; i < 3; i++) r[i] = ey[i * 64] + f32x4{o[4 * i], o[4 * i + 1], o[4 * i + 2], o[4 * i + 3]};
    const float il = __builtin_amdgcn_rcpf(__shfl(r[2][0], (lane & 31) + 32));
    const unsigned ob = OB + ((4 * qt + wm) * 32 + head) * 1280;
    const int sl_ = ((lane << 2) & 16) + ((lane >> 1) & 12) + (lane & 3);
    st128<16>(rw, ob + h * 512 + sl_ * 16, clean(pk8(r[0] * il, r[1] * il)));
    const u32x4 t = clean(pk8(r[2] * il, r[2] * il));
    if (!h) st64<16>(rw, ob + 1024 + sl_ * 8, u32x2{t[0], t[1]});
  }
  const int prow = 128 * qt + 64 * hf + 16 * wm, pblk = prow & ~31;
  const int rp0 = 1024 * (wm & 1) + (pblk >> 1);
  MARK(MK + 4);
  u32x2 x4v[3]; float dc = 0.f;
  const lptr wa = lds + L_A + wn * 35 * 1024 + lane * 16, wb = ring + lane * 16;
  const unsigned obase = OB + ((prow >> 5) * 32 + 16 * wn) * 1280;
  unsigned o1 = obase + (g >> 1) * 1280 + (g & 1) * 512 + (wm & 1) * 256 + r16 * 16, o2 = obase + 2 * g * 1280 + 1024 + (wm & 1) * 128 + r16 * 8;
  constexpr int NPRE = 15;
  u32x4 bf[50];
  f32x4 acc[5] = {};
  if (!FINAL) {
#pragma unroll
    for (int i = 0; i < 3; i++) {
      const int j = 80 * ct + 48 * wn + 16 * (wn ? (i & 1) : i) + r16;
      x4v[i] = gl64<16>(rw, (j == 1 ? WS_RB0 + ((pblk >> 5) * 641 + ((pblk >> 5) & 1) + 1) * 64 : WS_RB1 + (j * RBP + pblk) * 2) + 16 * g + 8 * (wm & 1));
    }
  } else {
    dc = __uint_as_float(gl32<16>(rw, WS_R3 + (RP + rp0 + r16) * 4));
    unsigned o3[3];
#pragma unroll
    for (int i = 0; i < 3; i++) {
      const int cg = 12 * wn + 4 * ((i == 2 && wn) ? 0 : i) + g, pw = cg / 5, gq = cg - 5 * pw;
      o3[i] = WS_X4 + ((((2 * ct + (pw >> 1)) * 16 + qt) * 4 + 2 * hf + (wm >> 1)) * 2 + (pw & 1)) * 1280 + (gq * 16 + (trow(wm & 1, r16) & 15)) * 16 + ((((r16 >> 3) ^ pw) & 1) * 8);
    }
#pragma unroll 1
    for (int spins = 0; spins < (1 << 18); spins++) {
#pragma unroll
      for (int i = 0; i < 3; i++) x4v[i] = gl64<16>(rw, o3[i]);
      if (!__builtin_amdgcn_ballot_w64(umax4(max(x4v[0][0], x4v[0][1]), u32x4{x4v[1][0], x4v[1][1], x4v[2][0], x4v[2][1]}) == EMPTY)) break;
      nap<8>();
    }
  }
#pragma unroll
  for (int j = 0; j < NPRE; j++) bf[j] = ld128(wa + j * 1024);
#pragma unroll
  for (int j = 0; j < 5; j++) asm volatile("" : "+v"(acc[j]));
  asm volatile("s_waitcnt vmcnt(0)" : "+v"(o1), "+v"(o2) :: "memory");
  MARK(MK + 5);
  {
    u32x4 af[10];
    auto ldf = [&](int i) {
      if (i < 8) af[i] = __builtin_amdgcn_raw_buffer_load_b128(rw, o1, i * 2 * 1280, 16);
      else { const u32x2 a = __builtin_amdgcn_raw_buffer_load_b64(rw, o2, (i - 8) * 8 * 1280, 16), b = __builtin_amdgcn_raw_buffer_load_b64(rw, o2, ((i - 8) * 8 + 1) * 1280, 16);
             af[i] = u32x4{a[0], a[1], b[0], b[1]}; }
    };
#pragma unroll
    for (int i = 0; i < 10; i++) ldf(i);
#pragma unroll 1
    for (int spins = 0; spins < (1 << 18); spins++) {
      unsigned m = 0;
#pragma unroll
      for (int i = 0; i < 10; i++) m = umax4(m, af[i]);
      if (!__builtin_amdgcn_ballot_w64(m == EMPTY)) break;
      nap<RSLEEP>();
#pragma unroll
      for (int i = 0; i < 10; i++) if (__builtin_amdgcn_ballot_w64(umax4(0, af[i]) == EMPTY)) ldf(i);
    }
    MARK(MK + 8);
#pragma unroll
    for (int i = 0; i < 10; i++) asm volatile("" : "+v"(af[i]));
    asm volatile("s_nop 1" : "+v"(acc[0]), "+v"(acc[1]), "+v"(acc[2]), "+v"(acc[3]), "+v"(acc[4]));
    SB();
#pragma unroll
    for (int i = 0; i < 10; i++) {
#pragma unroll
      for (int a = 0; a < 5; a++) {
        if (FINAL) mfma16v(acc[a], bf[5 * i + a], af[i]); else mfma16v(acc[a], af[i], bf[5 * i + a]);
        const int j = 5 * i + a + NPRE; if (j < 50) bf[j] = ld128(j < 35 ? wa + j * 1024 : wb + (j - 35) * 1024);
        SB();
      }
    }
  }
  asm volatile("s_nop 7\n\ts_nop 1" : "+v"(acc[0]), "+v"(acc[1]), "+v"(acc[2]), "+v"(acc[3]), "+v"(acc[4]) :: "memory");
  MARK(MK + 6);
  {
    LDS_AS f32x4* ex = (LDS_AS f32x4*)(ring + L_EX + 12288 + wm * 3072) + lane;
    LDS_AS f32x4* ey = (LDS_AS f32x4*)(lds + (wn ^ 1) * HR + L_EX + 12288 + wm * 3072) + lane + 64 * wn;
    ex[0] = acc[2]; ex[64] = acc[3]; ex[128] = acc[4];
    dc *= 0.039528470752104741f;
#pragma unroll
    for (int i = 0; i < 3; i++) { acc[i] += f32x4{bflo(x4v[i][0]), bfhi(x4v[i][0]), bflo(x4v[i][1]), bfhi(x4v[i][1])}; if (FINAL) acc[i] += dc; }
    lds_bar();
#pragma unroll
    for (int i = 0; i < 3; i++) acc[i] += ey[64 * i];
  }
  if (FINAL) {
    const rsrc_t ro = mk_rsrc(out, 2048 * 640 * 2);
    const unsigned op = (rowof(pblk + trow(wm & 1, r16)) * 640 + 80 * ct + 48 * wn + 4 * g) * 2;
#pragma unroll
    for (int i = 0; i < 3; i++)
      if (i < 2 || wn == 0) st64<OUTAUX>(ro, op + 32 * i, pk4(acc[i]));
  } else {
    f32x4 sq = {0.f, 0.f, 0.f, 0.f};
#pragma unroll
    for (int i = 0; i < 3; i++)
      if (i < 2 || wn == 0) {
        const int j = 80 * ct + 48 * wn + 16 * i + r16;
        st128<16>(rw, WS_R3 + (j * RP + rp0 + 4 * g) * 4, clean(asu(acc[i])));
        if (j != 1) sq += acc[i] * acc[i];
      }
#pragma unroll
    for (int e = 0; e < 4; e++) sq[e] = row16_sum(sq[e]);
    LDS_AS float* sl = (LDS_AS float*)(lds + L_MISC);
    if (r16 == 0) *(LDS_AS f32x4*)(sl + wn * 64 + 32 * (wm & 1) + 16 * (wm >> 1) + 4 * g) = sq;
    lds_bar();
    if (w == 0 && lane < 8) {
      LDS_AS f32x4* s4 = (LDS_AS f32x4*)sl + 2 * lane;
      st128<16>(rw, WS_SSQ + (ct * 2048 + 1024 * (lane >> 2) + 64 * qt + 32 * hf + 8 * (lane & 3)) * 2, clean(pk8(s4[0] + s4[16], s4[1] + s4[17])));
    }
  }
  MARK(MK + 7);
}

__global__ void __launch_bounds__(768) k_main(char* ws, u16* out) {
  STAMP(1);
  if (__builtin_expect(wave_id() >= 8, 1)) {
    extern __shared__ __attribute__((aligned(16))) char smem[];
    __builtin_amdgcn_s_setprio(3);
    if (threadIdx.x < 528) ((LDS_AS unsigned*)((lptr)smem + L_MISC + 1024))[threadIdx.x - 512] = 0u;
    asm volatile("s_waitcnt lgkmcnt(0)" ::: "memory");
    loader<false>(ws SPASS);
    loader<true>(ws SPASS);
  } else {
    compute<false>(ws, out SPASS);
    compute<true>(ws, out SPASS);
  }
}

#define STOPAT(n)
extern "C" void kernel_launch(void* const* d_in, const int* in_sizes, int n_in, void* d_out, int out_size, void* d_ws, size_t ws_size,
                              hipStream_t stream) {
  const u16* x = (const u16*)d_in[0];
  const u16* wr = (const u16*)d_in[1];
  const u16* wi = (const u16*)d_in[2];
  const u16* wqkv = (const u16*)d_in[4];
  const u16* wo = (const u16*)d_in[5];
  char* ws = (char*)d_ws;
  (void)hipFuncSetAttribute((const void*)k_prep, hipFuncAttributeMaxDynamicSharedMemorySize, LDS_PREP);
  (void)hipFuncSetAttribute((const void*)k_main, hipFuncAttributeMaxDynamicSharedMemorySize, LDS_FUSED);
  k_prep<<<256, 512, LDS_PREP, stream>>>(x, wr, wi, wqkv, wo, ws);
  STOPAT(1)
  k_main<<<256, 768, LDS_FUSED, stream>>>(ws, (u16*)d_out);
}
```

```cpp
#include <hip/hip_runtime.h>
#include <hip/hip_bf16.h>
#include <utility>
#include <type_traits>

typedef unsigned short u16;
typedef __attribute__((ext_vector_type(16))) float f32x16;
typedef __attribute__((ext_vector_type(4))) float f32x4;
typedef __attribute__((ext_vector_type(2))) float f32x2;
typedef __attribute__((ext_vector_type(8))) __bf16 bf16x8;
typedef __attribute__((ext_vector_type(2))) __bf16 bf16x2;
typedef __attribute__((ext_vector_type(4))) short s16x4;
typedef __attribute__((ext_vector_type(4))) unsigned u32x4;
typedef __attribute__((ext_vector_type(2))) unsigned u32x2;
#define DI __device__ __forceinline__
#define LDS_AS __attribute__((address_space(3)))
typedef LDS_AS char* lptr;

constexpr double cx_cos_small(double x) { double x2 = x * x, t = 1, s = 1; for (int i = 1; i < 12; i++) { t *= -x2 / ((2 * i - 1) * (2 * i)); s += t; } return s; }
constexpr double cx_sin_small(double x) { double x2 = x * x, t = x, s = x; for (int i = 1; i < 12; i++) { t *= -x2 / ((2 * i) * (2 * i + 1)); s += t; } return s; }
constexpr double cx_cos640(int m) {
  m %= 640; if (m > 320) m = 640 - m; double sg = 1; if (m > 160) { m = 320 - m; sg = -1; }
  const double w = 6.283185307179586476925 / 640; return sg * (m <= 80 ? cx_cos_small(w * m) : cx_sin_small(w * (160 - m)));
}
constexpr u16 cx_bf16(double d) { float f = (float)d; unsigned u = __builtin_bit_cast(unsigned, f); u += 0x7FFFu + ((u >> 16) & 1u); return (u16)(u >> 16); }
struct Cos640 { double v[640]; };
constexpr Cos640 mk_cos() { Cos640 c{}; for (int m = 0; m < 640; m++) c.v[m] = cx_cos640(m); return c; }
constexpr Cos640 COS = mk_cos();
__host__ __device__ constexpr int kf_of(int G, int i) { return G ? G + 40 * i : (i == 0 ? 320 : i < 8 ? 40 * i : 40 * i - 300); }
__host__ __device__ constexpr int freq_of(int G, int i) { return kf_of(G, i) > 320 ? 640 - kf_of(G, i) : kf_of(G, i); }
constexpr double a1val(int out, int a) {
  if (out >= 40 || a >= 40) return 0;
  const int G = out >> 1, part = out & 1;
  if (G == 0) return (part && (a & 1)) ? -1 : 1;
  return COS.v[(16 * a * G + 160 * part) % 640];
}
constexpr double m2val(int G, int o, int k) {
  const int i = o >> 1, po = o & 1, part = k >> 4, b = k & 15, kf = kf_of(G, i);
  const double c = COS.v[(b * kf) % 640], sn = COS.v[(b * kf + 480) % 640];
  if (G == 0) {
    if (part != (i >= 8)) return 0;
    if (i == 0) return 0.039528470752104741 * (po ? 1.0 : c);
    return 0.055901699437494742 * (po ? -sn : c);
  }
  const double v = po ? (part ? c : -sn) : (part ? sn : c);
  return 0.055901699437494742 * ((po && kf > 320) ? -v : v);
}
struct DTab { u16 a1[3][64][8]; u16 a1b[3][64][4]; u16 pad[256]; u16 m2[20][2][64][8]; };
constexpr DTab mk_dtab() {
  DTab t{};
  for (int l = 0; l < 64; l++) {
    const int r = l & 15, g = l >> 4;
    for (int T = 0; T < 3; T++) {
      for (int e = 0; e < 8; e++) t.a1[T][l][e] = cx_bf16(a1val(16 * T + r, 16 * (e >> 2) + 4 * g + (e & 3)));
      for (int e = 0; e < 4; e++) t.a1b[T][l][e] = cx_bf16(a1val(16 * T + r, 32 + 4 * g + e));
    }
    for (int G = 0; G < 20; G++) for (int T = 0; T < 2; T++) for (int e = 0; e < 8; e++)
      t.m2[G][T][l][e] = cx_bf16(m2val(G, 8 * (r >> 2) + 4 * T + (r & 3), 8 * g + e));
  }
  return t;
}
static __device__ const __attribute__((aligned(1024))) DTab g_dt = mk_dtab();

struct CTab { u16 v[10 * 512]; };
constexpr CTab mk_ctab() {
  CTab c{}; u16 cs[64] = {};
  for (int m = 0; m < 64; m++) cs[m] = cx_bf16(cx_cos640(10 * m));
  for (int f = 0; f < 10; f++) for (int l = 0; l < 64; l++) for (int e = 0; e < 8; e++) {
    const int r = l & 15, g = l >> 4;
    const int m = f < 8 ? (8 * g + e) * (16 * (f >> 1) + r) : 2 * r * (16 * (e >> 2) + 4 * g + (e & 3));
    c.v[(f * 64 + l) * 8 + e] = cs[(m + (f & 1) * 48) & 63];
  }
  return c;
}
static __device__ const __attribute__((aligned(1024))) CTab g_ct = mk_ctab();

constexpr int RP = 2048 + 64, RBP = 2048 + 128;
constexpr unsigned RBSZ = 640 * RBP * 2;
constexpr unsigned WS_FL = 0;
constexpr unsigned WS_AUX = WS_FL + 3 * 32768;
constexpr unsigned WS_RS1 = WS_AUX + 2048;
constexpr unsigned WS_RB0 = WS_RS1 + 8192;
constexpr unsigned WS_RB1 = WS_RB0 + RBSZ;
constexpr unsigned WS_WF = WS_RB1 + RBSZ;
constexpr unsigned WS_WQ = WS_WF + 320 * 8192;
constexpr unsigned WS_WOF = WS_WQ + 16 * 10 * 20 * 1024;
constexpr unsigned WS_WOP = WS_WOF + 40 * 20 * 1024;
constexpr int KVC = 11264;
constexpr unsigned WS_KV = WS_WOP + 40 * 20 * 1024;
constexpr unsigned WS_O = WS_KV + 32 * 16 * KVC;
constexpr unsigned LAYER2 = 32 * 16 * KVC + 32 * 64 * 1280;
constexpr unsigned WS_X4 = WS_KV + 2 * LAYER2;
constexpr unsigned WS_R3 = WS_X4 + 2048 * 640 * 2;
constexpr unsigned WS_RB2 = WS_R3 + 640 * RP * 4;
constexpr unsigned WS_SSQ = WS_RB2 + RBSZ;
constexpr unsigned EMPTY = 0xFFFFFFFFu;
constexpr unsigned WS_END = WS_SSQ + 8 * 2048 * 16;
#define FILLDELAY 10
#define OUTAUX 0
#define XDELAY 14
#define SW1 50
#define SW2 46
#define XNL 0
#define K1DELAY 40
#define YDELAY 0
#define ODELAY 0
#define PFDELAY 40
#define YSLEEP 4
#define XSLEEP 1
#define RSLEEP 2
#define AUXB 16

#define STW 0
#define STAMP(k)
#define MARK(i)
#define SARG
#define SPASS
#define SB() __builtin_amdgcn_sched_barrier(0)
typedef __amdgpu_buffer_rsrc_t rsrc_t;
DI rsrc_t mk_rsrc(const void* p, unsigned bytes) { return __builtin_amdgcn_make_buffer_rsrc((void*)p, 0, bytes, 0x00020000); }
template <int AUX> DI void st128(rsrc_t r, unsigned off, u32x4 v) { __builtin_amdgcn_raw_buffer_store_b128(v, r, off, 0, AUX); }
template <int AUX> DI void st64(rsrc_t r, unsigned off, u32x2 v) { __builtin_amdgcn_raw_buffer_store_b64(v, r, off, 0, AUX); }
template <int AUX> DI void st32(rsrc_t r, unsigned off, unsigned v) { __builtin_amdgcn_raw_buffer_store_b32(v, r, off, 0, AUX); }
template <int AUX> DI void st16(rsrc_t r, unsigned off, u16 v) { __builtin_amdgcn_raw_buffer_store_b16(v, r, off, 0, AUX); }
template <int AUX> DI u32x4 gl128(rsrc_t r, unsigned off) { return __builtin_amdgcn_raw_buffer_load_b128(r, off, 0, AUX); }
template <int AUX> DI u32x2 gl64(rsrc_t r, unsigned off) { return __builtin_amdgcn_raw_buffer_load_b64(r, off, 0, AUX); }
template <int AUX> DI unsigned gl32(rsrc_t r, unsigned off) { return __builtin_amdgcn_raw_buffer_load_b32(r, off, 0, AUX); }
DI u32x4 asu(f32x4 v) { return __builtin_bit_cast(u32x4, v); }
DI f32x4 asf(u32x4 v) { return __builtin_bit_cast(f32x4, v); }
DI unsigned pk2(float a, float b) { f32x2 v = {a, b}; return __builtin_bit_cast(unsigned, __builtin_convertvector(v, bf16x2)); }
DI u32x2 pk4(f32x4 a) { return u32x2{pk2(a[0], a[1]), pk2(a[2], a[3])}; }
DI u32x4 pk8(f32x4 a, f32x4 b) { return u32x4{pk2(a[0], a[1]), pk2(a[2], a[3]), pk2(b[0], b[1]), pk2(b[2], b[3])}; }
DI u32x4 clean(u32x4 v) { return __builtin_elementwise_min(v, u32x4{EMPTY - 1, EMPTY - 1, EMPTY - 1, EMPTY - 1}); }
DI unsigned umax4(unsigned m, u32x4 v) { return max(max(max(m, v[0]), v[1]), max(v[2], v[3])); }
DI float bflo(unsigned u) { return __uint_as_float(u << 16); }
DI float bfhi(unsigned u) { return __uint_as_float(u & 0xFFFF0000u); }
DI float dot2(unsigned a, unsigned b, float c) { return __builtin_amdgcn_fdot2_f32_bf16(__builtin_bit_cast(bf16x2, a), __builtin_bit_cast(bf16x2, b), c, false); }
DI int rowof(int p) { return 32 * (((p >> 5) & 0x3C) + (p & 3)) + ((p << 2) & 16) + ((p >> 3) & 15); }
DI int rpos(int p) { return ((p << 8) & 1024) + ((p >> 3) << 2) + (p & 3); }
DI int trow(int T, int r) { return 8 * (r >> 2) + 4 * T + (r & 3); }
template <int CTRL> DI float dpp_add(float v) { return v + __int_as_float(__builtin_amdgcn_update_dpp(0, __float_as_int(v), CTRL, 0xF, 0xF, true)); }
DI float row16_sum(float v) { v = dpp_add<0x128>(v); v = dpp_add<0x124>(v); v = dpp_add<0x122>(v); return dpp_add<0x121>(v); }
DI f32x4 mfma16(u32x4 a, u32x4 b, f32x4 c) {
  return __builtin_amdgcn_mfma_f32_16x16x32_bf16(__builtin_bit_cast(bf16x8, a), __builtin_bit_cast(bf16x8, b), c, 0, 0, 0);
}
DI void mfma16v(f32x4& c, u32x4 a, u32x4 b) { asm volatile("v_mfma_f32_16x16x32_bf16 %0, %1, %2, %0" : "+v"(c) : "v"(a), "v"(b)); }
DI u32x2 ldtr(lptr p) { return __builtin_bit_cast(u32x2, __builtin_amdgcn_ds_read_tr16_b64_v4i16((LDS_AS s16x4*)p)); }
DI u32x4 ld128(lptr p) { return *(LDS_AS u32x4*)p; }
DI u32x2 ld64(lptr p) { return *(LDS_AS u32x2*)p; }
#define GL(v, s, o, x) "global_load_lds_dwordx4 " v ", " s " offset:" #o x "\n\t"
#define M0SET "s_mov_b32 %[sv], m0\n\ts_mov_b32 m0, %[l]\n\ts_nop 2\n\t"
#define M0ADD(x) "s_add_u32 m0, m0, " x "\n\ts_nop 0\n\t"
#define M0RST "s_mov_b32 m0, %[sv]"
DI void dma2(unsigned v, const char* sb, unsigned l) { unsigned sv;
  asm volatile(M0SET GL("%[v]", "%[s]", 0, "") GL("%[v]", "%[s]", 1024, "") M0RST : [sv] "=&s"(sv) : [v] "v"(v), [s] "s"(sb), [l] "s"(l) : "memory"); }
DI void dma4(unsigned v, const char* sb, unsigned l) { unsigned sv;
  asm volatile(M0SET GL("%[v]", "%[s]", 0, "") GL("%[v]", "%[s]", 1024, "") GL("%[v]", "%[s]", 2048, "") GL("%[v]", "%[s]", 3072, "") M0RST
               : [sv] "=&s"(sv) : [v] "v"(v), [s] "s"(sb), [l] "s"(l) : "memory"); }
DI void dma6_sc1(unsigned v, const char* sb, unsigned l) { unsigned sv;
  asm volatile(M0SET GL("%[v]", "%[s]", 0, " sc1") GL("%[v]", "%[s]", 1024, " sc1") GL("%[v]", "%[s]", 2048, " sc1") M0ADD("3072")
               GL("%[w]", "%[s]", 0, " sc1") GL("%[w]", "%[s]", 1024, " sc1") GL("%[w]", "%[s]", 2048, " sc1") M0RST
               : [sv] "=&s"(sv) : [v] "v"(v), [w] "v"(v + 3072), [s] "s"(sb), [l] "s"(l) : "memory", "scc"); }
template <int N> DI void vm_bar() { asm volatile("s_waitcnt vmcnt(%0)\n\ts_barrier" ::"i"(N) : "memory"); }
DI void lds_bar() { asm volatile("s_waitcnt lgkmcnt(0)\n\ts_barrier" ::: "memory"); }
DI void raw_bar() { asm volatile("s_barrier" ::: "memory"); }
template <int N> DI void nap() { __builtin_amdgcn_s_sleep(N); asm volatile("" ::: "memory"); }
DI void drain() { asm volatile("s_waitcnt vmcnt(0)" ::: "memory"); }
#define CODE_ADDR(sym) ({ unsigned lo_, hi_; \
    asm volatile("s_getpc_b64 vcc\n\ts_add_u32 %0, vcc_lo, " sym "@rel32@lo+4\n\ts_addc_u32 %1, vcc_hi, " sym "@rel32@hi+12" : "=s"(lo_), "=s"(hi_) :: "vcc", "scc"); \
    (const unsigned*)(((unsigned long long)hi_ << 32) | lo_); })
#define SYM_PREP "_Z6k_prepPKtS0_S0_S0_S0_Pc"
#define SYM_MAIN "_Z6k_mainPcPt"
DI int wave_id() { return __builtin_amdgcn_readfirstlane(threadIdx.x >> 6); }
DI unsigned lds_addr(lptr p) { return (unsigned)(size_t)p; }

DI int kappa_to_i(int kap) {
  int ks = kap >> 5, g = (kap >> 3) & 3, e = kap & 7;
  if (ks < 16) return (2 * ks + (g >> 1)) * 20 + 8 * (e >> 2) + 4 * (g & 1) + (e & 3);
  return (8 * (ks - 16) + 2 * g + (e >> 2)) * 20 + 16 + (e & 3);
}

DI unsigned wo_frag(int nt, int ks) {
  const int ct = nt / 5, c5 = nt - 5 * ct, kh = ks < 16 ? ks >> 3 : (ks - 16) >> 1, i = ks < 16 ? ks & 7 : 8 + (ks & 1);
  const int a = kh ? (c5 >= 3 ? c5 - 3 : c5 + 2) : c5;
  return ((ct * 2 + kh) * 50 + 5 * i + a) * 1024;
}

constexpr int SLOT32 = 36864;
DI void dma_a(const unsigned* vm, const char* mb, unsigned l) { unsigned sv;
  asm volatile(M0SET GL("%[a0]", "%[m]", 0, " sc1") GL("%[a1]", "%[m]", 1024, " sc1") GL("%[a2]", "%[m]", 2048, " sc1") GL("%[a3]", "%[m]", 3072, " sc1") M0RST
               : [sv] "=&s"(sv) : [a0] "v"(vm[0]), [a1] "v"(vm[1]), [a2] "v"(vm[2]), [a3] "v"(vm[3]), [m] "s"(mb), [l] "s"(l) : "memory"); }
DI void dma_s(unsigned v0, unsigned v1, const char* sb, unsigned l) { unsigned sv;
  asm volatile(M0SET GL("%[v0]", "%[s]", 0, " sc1") GL("%[v0]", "%[s]", 1024, " sc1") GL("%[v1]", "%[s]", 2048, " sc1") GL("%[v1]", "%[s]", 3072, " sc1") M0RST
               : [sv] "=&s"(sv) : [v0] "v"(v0), [v1] "v"(v1), [s] "s"(sb), [l] "s"(l) : "memory"); }
DI void dma1_sc1(unsigned v, const char* sb, unsigned l) { unsigned sv;
  asm volatile(M0SET GL("%[v]", "%[s]", 0, " sc1") M0RST : [sv] "=&s"(sv) : [v] "v"(v), [s] "s"(sb), [l] "s"(l) : "memory"); }
DI void dma1(unsigned v, const char* sb, unsigned l) { unsigned sv;
  asm volatile(M0SET GL("%[v]", "%[s]", 0, "") M0RST : [sv] "=&s"(sv) : [v] "v"(v), [s] "s"(sb), [l] "s"(l) : "memory"); }

constexpr int L_I2 = 40 * 1056, LDS_PREP = L_I2 + 20 * 2064 + 1024;
__global__ void __launch_bounds__(512, 2) k_prep(const u16* __restrict__ x, const u16* __restrict__ wr, const u16* __restrict__ wi,
                                                 const u16* __restrict__ wqkv, const u16* __restrict__ wo, char* __restrict__ ws) {
  STAMP(0);
  extern __shared__ __attribute__((aligned(16))) char smem[];
  const lptr lds = (lptr)smem;
  const int tid = threadIdx.x, lane = tid & 63, w = wave_id(), g = lane >> 4, r16 = lane & 15;
  const int job = blockIdx.x;
  if (__builtin_expect(job < 144, 1)) {
    const bool cols = job >= 64 && job < 124;
    const int cj = job - 64, cb = cj < 48 ? (cj & 48) + 2 * (cj & 7) + ((cj >> 3) & 1) : cj;
    u32x4 a1[3], m2[3][2]; u32x2 a1b[3];
#pragma unroll
    for (int T = 0; T < 3; T++) { a1[T] = *(const u32x4*)g_dt.a1[T][lane]; a1b[T] = *(const u32x2*)g_dt.a1b[T][lane]; }
#pragma unroll
    for (int i = 0; i < 3; i++)
#pragma unroll
      for (int T = 0; T < 2; T++) m2[i][T] = *(const u32x4*)g_dt.m2[(i < 2 || w < 4) ? w + 8 * i : 0][T][lane];
    SB();
    if (cols) {
#pragma unroll
      for (int i = 0; i < 5; i++) dma1((16 * (5 * w + i) + (lane >> 2)) * 3840 + cb * 64 + (lane & 3) * 16, (const char*)wqkv, lds_addr(lds) + (5 * w + i) * 1056);
    } else {
      const char* src = (const char*)(job < 64 ? x : wo);
      const int sl = 8 * (w & 3) + 4 * (w >> 2);
      src += (job < 64 ? rowof(32 * job + sl) : kappa_to_i(32 * (job - 124) + sl)) * 1280;
      const int st = (job < 64 ? 31 : 0) * 1280;
#pragma unroll
      for (int i = 0; i < 5; i++) dma1(1024 * i + lane * 16 + ((i == 4 || lane < 16 * i) ? i - 1 : i) * st, src, lds_addr(lds) + (5 * w + i) * 1024);
    }
    unsigned so[3][2]; float sc[2] = {1.f, 1.f};
    if (cols) {
#pragma unroll
      for (int m = 0; m < 2; m++) {
        const int c = 32 * cb + 16 * m + r16;
        const int which = c / 640, rem = c - which * 640, head = rem / 20, d = rem - head * 20, grp = d >> 2;
        const int T = grp < 4 ? (which == 2 ? 2 : 0) + (grp & 1) : (which == 1 ? 3 : 2), gs = grp < 4 ? (which == 1 ? 2 : 0) + (grp >> 1) : (which == 2 ? 3 : 2);
        if (which == 0) sc[m] = 0.22360679774997896f * 1.4426950408889634f;
#pragma unroll
        for (int i = 0; i < 3; i++) so[i][m] = WS_WQ + (((head >> 1) * 10 + 4 * (head & 1) + T) * 20 + w + 8 * i) * 1024 + (g * 16 + 4 * gs + (d & 3)) * 16;
      }
    } else {
#pragma unroll
      for (int i = 0; i < 3; i++) {
        const int G = w + 8 * i;
        const unsigned f0 = wo_frag(2 * G, job - 124), f1 = wo_frag(2 * G + 1, job - 124);
#pragma unroll
        for (int T = 0; T < 2; T++)
          so[i][T] = job < 64 ? WS_RB0 + (job * 641 + 32 * G + trow(T, r16)) * 64 + (job & 1) * 64 + g * 16 : WS_WOF + (r16 < 8 ? f0 : f1) + (g * 16 + (trow(T, r16) & 15)) * 16;
      }
    }
    if (w >= 4) { so[2][0] = WS_END; so[2][1] = WS_END; }
    asm volatile("" : "+v"(so[0][0]), "+v"(so[0][1]), "+v"(so[1][0]), "+v"(so[1][1]), "+v"(so[2][0]), "+v"(so[2][1]), "+v"(sc[0]), "+v"(sc[1]));
    const f32x4 z4 = {0.f, 0.f, 0.f, 0.f};
    const lptr i2 = lds + L_I2;
    asm volatile("s_waitcnt vmcnt(0)" : "+v"(a1[0]), "+v"(a1[1]), "+v"(a1[2]), "+v"(a1b[0]), "+v"(a1b[1]), "+v"(a1b[2]) :: "memory");
    MARK(0);
    f32x4 t1[4][3];
    if (cols) {
      raw_bar();
      const lptr src = lds + (4 * g + (r16 >> 2)) * 1056 + (w >> 1) * 256 + (w & 1) * 32 + (lane & 3) * 8, srb = src + (32 - 8 * (g >> 1)) * 1056;
#pragma unroll
      for (int i = 0; i < 4; i++) {
        const u32x2 f0 = ldtr(src + i * 64), f1 = ldtr(src + 16 * 1056 + i * 64), f2 = ldtr(srb + i * 64);
#pragma unroll
        for (int T = 0; T < 3; T++) t1[i][T] = mfma16(u32x4{a1b[T][0], a1b[T][1], 0u, 0u}, u32x4{f2[0], f2[1], 0u, 0u}, mfma16(a1[T], u32x4{f0[0], f0[1], f1[0], f1[1]}, z4));
      }
      const int xo = ((w >> 1) * 8) ^ (((r16 >> 1) & 3) * 16);
      const lptr dst = i2 + 2 * g * 2064 + (16 * (w & 1) + r16) * 64;
#pragma unroll
      for (int T = 0; T < 3; T++)
#pragma unroll
        for (int e = 0; e < 4; e++)
          if (T < 2 || g < 2) *(LDS_AS u32x2*)(dst + (8 * T + (e >> 1)) * 2064 + (xo ^ ((e & 1) * 32))) = u32x2{pk2(t1[0][T][e], t1[1][T][e]), pk2(t1[2][T][e], t1[3][T][e])};
    } else {
      const lptr src = lds + w * 5120 + lane * 8, srb = lds + w * 5120 + 1024 + (lane & 31) * 8;
#pragma unroll
      for (int i = 0; i < 4; i++) {
        const u32x2 f0 = ldtr(src + i * 1280), f1 = ldtr(src + i * 1280 + 512), f2 = ldtr(srb + i * 1280);
#pragma unroll
        for (int T = 0; T < 3; T++) t1[i][T] = mfma16(u32x4{f2[0], f2[1], 0u, 0u}, u32x4{a1b[T][0], a1b[T][1], 0u, 0u}, mfma16(u32x4{f0[0], f0[1], f1[0], f1[1]}, a1[T], z4));
      }
      const int xo = ((r16 & 1) * 32 + g * 8) ^ ((w & 1) * 32);
      const lptr dst = i2 + (r16 >> 1) * 2064 + w * 256;
#pragma unroll
      for (int i = 0; i < 4; i++)
#pragma unroll
        for (int T = 0; T < 3; T++)
          if (T < 2 || r16 < 8) *(LDS_AS u32x2*)(dst + T * 8 * 2064 + i * 64 + (xo ^ ((i >> 1) * 16))) = pk4(t1[i][T]);
    }
    lds_bar();
    MARK(1);
    const rsrc_t rw = mk_rsrc(ws, WS_END);
    f32x4 sq[2] = {z4, z4};
    u32x4 f[3][2];
#pragma unroll
    for (int i = 0; i < 3; i++) {
      const lptr fp = i2 + ((i < 2 || w < 4) ? w + 8 * i : 0) * 2064 + r16 * 64 + ((g ^ ((r16 >> 1) & 3)) * 16);
      f[i][0] = ld128(fp); f[i][1] = ld128(fp + 1024);
    }
    if (cols) {
#pragma unroll
      for (int i = 0; i < 3; i++)
#pragma unroll
        for (int m = 0; m < 2; m++) st128<AUXB>(rw, so[i][m], pk8(mfma16(m2[i][0], f[i][m], z4) * sc[m], mfma16(m2[i][1], f[i][m], z4) * sc[m]));
    } else {
#pragma unroll
      for (int i = 0; i < 3; i++)
#pragma unroll
        for (int T = 0; T < 2; T++) {
          const f32x4 d0 = mfma16(f[i][0], m2[i][T], z4), d1 = mfma16(f[i][1], m2[i][T], z4);
          st128<AUXB>(rw, so[i][T], pk8(d0, d1));
          const float k = ((i == 2 && w >= 4) || (i == 0 && T == 0 && tid < 64 && r16 == 1)) ? 0.f : 1.f;
          sq[0] += d0 * d0 * k; sq[1] += d1 * d1 * k;
        }
    }
    LDS_AS float* sl = (LDS_AS float*)(lds + LDS_PREP - 1024);
    if (job < 64) {
#pragma unroll
      for (int m = 0; m < 2; m++) {
#pragma unroll
        for (int e = 0; e < 4; e++) sq[m][e] = row16_sum(sq[m][e]);
        if (r16 == 0) *(LDS_AS f32x4*)(sl + w * 32 + 8 * g + 4 * m) = sq[m];
      }
    }
    MARK(2);
    if (job < 64) {
      lds_bar();
      if (tid < 32) {
        float v = 0.f;
#pragma unroll
        for (int i = 0; i < 8; i++) v += sl[i * 32 + tid];
        st32<AUXB>(rw, WS_RS1 + rpos(32 * job + tid) * 4, __float_as_uint(__builtin_amdgcn_rsqf((v + 640.f * 1e-5f) * (2048.f * 2048.f / 640.f))));
      }
    }
    return;
  }
  const int mi = job - 144;
  const rsrc_t rw = mk_rsrc(ws, WS_END);
  if (mi < 64) {
    const int m = mi >> 3, oc = mi & 7;
    const char* src = (const char*)((w >> 2) ? wi : wr) + (64 * (4 * m + (w & 3)) + 8 * oc) * 642;
    dma4(lane * 16, src, lds_addr(lds) + w * 5152);
    dma1(4096 + lane * 16, src, lds_addr(lds) + w * 5152 + 4096);
    if (lane == 0) dma1(5120, src, lds_addr(lds) + w * 5152 + 5120);
    vm_bar<0>();
    MARK(3);
    LDS_AS u16* L = (LDS_AS u16*)lds + (tid & 7) * 321;
#pragma unroll
    for (int it = 0; it < 5; it++) {
      const int c = (tid >> 3) + 64 * it, col = freq_of(c >> 4, c & 15);
      u32x4 o;
#pragma unroll
      for (int e = 0; e < 4; e++) o[e] = (unsigned)L[e * 2576 + col] | ((unsigned)L[(4 + e) * 2576 + col] << 16);
      st128<AUXB>(rw, WS_WF + c * 8192 + (m * 64 + 8 * oc + (tid & 7)) * 16, o);
    }
    MARK(4);
#pragma unroll
    for (int it = 0; it < 3; it++) st128<AUXB>(rw, WS_RB1 + ((it * 64 + mi) * 512 + tid) * 16, u32x4{EMPTY, EMPTY, EMPTY, EMPTY});
  } else if (mi < 84) {
    const char* src = (const char*)wo + kappa_to_i(32 * (mi - 64) + 8 * (w & 3) + 4 * (w >> 2)) * 1280;
    dma4(lane * 16, src, lds_addr(lds) + w * 5152);
    dma1(4096 + lane * 16, src, lds_addr(lds) + w * 5152 + 4096);
    vm_bar<0>();
#pragma unroll
    for (int i = 0; i < 5; i++) {
      const lptr a = lds + g * 5152 + (r16 >> 2) * 1280 + (5 * w + i) * 32 + (lane & 3) * 8;
      const u32x2 lo = ldtr(a), hi = ldtr(a + 4 * 5152);
      st128<AUXB>(rw, WS_WOP + wo_frag(5 * w + i, mi - 64) + lane * 16, u32x4{lo[0], lo[1], hi[0], hi[1]});
    }
  } else {
    const int i0 = (mi - 84) * 512 + tid;
    unsigned pf0, pf1;
    {
      const unsigned* cp = CODE_ADDR(SYM_PREP); const unsigned* cm = CODE_ADDR(SYM_MAIN);
      const unsigned* p = (const unsigned*)wqkv + min(14336 + i0 - 4096, 19199) * 32;
      if (mi < 92) p = tid < 72 ? cp + tid * 32 : tid < 152 ? (const unsigned*)&g_ct + (tid - 72) * 32 : cm + min(tid - 152, 259) * 32;
      asm volatile("global_load_dword %0, %1, off" : "=v"(pf0) : "v"(p) : "memory");
      asm volatile("global_load_dword %0, %1, off" : "=v"(pf1) : "v"((const unsigned*)wqkv + i0 * 32) : "memory");
    }
    nap<K1DELAY>();
    if (i0 < 6272) st128<AUXB>(rw, WS_FL + i0 * 16, u32x4{0u, 0u, 0u, 0u});
#pragma unroll
    for (int it = 0; it < 6; it++) { const int i = 98304 + i0 + it * 14336; if (i < (int)RBSZ / 16) st128<AUXB>(rw, WS_RB1 + i * 16, u32x4{EMPTY, EMPTY, EMPTY, EMPTY}); }
    asm volatile("s_waitcnt vmcnt(0)" : "+v"(pf0), "+v"(pf1));
  }
}

#define PINF(x) asm volatile("" : "+v"(x))
template <bool FIRST>
DI void col_phase(rsrc_t rw, lptr lds, int c0 SARG) {
  int lane = threadIdx.x & 63; PINF(lane);
  const int wv = wave_id(), w = wv & 3, q = wv >> 2, tid = w * 64 + lane, g = lane >> 4, r16 = lane & 15;
  const int c = c0 + q;
  const lptr img = lds + q * SLOT32;
  f32x4 xr[2], xi[2], rs[2]; u32x4 wf[2], xb[2];
  if (FIRST) {
#pragma unroll
    for (int t = 0; t < 2; t++) { xb[t] = gl128<0>(rw, WS_RB0 + ((tid >> 2) * 641 + ((tid >> 2) & 1) + 2 * c + t) * 64 + (tid & 3) * 16); rs[t] = asf(gl128<0>(rw, WS_RS1 + (1024 * t + 4 * tid) * 4)); }
  }
  u32x4 c64[2], s64[2], n64[2], c32[2], s32[2], n32[2];
  const u32x4* ct = (const u32x4*)&g_ct + lane;
  auto ld_const = [&]() {
  c64[0] = ct[(2 * w) * 64]; s64[0] = ct[(2 * w + 1) * 64]; c32[0] = ct[8 * 64]; s32[0] = ct[9 * 64];
#pragma unroll
  for (int t = 0; t < 2; t++) wf[t] = gl128<0>(rw, WS_WF + c * 8192 + ((4 * t + g) * 64 + 16 * w + r16) * 16);
  };
  if (FIRST) ld_const();
  SB();
  f32x4 t1c[2], t1s[2], t2c[2], t2s[2];
  {
    const float f1 = (float)(16 * w + r16) * (1.f / 2048.f), b1 = (float)(4 * g), f2 = (float)(16 * w + 4 * g) * (1.f / 2048.f), b2 = (float)r16;
#pragma unroll
    for (int t = 0; t < 2; t++) {
#pragma unroll
      for (int e = 0; e < 4; e++) {
        const float a1 = (b1 + (float)(16 * t + e)) * f1, a2 = (b2 + (float)(16 * t)) * (f2 + (float)e * (1.f / 2048.f));
        t1c[t][e] = __builtin_amdgcn_cosf(a1); t1s[t][e] = __builtin_amdgcn_sinf(-a1);
        t2c[t][e] = __builtin_amdgcn_cosf(a2); t2s[t][e] = __builtin_amdgcn_sinf(a2);
      }
      PINF(t1c[t]); PINF(t1s[t]); PINF(t2c[t]); PINF(t2s[t]);
    }
  }
  int wo_ = r16 * 128 + (((2 * w + (g >> 1)) ^ (r16 >> 1)) * 16) + (g & 1) * 8, ro_ = r16 * 128 + ((g ^ (r16 >> 1)) * 16), ro2 = ro_ ^ 64;
  PINF(wo_); PINF(ro_); PINF(ro2);
  SB();
  if (FIRST) {
#pragma unroll
    for (int t = 0; t < 2; t++) {
      xr[t] = f32x4{bflo(xb[0][2 * t]), bfhi(xb[0][2 * t]), bflo(xb[0][2 * t + 1]), bfhi(xb[0][2 * t + 1])};
      xi[t] = f32x4{bflo(xb[1][2 * t]), bfhi(xb[1][2 * t]), bflo(xb[1][2 * t + 1]), bfhi(xb[1][2 * t + 1])};
    }
  } else {
    MARK(23);
    drain();
    MARK(11);
    nap<YDELAY>();
    u32x2 sp[8];
    const unsigned so = WS_SSQ + (blockIdx.x & 7) * 32768 + (1024 * q + 4 * tid) * 2, xo = WS_R3 + ((tid >> 2) * 642 + 2 * c) * 64 + (tid & 3) * 16;
    bool bs = true, bx = true;
#pragma unroll 1
    for (int spins = 0; spins < (1 << 18); spins++) {
      if (bx) {
#pragma unroll
        for (int t = 0; t < 2; t++) { xr[t] = asf(gl128<16>(rw, xo + 64 * 642 * 64 * t)); xi[t] = asf(gl128<16>(rw, xo + 64 * 642 * 64 * t + 64)); }
      }
      if (bs) {
#pragma unroll
        for (int i = 0; i < 8; i++) sp[i] = gl64<16>(rw, so + i * 4096);
      }
      unsigned m = 0;
#pragma unroll
      for (int t = 0; t < 2; t++) m = umax4(umax4(m, asu(xr[t])), asu(xi[t]));
      bx = __builtin_amdgcn_ballot_w64(m == EMPTY) != 0;
      m = 0;
#pragma unroll
      for (int i = 0; i < 8; i++) m = max(max(m, sp[i][0]), sp[i][1]);
      bs = __builtin_amdgcn_ballot_w64(m == EMPTY) != 0;
      if (!bs && !bx) break;
      nap<YSLEEP>();
    }
    ld_const();
    SB();
    f32x4 r = {640.f * 1e-5f, 640.f * 1e-5f, 640.f * 1e-5f, 640.f * 1e-5f};
#pragma unroll
    for (int i = 0; i < 8; i++) r += f32x4{bflo(sp[i][0]), bfhi(sp[i][0]), bflo(sp[i][1]), bfhi(sp[i][1])};
#pragma unroll
    for (int e = 0; e < 4; e++) r[e] = __builtin_amdgcn_rsqf(r[e] * (2048.f * 2048.f / 640.f));
    LDS_AS f32x4* ex = (LDS_AS f32x4*)(lds + 2 * SLOT32) + tid;
    ex[q * 256] = r;
    lds_bar();
    rs[0] = ex[0]; rs[1] = ex[256];
  }
  MARK(FIRST ? 9 : 21);
#pragma unroll
  for (int t = 0; t < 2; t++) {
    u32x2 pb = pk4(xi[t] * rs[t]);
    if (c == 0) pb = u32x2{0u, 0u};
    *(LDS_AS u32x2*)(img + wo_ + t * 2048) = pk4(xr[t] * rs[t]);
    *(LDS_AS u32x2*)(img + 4096 + wo_ + t * 2048) = pb;
  }
  lds_bar();
  u32x4 ur[2][2], ui[2][2], br, bi;
  f32x4 ar[2], ai[2];
  const f32x4 z4 = {0.f, 0.f, 0.f, 0.f};
  auto rd_in = [&](int off) {
#pragma unroll
    for (int ks = 0; ks < 2; ks++)
#pragma unroll
      for (int t = 0; t < 2; t++) { const lptr p = img + off + (ks ? ro2 : ro_) + t * 2048; ur[t][ks] = ld128(p); ui[t][ks] = ld128(p + 4096); }
    SB();
  };
  auto cmul = [&](const f32x4* cc, const f32x4* sn) {
    br = pk8(ar[0] * cc[0] - ai[0] * sn[0], ar[1] * cc[1] - ai[1] * sn[1]);
    bi = pk8(ai[0] * cc[0] + ar[0] * sn[0], ai[1] * cc[1] + ar[1] * sn[1]);
  };
  rd_in(0);
  {
    const unsigned par = (lane & 1) ? 0x80008000u : 0u;
    c64[1] = c64[0] ^ par; s64[1] = s64[0] ^ par; n64[0] = s64[0] ^ 0x80008000u; n64[1] = s64[1] ^ 0x80008000u;
    c32[1] = c32[0] ^ 0x80000000u; s32[1] = s32[0] ^ 0x80000000u; n32[0] = s32[0] ^ 0x80008000u; n32[1] = s32[0] ^ 0x00008000u;
    PINF(c64[1]); PINF(s64[1]); PINF(n64[0]); PINF(n64[1]); PINF(c32[1]); PINF(s32[1]); PINF(n32[0]); PINF(n32[1]);
  }
  SB();
#pragma unroll
  for (int ks = 0; ks < 2; ks++) {
#pragma unroll
    for (int t = 0; t < 2; t++) { ar[t] = mfma16(ur[t][ks], c64[ks], ks ? ar[t] : z4); ai[t] = mfma16(ui[t][ks], c64[ks], ks ? ai[t] : z4); }
#pragma unroll
    for (int t = 0; t < 2; t++) { ar[t] = mfma16(ui[t][ks], s64[ks], ar[t]); ai[t] = mfma16(ur[t][ks], n64[ks], ai[t]); }
  }
  const f32x4 fa[2] = {asf(wf[0] << 16), asf(wf[1] << 16)}, fb[2] = {asf(wf[0] & 0xFFFF0000u), asf(wf[1] & 0xFFFF0000u)};
  cmul(t1c, t1s);
#pragma unroll
  for (int t = 0; t < 2; t++) { ar[t] = mfma16(c32[t], br, z4); ai[t] = mfma16(c32[t], bi, z4); }
#pragma unroll
  for (int t = 0; t < 2; t++) { ar[t] = mfma16(s32[t], bi, ar[t]); ai[t] = mfma16(n32[t], br, ai[t]); }
  cmul(fa, fb);
#pragma unroll
  for (int t = 0; t < 2; t++) { ar[t] = mfma16(br, c32[t], z4); ai[t] = mfma16(bi, c32[t], z4); }
#pragma unroll
  for (int t = 0; t < 2; t++) { ar[t] = mfma16(bi, n32[t], ar[t]); ai[t] = mfma16(br, s32[t], ai[t]); }
  cmul(t2c, t2s);
#pragma unroll
  for (int t = 0; t < 2; t++) {
    *(LDS_AS u32x2*)(img + 8192 + wo_ + t * 2048) = u32x2{br[2 * t], br[2 * t + 1]};
    *(LDS_AS u32x2*)(img + 12288 + wo_ + t * 2048) = u32x2{bi[2 * t], bi[2 * t + 1]};
  }
  lds_bar();
  rd_in(8192);
#pragma unroll
  for (int ks = 0; ks < 2; ks++) {
#pragma unroll
    for (int t = 0; t < 2; t++) { ar[t] = mfma16(c64[ks], ur[t][ks], ks ? ar[t] : xr[t]); ai[t] = mfma16(c64[ks], ui[t][ks], ks ? ai[t] : xi[t]); }
#pragma unroll
    for (int t = 0; t < 2; t++) { ar[t] = mfma16(n64[ks], ui[t][ks], ar[t]); ai[t] = mfma16(s64[ks], ur[t][ks], ai[t]); }
  }
  const unsigned ob = (FIRST ? WS_RB1 : WS_RB2) + (2 * c * RBP + 8 * tid) * 2;
  st128<16>(rw, ob, clean(pk8(ar[0], ar[1])));
  st128<16>(rw, ob + RBP * 2, c == 0 ? u32x4{0u, 0u, 0u, 0u} : clean(pk8(ai[0], ai[1])));
}

DI f32x16 mfma32(u32x4 a, u32x4 b, f32x16 c) {
  return __builtin_amdgcn_mfma_f32_32x32x16_bf16(__builtin_bit_cast(bf16x8, a), __builtin_bit_cast(bf16x8, b), c, 0, 0, 0);
}
DI f32x16 mfma32k8(u32x2 a, u32x2 b, f32x16 c) {
  return __builtin_amdgcn_mfma_f32_32x32x8bf16_1k(__builtin_bit_cast(s16x4, a), __builtin_bit_cast(s16x4, b), c, 0, 0, 0);
}
DI f32x16 scores(u32x4 k1, u32x2 k2, u32x4 q1, u32x2 q2) {
  f32x16 z;
#pragma unroll
  for (int i = 0; i < 16; i++) z[i] = 0.f;
  f32x16 s = mfma32(k1, q1, z);
  asm volatile("s_nop 11" : "+v"(s));
  return mfma32k8(k2, q2, s);
}
DI float max16(const f32x16& s) {
  float m = fmaxf(s[0], s[1]);
#pragma unroll
  for (int i = 2; i < 16; i++) m = fmaxf(m, s[i]);
  return m;
}
#define STR_(x) #x
#define STR(x) STR_(x)
#define KOF(t) ((((t)&7)>>1)*11264+((t)&1)*1280)
#define VOF(t) ((((t)&7)>>1)*11264+5120+((t)&1)*1536)
#define EXP(d, s) "v_exp_f32 v" #d ", v" #s "\n\t"
#define CVT(d, s) "v_cvt_pk_bf16_f32 v" #d ", v" #s ", v[" #s "+1]\n\t"
#define STEPA(t) \
  "v_mfma_f32_32x32x16_bf16 v[48:63], v[80:83], v[136:139], v[120:135]\n\t" \
  "ds_read_b128 v[80:83], v100 offset:" STR(KOF(t+1)) "\n\t" \
  EXP(64,32) EXP(65,33) CVT(78,68) CVT(79,70) EXP(66,34) EXP(67,35) \
  "v_mfma_f32_32x32x16_bf16 v[104:119], v[90:93], v[76:79], v[104:119]\n\t" \
  "ds_read_b64_tr_b16 v[90:91], v102 offset:" STR(VOF(t)+768) "\n\t" \
  "ds_read_b64_tr_b16 v[92:93], v102 offset:" STR(VOF(t)+1152) "\n\t" \
  EXP(68,36) EXP(69,37) CVT(72,64) CVT(73,66) EXP(70,38) EXP(71,39) \
  "v_mfma_f32_32x32x8_bf16 v[48:63], v[84:85], v[140:141], v[48:63]\n\t" \
  "ds_read_b64 v[84:85], v101 offset:" STR(KOF(t+1)) "\n\t" \
  EXP(64,40) EXP(65,41) CVT(74,68) CVT(75,70) EXP(66,42) EXP(67,43) \
  "s_waitcnt lgkmcnt(4)\n\t" \
  "v_mfma_f32_32x32x16_bf16 v[0:15], v[86:89], v[72:75], v[0:15]\n\t" \
  EXP(68,44) EXP(69,45) CVT(76,64) CVT(77,66) EXP(70,46) EXP(71,47)
#define STEPB(t) \
  "s_waitcnt lgkmcnt(0)\n\t" \
  "v_mfma_f32_32x32x16_bf16 v[32:47], v[80:83], v[94:97], v[16:31]\n\t" \
  EXP(64,48) EXP(65,49) CVT(78,68) CVT(79,70) EXP(66,50) EXP(67,51) \
  "v_mfma_f32_32x32x16_bf16 v[0:15], v[90:93], v[76:79], v[0:15]\n\t" \
  EXP(68,52) EXP(69,53) CVT(72,64) CVT(73,66) EXP(70,54) EXP(71,55) \
  "v_mfma_f32_32x32x8_bf16 v[32:47], v[84:85], v[98:99], v[32:47]\n\t" \
  EXP(64,56) EXP(65,57) CVT(74,68) CVT(75,70) EXP(66,58) EXP(67,59) \
  "v_mfma_f32_32x32x16_bf16 v[104:119], v[86:89], v[72:75], v[104:119]\n\t" \
  "ds_read_b64_tr_b16 v[86:87], v102 offset:" STR(VOF(t+1)) "\n\t" \
  "ds_read_b64_tr_b16 v[88:89], v102 offset:" STR(VOF(t+1)+384) "\n\t" \
  EXP(68,60) EXP(69,61) CVT(76,64) CVT(77,66) EXP(70,62) EXP(71,63)
#define PR(n) "s_setprio " #n "\n\t"
#define CHUNK(i) PR(1) STEPA(2*(i)) PR(0) STEPB(2*(i)) "s_barrier\n\t" PR(2) STEPA(2*(i)+1) STEPB(2*(i)+1)
#define Z4(a) "v_mov_b64 v[" #a ":" #a "+1], 0\n\tv_mov_b64 v[" #a "+2:" #a "+3], 0\n\t"

#define R4(b) "v[" STR(b) ":" STR(b) "+3]"
#define MM(d, a, b) "v_mfma_f32_16x16x32_bf16 " R4(d) ", " R4(a) ", " R4(b) ", " R4(d) "\n\t"
#define GM(B, m, i) MM(20*m+4*i, B+8+4*i, B+4*m)
#define RT(B, m, hf, k) "ds_read_b64_tr_b16 v[" STR(B+4*m+2*hf) ":" STR(B+4*m+2*hf+1) "], v10" #m " offset:" STR(k*8192+hf*1024) "\n\t"
#define RS(B, i, k) "ds_read_b128 " R4(B+8+4*i) ", v102 offset:" STR(i*2048+k*1024) "\n\t"
#define IF5_4(x) ""
#define IF5_5(x) x
#define GSTEP(N, B, C, k) GM(B,0,0) RT(C,0,0,k) RT(C,0,1,k) GM(B,1,0) RS(C,0,k) RT(C,1,0,k) GM(B,0,1) RT(C,1,1,k) RS(C,1,k) GM(B,1,1) RS(C,2,k) \
  GM(B,0,2) RS(C,3,k) GM(B,1,2) IF5_##N(RS(C,4,k)) GM(B,0,3) GM(B,1,3) IF5_##N(GM(B,0,4) GM(B,1,4)) MM(40,B,B)
#define GEMM_ASM(N) \
  Z4(0) Z4(4) Z4(8) Z4(12) Z4(16) Z4(20) Z4(24) Z4(28) Z4(32) Z4(36) Z4(40) "s_mov_b32 %[c], 0\n\ts_barrier\n\t" \
  RT(44,0,0,0) RT(44,0,1,0) RS(44,0,0) RT(44,1,0,0) RT(44,1,1,0) RS(44,1,0) RS(44,2,0) RS(44,3,0) IF5_##N(RS(44,4,0)) \
  "L_gemm_%=:\n\ts_add_u32 %[c], %[c], 1\n\ts_and_b32 %[t], %[c], 3\n\ts_cselect_b32 %[t], 1, -3\n\ts_mul_i32 %[t], %[t], 36864\n\ts_waitcnt lgkmcnt(0)\n\t" \
  GSTEP(N,44,72,1) \
  "v_add_u32 v100, %[t], v100\n\tv_add_u32 v101, %[t], v101\n\tv_add_u32 v102, %[t], v102\n\ts_waitcnt lgkmcnt(0)\n\ts_barrier\n\t" \
  GSTEP(N,72,44,0) \
  "s_cmp_lg_u32 %[c], 9\n\ts_cbranch_scc1 L_gemm_%=\n\ts_waitcnt lgkmcnt(0)\n\t" \
  GSTEP(N,44,72,1) "s_waitcnt lgkmcnt(0)\n\t" \
  MM(40,72,72) GM(72,0,0) GM(72,1,0) GM(72,0,1) GM(72,1,1) "s_nop 4\n\t" \
  "v_cndmask_b32 v44, v40, v41, %[m1]\n\tv_cndmask_b32 v45, v42, v43, %[m1]\n\tv_mov_b32 v46, 0x3727c5ac\n\tv_cndmask_b32 v44, v44, v45, %[m2]\n\t" \
  "v_fmac_f32 v46, 0x3acccccd, v44\n\tv_cmp_u_f32 vcc, v44, v44\n\tv_rsq_f32 v46, v46\n\ts_cbranch_vccz L_gok_%=\n\tds_write_b32 v106, v106\n\t" \
  "L_gok_%=:\n\tds_write_b32 v103, v46\n\ts_waitcnt lgkmcnt(0)\n\ts_barrier\n\t" \
  "ds_read_b32 v44, v104\n\tds_read_b32 v45, v105\n\tds_read_b32 v46, v106\n\t" \
  GM(72,0,2) GM(72,1,2) GM(72,0,3) GM(72,1,3) IF5_##N(GM(72,0,4) GM(72,1,4)) "s_waitcnt lgkmcnt(0)"
#define GEMM_OPS \
  : "={v[0:3]}"(acc[0][0]), "={v[4:7]}"(acc[0][1]), "={v[8:11]}"(acc[0][2]), "={v[12:15]}"(acc[0][3]), "={v[16:19]}"(acc[0][4]), \
    "={v[20:23]}"(acc[1][0]), "={v[24:27]}"(acc[1][1]), "={v[28:31]}"(acc[1][2]), "={v[32:35]}"(acc[1][3]), "={v[36:39]}"(acc[1][4]), \
    "={v44}"(rs[0]), "={v45}"(rs[1]), "={v46}"(vote), "+{v100}"(am0), "+{v101}"(am1), "+{v102}"(as), [c] "=&s"(c_), [t] "=&s"(t_) \
  : "{v103}"(a_w), "{v104}"(a_r[0]), "{v105}"(a_r[1]), "{v106}"(a_v), [m1] "s"(0xAAAAAAAAAAAAAAAAull), [m2] "s"(0xCCCCCCCCCCCCCCCCull) \
  : "memory", "scc", "vcc", "v40", "v41", "v42", "v43", "v47", "v48", "v49", "v50", "v51", "v52", "v53", "v54", "v55", "v56", "v57", "v58", "v59", "v60", \
    "v61", "v62", "v63", "v64", "v65", "v66", "v67", "v68", "v69", "v70", "v71", "v72", "v73", "v74", "v75", "v76", "v77", "v78", "v79", "v80", "v81", \
    "v82", "v83", "v84", "v85", "v86", "v87", "v88", "v89", "v90", "v91", "v92", "v93", "v94", "v95", "v96", "v97", "v98", "v99"

constexpr int HR = 4 * KVC;
constexpr int L_A = 2 * HR;
constexpr int L_EX = 15360;
constexpr int L_MISC = L_A + 70 * 1024;
constexpr int LDS_FUSED = L_MISC + 2048;

DI bool poll16(rsrc_t rw, unsigned off, unsigned need) {
  const int lane = threadIdx.x & 63;
#pragma unroll 1
  for (int spins = 0; spins < (1 << 20); spins++) {
    const unsigned v = gl32<16>(rw, off + (lane & 15) * 4);
    const unsigned got = (unsigned)__builtin_amdgcn_ballot_w64(v != 0) & 0xFFFFu;
    if ((got & need) == need) return true;
    nap<1>();
  }
  return false;
}

DI void poll_aux(rsrc_t rw, unsigned off) {
#pragma unroll 1
  for (int spins = 0; spins < (1 << 20); spins++) {
    const u32x4 v = gl128<16>(rw, off + (threadIdx.x & 63) * 16);
    if (!__builtin_amdgcn_ballot_w64(min(min(v[0], v[1]), min(v[2], v[3])) == 0u)) break;
    nap<4>();
  }
}
#define LAYER_VARS \
  extern __shared__ __attribute__((aligned(16))) char smem[]; \
  const lptr lds = (lptr)smem; \
  const rsrc_t rw = mk_rsrc(ws, WS_END); \
  int lane = threadIdx.x & 63; asm volatile("" : "+v"(lane)); \
  const int w = wave_id(), g = lane >> 4, r16 = lane & 15; \
  const int xcd = blockIdx.x & 7, bi = blockIdx.x >> 3, u = 8 * (xcd & 1) + (bi & 7), qt = 4 * (xcd >> 1) + (bi >> 3); \
  const int hf = u & 1, ct = u >> 1;     \
  constexpr unsigned FKV = WS_FL + (FINAL ? 32768 : 0), KV = WS_KV + (FINAL ? LAYER2 : 0), OB = WS_O + (FINAL ? LAYER2 : 0); \
  constexpr unsigned AUX = WS_AUX + (FINAL ? 1024 : 0), RB = FINAL ? WS_RB2 : WS_RB1; \
  constexpr int MK = FINAL ? 12 : 0; (void)MK; (void)hf; (void)AUX; (void)RB; (void)FKV; \
  LDS_AS unsigned* misc = (LDS_AS unsigned*)(lds + L_MISC + 1024) + (FINAL ? 8 : 0);

template <bool FINAL>
DI void loader(char* ws SARG) {
  LAYER_VARS
  {
    const int lw = w - 8;
    {
      unsigned vm[4];
#pragma unroll
      for (int r = 0; r < 4; r++) vm[r] = (4 * (4 * lw + r) + g) * (RBP * 2) + 256 * qt + (((r16 >> 1) ^ (g | ((r & 2) << 1))) * 32) + (r16 & 1) * 16 + 3072 - 1024 * r;
      const unsigned l0 = lds_addr(lds) + lw * 4096, v0 = 2 * lw * 20480 + lane * 16, v2 = ((8 + (lw >> 1)) * 20 + (lw & 1)) * 1024 + lane * 16;
      constexpr int CNT = FINAL ? 9 : 8;
      auto lds_w = [&](int s) {
        const char* sb = ws + WS_WQ + u * 204800 + s * 2048;
        dma_s(v0, v0 + 20480 - 2048, sb, l0 + (s & 3) * SLOT32 + 16384 + (lw >> 1) * 2048);
        if (FINAL) dma1_sc1(v2, sb, lds_addr(lds) + (s & 3) * SLOT32 + 24576 + (lw >> 1) * 10240 + (lw & 1) * 1024);
      };
      auto lds_a = [&](int s) {
        dma_a(vm, ws + RB - 3072 + s * (64 * RBP * 2), l0 + (s & 3) * SLOT32); };
#pragma unroll 1
      for (int att = 0;; att++) {
        const bool cb = att == 0 && blockIdx.x < 160;
        if (att == 0) { if (cb) raw_bar(); else nap<FINAL ? 100 : PFDELAY>(); }
        lds_w(0); lds_w(1);
        if (cb) raw_bar();
        lds_w(2); lds_w(3);
        if (att == 0) {
          if (FINAL && cb) raw_bar();
          raw_bar();
          if (cb) nap<XDELAY>(); else nap<FINAL ? SW2 : SW1>();
          MARK(MK + 7);
          const int pl = min(lane, 39);
          const unsigned po = RB + ((64 * (pl >> 2) + 16 * lw + 4 * (pl & 3) + 1) * RBP + 128 * qt + 126) * 2;
#pragma unroll 1
          for (int spins = 0; spins < (1 << 20); spins++) {
            const unsigned x0 = gl32<16>(rw, po), x1 = gl32<16>(rw, po + 2 * RBP * 2);
            if (!__builtin_amdgcn_ballot_w64(max(x0, x1) == EMPTY)) break;
            nap<XSLEEP>();
          }
        }
        drain();
        MARK(MK + 8);
#pragma unroll 1
        for (int s = 0; s < 4; s++) lds_a(s);
        vm_bar<12>(); vm_bar<8>();
        lds_a(4); lds_w(4);
        vm_bar<4 + CNT>();
#pragma unroll 1
        for (int s = 5; s < 10; s++) { lds_a(s); lds_w(s); vm_bar<2 * CNT>(); }
        vm_bar<CNT>();
        poll_aux(rw, AUX);
        vm_bar<0>();
        raw_bar();
        if (XNL || att || !*(volatile LDS_AS unsigned*)(misc + 2)) break;
      }
    }
    MARK(MK + 0);
    const int hh = lw >> 1, hp = lw & 1;
    const unsigned kvv = KV + (2 * u + hh) * 16 * KVC + hp * 5120 + lane * 16;
    const unsigned lkv = lds_addr(lds) + hh * HR + hp * 5120;
    const unsigned wv = (FINAL ? WS_WOP : WS_WOF) + (ct * 2 + hh) * 51200 + lane * 16;
    const unsigned myfl = FKV + (u * 16 + qt) * 128;
    raw_bar();
    MARK(MK + 1);
    {
      u32x4 v[6];
#pragma unroll
      for (int i = 0; i < 6; i++) v[i] = ld128(lds + hh * HR + hp * 5120 + lane * 16 + i * 1024);
#pragma unroll
      for (int i = 0; i < 6; i++) st128<16>(rw, kvv + qt * KVC + i * 1024, clean(v[i]));
    }
    drain();
    unsigned cnt = 0;
    if (lane == 0) cnt = __hip_atomic_fetch_add(misc, 1u, __ATOMIC_RELAXED, __HIP_MEMORY_SCOPE_WORKGROUP);
    if (__builtin_amdgcn_readfirstlane(cnt) == 3 && lane < 16) st32<16>(rw, FKV + (u * 16 + lane) * 128 + qt * 4, 1u);
    MARK(MK + 2);
    auto ldkv = [&](int seq) {
      const int ch = seq == 0 ? qt : seq < 13 ? ((qt & 12) + 3 + seq) & 15 : (qt & 12) | ((qt + seq) & 3);
      dma6_sc1(kvv + ch * KVC, ws, lkv + (seq & 3) * KVC);
    };
    auto fetch = [&](int seq) {
      const unsigned src = kvv + ((((qt & 12) + 3 + seq) & 15)) * KVC;
      u32x4 v[6];
#pragma unroll 1
      for (int spins = 0; spins < (1 << 18); spins++) {
#pragma unroll
        for (int i = 0; i < 6; i++) v[i] = gl128<16>(rw, src + i * 1024);
        unsigned m = 0;
#pragma unroll
        for (int i = 0; i < 6; i++) m = umax4(m, v[i]);
        if (!__builtin_amdgcn_ballot_w64(m == EMPTY)) break;
        nap<2>();
      }
#pragma unroll
      for (int i = 0; i < 6; i++) *(LDS_AS u32x4*)(lds + hh * HR + hp * 5120 + seq * KVC + lane * 16 + i * 1024) = v[i];
    };
#pragma unroll 1
    for (int attempt = 0;; attempt++) {
      int c = -2;
      if (attempt) { ldkv(0); vm_bar<0>(); }
      else {
        fetch(1); MARK(MK + 3); lds_bar();
        MARK(MK + 4);
        fetch(2); poll16(rw, myfl, 12u << (((qt & 12) + 4) & 15)); ldkv(3); MARK(MK + 5); lds_bar();
        c = 1;
      }
#pragma unroll 1
      for (; c < 13; c++) {
        if (c == 2 && !attempt) poll16(rw, myfl, 0xFFFFu);
        if (!FINAL && !attempt) {
          if (c == 10) {
            unsigned n = 0;
            if (lane == 0) n = __hip_atomic_fetch_add(misc + 3, 1u, __ATOMIC_RELAXED, __HIP_MEMORY_SCOPE_WORKGROUP);
            if (__builtin_amdgcn_readfirstlane(n) == 3 && lane < 32) st32<16>(rw, WS_FL + 65536 + (lane < 16 ? (u * 16 + lane) * 128 + qt * 4 : (r16 * 16 + qt) * 128 + 64 + u * 4), 1u);
          }
        }
        if (c >= 2) { const int e = min(4 * (c - 2) + 2 * hp, 33); dma2(wv + e * 1024, ws, lds_addr(lds) + L_A + (hh * 35 + e) * 1024); }
        ldkv(c + 3);
        if (c == 0) vm_bar<12>();
        if (!FINAL && !attempt && c >= 2 && c < 9) {
          const u32x4 e = {EMPTY, EMPTY, EMPTY, EMPTY};
          const unsigned o = OB + LAYER2 + (((c - 2) * 256 + blockIdx.x) * 4 + lw) * 2048 + lane * 16;
          st128<16>(rw, o, e); st128<16>(rw, o + 1024, e);
          st128<16>(rw, ((qt & 3) < 2 && c < 8) ? kvv + LAYER2 + qt * KVC + (c - 2) * 1024 : WS_END, e);
          st128<16>(rw, c < 5 ? OB + ((4 * qt + lw) * 32 + 2 * u) * 1280 + min((c - 2) * 1024, 1536) + lane * 16 : WS_END, e);
          if (c == 2) vm_bar<12>(); else vm_bar<16>();
        } else if (c >= 2) vm_bar<8>(); else if (c >= 0) vm_bar<6>();
      }
      if (!FINAL && !attempt) poll16(rw, myfl + 65536 + hp * 64, 0xFFFFu);
      vm_bar<0>();
      dma4(wv + (35 + 7 * hp) * 1024, ws, lds_addr(lds) + hh * HR + 7 * hp * 1024);
      dma4(wv + (39 + 7 * hp) * 1024, ws, lds_addr(lds) + hh * HR + (4 + 7 * hp) * 1024);
      raw_bar();
      vm_bar<0>();
      if (attempt || !*(volatile LDS_AS unsigned*)(misc + 1)) break;
      raw_bar();
    }
    if (!FINAL) { raw_bar(); raw_bar(); }
  }
}

template <bool FINAL>
DI void compute(char* ws, u16* out SARG) {
  LAYER_VARS
  const int wm = w & 3, wn = w >> 2, h = lane >> 5;
  if (blockIdx.x < 160) col_phase<!FINAL>(rw, lds, 2 * blockIdx.x SPASS);
  else if (!FINAL) {
    for (int pc = (blockIdx.x - 160) * 512 + threadIdx.x; pc < 640 * 80; pc += 96 * 512) {
      const int n = pc % 640, kq = pc / 640;
      const int u_ = n / 40, rn = n - 40 * u_, hn = rn / 20, dn = rn - 20 * hn, gq = dn >> 2;
      const int T = gq < 4 ? 8 + hn : 4 * hn + 3, gs = gq < 4 ? gq : 3;
      float v[8];
#pragma unroll
      for (int e = 0; e < 4; e++) {
        const float a = (float)((freq_of(kq >> 2, 4 * (kq & 3) + e) * n) % 640) * (1.f / 640.f);
        v[2 * e] = 0.055901699437494742f * __builtin_amdgcn_cosf(a);
        v[2 * e + 1] = -0.055901699437494742f * __builtin_amdgcn_sinf(a);
      }
      if (kq == 0) { v[0] = (n & 1) ? -0.039528470752104741f : 0.039528470752104741f; v[1] = 0.039528470752104741f; }
      st128<16>(rw, WS_WQ + ((u_ * 10 + T) * 20 + (kq >> 2)) * 1024 + ((kq & 3) * 16 + 4 * gs + (dn & 3)) * 16,
                  u32x4{pk2(v[0], v[1]), pk2(v[2], v[3]), pk2(v[4], v[5]), pk2(v[6], v[7])});
    }
    nap<FILLDELAY>();
#pragma unroll
    for (int it = 0; it < 4; it++) {
      const int k = (it * 96 + blockIdx.x - 160) * 512 + threadIdx.x, b = k / 1408;
      st128<16>(rw, k < 128 * 1408 ? WS_KV + b * 4 * KVC + (k - b * 1408) * 16 : WS_END, u32x4{EMPTY, EMPTY, EMPTY, EMPTY});
    }
  }
  MARK(MK + 10);
  lds_bar();
  {
    drain();
    unsigned n = 0;
    if (lane == 0) n = __hip_atomic_fetch_add(misc + 4, 1u, __ATOMIC_RELAXED, __HIP_MEMORY_SCOPE_WORKGROUP);
    if (n == 7) st32<16>(rw, AUX + blockIdx.x * 4, 1u);
  }

  {
    constexpr int NT = FINAL ? 5 : 4;
    f32x4 acc[2][5]; float rs[2];
    unsigned mo[2], a0[2], a1[2], a2[2], a_r[2];
#pragma unroll
    for (int m = 0; m < 2; m++) {
      const int q = r16 >> 2, pr = 16 * (m ^ wn) + r16;
      mo[m] = lds_addr(lds) + (8 * g + q) * 256 + (((2 * wm + (m ^ wn)) ^ (q | ((g & 1) << 2))) * 32) + (lane & 3) * 8;
      const unsigned qb = L_A + (wn * 4 + wm) * 1280, kb = wn * HR + wm * 1280, vr = wn * HR + 5120 + (32 * wm + pr) * 48 + (g == 3 ? 32 : g * 16);
      a0[m] = ((g >> 1) ? kb : qb) + (g & 1) * 512 + pr * 16;
      a1[m] = g == 2 ? qb + 1024 + pr * 8 : vr; a2[m] = g == 2 ? kb + 1024 + pr * 8 : vr + 8;
      a_r[m] = lds_addr(lds) + L_MISC + (32 * wm + pr) * 4;
      asm volatile("" : "+v"(a0[m]), "+v"(a1[m]), "+v"(a2[m]));
    }
    const unsigned a_w = a_r[0] + (g == (r16 >> 2) ? 0 : 512), a_v = lds_addr((lptr)(misc + 2));
#pragma unroll 1
    for (int att = 0;; att++) {
      unsigned am0 = mo[0], am1 = mo[1], as = lds_addr(lds) + 16384 + wn * 10240 + lane * 16, vote; int c_, t_;
      if (FINAL) asm volatile(GEMM_ASM(5) GEMM_OPS); else asm volatile(GEMM_ASM(4) GEMM_OPS);
      if (XNL || att || !__builtin_amdgcn_readfirstlane(vote)) break;
    }
    SB();
    MARK(MK + 0);
#pragma unroll
    for (int m = 0; m < 2; m++) *(LDS_AS u32x4*)(lds + a0[m]) = pk8(acc[m][0] * rs[m], acc[m][1] * rs[m]);
    asm volatile("s_nop 1" : "+v"(acc[0][2]), "+v"(acc[1][2]), "+v"(acc[0][3]), "+v"(acc[1][3]), "+v"(acc[0][NT - 1]), "+v"(acc[1][NT - 1]) :: "memory");
#pragma unroll
    for (int m = 0; m < 2; m++) {
      u32x2 hi = pk4(acc[m][3] * rs[m]);
      if (g == 3) hi = u32x2{0x3F80u, 0u};
      *(LDS_AS u32x2*)(lds + a1[m]) = pk4(acc[m][2] * rs[m]);
      *(LDS_AS u32x2*)(lds + a2[m]) = hi;
    }
    lds_bar();
    if (FINAL) {
      const unsigned x4 = WS_X4 + (((u * 16 + qt) * 4 + wm) * 2 + wn) * 1280;
      const u32x2 p0 = pk4(acc[0][NT - 1]), p1 = pk4(acc[1][NT - 1]), q0 = pk4(acc[0][3]), q1 = pk4(acc[1][3]);
      st128<16>(rw, x4 + lane * 16, clean(u32x4{p0[0], p0[1], p1[0], p1[1]}));
      if (g == 3) st128<16>(rw, x4 + 1024 + r16 * 16, clean(u32x4{q0[0], q0[1], q1[0], q1[1]}));
    }
  }
  MARK(MK + 1);
  const int head = 2 * u + wn, kh = w & 1;
  const lptr ring = lds + wn * HR;
  const int pblk = 128 * qt + 64 * hf + 32 * (wm >> 1);
  const unsigned r3t = WS_R3 + (64 * (wm & 1) + (pblk >> 5)) * (642 * 64);
  u32x2 x4v[3]; float dc = 0.f;
  unsigned os = OB + ((4 * qt + wm) * 32 + head) * 1280 + (((lane << 2) & 16) + ((lane >> 1) & 12) + (lane & 3)) * 8;
  unsigned o1 = OB + ((pblk >> 5) * 32 + 16 * wn) * 1280 + (g >> 1) * 1280 + (g & 1) * 512 + (wm & 1) * 256 + r16 * 16;
  unsigned o2 = OB + ((pblk >> 5) * 32 + 16 * wn) * 1280 + 2 * g * 1280 + 1024 + (wm & 1) * 128 + r16 * 8;
  u32x4 o3 = {0u, 0u, 0u, 0u};
  if (!FINAL) {
#pragma unroll
    for (int i = 0; i < 3; i++) {
      const int j = 80 * ct + 48 * wn + 16 * (wn ? (i & 1) : i) + r16;
      o3[i] = (j == 1 ? WS_RB0 + ((pblk >> 5) * 641 + ((pblk >> 5) & 1) + 1) * 64 : WS_RB1 + (j * RBP + pblk) * 2) + 16 * g + 8 * (wm & 1);
    }
  } else {
    o3[3] = r3t + 64 + r16 * 4;
#pragma unroll
    for (int i = 0; i < 3; i++) {
      const int cg = 12 * wn + 4 * ((i == 2 && wn) ? 0 : i) + g, pw = cg / 5, gq = cg - 5 * pw;
      o3[i] = WS_X4 + ((((2 * ct + (pw >> 1)) * 16 + qt) * 4 + 2 * hf + (wm >> 1)) * 2 + (pw & 1)) * 1280 + (gq * 16 + (trow(wm & 1, r16) & 15)) * 16 + ((((r16 >> 3) ^ pw) & 1) * 8);
    }
  }
  asm volatile("" : "+v"(os), "+v"(o1), "+v"(o2), "+v"(o3));
  f32x4 r[3];
  {
    const lptr qpa = lds + L_A + (wn * 4 + wm) * 1280, qpb = lds + L_A + (wn * 4 + (wm ^ 1)) * 1280;
    const u32x4 qa1 = ld128(qpa + lane * 16), qb1 = ld128(qpb + lane * 16);
    u32x2 qa2 = ld64(qpa + 1024 + (lane & 31) * 8), qb2 = ld64(qpb + 1024 + (lane & 31) * 8);
    if (h) { qa2 = u32x2{0u, 0u}; qb2 = qa2; }
    const unsigned ka = lds_addr(ring) + kh * 2560 + lane * 16, kb = lds_addr(ring) + kh * 2560 + 1024 + (lane & 31) * 8;
    const unsigned va = lds_addr(ring) + kh * 3072 + (4 * h + (r16 >> 2)) * 48 + ((g & 1) ? ((lane & 3) ? 40 : 32) : (lane & 3) * 8);
    float ma, mb;
    {
      const u32x4 k1 = ld128(ring + lane * 16); const u32x2 k2 = ld64(ring + 1024 + (lane & 31) * 8);
      ma = max16(scores(k1, k2, qa1, qa2)); mb = max16(scores(k1, k2, qb1, qb2));
    }
    for (int attempt = 0;; attempt++) {
      ma = fmaxf(ma, __shfl_xor(ma, 32)); mb = fmaxf(mb, __shfl_xor(mb, 32));
      f32x16 cia, cib, oa, ob;
#pragma unroll
      for (int i = 0; i < 16; i++) { cia[i] = -ma; cib[i] = -mb; }
      int cnt = 4;
      MARK(MK + 2);
      asm volatile(
          "ds_read_b128 v[80:83], v100\n\t"
          "ds_read_b64 v[84:85], v101\n\t"
          "ds_read_b64_tr_b16 v[86:87], v102 offset:5120\n\t"
          "ds_read_b64_tr_b16 v[88:89], v102 offset:5504\n\t"
          Z4(0) Z4(68) Z4(76) Z4(90)
          "s_waitcnt lgkmcnt(0)\n\t"
          "v_mfma_f32_32x32x16_bf16 v[32:47], v[80:83], v[94:97], v[16:31]\n\t"
          Z4(4) Z4(8) Z4(12) Z4(104) Z4(108) Z4(112) Z4(116)
          "v_mfma_f32_32x32x8_bf16 v[32:47], v[84:85], v[98:99], v[32:47]\n\ts_nop 9\n\t"
          "L_attn_%=:\n\t"
          CHUNK(0) CHUNK(1) CHUNK(2) CHUNK(3)
          "s_sub_u32 %[cnt], %[cnt], 1\n\ts_cmp_lg_u32 %[cnt], 0\n\ts_cbranch_scc1 L_attn_%=\n\t"
          "s_setprio 0\n\t" CVT(78,68) CVT(79,70) "s_waitcnt lgkmcnt(0)\n\ts_nop 1\n\t"
          "v_mfma_f32_32x32x16_bf16 v[104:119], v[90:93], v[76:79], v[104:119]\n\ts_nop 15\n\t"
          : "={v[0:15]}"(oa), "={v[104:119]}"(ob), [cnt] "+s"(cnt)
          : "{v[16:31]}"(cia), "{v[120:135]}"(cib), "{v[94:97]}"(qa1), "{v[98:99]}"(qa2), "{v[136:139]}"(qb1), "{v[140:141]}"(qb2),
            "{v100}"(ka), "{v101}"(kb), "{v102}"(va)
          : "memory", "scc", "v32", "v33", "v34", "v35", "v36", "v37", "v38", "v39", "v40", "v41", "v42", "v43", "v44", "v45", "v46", "v47",
            "v48", "v49", "v50", "v51", "v52", "v53", "v54", "v55", "v56", "v57", "v58", "v59", "v60", "v61", "v62", "v63", "v64", "v65",
            "v66", "v67", "v68", "v69", "v70", "v71", "v72", "v73", "v74", "v75", "v76", "v77", "v78", "v79", "v80", "v81", "v82", "v83",
            "v84", "v85", "v86", "v87", "v88", "v89", "v90", "v91", "v92", "v93");
      MARK(MK + 3);
      LDS_AS f32x4* ex = (LDS_AS f32x4*)(ring + L_EX + wm * 3072) + lane;
#pragma unroll
      for (int i = 0; i < 3; i++) ex[i * 64] = f32x4{ob[4 * i], ob[4 * i + 1], ob[4 * i + 2], ob[4 * i + 3]};
      f32x4 cs = {0.f, 0.f, 0.f, 0.f};
#pragma unroll
      for (int i = 0; i < 3; i++) cs += f32x4{oa[4 * i], oa[4 * i + 1], oa[4 * i + 2], oa[4 * i + 3]} + f32x4{ob[4 * i], ob[4 * i + 1], ob[4 * i + 2], ob[4 * i + 3]};
      if (!(fabsf(cs[0] + cs[1] + cs[2] + cs[3]) < INFINITY) || (h && !(oa[8] + ob[8] < 0x1p60f))) misc[1] = 1u;
      lds_bar();
      const unsigned vote = *(volatile LDS_AS unsigned*)(misc + 1);
      LDS_AS f32x4* ey = (LDS_AS f32x4*)(ring + L_EX + (wm ^ 1) * 3072) + lane;
#pragma unroll
      for (int i = 0; i < 3; i++) r[i] = ey[i * 64] + f32x4{oa[4 * i], oa[4 * i + 1], oa[4 * i + 2], oa[4 * i + 3]};
      if (attempt || !vote) break;
      lds_bar();
      ma = -INFINITY; mb = -INFINITY;
      for (int t = 0; t < 64; t++) {
        const unsigned kb_ = KV + (head * 16 + (t >> 2)) * KVC + (t & 3) * 1280;
        const u32x4 k1 = gl128<16>(rw, kb_ + lane * 16); const u32x2 k2 = gl64<16>(rw, kb_ + 1024 + (lane & 31) * 8);
        ma = fmaxf(ma, max16(scores(k1, k2, qa1, qa2))); mb = fmaxf(mb, max16(scores(k1, k2, qb1, qb2)));
      }
      raw_bar();
    }
  }
  {
    const unsigned lb = __float_as_uint(r[2][0]);
    const float il = __builtin_amdgcn_rcpf(__uint_as_float(__builtin_amdgcn_permlane32_swap(lb, lb, false, false)[1]));
    const unsigned sl8 = os & 255;
    st128<16>(rw, os + sl8 + h * 512, clean(pk8(r[0] * il, r[1] * il)));
    const u32x4 t = clean(pk8(r[2] * il, r[2] * il));
    st64<16>(rw, h ? WS_END : os + 1024, u32x2{t[0], t[1]});
  }
  MARK(MK + 4);
  const lptr wa = lds + L_A + wn * 35 * 1024 + lane * 16, wb = ring + lane * 16;
  constexpr int NPRE = 15;
  u32x4 bf[50];
  f32x4 acc[5] = {};
#pragma unroll
  for (int j = 0; j < NPRE; j++) bf[j] = ld128(wa + j * 1024);
#pragma unroll
  for (int j = 0; j < 5; j++) asm volatile("" : "+v"(acc[j]));
  if (FINAL) dc = __uint_as_float(gl32<16>(rw, o3[3]));
#pragma unroll 1
  for (int spins = 0; spins < (1 << 18); spins++) {
#pragma unroll
    for (int i = 0; i < 3; i++) x4v[i] = gl64<16>(rw, o3[i]);
    if (!FINAL || !__builtin_amdgcn_ballot_w64(umax4(max(x4v[0][0], x4v[0][1]), u32x4{x4v[1][0], x4v[1][1], x4v[2][0], x4v[2][1]}) == EMPTY)) break;
    nap<8>();
  }
  drain();
  nap<ODELAY>();
  MARK(MK + 5);
  {
    u32x4 af[10];
    auto ldf = [&](int i) {
      if (i < 8) af[i] = __builtin_amdgcn_raw_buffer_load_b128(rw, o1, i * 2 * 1280, 16);
      else { const u32x2 a = __builtin_amdgcn_raw_buffer_load_b64(rw, o2, (i - 8) * 8 * 1280, 16), b = __builtin_amdgcn_raw_buffer_load_b64(rw, o2, ((i - 8) * 8 + 1) * 1280, 16);
             af[i] = u32x4{a[0], a[1], b[0], b[1]}; }
    };
#pragma unroll
    for (int i = 0; i < 10; i++) ldf(i);
#pragma unroll 1
    for (int spins = 0; spins < (1 << 18); spins++) {
      unsigned m = 0;
#pragma unroll
      for (int i = 0; i < 10; i++) m = umax4(m, af[i]);
      if (!__builtin_amdgcn_ballot_w64(m == EMPTY)) break;
      nap<RSLEEP>();
#pragma unroll
      for (int i = 0; i < 10; i++) if (__builtin_amdgcn_ballot_w64(umax4(0, af[i]) == EMPTY)) ldf(i);
    }
    MARK(MK + 8);
#pragma unroll
    for (int i = 0; i < 10; i++) asm volatile("" : "+v"(af[i]));
    asm volatile("s_nop 1" : "+v"(acc[0]), "+v"(acc[1]), "+v"(acc[2]), "+v"(acc[3]), "+v"(acc[4]));
    SB();
#pragma unroll
    for (int i = 0; i < 10; i++) {
#pragma unroll
      for (int a = 0; a < 5; a++) {
        if (FINAL) mfma16v(acc[a], bf[5 * i + a], af[i]); else mfma16v(acc[a], af[i], bf[5 * i + a]);
        const int j = 5 * i + a + NPRE; if (j < 50) bf[j] = ld128(j < 35 ? wa + j * 1024 : wb + (j - 35) * 1024);
        SB();
      }
    }
  }
  asm volatile("s_nop 7\n\ts_nop 1" : "+v"(acc[0]), "+v"(acc[1]), "+v"(acc[2]), "+v"(acc[3]), "+v"(acc[4]) :: "memory");
  MARK(MK + 6);
  {
    LDS_AS f32x4* ex = (LDS_AS f32x4*)(ring + L_EX + 12288 + wm * 3072) + lane;
    LDS_AS f32x4* ey = (LDS_AS f32x4*)(lds + (wn ^ 1) * HR + L_EX + 12288 + wm * 3072) + lane + 64 * wn;
    ex[0] = acc[2]; ex[64] = acc[3]; ex[128] = acc[4];
    dc *= 0.039528470752104741f;
#pragma unroll
    for (int i = 0; i < 3; i++) { acc[i] += f32x4{bflo(x4v[i][0]), bfhi(x4v[i][0]), bflo(x4v[i][1]), bfhi(x4v[i][1])}; if (FINAL) acc[i] += dc; }
    lds_bar();
#pragma unroll
    for (int i = 0; i < 3; i++) acc[i] += ey[64 * i];
  }
  if (FINAL) {
    const rsrc_t ro = mk_rsrc(out, 2048 * 640 * 2);
    const unsigned op = (rowof(pblk + trow(wm & 1, r16)) * 640 + 80 * ct + 48 * wn + 4 * g) * 2;
#pragma unroll
    for (int i = 0; i < 3; i++)
      if (i < 2 || wn == 0) st64<OUTAUX>(ro, op + 32 * i, pk4(acc[i]));
  } else {
    f32x4 sq = {0.f, 0.f, 0.f, 0.f};
#pragma unroll
    for (int i = 0; i < 3; i++)
      if (i < 2 || wn == 0) {
        const int j = 80 * ct + 48 * wn + 16 * i + r16;
        st128<16>(rw, r3t + j * 64 + g * 16, clean(asu(acc[i])));
        if (j != 1) sq += acc[i] * acc[i];
      }
#pragma unroll
    for (int e = 0; e < 4; e++) sq[e] = row16_sum(sq[e]);
    LDS_AS float* sl = (LDS_AS float*)(lds + L_MISC);
    if (r16 == 0) *(LDS_AS f32x4*)(sl + wn * 64 + 32 * (wm & 1) + 16 * (wm >> 1) + 4 * g) = sq;
    lds_bar();
    if (w == 0) {
      LDS_AS f32x4* s4 = (LDS_AS f32x4*)sl + 2 * (lane & 7);
      st128<16>(rw, WS_SSQ + (lane >> 3) * 32768 + (ct * 2048 + 1024 * ((lane >> 2) & 1) + 64 * qt + 32 * hf + 8 * (lane & 3)) * 2, clean(pk8(s4[0] + s4[16], s4[1] + s4[17])));
    }
  }
  MARK(MK + 7);
}

__global__ void __launch_bounds__(768) k_main(char* ws, u16* out) {
  STAMP(1);
  if (__builtin_expect(wave_id() >= 8, 1)) {
    extern __shared__ __attribute__((aligned(16))) char smem[];
    __builtin_amdgcn_s_setprio(3);
    if (threadIdx.x < 528) ((LDS_AS unsigned*)((lptr)smem + L_MISC + 1024))[threadIdx.x - 512] = 0u;
    asm volatile("s_waitcnt lgkmcnt(0)" ::: "memory");
    loader<false>(ws SPASS);
    loader<true>(ws SPASS);
  } else {
    compute<false>(ws, out SPASS);
    compute<true>(ws, out SPASS);
  }
}

#define STOPAT(n)
extern "C" void kernel_launch(void* const* d_in, const int* in_sizes, int n_in, void* d_out, int out_size, void* d_ws, size_t ws_size,
                              hipStream_t stream) {
  const u16* x = (const u16*)d_in[0];
  const u16* wr = (const u16*)d_in[1];
  const u16* wi = (const u16*)d_in[2];
  const u16* wqkv = (const u16*)d_in[4];
  const u16* wo = (const u16*)d_in[5];
  char* ws = (char*)d_ws;
  (void)hipFuncSetAttribute((const void*)k_prep, hipFuncAttributeMaxDynamicSharedMemorySize, LDS_PREP);
  (void)hipFuncSetAttribute((const void*)k_main, hipFuncAttributeMaxDynamicSharedMemorySize, LDS_FUSED);
  k_prep<<<256, 512, LDS_PREP, stream>>>(x, wr, wi, wqkv, wo, ws);
  STOPAT(1)
  k_main<<<256, 768, LDS_FUSED, stream>>>(ws, (u16*)d_out);
}
```

```cpp
#include <hip/hip_runtime.h>
#include <hip/hip_bf16.h>
#include <utility>
#include <type_traits>

typedef unsigned short u16;
typedef __attribute__((ext_vector_type(16))) float f32x16;
typedef __attribute__((ext_vector_type(4))) float f32x4;
typedef __attribute__((ext_vector_type(2))) float f32x2;
typedef __attribute__((ext_vector_type(8))) __bf16 bf16x8;
typedef __attribute__((ext_vector_type(2))) __bf16 bf16x2;
typedef __attribute__((ext_vector_type(4))) short s16x4;
typedef __attribute__((ext_vector_type(4))) unsigned u32x4;
typedef __attribute__((ext_vector_type(2))) unsigned u32x2;
#define DI __device__ __forceinline__
#define LDS_AS __attribute__((address_space(3)))
typedef LDS_AS char* lptr;

constexpr double cx_cos_small(double x) { double x2 = x * x, t = 1, s = 1; for (int i = 1; i < 12; i++) { t *= -x2 / ((2 * i - 1) * (2 * i)); s += t; } return s; }
constexpr double cx_sin_small(double x) { double x2 = x * x, t = x, s = x; for (int i = 1; i < 12; i++) { t *= -x2 / ((2 * i) * (2 * i + 1)); s += t; } return s; }
constexpr double cx_cos640(int m) {
  m %= 640; if (m > 320) m = 640 - m; double sg = 1; if (m > 160) { m = 320 - m; sg = -1; }
  const double w = 6.283185307179586476925 / 640; return sg * (m <= 80 ? cx_cos_small(w * m) : cx_sin_small(w * (160 - m)));
}
constexpr u16 cx_bf16(double d) { float f = (float)d; unsigned u = __builtin_bit_cast(unsigned, f); u += 0x7FFFu + ((u >> 16) & 1u); return (u16)(u >> 16); }
struct Cos640 { double v[640]; };
constexpr Cos640 mk_cos() { Cos640 c{}; for (int m = 0; m < 640; m++) c.v[m] = cx_cos640(m); return c; }
constexpr Cos640 COS = mk_cos();
__host__ __device__ constexpr int kf_of(int G, int i) { return G ? G + 40 * i : (i == 0 ? 320 : i < 8 ? 40 * i : 40 * i - 300); }
__host__ __device__ constexpr int freq_of(int G, int i) { return kf_of(G, i) > 320 ? 640 - kf_of(G, i) : kf_of(G, i); }
constexpr double a1val(int out, int a) {
  if (out >= 40 || a >= 40) return 0;
  const int G = out >> 1, part = out & 1;
  if (G == 0) return (part && (a & 1)) ? -1 : 1;
  return COS.v[(16 * a * G + 160 * part) % 640];
}
constexpr double m2val(int G, int o, int k) {
  const int i = o >> 1, po = o & 1, part = k >> 4, b = k & 15, kf = kf_of(G, i);
  const double c = COS.v[(b * kf) % 640], sn = COS.v[(b * kf + 480) % 640];
  if (G == 0) {
    if (part != (i >= 8)) return 0;
    if (i == 0) return 0.039528470752104741 * (po ? 1.0 : c);
    return 0.055901699437494742 * (po ? -sn : c);
  }
  const double v = po ? (part ? c : -sn) : (part ? sn : c);
  return 0.055901699437494742 * ((po && kf > 320) ? -v : v);
}
struct DTab { u16 a1[3][64][8]; u16 a1b[3][64][4]; u16 pad[256]; u16 m2[20][2][64][8]; };
constexpr DTab mk_dtab() {
  DTab t{};
  for (int l = 0; l < 64; l++) {
    const int r = l & 15, g = l >> 4;
    for (int T = 0; T < 3; T++) {
      for (int e = 0; e < 8; e++) t.a1[T][l][e] = cx_bf16(a1val(16 * T + r, 16 * (e >> 2) + 4 * g + (e & 3)));
      for (int e = 0; e < 4; e++) t.a1b[T][l][e] = cx_bf16(a1val(16 * T + r, 32 + 4 * g + e));
    }
    for (int G = 0; G < 20; G++) for (int T = 0; T < 2; T++) for (int e = 0; e < 8; e++)
      t.m2[G][T][l][e] = cx_bf16(m2val(G, 8 * (r >> 2) + 4 * T + (r & 3), 8 * g + e));
  }
  return t;
}
static __device__ const __attribute__((aligned(1024))) DTab g_dt = mk_dtab();

struct CTab { u16 v[10 * 512]; };
constexpr CTab mk_ctab() {
  CTab c{}; u16 cs[64] = {};
  for (int m = 0; m < 64; m++) cs[m] = cx_bf16(cx_cos640(10 * m));
  for (int f = 0; f < 10; f++) for (int l = 0; l < 64; l++) for (int e = 0; e < 8; e++) {
    const int r = l & 15, g = l >> 4;
    const int m = f < 8 ? (8 * g + e) * (16 * (f >> 1) + r) : 2 * r * (16 * (e >> 2) + 4 * g + (e & 3));
    c.v[(f * 64 + l) * 8 + e] = cs[(m + (f & 1) * 48) & 63];
  }
  return c;
}
static __device__ const __attribute__((aligned(1024))) CTab g_ct = mk_ctab();

constexpr int RP = 2048 + 64, RBP = 2048 + 128;
constexpr unsigned RBSZ = 640 * RBP * 2;
constexpr unsigned WS_FL = 0;
constexpr unsigned WS_AUX = WS_FL + 3 * 32768;
constexpr unsigned WS_RS1 = WS_AUX + 2048;
constexpr unsigned WS_RB0 = WS_RS1 + 8192;
constexpr unsigned WS_RB1 = WS_RB0 + RBSZ;
constexpr unsigned WS_WF = WS_RB1 + RBSZ;
constexpr unsigned WS_WQ = WS_WF + 320 * 8192;
constexpr unsigned WS_WOF = WS_WQ + 16 * 10 * 20 * 1024;
constexpr unsigned WS_WOP = WS_WOF + 40 * 20 * 1024;
constexpr int KVC = 11264;
constexpr unsigned WS_KV = WS_WOP + 40 * 20 * 1024;
constexpr unsigned WS_O = WS_KV + 32 * 16 * KVC;
constexpr unsigned LAYER2 = 32 * 16 * KVC + 32 * 64 * 1280;
constexpr unsigned WS_X4 = WS_KV + 2 * LAYER2;
constexpr unsigned WS_R3 = WS_X4 + 2048 * 640 * 2;
constexpr unsigned WS_RB2 = WS_R3 + 640 * RP * 4;
constexpr unsigned WS_SSQ = WS_RB2 + RBSZ;
constexpr unsigned EMPTY = 0xFFFFFFFFu;
constexpr unsigned WS_END = WS_SSQ + 8 * 2048 * 16;
#define FILLDELAY 10
#define OUTAUX 0
#define XDELAY 14
#define SW1 50
#define SW2 46
#define XNL 0
#define K1DELAY 40
#define YDELAY 0
#define ODELAY 0
#define PFDELAY 40
#define YSLEEP 4
#define XSLEEP 1
#define RSLEEP 2
#define AUXB 16

#define STW 0
#define STAMP(k)
#define MARK(i)
#define SARG
#define SPASS
#define SB() __builtin_amdgcn_sched_barrier(0)
typedef __amdgpu_buffer_rsrc_t rsrc_t;
DI rsrc_t mk_rsrc(const void* p, unsigned bytes) { return __builtin_amdgcn_make_buffer_rsrc((void*)p, 0, bytes, 0x00020000); }
template <int AUX> DI void st128(rsrc_t r, unsigned off, u32x4 v) { __builtin_amdgcn_raw_buffer_store_b128(v, r, off, 0, AUX); }
template <int AUX> DI void st64(rsrc_t r, unsigned off, u32x2 v) { __builtin_amdgcn_raw_buffer_store_b64(v, r, off, 0, AUX); }
template <int AUX> DI void st32(rsrc_t r, unsigned off, unsigned v) { __builtin_amdgcn_raw_buffer_store_b32(v, r, off, 0, AUX); }
template <int AUX> DI void st16(rsrc_t r, unsigned off, u16 v) { __builtin_amdgcn_raw_buffer_store_b16(v, r, off, 0, AUX); }
template <int AUX> DI u32x4 gl128(rsrc_t r, unsigned off) { return __builtin_amdgcn_raw_buffer_load_b128(r, off, 0, AUX); }
template <int AUX> DI u32x2 gl64(rsrc_t r, unsigned off) { return __builtin_amdgcn_raw_buffer_load_b64(r, off, 0, AUX); }
template <int AUX> DI unsigned gl32(rsrc_t r, unsigned off) { return __builtin_amdgcn_raw_buffer_load_b32(r, off, 0, AUX); }
DI u32x4 asu(f32x4 v) { return __builtin_bit_cast(u32x4, v); }
DI f32x4 asf(u32x4 v) { return __builtin_bit_cast(f32x4, v); }
DI unsigned pk2(float a, float b) { f32x2 v = {a, b}; return __builtin_bit_cast(unsigned, __builtin_convertvector(v, bf16x2)); }
DI u32x2 pk4(f32x4 a) { return u32x2{pk2(a[0], a[1]), pk2(a[2], a[3])}; }
DI u32x4 pk8(f32x4 a, f32x4 b) { return u32x4{pk2(a[0], a[1]), pk2(a[2], a[3]), pk2(b[0], b[1]), pk2(b[2], b[3])}; }
DI u32x4 clean(u32x4 v) { return __builtin_elementwise_min(v, u32x4{EMPTY - 1, EMPTY - 1, EMPTY - 1, EMPTY - 1}); }
DI unsigned umax4(unsigned m, u32x4 v) { return max(max(max(m, v[0]), v[1]), max(v[2], v[3])); }
DI float bflo(unsigned u) { return __uint_as_float(u << 16); }
DI float bfhi(unsigned u) { return __uint_as_float(u & 0xFFFF0000u); }
DI float dot2(unsigned a, unsigned b, float c) { return __builtin_amdgcn_fdot2_f32_bf16(__builtin_bit_cast(bf16x2, a), __builtin_bit_cast(bf16x2, b), c, false); }
DI int rowof(int p) { return 32 * (((p >> 5) & 0x3C) + (p & 3)) + ((p << 2) & 16) + ((p >> 3) & 15); }
DI int rpos(int p) { return ((p << 8) & 1024) + ((p >> 3) << 2) + (p & 3); }
DI int trow(int T, int r) { return 8 * (r >> 2) + 4 * T + (r & 3); }
template <int CTRL> DI float dpp_add(float v) { return v + __int_as_float(__builtin_amdgcn_update_dpp(0, __float_as_int(v), CTRL, 0xF, 0xF, true)); }
DI float row16_sum(float v) { v = dpp_add<0x128>(v); v = dpp_add<0x124>(v); v = dpp_add<0x122>(v); return dpp_add<0x121>(v); }
DI f32x4 mfma16(u32x4 a, u32x4 b, f32x4 c) {
  return __builtin_amdgcn_mfma_f32_16x16x32_bf16(__builtin_bit_cast(bf16x8, a), __builtin_bit_cast(bf16x8, b), c, 0, 0, 0);
}
DI void mfma16v(f32x4& c, u32x4 a, u32x4 b) { asm volatile("v_mfma_f32_16x16x32_bf16 %0, %1, %2, %0" : "+v"(c) : "v"(a), "v"(b)); }
DI u32x2 ldtr(lptr p) { return __builtin_bit_cast(u32x2, __builtin_amdgcn_ds_read_tr16_b64_v4i16((LDS_AS s16x4*)p)); }
DI u32x4 ld128(lptr p) { return *(LDS_AS u32x4*)p; }
DI u32x2 ld64(lptr p) { return *(LDS_AS u32x2*)p; }
#define GL(v, s, o, x) "global_load_lds_dwordx4 " v ", " s " offset:" #o x "\n\t"
#define M0SET "s_mov_b32 %[sv], m0\n\ts_mov_b32 m0, %[l]\n\ts_nop 2\n\t"
#define M0ADD(x) "s_add_u32 m0, m0, " x "\n\ts_nop 0\n\t"
#define M0RST "s_mov_b32 m0, %[sv]"
DI void dma2(unsigned v, const char* sb, unsigned l) { unsigned sv;
  asm volatile(M0SET GL("%[v]", "%[s]", 0, "") GL("%[v]", "%[s]", 1024, "") M0RST : [sv] "=&s"(sv) : [v] "v"(v), [s] "s"(sb), [l] "s"(l) : "memory"); }
DI void dma4(unsigned v, const char* sb, unsigned l) { unsigned sv;
  asm volatile(M0SET GL("%[v]", "%[s]", 0, "") GL("%[v]", "%[s]", 1024, "") GL("%[v]", "%[s]", 2048, "") GL("%[v]", "%[s]", 3072, "") M0RST
               : [sv] "=&s"(sv) : [v] "v"(v), [s] "s"(sb), [l] "s"(l) : "memory"); }
DI void dma6_sc1(unsigned v, const char* sb, unsigned l) { unsigned sv;
  asm volatile(M0SET GL("%[v]", "%[s]", 0, " sc1") GL("%[v]", "%[s]", 1024, " sc1") GL("%[v]", "%[s]", 2048, " sc1") M0ADD("3072")
               GL("%[w]", "%[s]", 0, " sc1") GL("%[w]", "%[s]", 1024, " sc1") GL("%[w]", "%[s]", 2048, " sc1") M0RST
               : [sv] "=&s"(sv) : [v] "v"(v), [w] "v"(v + 3072), [s] "s"(sb), [l] "s"(l) : "memory", "scc"); }
template <int N> DI void vm_bar() { asm volatile("s_waitcnt vmcnt(%0)\n\ts_barrier" ::"i"(N) : "memory"); }
DI void lds_bar() { asm volatile("s_waitcnt lgkmcnt(0)\n\ts_barrier" ::: "memory"); }
DI void raw_bar() { asm volatile("s_barrier" ::: "memory"); }
template <int N> DI void nap() { __builtin_amdgcn_s_sleep(N); asm volatile("" ::: "memory"); }
DI void drain() { asm volatile("s_waitcnt vmcnt(0)" ::: "memory"); }
#define CODE_ADDR(sym) ({ unsigned lo_, hi_; \
    asm volatile("s_getpc_b64 vcc\n\ts_add_u32 %0, vcc_lo, " sym "@rel32@lo+4\n\ts_addc_u32 %1, vcc_hi, " sym "@rel32@hi+12" : "=s"(lo_), "=s"(hi_) :: "vcc", "scc"); \
    (const unsigned*)(((unsigned long long)hi_ << 32) | lo_); })
#define SYM_PREP "_Z6k_prepPKtS0_S0_S0_S0_Pc"
#define SYM_MAIN "_Z6k_mainPcPt"
DI int wave_id() { return __builtin_amdgcn_readfirstlane(threadIdx.x >> 6); }
DI unsigned lds_addr(lptr p) { return (unsigned)(size_t)p; }

DI int kappa_to_i(int kap) {
  int ks = kap >> 5, g = (kap >> 3) & 3, e = kap & 7;
  if (ks < 16) return (2 * ks + (g >> 1)) * 20 + 8 * (e >> 2) + 4 * (g & 1) + (e & 3);
  return (8 * (ks - 16) + 2 * g + (e >> 2)) * 20 + 16 + (e & 3);
}

DI unsigned wo_frag(int nt, int ks) {
  const int ct = nt / 5, c5 = nt - 5 * ct, kh = ks < 16 ? ks >> 3 : (ks - 16) >> 1, i = ks < 16 ? ks & 7 : 8 + (ks & 1);
  const int a = kh ? (c5 >= 3 ? c5 - 3 : c5 + 2) : c5;
  return ((ct * 2 + kh) * 50 + 5 * i + a) * 1024;
}

constexpr int SLOT32 = 36864;
DI void dma_a(const unsigned* vm, const char* mb, unsigned l) { unsigned sv;
  asm volatile(M0SET GL("%[a0]", "%[m]", 0, " sc1") GL("%[a1]", "%[m]", 1024, " sc1") GL("%[a2]", "%[m]", 2048, " sc1") GL("%[a3]", "%[m]", 3072, " sc1") M0RST
               : [sv] "=&s"(sv) : [a0] "v"(vm[0]), [a1] "v"(vm[1]), [a2] "v"(vm[2]), [a3] "v"(vm[3]), [m] "s"(mb), [l] "s"(l) : "memory"); }
DI void dma_s(unsigned v0, unsigned v1, const char* sb, unsigned l) { unsigned sv;
  asm volatile(M0SET GL("%[v0]", "%[s]", 0, " sc1") GL("%[v0]", "%[s]", 1024, " sc1") GL("%[v1]", "%[s]", 2048, " sc1") GL("%[v1]", "%[s]", 3072, " sc1") M0RST
               : [sv] "=&s"(sv) : [v0] "v"(v0), [v1] "v"(v1), [s] "s"(sb), [l] "s"(l) : "memory"); }
DI void dma1_sc1(unsigned v, const char* sb, unsigned l) { unsigned sv;
  asm volatile(M0SET GL("%[v]", "%[s]", 0, " sc1") M0RST : [sv] "=&s"(sv) : [v] "v"(v), [s] "s"(sb), [l] "s"(l) : "memory"); }
DI void dma1(unsigned v, const char* sb, unsigned l) { unsigned sv;
  asm volatile(M0SET GL("%[v]", "%[s]", 0, "") M0RST : [sv] "=&s"(sv) : [v] "v"(v), [s] "s"(sb), [l] "s"(l) : "memory"); }

constexpr int L_I2 = 40 * 1056, LDS_PREP = L_I2 + 20 * 2064 + 1024;
__global__ void __launch_bounds__(512, 2) k_prep(const u16* __restrict__ x, const u16* __restrict__ wr, const u16* __restrict__ wi,
                                                 const u16* __restrict__ wqkv, const u16* __restrict__ wo, char* __restrict__ ws) {
  STAMP(0);
  if (blockIdx.x == 255 && threadIdx.x == 0) *(unsigned long long*)(ws + (60u << 20)) = __builtin_amdgcn_s_memrealtime();
  extern __shared__ __attribute__((aligned(16))) char smem[];
  const lptr lds = (lptr)smem;
  const int tid = threadIdx.x, lane = tid & 63, w = wave_id(), g = lane >> 4, r16 = lane & 15;
  const int job = blockIdx.x;
  if (__builtin_expect(job < 144, 1)) {
    const bool cols = job >= 64 && job < 124;
    const int cj = job - 64, cb = cj < 48 ? (cj & 48) + 2 * (cj & 7) + ((cj >> 3) & 1) : cj;
    u32x4 a1[3], m2[3][2]; u32x2 a1b[3];
#pragma unroll
    for (int T = 0; T < 3; T++) { a1[T] = *(const u32x4*)g_dt.a1[T][lane]; a1b[T] = *(const u32x2*)g_dt.a1b[T][lane]; }
#pragma unroll
    for (int i = 0; i < 3; i++)
#pragma unroll
      for (int T = 0; T < 2; T++) m2[i][T] = *(const u32x4*)g_dt.m2[(i < 2 || w < 4) ? w + 8 * i : 0][T][lane];
    SB();
    if (cols) {
#pragma unroll
      for (int i = 0; i < 5; i++) dma1((16 * (5 * w + i) + (lane >> 2)) * 3840 + cb * 64 + (lane & 3) * 16, (const char*)wqkv, lds_addr(lds) + (5 * w + i) * 1056);
    } else {
      const char* src = (const char*)(job < 64 ? x : wo);
      const int sl = 8 * (w & 3) + 4 * (w >> 2);
      src += (job < 64 ? rowof(32 * job + sl) : kappa_to_i(32 * (job - 124) + sl)) * 1280;
      const int st = (job < 64 ? 31 : 0) * 1280;
#pragma unroll
      for (int i = 0; i < 5; i++) dma1(1024 * i + lane * 16 + ((i == 4 || lane < 16 * i) ? i - 1 : i) * st, src, lds_addr(lds) + (5 * w + i) * 1024);
    }
    unsigned so[3][2]; float sc[2] = {1.f, 1.f};
    if (cols) {
#pragma unroll
      for (int m = 0; m < 2; m++) {
        const int c = 32 * cb + 16 * m + r16;
        const int which = c / 640, rem = c - which * 640, head = rem / 20, d = rem - head * 20, grp = d >> 2;
        const int T = grp < 4 ? (which == 2 ? 2 : 0) + (grp & 1) : (which == 1 ? 3 : 2), gs = grp < 4 ? (which == 1 ? 2 : 0) + (grp >> 1) : (which == 2 ? 3 : 2);
        if (which == 0) sc[m] = 0.22360679774997896f * 1.4426950408889634f;
#pragma unroll
        for (int i = 0; i < 3; i++) so[i][m] = WS_WQ + (((head >> 1) * 10 + 4 * (head & 1) + T) * 20 + w + 8 * i) * 1024 + (g * 16 + 4 * gs + (d & 3)) * 16;
      }
    } else {
#pragma unroll
      for (int i = 0; i < 3; i++) {
        const int G = w + 8 * i;
        const unsigned f0 = wo_frag(2 * G, job - 124), f1 = wo_frag(2 * G + 1, job - 124);
#pragma unroll
        for (int T = 0; T < 2; T++)
          so[i][T] = job < 64 ? WS_RB0 + (job * 641 + 32 * G + trow(T, r16)) * 64 + (job & 1) * 64 + g * 16 : WS_WOF + (r16 < 8 ? f0 : f1) + (g * 16 + (trow(T, r16) & 15)) * 16;
      }
    }
    if (w >= 4) { so[2][0] = WS_END; so[2][1] = WS_END; }
    asm volatile("" : "+v"(so[0][0]), "+v"(so[0][1]), "+v"(so[1][0]), "+v"(so[1][1]), "+v"(so[2][0]), "+v"(so[2][1]), "+v"(sc[0]), "+v"(sc[1]));
    const f32x4 z4 = {0.f, 0.f, 0.f, 0.f};
    const lptr i2 = lds + L_I2;
    asm volatile("s_waitcnt vmcnt(0)" : "+v"(a1[0]), "+v"(a1[1]), "+v"(a1[2]), "+v"(a1b[0]), "+v"(a1b[1]), "+v"(a1b[2]) :: "memory");
    MARK(0);
    f32x4 t1[4][3];
    if (cols) {
      raw_bar();
      const lptr src = lds + (4 * g + (r16 >> 2)) * 1056 + (w >> 1) * 256 + (w & 1) * 32 + (lane & 3) * 8, srb = src + (32 - 8 * (g >> 1)) * 1056;
#pragma unroll
      for (int i = 0; i < 4; i++) {
        const u32x2 f0 = ldtr(src + i * 64), f1 = ldtr(src + 16 * 1056 + i * 64), f2 = ldtr(srb + i * 64);
#pragma unroll
        for (int T = 0; T < 3; T++) t1[i][T] = mfma16(u32x4{a1b[T][0], a1b[T][1], 0u, 0u}, u32x4{f2[0], f2[1], 0u, 0u}, mfma16(a1[T], u32x4{f0[0], f0[1], f1[0], f1[1]}, z4));
      }
      const int xo = ((w >> 1) * 8) ^ (((r16 >> 1) & 3) * 16);
      const lptr dst = i2 + 2 * g * 2064 + (16 * (w & 1) + r16) * 64;
#pragma unroll
      for (int T = 0; T < 3; T++)
#pragma unroll
        for (int e = 0; e < 4; e++)
          if (T < 2 || g < 2) *(LDS_AS u32x2*)(dst + (8 * T + (e >> 1)) * 2064 + (xo ^ ((e & 1) * 32))) = u32x2{pk2(t1[0][T][e], t1[1][T][e]), pk2(t1[2][T][e], t1[3][T][e])};
    } else {
      const lptr src = lds + w * 5120 + lane * 8, srb = lds + w * 5120 + 1024 + (lane & 31) * 8;
#pragma unroll
      for (int i = 0; i < 4; i++) {
        const u32x2 f0 = ldtr(src + i * 1280), f1 = ldtr(src + i * 1280 + 512), f2 = ldtr(srb + i * 1280);
#pragma unroll
        for (int T = 0; T < 3; T++) t1[i][T] = mfma16(u32x4{f2[0], f2[1], 0u, 0u}, u32x4{a1b[T][0], a1b[T][1], 0u, 0u}, mfma16(u32x4{f0[0], f0[1], f1[0], f1[1]}, a1[T], z4));
      }
      const int xo = ((r16 & 1) * 32 + g * 8) ^ ((w & 1) * 32);
      const lptr dst = i2 + (r16 >> 1) * 2064 + w * 256;
#pragma unroll
      for (int i = 0; i < 4; i++)
#pragma unroll
        for (int T = 0; T < 3; T++)
          if (T < 2 || r16 < 8) *(LDS_AS u32x2*)(dst + T * 8 * 2064 + i * 64 + (xo ^ ((i >> 1) * 16))) = pk4(t1[i][T]);
    }
    lds_bar();
    MARK(1);
    const rsrc_t rw = mk_rsrc(ws, WS_END);
    f32x4 sq[2] = {z4, z4};
    u32x4 f[3][2];
#pragma unroll
    for (int i = 0; i < 3; i++) {
      const lptr fp = i2 + ((i < 2 || w < 4) ? w + 8 * i : 0) * 2064 + r16 * 64 + ((g ^ ((r16 >> 1) & 3)) * 16);
      f[i][0] = ld128(fp); f[i][1] = ld128(fp + 1024);
    }
    if (cols) {
#pragma unroll
      for (int i = 0; i < 3; i++)
#pragma unroll
        for (int m = 0; m < 2; m++) st128<AUXB>(rw, so[i][m], pk8(mfma16(m2[i][0], f[i][m], z4) * sc[m], mfma16(m2[i][1], f[i][m], z4) * sc[m]));
    } else {
#pragma unroll
      for (int i = 0; i < 3; i++)
#pragma unroll
        for (int T = 0; T < 2; T++) {
          const f32x4 d0 = mfma16(f[i][0], m2[i][T], z4), d1 = mfma16(f[i][1], m2[i][T], z4);
          st128<AUXB>(rw, so[i][T], pk8(d0, d1));
          const float k = ((i == 2 && w >= 4) || (i == 0 && T == 0 && tid < 64 && r16 == 1)) ? 0.f : 1.f;
          sq[0] += d0 * d0 * k; sq[1] += d1 * d1 * k;
        }
    }
    LDS_AS float* sl = (LDS_AS float*)(lds + LDS_PREP - 1024);
    if (job < 64) {
#pragma unroll
      for (int m = 0; m < 2; m++) {
#pragma unroll
        for (int e = 0; e < 4; e++) sq[m][e] = row16_sum(sq[m][e]);
        if (r16 == 0) *(LDS_AS f32x4*)(sl + w * 32 + 8 * g + 4 * m) = sq[m];
      }
    }
    MARK(2);
    if (job < 64) {
      lds_bar();
      if (tid < 32) {
        float v = 0.f;
#pragma unroll
        for (int i = 0; i < 8; i++) v += sl[i * 32 + tid];
        st32<AUXB>(rw, WS_RS1 + rpos(32 * job + tid) * 4, __float_as_uint(__builtin_amdgcn_rsqf((v + 640.f * 1e-5f) * (2048.f * 2048.f / 640.f))));
      }
    }
    return;
  }
  const int mi = job - 144;
  const rsrc_t rw = mk_rsrc(ws, WS_END);
  if (mi < 64) {
    const int m = mi >> 3, oc = mi & 7;
    const char* src = (const char*)((w >> 2) ? wi : wr) + (64 * (4 * m + (w & 3)) + 8 * oc) * 642;
    dma4(lane * 16, src, lds_addr(lds) + w * 5152);
    dma1(4096 + lane * 16, src, lds_addr(lds) + w * 5152 + 4096);
    if (lane == 0) dma1(5120, src, lds_addr(lds) + w * 5152 + 5120);
    vm_bar<0>();
    MARK(3);
    LDS_AS u16* L = (LDS_AS u16*)lds + (tid & 7) * 321;
#pragma unroll
    for (int it = 0; it < 5; it++) {
      const int c = (tid >> 3) + 64 * it, col = freq_of(c >> 4, c & 15);
      u32x4 o;
#pragma unroll
      for (int e = 0; e < 4; e++) o[e] = (unsigned)L[e * 2576 + col] | ((unsigned)L[(4 + e) * 2576 + col] << 16);
      st128<AUXB>(rw, WS_WF + c * 8192 + (m * 64 + 8 * oc + (tid & 7)) * 16, o);
    }
    MARK(4);
#pragma unroll
    for (int it = 0; it < 3; it++) st128<AUXB>(rw, WS_RB1 + ((it * 64 + mi) * 512 + tid) * 16, u32x4{EMPTY, EMPTY, EMPTY, EMPTY});
  } else if (mi < 84) {
    const char* src = (const char*)wo + kappa_to_i(32 * (mi - 64) + 8 * (w & 3) + 4 * (w >> 2)) * 1280;
    dma4(lane * 16, src, lds_addr(lds) + w * 5152);
    dma1(4096 + lane * 16, src, lds_addr(lds) + w * 5152 + 4096);
    vm_bar<0>();
#pragma unroll
    for (int i = 0; i < 5; i++) {
      const lptr a = lds + g * 5152 + (r16 >> 2) * 1280 + (5 * w + i) * 32 + (lane & 3) * 8;
      const u32x2 lo = ldtr(a), hi = ldtr(a + 4 * 5152);
      st128<AUXB>(rw, WS_WOP + wo_frag(5 * w + i, mi - 64) + lane * 16, u32x4{lo[0], lo[1], hi[0], hi[1]});
    }
  } else {
    const int i0 = (mi - 84) * 512 + tid;
    unsigned pf0, pf1;
    {
      const unsigned* cp = CODE_ADDR(SYM_PREP); const unsigned* cm = CODE_ADDR(SYM_MAIN);
      const unsigned* p = (const unsigned*)wqkv + min(14336 + i0 - 4096, 19199) * 32;
      if (mi < 92) p = tid < 72 ? cp + tid * 32 : tid < 152 ? (const unsigned*)&g_ct + (tid - 72) * 32 : cm + min(tid - 152, 259) * 32;
      asm volatile("global_load_dword %0, %1, off" : "=v"(pf0) : "v"(p) : "memory");
      asm volatile("global_load_dword %0, %1, off" : "=v"(pf1) : "v"((const unsigned*)wqkv + i0 * 32) : "memory");
    }
    nap<K1DELAY>();
    if (i0 < 6272) st128<AUXB>(rw, WS_FL + i0 * 16, u32x4{0u, 0u, 0u, 0u});
#pragma unroll
    for (int it = 0; it < 6; it++) { const int i = 98304 + i0 + it * 14336; if (i < (int)RBSZ / 16) st128<AUXB>(rw, WS_RB1 + i * 16, u32x4{EMPTY, EMPTY, EMPTY, EMPTY}); }
    asm volatile("s_waitcnt vmcnt(0)" : "+v"(pf0), "+v"(pf1));
  }
}

#define PINF(x) asm volatile("" : "+v"(x))
template <bool FIRST>
DI void col_phase(rsrc_t rw, lptr lds, int c0 SARG) {
  int lane = threadIdx.x & 63; PINF(lane);
  const int wv = wave_id(), w = wv & 3, q = wv >> 2, tid = w * 64 + lane, g = lane >> 4, r16 = lane & 15;
  const int c = c0 + q;
  const lptr img = lds + q * SLOT32;
  f32x4 xr[2], xi[2], rs[2]; u32x4 wf[2], xb[2];
  if (FIRST) {
#pragma unroll
    for (int t = 0; t < 2; t++) { xb[t] = gl128<0>(rw, WS_RB0 + ((tid >> 2) * 641 + ((tid >> 2) & 1) + 2 * c + t) * 64 + (tid & 3) * 16); rs[t] = asf(gl128<0>(rw, WS_RS1 + (1024 * t + 4 * tid) * 4)); }
  }
  u32x4 c64[2], s64[2], n64[2], c32[2], s32[2], n32[2];
  const u32x4* ct = (const u32x4*)&g_ct + lane;
  auto ld_const = [&]() {
  c64[0] = ct[(2 * w) * 64]; s64[0] = ct[(2 * w + 1) * 64]; c32[0] = ct[8 * 64]; s32[0] = ct[9 * 64];
#pragma unroll
  for (int t = 0; t < 2; t++) wf[t] = gl128<0>(rw, WS_WF + c * 8192 + ((4 * t + g) * 64 + 16 * w + r16) * 16);
  };
  if (FIRST) ld_const();
  SB();
  f32x4 t1c[2], t1s[2], t2c[2], t2s[2];
  {
    const float f1 = (float)(16 * w + r16) * (1.f / 2048.f), b1 = (float)(4 * g), f2 = (float)(16 * w + 4 * g) * (1.f / 2048.f), b2 = (float)r16;
#pragma unroll
    for (int t = 0; t < 2; t++) {
#pragma unroll
      for (int e = 0; e < 4; e++) {
        const float a1 = (b1 + (float)(16 * t + e)) * f1, a2 = (b2 + (float)(16 * t)) * (f2 + (float)e * (1.f / 2048.f));
        t1c[t][e] = __builtin_amdgcn_cosf(a1); t1s[t][e] = __builtin_amdgcn_sinf(-a1);
        t2c[t][e] = __builtin_amdgcn_cosf(a2); t2s[t][e] = __builtin_amdgcn_sinf(a2);
      }
      PINF(t1c[t]); PINF(t1s[t]); PINF(t2c[t]); PINF(t2s[t]);
    }
  }
  int wo_ = r16 * 128 + (((2 * w + (g >> 1)) ^ (r16 >> 1)) * 16) + (g & 1) * 8, ro_ = r16 * 128 + ((g ^ (r16 >> 1)) * 16), ro2 = ro_ ^ 64;
  PINF(wo_); PINF(ro_); PINF(ro2);
  SB();
  if (FIRST) {
#pragma unroll
    for (int t = 0; t < 2; t++) {
      xr[t] = f32x4{bflo(xb[0][2 * t]), bfhi(xb[0][2 * t]), bflo(xb[0][2 * t + 1]), bfhi(xb[0][2 * t + 1])};
      xi[t] = f32x4{bflo(xb[1][2 * t]), bfhi(xb[1][2 * t]), bflo(xb[1][2 * t + 1]), bfhi(xb[1][2 * t + 1])};
    }
  } else {
    MARK(23);
    drain();
    MARK(11);
    nap<YDELAY>();
    u32x2 sp[8];
    const unsigned so = WS_SSQ + (blockIdx.x & 7) * 32768 + (1024 * q + 4 * tid) * 2, xo = WS_R3 + ((tid >> 2) * 642 + 2 * c) * 64 + (tid & 3) * 16;
    bool bs = true, bx = true;
#pragma unroll 1
    for (int spins = 0; spins < (1 << 18); spins++) {
      if (bx) {
#pragma unroll
        for (int t = 0; t < 2; t++) { xr[t] = asf(gl128<16>(rw, xo + 64 * 642 * 64 * t)); xi[t] = asf(gl128<16>(rw, xo + 64 * 642 * 64 * t + 64)); }
      }
      if (bs) {
#pragma unroll
        for (int i = 0; i < 8; i++) sp[i] = gl64<16>(rw, so + i * 4096);
      }
      unsigned m = 0;
#pragma unroll
      for (int t = 0; t < 2; t++) m = umax4(umax4(m, asu(xr[t])), asu(xi[t]));
      bx = __builtin_amdgcn_ballot_w64(m == EMPTY) != 0;
      m = 0;
#pragma unroll
      for (int i = 0; i < 8; i++) m = max(max(m, sp[i][0]), sp[i][1]);
      bs = __builtin_amdgcn_ballot_w64(m == EMPTY) != 0;
      if (!bs && !bx) break;
      nap<YSLEEP>();
    }
    ld_const();
    SB();
    f32x4 r = {640.f * 1e-5f, 640.f * 1e-5f, 640.f * 1e-5f, 640.f * 1e-5f};
#pragma unroll
    for (int i = 0; i < 8; i++) r += f32x4{bflo(sp[i][0]), bfhi(sp[i][0]), bflo(sp[i][1]), bfhi(sp[i][1])};
#pragma unroll
    for (int e = 0; e < 4; e++) r[e] = __builtin_amdgcn_rsqf(r[e] * (2048.f * 2048.f / 640.f));
    LDS_AS f32x4* ex = (LDS_AS f32x4*)(lds + 2 * SLOT32) + tid;
    ex[q * 256] = r;
    lds_bar();
    rs[0] = ex[0]; rs[1] = ex[256];
  }
  MARK(FIRST ? 9 : 21);
#pragma unroll
  for (int t = 0; t < 2; t++) {
    u32x2 pb = pk4(xi[t] * rs[t]);
    if (c == 0) pb = u32x2{0u, 0u};
    *(LDS_AS u32x2*)(img + wo_ + t * 2048) = pk4(xr[t] * rs[t]);
    *(LDS_AS u32x2*)(img + 4096 + wo_ + t * 2048) = pb;
  }
  lds_bar();
  u32x4 ur[2][2], ui[2][2], br, bi;
  f32x4 ar[2], ai[2];
  const f32x4 z4 = {0.f, 0.f, 0.f, 0.f};
  auto rd_in = [&](int off) {
#pragma unroll
    for (int ks = 0; ks < 2; ks++)
#pragma unroll
      for (int t = 0; t < 2; t++) { const lptr p = img + off + (ks ? ro2 : ro_) + t * 2048; ur[t][ks] = ld128(p); ui[t][ks] = ld128(p + 4096); }
    SB();
  };
  auto cmul = [&](const f32x4* cc, const f32x4* sn) {
    br = pk8(ar[0] * cc[0] - ai[0] * sn[0], ar[1] * cc[1] - ai[1] * sn[1]);
    bi = pk8(ai[0] * cc[0] + ar[0] * sn[0], ai[1] * cc[1] + ar[1] * sn[1]);
  };
  rd_in(0);
  {
    const unsigned par = (lane & 1) ? 0x80008000u : 0u;
    c64[1] = c64[0] ^ par; s64[1] = s64[0] ^ par; n64[0] = s64[0] ^ 0x80008000u; n64[1] = s64[1] ^ 0x80008000u;
    c32[1] = c32[0] ^ 0x80000000u; s32[1] = s32[0] ^ 0x80000000u; n32[0] = s32[0] ^ 0x80008000u; n32[1] = s32[0] ^ 0x00008000u;
    PINF(c64[1]); PINF(s64[1]); PINF(n64[0]); PINF(n64[1]); PINF(c32[1]); PINF(s32[1]); PINF(n32[0]); PINF(n32[1]);
  }
  SB();
#pragma unroll
  for (int ks = 0; ks < 2; ks++) {
#pragma unroll
    for (int t = 0; t < 2; t++) { ar[t] = mfma16(ur[t][ks], c64[ks], ks ? ar[t] : z4); ai[t] = mfma16(ui[t][ks], c64[ks], ks ? ai[t] : z4); }
#pragma unroll
    for (int t = 0; t < 2; t++) { ar[t] = mfma16(ui[t][ks], s64[ks], ar[t]); ai[t] = mfma16(ur[t][ks], n64[ks], ai[t]); }
  }
  const f32x4 fa[2] = {asf(wf[0] << 16), asf(wf[1] << 16)}, fb[2] = {asf(wf[0] & 0xFFFF0000u), asf(wf[1] & 0xFFFF0000u)};
  cmul(t1c, t1s);
#pragma unroll
  for (int t = 0; t < 2; t++) { ar[t] = mfma16(c32[t], br, z4); ai[t] = mfma16(c32[t], bi, z4); }
#pragma unroll
  for (int t = 0; t < 2; t++) { ar[t] = mfma16(s32[t], bi, ar[t]); ai[t] = mfma16(n32[t], br, ai[t]); }
  cmul(fa, fb);
#pragma unroll
  for (int t = 0; t < 2; t++) { ar[t] = mfma16(br, c32[t], z4); ai[t] = mfma16(bi, c32[t], z4); }
#pragma unroll
  for (int t = 0; t < 2; t++) { ar[t] = mfma16(bi, n32[t], ar[t]); ai[t] = mfma16(br, s32[t], ai[t]); }
  cmul(t2c, t2s);
#pragma unroll
  for (int t = 0; t < 2; t++) {
    *(LDS_AS u32x2*)(img + 8192 + wo_ + t * 2048) = u32x2{br[2 * t], br[2 * t + 1]};
    *(LDS_AS u32x2*)(img + 12288 + wo_ + t * 2048) = u32x2{bi[2 * t], bi[2 * t + 1]};
  }
  lds_bar();
  rd_in(8192);
#pragma unroll
  for (int ks = 0; ks < 2; ks++) {
#pragma unroll
    for (int t = 0; t < 2; t++) { ar[t] = mfma16(c64[ks], ur[t][ks], ks ? ar[t] : xr[t]); ai[t] = mfma16(c64[ks], ui[t][ks], ks ? ai[t] : xi[t]); }
#pragma unroll
    for (int t = 0; t < 2; t++) { ar[t] = mfma16(n64[ks], ui[t][ks], ar[t]); ai[t] = mfma16(s64[ks], ur[t][ks], ai[t]); }
  }
  const unsigned ob = (FIRST ? WS_RB1 : WS_RB2) + (2 * c * RBP + 8 * tid) * 2;
  st128<16>(rw, ob, clean(pk8(ar[0], ar[1])));
  st128<16>(rw, ob + RBP * 2, c == 0 ? u32x4{0u, 0u, 0u, 0u} : clean(pk8(ai[0], ai[1])));
}

DI f32x16 mfma32(u32x4 a, u32x4 b, f32x16 c) {
  return __builtin_amdgcn_mfma_f32_32x32x16_bf16(__builtin_bit_cast(bf16x8, a), __builtin_bit_cast(bf16x8, b), c, 0, 0, 0);
}
DI f32x16 mfma32k8(u32x2 a, u32x2 b, f32x16 c) {
  return __builtin_amdgcn_mfma_f32_32x32x8bf16_1k(__builtin_bit_cast(s16x4, a), __builtin_bit_cast(s16x4, b), c, 0, 0, 0);
}
DI f32x16 scores(u32x4 k1, u32x2 k2, u32x4 q1, u32x2 q2) {
  f32x16 z;
#pragma unroll
  for (int i = 0; i < 16; i++) z[i] = 0.f;
  f32x16 s = mfma32(k1, q1, z);
  asm volatile("s_nop 11" : "+v"(s));
  return mfma32k8(k2, q2, s);
}
DI float max16(const f32x16& s) {
  float m = fmaxf(s[0], s[1]);
#pragma unroll
  for (int i = 2; i < 16; i++) m = fmaxf(m, s[i]);
  return m;
}
#define STR_(x) #x
#define STR(x) STR_(x)
#define KOF(t) ((((t)&7)>>1)*11264+((t)&1)*1280)
#define VOF(t) ((((t)&7)>>1)*11264+5120+((t)&1)*1536)
#define EXP(d, s) "v_exp_f32 v" #d ", v" #s "\n\t"
#define CVT(d, s) "v_cvt_pk_bf16_f32 v" #d ", v" #s ", v[" #s "+1]\n\t"
#define STEPA(t) \
  "v_mfma_f32_32x32x16_bf16 v[48:63], v[80:83], v[136:139], v[120:135]\n\t" \
  "ds_read_b128 v[80:83], v100 offset:" STR(KOF(t+1)) "\n\t" \
  EXP(64,32) EXP(65,33) CVT(78,68) CVT(79,70) EXP(66,34) EXP(67,35) \
  "v_mfma_f32_32x32x16_bf16 v[104:119], v[90:93], v[76:79], v[104:119]\n\t" \
  "ds_read_b64_tr_b16 v[90:91], v102 offset:" STR(VOF(t)+768) "\n\t" \
  "ds_read_b64_tr_b16 v[92:93], v102 offset:" STR(VOF(t)+1152) "\n\t" \
  EXP(68,36) EXP(69,37) CVT(72,64) CVT(73,66) EXP(70,38) EXP(71,39) \
  "v_mfma_f32_32x32x8_bf16 v[48:63], v[84:85], v[140:141], v[48:63]\n\t" \
  "ds_read_b64 v[84:85], v101 offset:" STR(KOF(t+1)) "\n\t" \
  EXP(64,40) EXP(65,41) CVT(74,68) CVT(75,70) EXP(66,42) EXP(67,43) \
  "s_waitcnt lgkmcnt(4)\n\t" \
  "v_mfma_f32_32x32x16_bf16 v[0:15], v[86:89], v[72:75], v[0:15]\n\t" \
  EXP(68,44) EXP(69,45) CVT(76,64) CVT(77,66) EXP(70,46) EXP(71,47)
#define STEPB(t) \
  "s_waitcnt lgkmcnt(0)\n\t" \
  "v_mfma_f32_32x32x16_bf16 v[32:47], v[80:83], v[94:97], v[16:31]\n\t" \
  EXP(64,48) EXP(65,49) CVT(78,68) CVT(79,70) EXP(66,50) EXP(67,51) \
  "v_mfma_f32_32x32x16_bf16 v[0:15], v[90:93], v[76:79], v[0:15]\n\t" \
  EXP(68,52) EXP(69,53) CVT(72,64) CVT(73,66) EXP(70,54) EXP(71,55) \
  "v_mfma_f32_32x32x8_bf16 v[32:47], v[84:85], v[98:99], v[32:47]\n\t" \
  EXP(64,56) EXP(65,57) CVT(74,68) CVT(75,70) EXP(66,58) EXP(67,59) \
  "v_mfma_f32_32x32x16_bf16 v[104:119], v[86:89], v[72:75], v[104:119]\n\t" \
  "ds_read_b64_tr_b16 v[86:87], v102 offset:" STR(VOF(t+1)) "\n\t" \
  "ds_read_b64_tr_b16 v[88:89], v102 offset:" STR(VOF(t+1)+384) "\n\t" \
  EXP(68,60) EXP(69,61) CVT(76,64) CVT(77,66) EXP(70,62) EXP(71,63)
#define PR(n) "s_setprio " #n "\n\t"
#define CHUNK(i) PR(1) STEPA(2*(i)) PR(0) STEPB(2*(i)) "s_barrier\n\t" PR(2) STEPA(2*(i)+1) STEPB(2*(i)+1)
#define Z4(a) "v_mov_b64 v[" #a ":" #a "+1], 0\n\tv_mov_b64 v[" #a "+2:" #a "+3], 0\n\t"

#define R4(b) "v[" STR(b) ":" STR(b) "+3]"
#define MM(d, a, b) "v_mfma_f32_16x16x32_bf16 " R4(d) ", " R4(a) ", " R4(b) ", " R4(d) "\n\t"
#define GM(B, m, i) MM(20*m+4*i, B+8+4*i, B+4*m)
#define RT(B, m, hf, k) "ds_read_b64_tr_b16 v[" STR(B+4*m+2*hf) ":" STR(B+4*m+2*hf+1) "], v10" #m " offset:" STR(k*8192+hf*1024) "\n\t"
#define RS(B, i, k) "ds_read_b128 " R4(B+8+4*i) ", v102 offset:" STR(i*2048+k*1024) "\n\t"
#define IF5_4(x) ""
#define IF5_5(x) x
#define GSTEP(N, B, C, k) GM(B,0,0) RT(C,0,0,k) RT(C,0,1,k) GM(B,1,0) RS(C,0,k) RT(C,1,0,k) GM(B,0,1) RT(C,1,1,k) RS(C,1,k) GM(B,1,1) RS(C,2,k) \
  GM(B,0,2) RS(C,3,k) GM(B,1,2) IF5_##N(RS(C,4,k)) GM(B,0,3) GM(B,1,3) IF5_##N(GM(B,0,4) GM(B,1,4)) MM(40,B,B)
#define GEMM_ASM(N) \
  Z4(0) Z4(4) Z4(8) Z4(12) Z4(16) Z4(20) Z4(24) Z4(28) Z4(32) Z4(36) Z4(40) "s_mov_b32 %[c], 0\n\ts_barrier\n\t" \
  RT(44,0,0,0) RT(44,0,1,0) RS(44,0,0) RT(44,1,0,0) RT(44,1,1,0) RS(44,1,0) RS(44,2,0) RS(44,3,0) IF5_##N(RS(44,4,0)) \
  "L_gemm_%=:\n\ts_add_u32 %[c], %[c], 1\n\ts_and_b32 %[t], %[c], 3\n\ts_cselect_b32 %[t], 1, -3\n\ts_mul_i32 %[t], %[t], 36864\n\ts_waitcnt lgkmcnt(0)\n\t" \
  GSTEP(N,44,72,1) \
  "v_add_u32 v100, %[t], v100\n\tv_add_u32 v101, %[t], v101\n\tv_add_u32 v102, %[t], v102\n\ts_waitcnt lgkmcnt(0)\n\ts_barrier\n\t" \
  GSTEP(N,72,44,0) \
  "s_cmp_lg_u32 %[c], 9\n\ts_cbranch_scc1 L_gemm_%=\n\ts_waitcnt lgkmcnt(0)\n\t" \
  GSTEP(N,44,72,1) "s_waitcnt lgkmcnt(0)\n\t" \
  MM(40,72,72) GM(72,0,0) GM(72,1,0) GM(72,0,1) GM(72,1,1) "s_nop 4\n\t" \
  "v_cndmask_b32 v44, v40, v41, %[m1]\n\tv_cndmask_b32 v45, v42, v43, %[m1]\n\tv_mov_b32 v46, 0x3727c5ac\n\tv_cndmask_b32 v44, v44, v45, %[m2]\n\t" \
  "v_fmac_f32 v46, 0x3acccccd, v44\n\tv_cmp_u_f32 vcc, v44, v44\n\tv_rsq_f32 v46, v46\n\ts_cbranch_vccz L_gok_%=\n\tds_write_b32 v106, v106\n\t" \
  "L_gok_%=:\n\tds_write_b32 v103, v46\n\ts_waitcnt lgkmcnt(0)\n\ts_barrier\n\t" \
  "ds_read_b32 v44, v104\n\tds_read_b32 v45, v105\n\tds_read_b32 v46, v106\n\t" \
  GM(72,0,2) GM(72,1,2) GM(72,0,3) GM(72,1,3) IF5_##N(GM(72,0,4) GM(72,1,4)) "s_waitcnt lgkmcnt(0)"
#define GEMM_OPS \
  : "={v[0:3]}"(acc[0][0]), "={v[4:7]}"(acc[0][1]), "={v[8:11]}"(acc[0][2]), "={v[12:15]}"(acc[0][3]), "={v[16:19]}"(acc[0][4]), \
    "={v[20:23]}"(acc[1][0]), "={v[24:27]}"(acc[1][1]), "={v[28:31]}"(acc[1][2]), "={v[32:35]}"(acc[1][3]), "={v[36:39]}"(acc[1][4]), \
    "={v44}"(rs[0]), "={v45}"(rs[1]), "={v46}"(vote), "+{v100}"(am0), "+{v101}"(am1), "+{v102}"(as), [c] "=&s"(c_), [t] "=&s"(t_) \
  : "{v103}"(a_w), "{v104}"(a_r[0]), "{v105}"(a_r[1]), "{v106}"(a_v), [m1] "s"(0xAAAAAAAAAAAAAAAAull), [m2] "s"(0xCCCCCCCCCCCCCCCCull) \
  : "memory", "scc", "vcc", "v40", "v41", "v42", "v43", "v47", "v48", "v49", "v50", "v51", "v52", "v53", "v54", "v55", "v56", "v57", "v58", "v59", "v60", \
    "v61", "v62", "v63", "v64", "v65", "v66", "v67", "v68", "v69", "v70", "v71", "v72", "v73", "v74", "v75", "v76", "v77", "v78", "v79", "v80", "v81", \
    "v82", "v83", "v84", "v85", "v86", "v87", "v88", "v89", "v90", "v91", "v92", "v93", "v94", "v95", "v96", "v97", "v98", "v99"

constexpr int HR = 4 * KVC;
constexpr int L_A = 2 * HR;
constexpr int L_EX = 15360;
constexpr int L_MISC = L_A + 70 * 1024;
constexpr int LDS_FUSED = L_MISC + 2048;

DI bool poll16(rsrc_t rw, unsigned off, unsigned need) {
  const int lane = threadIdx.x & 63;
#pragma unroll 1
  for (int spins = 0; spins < (1 << 20); spins++) {
    const unsigned v = gl32<16>(rw, off + (lane & 15) * 4);
    const unsigned got = (unsigned)__builtin_amdgcn_ballot_w64(v != 0) & 0xFFFFu;
    if ((got & need) == need) return true;
    nap<1>();
  }
  return false;
}

DI void poll_aux(rsrc_t rw, unsigned off) {
#pragma unroll 1
  for (int spins = 0; spins < (1 << 20); spins++) {
    const u32x4 v = gl128<16>(rw, off + (threadIdx.x & 63) * 16);
    if (!__builtin_amdgcn_ballot_w64(min(min(v[0], v[1]), min(v[2], v[3])) == 0u)) break;
    nap<4>();
  }
}
#define LAYER_VARS \
  extern __shared__ __attribute__((aligned(16))) char smem[]; \
  const lptr lds = (lptr)smem; \
  const rsrc_t rw = mk_rsrc(ws, WS_END); \
  int lane = threadIdx.x & 63; asm volatile("" : "+v"(lane)); \
  const int w = wave_id(), g = lane >> 4, r16 = lane & 15; \
  const int xcd = blockIdx.x & 7, bi = blockIdx.x >> 3, u = 8 * (xcd & 1) + (bi & 7), qt = 4 * (xcd >> 1) + (bi >> 3); \
  const int hf = u & 1, ct = u >> 1;     \
  constexpr unsigned FKV = WS_FL + (FINAL ? 32768 : 0), KV = WS_KV + (FINAL ? LAYER2 : 0), OB = WS_O + (FINAL ? LAYER2 : 0); \
  constexpr unsigned AUX = WS_AUX + (FINAL ? 1024 : 0), RB = FINAL ? WS_RB2 : WS_RB1; \
  constexpr int MK = FINAL ? 12 : 0; (void)MK; (void)hf; (void)AUX; (void)RB; (void)FKV; \
  LDS_AS unsigned* misc = (LDS_AS unsigned*)(lds + L_MISC + 1024) + (FINAL ? 8 : 0);

template <bool FINAL>
DI void loader(char* ws SARG) {
  LAYER_VARS
  {
    const int lw = w - 8;
    {
      unsigned vm[4];
#pragma unroll
      for (int r = 0; r < 4; r++) vm[r] = (4 * (4 * lw + r) + g) * (RBP * 2) + 256 * qt + (((r16 >> 1) ^ (g | ((r & 2) << 1))) * 32) + (r16 & 1) * 16 + 3072 - 1024 * r;
      const unsigned l0 = lds_addr(lds) + lw * 4096, v0 = 2 * lw * 20480 + lane * 16, v2 = ((8 + (lw >> 1)) * 20 + (lw & 1)) * 1024 + lane * 16;
      constexpr int CNT = FINAL ? 9 : 8;
      auto lds_w = [&](int s) {
        const char* sb = ws + WS_WQ + u * 204800 + s * 2048;
        dma_s(v0, v0 + 20480 - 2048, sb, l0 + (s & 3) * SLOT32 + 16384 + (lw >> 1) * 2048);
        if (FINAL) dma1_sc1(v2, sb, lds_addr(lds) + (s & 3) * SLOT32 + 24576 + (lw >> 1) * 10240 + (lw & 1) * 1024);
      };
      auto lds_a = [&](int s) {
        dma_a(vm, ws + RB - 3072 + s * (64 * RBP * 2), l0 + (s & 3) * SLOT32); };
#pragma unroll 1
      for (int att = 0;; att++) {
        const bool cb = att == 0 && blockIdx.x < 160;
        if (att == 0) { if (cb) raw_bar(); else nap<FINAL ? 100 : PFDELAY>(); }
        lds_w(0); lds_w(1);
        if (cb) raw_bar();
        lds_w(2); lds_w(3);
        if (att == 0) {
          if (FINAL && cb) raw_bar();
          raw_bar();
          if (cb) nap<XDELAY>(); else nap<FINAL ? SW2 : SW1>();
          MARK(MK + 7);
          const int pl = min(lane, 39);
          const unsigned po = RB + ((64 * (pl >> 2) + 16 * lw + 4 * (pl & 3) + 1) * RBP + 128 * qt + 126) * 2;
#pragma unroll 1
          for (int spins = 0; spins < (1 << 20); spins++) {
            const unsigned x0 = gl32<16>(rw, po), x1 = gl32<16>(rw, po + 2 * RBP * 2);
            if (!__builtin_amdgcn_ballot_w64(max(x0, x1) == EMPTY)) break;
            nap<XSLEEP>();
          }
        }
        drain();
        MARK(MK + 8);
#pragma unroll 1
        for (int s = 0; s < 4; s++) lds_a(s);
        vm_bar<12>(); vm_bar<8>();
        lds_a(4); lds_w(4);
        vm_bar<4 + CNT>();
#pragma unroll 1
        for (int s = 5; s < 10; s++) { lds_a(s); lds_w(s); vm_bar<2 * CNT>(); }
        vm_bar<CNT>();
        poll_aux(rw, AUX);
        vm_bar<0>();
        raw_bar();
        if (XNL || att || !*(volatile LDS_AS unsigned*)(misc + 2)) break;
      }
    }
    MARK(MK + 0);
    const int hh = lw >> 1, hp = lw & 1;
    const unsigned kvv = KV + (2 * u + hh) * 16 * KVC + hp * 5120 + lane * 16;
    const unsigned lkv = lds_addr(lds) + hh * HR + hp * 5120;
    const unsigned wv = (FINAL ? WS_WOP : WS_WOF) + (ct * 2 + hh) * 51200 + lane * 16;
    const unsigned myfl = FKV + (u * 16 + qt) * 128;
    raw_bar();
    MARK(MK + 1);
    {
      u32x4 v[6];
#pragma unroll
      for (int i = 0; i < 6; i++) v[i] = ld128(lds + hh * HR + hp * 5120 + lane * 16 + i * 1024);
#pragma unroll
      for (int i = 0; i < 6; i++) st128<16>(rw, kvv + qt * KVC + i * 1024, clean(v[i]));
    }
    drain();
    unsigned cnt = 0;
    if (lane == 0) cnt = __hip_atomic_fetch_add(misc, 1u, __ATOMIC_RELAXED, __HIP_MEMORY_SCOPE_WORKGROUP);
    if (__builtin_amdgcn_readfirstlane(cnt) == 3 && lane < 16) st32<16>(rw, FKV + (u * 16 + lane) * 128 + qt * 4, 1u);
    MARK(MK + 2);
    auto ldkv = [&](int seq) {
      const int ch = seq == 0 ? qt : seq < 13 ? ((qt & 12) + 3 + seq) & 15 : (qt & 12) | ((qt + seq) & 3);
      dma6_sc1(kvv + ch * KVC, ws, lkv + (seq & 3) * KVC);
    };
    auto fetch = [&](int seq) {
      const unsigned src = kvv + ((((qt & 12) + 3 + seq) & 15)) * KVC;
      u32x4 v[6];
#pragma unroll 1
      for (int spins = 0; spins < (1 << 18); spins++) {
#pragma unroll
        for (int i = 0; i < 6; i++) v[i] = gl128<16>(rw, src + i * 1024);
        unsigned m = 0;
#pragma unroll
        for (int i = 0; i < 6; i++) m = umax4(m, v[i]);
        if (!__builtin_amdgcn_ballot_w64(m == EMPTY)) break;
        nap<2>();
      }
#pragma unroll
      for (int i = 0; i < 6; i++) *(LDS_AS u32x4*)(lds + hh * HR + hp * 5120 + seq * KVC + lane * 16 + i * 1024) = v[i];
    };
#pragma unroll 1
    for (int attempt = 0;; attempt++) {
      int c = -2;
      if (attempt) { ldkv(0); vm_bar<0>(); }
      else {
        fetch(1); MARK(MK + 3); lds_bar();
        MARK(MK + 4);
        fetch(2); poll16(rw, myfl, 12u << (((qt & 12) + 4) & 15)); ldkv(3); MARK(MK + 5); lds_bar();
        c = 1;
      }
#pragma unroll 1
      for (; c < 13; c++) {
        if (c == 2 && !attempt) poll16(rw, myfl, 0xFFFFu);
        if (!FINAL && !attempt) {
          if (c == 10) {
            unsigned n = 0;
            if (lane == 0) n = __hip_atomic_fetch_add(misc + 3, 1u, __ATOMIC_RELAXED, __HIP_MEMORY_SCOPE_WORKGROUP);
            if (__builtin_amdgcn_readfirstlane(n) == 3 && lane < 32) st32<16>(rw, WS_FL + 65536 + (lane < 16 ? (u * 16 + lane) * 128 + qt * 4 : (r16 * 16 + qt) * 128 + 64 + u * 4), 1u);
          }
        }
        if (c >= 2) { const int e = min(4 * (c - 2) + 2 * hp, 33); dma2(wv + e * 1024, ws, lds_addr(lds) + L_A + (hh * 35 + e) * 1024); }
        ldkv(c + 3);
        if (c == 0) vm_bar<12>();
        if (!FINAL && !attempt && c >= 2 && c < 9) {
          const u32x4 e = {EMPTY, EMPTY, EMPTY, EMPTY};
          const unsigned o = OB + LAYER2 + (((c - 2) * 256 + blockIdx.x) * 4 + lw) * 2048 + lane * 16;
          st128<16>(rw, o, e); st128<16>(rw, o + 1024, e);
          st128<16>(rw, ((qt & 3) < 2 && c < 8) ? kvv + LAYER2 + qt * KVC + (c - 2) * 1024 : WS_END, e);
          st128<16>(rw, c < 5 ? OB + ((4 * qt + lw) * 32 + 2 * u) * 1280 + min((c - 2) * 1024, 1536) + lane * 16 : WS_END, e);
          if (c == 2) vm_bar<12>(); else vm_bar<16>();
        } else if (c >= 2) vm_bar<8>(); else if (c >= 0) vm_bar<6>();
      }
      if (!FINAL && !attempt) poll16(rw, myfl + 65536 + hp * 64, 0xFFFFu);
      vm_bar<0>();
      dma4(wv + (35 + 7 * hp) * 1024, ws, lds_addr(lds) + hh * HR + 7 * hp * 1024);
      dma4(wv + (39 + 7 * hp) * 1024, ws, lds_addr(lds) + hh * HR + (4 + 7 * hp) * 1024);
      raw_bar();
      vm_bar<0>();
      if (attempt || !*(volatile LDS_AS unsigned*)(misc + 1)) break;
      raw_bar();
    }
    if (!FINAL) { raw_bar(); raw_bar(); }
  }
}

template <bool FINAL>
DI void compute(char* ws, u16* out SARG) {
  LAYER_VARS
  const int wm = w & 3, wn = w >> 2, h = lane >> 5;
  if (blockIdx.x < 160) col_phase<!FINAL>(rw, lds, 2 * blockIdx.x SPASS);
  else if (!FINAL) {
    for (int pc = (blockIdx.x - 160) * 512 + threadIdx.x; pc < 640 * 80; pc += 96 * 512) {
      const int n = pc % 640, kq = pc / 640;
      const int u_ = n / 40, rn = n - 40 * u_, hn = rn / 20, dn = rn - 20 * hn, gq = dn >> 2;
      const int T = gq < 4 ? 8 + hn : 4 * hn + 3, gs = gq < 4 ? gq : 3;
      float v[8];
#pragma unroll
      for (int e = 0; e < 4; e++) {
        const float a = (float)((freq_of(kq >> 2, 4 * (kq & 3) + e) * n) % 640) * (1.f / 640.f);
        v[2 * e] = 0.055901699437494742f * __builtin_amdgcn_cosf(a);
        v[2 * e + 1] = -0.055901699437494742f * __builtin_amdgcn_sinf(a);
      }
      if (kq == 0) { v[0] = (n & 1) ? -0.039528470752104741f : 0.039528470752104741f; v[1] = 0.039528470752104741f; }
      st128<16>(rw, WS_WQ + ((u_ * 10 + T) * 20 + (kq >> 2)) * 1024 + ((kq & 3) * 16 + 4 * gs + (dn & 3)) * 16,
                  u32x4{pk2(v[0], v[1]), pk2(v[2], v[3]), pk2(v[4], v[5]), pk2(v[6], v[7])});
    }
    nap<FILLDELAY>();
#pragma unroll
    for (int it = 0; it < 4; it++) {
      const int k = (it * 96 + blockIdx.x - 160) * 512 + threadIdx.x, b = k / 1408;
      st128<16>(rw, k < 128 * 1408 ? WS_KV + b * 4 * KVC + (k - b * 1408) * 16 : WS_END, u32x4{EMPTY, EMPTY, EMPTY, EMPTY});
    }
  }
  MARK(MK + 10);
  lds_bar();
  {
    drain();
    unsigned n = 0;
    if (lane == 0) n = __hip_atomic_fetch_add(misc + 4, 1u, __ATOMIC_RELAXED, __HIP_MEMORY_SCOPE_WORKGROUP);
    if (n == 7) st32<16>(rw, AUX + blockIdx.x * 4, 1u);
  }

  {
    constexpr int NT = FINAL ? 5 : 4;
    f32x4 acc[2][5]; float rs[2];
    unsigned mo[2], a0[2], a1[2], a2[2], a_r[2];
#pragma unroll
    for (int m = 0; m < 2; m++) {
      const int q = r16 >> 2, pr = 16 * (m ^ wn) + r16;
      mo[m] = lds_addr(lds) + (8 * g + q) * 256 + (((2 * wm + (m ^ wn)) ^ (q | ((g & 1) << 2))) * 32) + (lane & 3) * 8;
      const unsigned qb = L_A + (wn * 4 + wm) * 1280, kb = wn * HR + wm * 1280, vr = wn * HR + 5120 + (32 * wm + pr) * 48 + (g == 3 ? 32 : g * 16);
      a0[m] = ((g >> 1) ? kb : qb) + (g & 1) * 512 + pr * 16;
      a1[m] = g == 2 ? qb + 1024 + pr * 8 : vr; a2[m] = g == 2 ? kb + 1024 + pr * 8 : vr + 8;
      a_r[m] = lds_addr(lds) + L_MISC + (32 * wm + pr) * 4;
      asm volatile("" : "+v"(a0[m]), "+v"(a1[m]), "+v"(a2[m]));
    }
    const unsigned a_w = a_r[0] + (g == (r16 >> 2) ? 0 : 512), a_v = lds_addr((lptr)(misc + 2));
#pragma unroll 1
    for (int att = 0;; att++) {
      unsigned am0 = mo[0], am1 = mo[1], as = lds_addr(lds) + 16384 + wn * 10240 + lane * 16, vote; int c_, t_;
      if (FINAL) asm volatile(GEMM_ASM(5) GEMM_OPS); else asm volatile(GEMM_ASM(4) GEMM_OPS);
      if (XNL || att || !__builtin_amdgcn_readfirstlane(vote)) break;
    }
    SB();
    MARK(MK + 0);
#pragma unroll
    for (int m = 0; m < 2; m++) *(LDS_AS u32x4*)(lds + a0[m]) = pk8(acc[m][0] * rs[m], acc[m][1] * rs[m]);
    asm volatile("s_nop 1" : "+v"(acc[0][2]), "+v"(acc[1][2]), "+v"(acc[0][3]), "+v"(acc[1][3]), "+v"(acc[0][NT - 1]), "+v"(acc[1][NT - 1]) :: "memory");
#pragma unroll
    for (int m = 0; m < 2; m++) {
      u32x2 hi = pk4(acc[m][3] * rs[m]);
      if (g == 3) hi = u32x2{0x3F80u, 0u};
      *(LDS_AS u32x2*)(lds + a1[m]) = pk4(acc[m][2] * rs[m]);
      *(LDS_AS u32x2*)(lds + a2[m]) = hi;
    }
    lds_bar();
    if (FINAL) {
      const unsigned x4 = WS_X4 + (((u * 16 + qt) * 4 + wm) * 2 + wn) * 1280;
      const u32x2 p0 = pk4(acc[0][NT - 1]), p1 = pk4(acc[1][NT - 1]), q0 = pk4(acc[0][3]), q1 = pk4(acc[1][3]);
      st128<16>(rw, x4 + lane * 16, clean(u32x4{p0[0], p0[1], p1[0], p1[1]}));
      if (g == 3) st128<16>(rw, x4 + 1024 + r16 * 16, clean(u32x4{q0[0], q0[1], q1[0], q1[1]}));
    }
  }
  MARK(MK + 1);
  const int head = 2 * u + wn, kh = w & 1;
  const lptr ring = lds + wn * HR;
  const int pblk = 128 * qt + 64 * hf + 32 * (wm >> 1);
  const unsigned r3t = WS_R3 + (64 * (wm & 1) + (pblk >> 5)) * (642 * 64);
  u32x2 x4v[3]; float dc = 0.f;
  unsigned os = OB + ((4 * qt + wm) * 32 + head) * 1280 + (((lane << 2) & 16) + ((lane >> 1) & 12) + (lane & 3)) * 8;
  unsigned o1 = OB + ((pblk >> 5) * 32 + 16 * wn) * 1280 + (g >> 1) * 1280 + (g & 1) * 512 + (wm & 1) * 256 + r16 * 16;
  unsigned o2 = OB + ((pblk >> 5) * 32 + 16 * wn) * 1280 + 2 * g * 1280 + 1024 + (wm & 1) * 128 + r16 * 8;
  u32x4 o3 = {0u, 0u, 0u, 0u};
  if (!FINAL) {
#pragma unroll
    for (int i = 0; i < 3; i++) {
      const int j = 80 * ct + 48 * wn + 16 * (wn ? (i & 1) : i) + r16;
      o3[i] = (j == 1 ? WS_RB0 + ((pblk >> 5) * 641 + ((pblk >> 5) & 1) + 1) * 64 : WS_RB1 + (j * RBP + pblk) * 2) + 16 * g + 8 * (wm & 1);
    }
  } else {
    o3[3] = r3t + 64 + r16 * 4;
#pragma unroll
    for (int i = 0; i < 3; i++) {
      const int cg = 12 * wn + 4 * ((i == 2 && wn) ? 0 : i) + g, pw = cg / 5, gq = cg - 5 * pw;
      o3[i] = WS_X4 + ((((2 * ct + (pw >> 1)) * 16 + qt) * 4 + 2 * hf + (wm >> 1)) * 2 + (pw & 1)) * 1280 + (gq * 16 + (trow(wm & 1, r16) & 15)) * 16 + ((((r16 >> 3) ^ pw) & 1) * 8);
    }
  }
  asm volatile("" : "+v"(os), "+v"(o1), "+v"(o2), "+v"(o3));
  f32x4 r[3];
  {
    const lptr qpa = lds + L_A + (wn * 4 + wm) * 1280, qpb = lds + L_A + (wn * 4 + (wm ^ 1)) * 1280;
    const u32x4 qa1 = ld128(qpa + lane * 16), qb1 = ld128(qpb + lane * 16);
    u32x2 qa2 = ld64(qpa + 1024 + (lane & 31) * 8), qb2 = ld64(qpb + 1024 + (lane & 31) * 8);
    if (h) { qa2 = u32x2{0u, 0u}; qb2 = qa2; }
    const unsigned ka = lds_addr(ring) + kh * 2560 + lane * 16, kb = lds_addr(ring) + kh * 2560 + 1024 + (lane & 31) * 8;
    const unsigned va = lds_addr(ring) + kh * 3072 + (4 * h + (r16 >> 2)) * 48 + ((g & 1) ? ((lane & 3) ? 40 : 32) : (lane & 3) * 8);
    float ma, mb;
    {
      const u32x4 k1 = ld128(ring + lane * 16); const u32x2 k2 = ld64(ring + 1024 + (lane & 31) * 8);
      ma = max16(scores(k1, k2, qa1, qa2)); mb = max16(scores(k1, k2, qb1, qb2));
    }
    for (int attempt = 0;; attempt++) {
      ma = fmaxf(ma, __shfl_xor(ma, 32)); mb = fmaxf(mb, __shfl_xor(mb, 32));
      f32x16 cia, cib, oa, ob;
#pragma unroll
      for (int i = 0; i < 16; i++) { cia[i] = -ma; cib[i] = -mb; }
      int cnt = 4;
      MARK(MK + 2);
      asm volatile(
          "ds_read_b128 v[80:83], v100\n\t"
          "ds_read_b64 v[84:85], v101\n\t"
          "ds_read_b64_tr_b16 v[86:87], v102 offset:5120\n\t"
          "ds_read_b64_tr_b16 v[88:89], v102 offset:5504\n\t"
          Z4(0) Z4(68) Z4(76) Z4(90)
          "s_waitcnt lgkmcnt(0)\n\t"
          "v_mfma_f32_32x32x16_bf16 v[32:47], v[80:83], v[94:97], v[16:31]\n\t"
          Z4(4) Z4(8) Z4(12) Z4(104) Z4(108) Z4(112) Z4(116)
          "v_mfma_f32_32x32x8_bf16 v[32:47], v[84:85], v[98:99], v[32:47]\n\ts_nop 9\n\t"
          "L_attn_%=:\n\t"
          CHUNK(0) CHUNK(1) CHUNK(2) CHUNK(3)
          "s_sub_u32 %[cnt], %[cnt], 1\n\ts_cmp_lg_u32 %[cnt], 0\n\ts_cbranch_scc1 L_attn_%=\n\t"
          "s_setprio 0\n\t" CVT(78,68) CVT(79,70) "s_waitcnt lgkmcnt(0)\n\ts_nop 1\n\t"
          "v_mfma_f32_32x32x16_bf16 v[104:119], v[90:93], v[76:79], v[104:119]\n\ts_nop 15\n\t"
          : "={v[0:15]}"(oa), "={v[104:119]}"(ob), [cnt] "+s"(cnt)
          : "{v[16:31]}"(cia), "{v[120:135]}"(cib), "{v[94:97]}"(qa1), "{v[98:99]}"(qa2), "{v[136:139]}"(qb1), "{v[140:141]}"(qb2),
            "{v100}"(ka), "{v101}"(kb), "{v102}"(va)
          : "memory", "scc", "v32", "v33", "v34", "v35", "v36", "v37", "v38", "v39", "v40", "v41", "v42", "v43", "v44", "v45", "v46", "v47",
            "v48", "v49", "v50", "v51", "v52", "v53", "v54", "v55", "v56", "v57", "v58", "v59", "v60", "v61", "v62", "v63", "v64", "v65",
            "v66", "v67", "v68", "v69", "v70", "v71", "v72", "v73", "v74", "v75", "v76", "v77", "v78", "v79", "v80", "v81", "v82", "v83",
            "v84", "v85", "v86", "v87", "v88", "v89", "v90", "v91", "v92", "v93");
      MARK(MK + 3);
      LDS_AS f32x4* ex = (LDS_AS f32x4*)(ring + L_EX + wm * 3072) + lane;
#pragma unroll
      for (int i = 0; i < 3; i++) ex[i * 64] = f32x4{ob[4 * i], ob[4 * i + 1], ob[4 * i + 2], ob[4 * i + 3]};
      f32x4 cs = {0.f, 0.f, 0.f, 0.f};
#pragma unroll
      for (int i = 0; i < 3; i++) cs += f32x4{oa[4 * i], oa[4 * i + 1], oa[4 * i + 2], oa[4 * i + 3]} + f32x4{ob[4 * i], ob[4 * i + 1], ob[4 * i + 2], ob[4 * i + 3]};
      if (!(fabsf(cs[0] + cs[1] + cs[2] + cs[3]) < INFINITY) || (h && !(oa[8] + ob[8] < 0x1p60f))) misc[1] = 1u;
      lds_bar();
      const unsigned vote = *(volatile LDS_AS unsigned*)(misc + 1);
      LDS_AS f32x4* ey = (LDS_AS f32x4*)(ring + L_EX + (wm ^ 1) * 3072) + lane;
#pragma unroll
      for (int i = 0; i < 3; i++) r[i] = ey[i * 64] + f32x4{oa[4 * i], oa[4 * i + 1], oa[4 * i + 2], oa[4 * i + 3]};
      if (attempt || !vote) break;
      lds_bar();
      ma = -INFINITY; mb = -INFINITY;
      for (int t = 0; t < 64; t++) {
        const unsigned kb_ = KV + (head * 16 + (t >> 2)) * KVC + (t & 3) * 1280;
        const u32x4 k1 = gl128<16>(rw, kb_ + lane * 16); const u32x2 k2 = gl64<16>(rw, kb_ + 1024 + (lane & 31) * 8);
        ma = fmaxf(ma, max16(scores(k1, k2, qa1, qa2))); mb = fmaxf(mb, max16(scores(k1, k2, qb1, qb2)));
      }
      raw_bar();
    }
  }
  {
    const unsigned lb = __float_as_uint(r[2][0]);
    const float il = __builtin_amdgcn_rcpf(__uint_as_float(__builtin_amdgcn_permlane32_swap(lb, lb, false, false)[1]));
    const unsigned sl8 = os & 255;
    st128<16>(rw, os + sl8 + h * 512, clean(pk8(r[0] * il, r[1] * il)));
    const u32x4 t = clean(pk8(r[2] * il, r[2] * il));
    st64<16>(rw, h ? WS_END : os + 1024, u32x2{t[0], t[1]});
  }
  MARK(MK + 4);
  const lptr wa = lds + L_A + wn * 35 * 1024 + lane * 16, wb = ring + lane * 16;
  constexpr int NPRE = 15;
  u32x4 bf[50];
  f32x4 acc[5] = {};
#pragma unroll
  for (int j = 0; j < NPRE; j++) bf[j] = ld128(wa + j * 1024);
#pragma unroll
  for (int j = 0; j < 5; j++) asm volatile("" : "+v"(acc[j]));
  if (FINAL) dc = __uint_as_float(gl32<16>(rw, o3[3]));
#pragma unroll 1
  for (int spins = 0; spins < (1 << 18); spins++) {
#pragma unroll
    for (int i = 0; i < 3; i++) x4v[i] = gl64<16>(rw, o3[i]);
    if (!FINAL || !__builtin_amdgcn_ballot_w64(umax4(max(x4v[0][0], x4v[0][1]), u32x4{x4v[1][0], x4v[1][1], x4v[2][0], x4v[2][1]}) == EMPTY)) break;
    nap<8>();
  }
  drain();
  nap<ODELAY>();
  MARK(MK + 5);
  {
    u32x4 af[10];
    auto ldf = [&](int i) {
      if (i < 8) af[i] = __builtin_amdgcn_raw_buffer_load_b128(rw, o1, i * 2 * 1280, 16);
      else { const u32x2 a = __builtin_amdgcn_raw_buffer_load_b64(rw, o2, (i - 8) * 8 * 1280, 16), b = __builtin_amdgcn_raw_buffer_load_b64(rw, o2, ((i - 8) * 8 + 1) * 1280, 16);
             af[i] = u32x4{a[0], a[1], b[0], b[1]}; }
    };
#pragma unroll
    for (int i = 0; i < 10; i++) ldf(i);
#pragma unroll 1
    for (int spins = 0; spins < (1 << 18); spins++) {
      unsigned m = 0;
#pragma unroll
      for (int i = 0; i < 10; i++) m = umax4(m, af[i]);
      if (!__builtin_amdgcn_ballot_w64(m == EMPTY)) break;
      nap<RSLEEP>();
#pragma unroll
      for (int i = 0; i < 10; i++) if (__builtin_amdgcn_ballot_w64(umax4(0, af[i]) == EMPTY)) ldf(i);
    }
    MARK(MK + 8);
#pragma unroll
    for (int i = 0; i < 10; i++) asm volatile("" : "+v"(af[i]));
    asm volatile("s_nop 1" : "+v"(acc[0]), "+v"(acc[1]), "+v"(acc[2]), "+v"(acc[3]), "+v"(acc[4]));
    SB();
#pragma unroll
    for (int i = 0; i < 10; i++) {
#pragma unroll
      for (int a = 0; a < 5; a++) {
        if (FINAL) mfma16v(acc[a], bf[5 * i + a], af[i]); else mfma16v(acc[a], af[i], bf[5 * i + a]);
        const int j = 5 * i + a + NPRE; if (j < 50) bf[j] = ld128(j < 35 ? wa + j * 1024 : wb + (j - 35) * 1024);
        SB();
      }
    }
  }
  asm volatile("s_nop 7\n\ts_nop 1" : "+v"(acc[0]), "+v"(acc[1]), "+v"(acc[2]), "+v"(acc[3]), "+v"(acc[4]) :: "memory");
  MARK(MK + 6);
  {
    LDS_AS f32x4* ex = (LDS_AS f32x4*)(ring + L_EX + 12288 + wm * 3072) + lane;
    LDS_AS f32x4* ey = (LDS_AS f32x4*)(lds + (wn ^ 1) * HR + L_EX + 12288 + wm * 3072) + lane + 64 * wn;
    ex[0] = acc[2]; ex[64] = acc[3]; ex[128] = acc[4];
    dc *= 0.039528470752104741f;
#pragma unroll
    for (int i = 0; i < 3; i++) { acc[i] += f32x4{bflo(x4v[i][0]), bfhi(x4v[i][0]), bflo(x4v[i][1]), bfhi(x4v[i][1])}; if (FINAL) acc[i] += dc; }
    lds_bar();
#pragma unroll
    for (int i = 0; i < 3; i++) acc[i] += ey[64 * i];
  }
  if (FINAL) {
    const rsrc_t ro = mk_rsrc(out, 2048 * 640 * 2);
    const unsigned op = (rowof(pblk + trow(wm & 1, r16)) * 640 + 80 * ct + 48 * wn + 4 * g) * 2;
#pragma unroll
    for (int i = 0; i < 3; i++)
      if (i < 2 || wn == 0) st64<OUTAUX>(ro, op + 32 * i, pk4(acc[i]));
  } else {
    f32x4 sq = {0.f, 0.f, 0.f, 0.f};
#pragma unroll
    for (int i = 0; i < 3; i++)
      if (i < 2 || wn == 0) {
        const int j = 80 * ct + 48 * wn + 16 * i + r16;
        st128<16>(rw, r3t + j * 64 + g * 16, clean(asu(acc[i])));
        if (j != 1) sq += acc[i] * acc[i];
      }
#pragma unroll
    for (int e = 0; e < 4; e++) sq[e] = row16_sum(sq[e]);
    LDS_AS float* sl = (LDS_AS float*)(lds + L_MISC);
    if (r16 == 0) *(LDS_AS f32x4*)(sl + wn * 64 + 32 * (wm & 1) + 16 * (wm >> 1) + 4 * g) = sq;
    lds_bar();
    if (w == 0) {
      LDS_AS f32x4* s4 = (LDS_AS f32x4*)sl + 2 * (lane & 7);
      st128<16>(rw, WS_SSQ + (lane >> 3) * 32768 + (ct * 2048 + 1024 * ((lane >> 2) & 1) + 64 * qt + 32 * hf + 8 * (lane & 3)) * 2, clean(pk8(s4[0] + s4[16], s4[1] + s4[17])));
    }
  }
  MARK(MK + 7);
}

__global__ void __launch_bounds__(768) k_main(char* ws, u16* out) {
  STAMP(1);
  const unsigned long long tm0 = __builtin_amdgcn_s_memrealtime();
  if (__builtin_expect(wave_id() >= 8, 1)) {
    extern __shared__ __attribute__((aligned(16))) char smem[];
    __builtin_amdgcn_s_setprio(3);
    if (threadIdx.x < 528) ((LDS_AS unsigned*)((lptr)smem + L_MISC + 1024))[threadIdx.x - 512] = 0u;
    asm volatile("s_waitcnt lgkmcnt(0)" ::: "memory");
    loader<false>(ws SPASS);
    loader<true>(ws SPASS);
  } else {
    compute<false>(ws, out SPASS);
    const unsigned long long tm1 = __builtin_amdgcn_s_memrealtime();
    compute<true>(ws, out SPASS);
    if (blockIdx.x == 0 && threadIdx.x < 64) {
      const unsigned long long te = __builtin_amdgcn_s_memrealtime(), tk = *(volatile unsigned long long*)(ws + (60u << 20));
      const unsigned long long pad = (tk & 0) + 1000;
      while (__builtin_amdgcn_s_memrealtime() - te < pad) __builtin_amdgcn_s_sleep(1);
    }
  }
}

#define STOPAT(n)
extern "C" void kernel_launch(void* const* d_in, const int* in_sizes, int n_in, void* d_out, int out_size, void* d_ws, size_t ws_size,
                              hipStream_t stream) {
  const u16* x = (const u16*)d_in[0];
  const u16* wr = (const u16*)d_in[1];
  const u16* wi = (const u16*)d_in[2];
  const u16* wqkv = (const u16*)d_in[4];
  const u16* wo = (const u16*)d_in[5];
  char* ws = (char*)d_ws;
  (void)hipFuncSetAttribute((const void*)k_prep, hipFuncAttributeMaxDynamicSharedMemorySize, LDS_PREP);
  (void)hipFuncSetAttribute((const void*)k_main, hipFuncAttributeMaxDynamicSharedMemorySize, LDS_FUSED);
  k_prep<<<256, 512, LDS_PREP, stream>>>(x, wr, wi, wqkv, wo, ws);
  STOPAT(1)
  k_main<<<256, 768, LDS_FUSED, stream>>>(ws, (u16*)d_out);
}
```

```cpp
#include <hip/hip_runtime.h>
#include <hip/hip_bf16.h>
#include <utility>
#include <type_traits>

typedef unsigned short u16;
typedef __attribute__((ext_vector_type(16))) float f32x16;
typedef __attribute__((ext_vector_type(4))) float f32x4;
typedef __attribute__((ext_vector_type(2))) float f32x2;
typedef __attribute__((ext_vector_type(8))) __bf16 bf16x8;
typedef __attribute__((ext_vector_type(2))) __bf16 bf16x2;
typedef __attribute__((ext_vector_type(4))) short s16x4;
typedef __attribute__((ext_vector_type(4))) unsigned u32x4;
typedef __attribute__((ext_vector_type(2))) unsigned u32x2;
#define DI __device__ __forceinline__
#define LDS_AS __attribute__((address_space(3)))
typedef LDS_AS char* lptr;

constexpr double cx_cos_small(double x) { double x2 = x * x, t = 1, s = 1; for (int i = 1; i < 12; i++) { t *= -x2 / ((2 * i - 1) * (2 * i)); s += t; } return s; }
constexpr double cx_sin_small(double x) { double x2 = x * x, t = x, s = x; for (int i = 1; i < 12; i++) { t *= -x2 / ((2 * i) * (2 * i + 1)); s += t; } return s; }
constexpr double cx_cos640(int m) {
  m %= 640; if (m > 320) m = 640 - m; double sg = 1; if (m > 160) { m = 320 - m; sg = -1; }
  const double w = 6.283185307179586476925 / 640; return sg * (m <= 80 ? cx_cos_small(w * m) : cx_sin_small(w * (160 - m)));
}
constexpr u16 cx_bf16(double d) { float f = (float)d; unsigned u = __builtin_bit_cast(unsigned, f); u += 0x7FFFu + ((u >> 16) & 1u); return (u16)(u >> 16); }
struct Cos640 { double v[640]; };
constexpr Cos640 mk_cos() { Cos640 c{}; for (int m = 0; m < 640; m++) c.v[m] = cx_cos640(m); return c; }
constexpr Cos640 COS = mk_cos();
__host__ __device__ constexpr int kf_of(int G, int i) { return G ? G + 40 * i : (i == 0 ? 320 : i < 8 ? 40 * i : 40 * i - 300); }
__host__ __device__ constexpr int freq_of(int G, int i) { return kf_of(G, i) > 320 ? 640 - kf_of(G, i) : kf_of(G, i); }
constexpr double a1val(int out, int a) {
  if (out >= 40 || a >= 40) return 0;
  const int G = out >> 1, part = out & 1;
  if (G == 0) return (part && (a & 1)) ? -1 : 1;
  return COS.v[(16 * a * G + 160 * part) % 640];
}
constexpr double m2val(int G, int o, int k) {
  const int i = o >> 1, po = o & 1, part = k >> 4, b = k & 15, kf = kf_of(G, i);
  const double c = COS.v[(b * kf) % 640], sn = COS.v[(b * kf + 480) % 640];
  if (G == 0) {
    if (part != (i >= 8)) return 0;
    if (i == 0) return 0.039528470752104741 * (po ? 1.0 : c);
    return 0.055901699437494742 * (po ? -sn : c);
  }
  const double v = po ? (part ? c : -sn) : (part ? sn : c);
  return 0.055901699437494742 * ((po && kf > 320) ? -v : v);
}
struct DTab { u16 a1[3][64][8]; u16 a1b[3][64][4]; u16 pad[256]; u16 m2[20][2][64][8]; };
constexpr DTab mk_dtab() {
  DTab t{};
  for (int l = 0; l < 64; l++) {
    const int r = l & 15, g = l >> 4;
    for (int T = 0; T < 3; T++) {
      for (int e = 0; e < 8; e++) t.a1[T][l][e] = cx_bf16(a1val(16 * T + r, 16 * (e >> 2) + 4 * g + (e & 3)));
      for (int e = 0; e < 4; e++) t.a1b[T][l][e] = cx_bf16(a1val(16 * T + r, 32 + 4 * g + e));
    }
    for (int G = 0; G < 20; G++) for (int T = 0; T < 2; T++) for (int e = 0; e < 8; e++)
      t.m2[G][T][l][e] = cx_bf16(m2val(G, 8 * (r >> 2) + 4 * T + (r & 3), 8 * g + e));
  }
  return t;
}
static __device__ const __attribute__((aligned(1024))) DTab g_dt = mk_dtab();

struct CTab { u16 v[10 * 512]; };
constexpr CTab mk_ctab() {
  CTab c{}; u16 cs[64] = {};
  for (int m = 0; m < 64; m++) cs[m] = cx_bf16(cx_cos640(10 * m));
  for (int f = 0; f < 10; f++) for (int l = 0; l < 64; l++) for (int e = 0; e < 8; e++) {
    const int r = l & 15, g = l >> 4;
    const int m = f < 8 ? (8 * g + e) * (16 * (f >> 1) + r) : 2 * r * (16 * (e >> 2) + 4 * g + (e & 3));
    c.v[(f * 64 + l) * 8 + e] = cs[(m + (f & 1) * 48) & 63];
  }
  return c;
}
static __device__ const __attribute__((aligned(1024))) CTab g_ct = mk_ctab();

constexpr int RP = 2048 + 64, RBP = 2048 + 128;
constexpr unsigned RBSZ = 640 * RBP * 2;
constexpr unsigned WS_FL = 0;
constexpr unsigned WS_AUX = WS_FL + 3 * 32768;
constexpr unsigned WS_RS1 = WS_AUX + 2048;
constexpr unsigned WS_RB0 = WS_RS1 + 8192;
constexpr unsigned WS_RB1 = WS_RB0 + RBSZ;
constexpr unsigned WS_WF = WS_RB1 + RBSZ;
constexpr unsigned WS_WQ = WS_WF + 320 * 8192;
constexpr unsigned WS_WOF = WS_WQ + 16 * 10 * 20 * 1024;
constexpr unsigned WS_WOP = WS_WOF + 40 * 20 * 1024;
constexpr int KVC = 11264;
constexpr unsigned WS_KV = WS_WOP + 40 * 20 * 1024;
constexpr unsigned WS_O = WS_KV + 32 * 16 * KVC;
constexpr unsigned LAYER2 = 32 * 16 * KVC + 32 * 64 * 1280;
constexpr unsigned WS_X4 = WS_KV + 2 * LAYER2;
constexpr unsigned WS_R3 = WS_X4 + 2048 * 640 * 2;
constexpr unsigned WS_RB2 = WS_R3 + 640 * RP * 4;
constexpr unsigned WS_SSQ = WS_RB2 + RBSZ;
constexpr unsigned EMPTY = 0xFFFFFFFFu;
constexpr unsigned WS_END = WS_SSQ + 8 * 2048 * 16;
#define FILLDELAY 10
#define OUTAUX 0
#define XDELAY 20
#define SW1 50
#define SW2 46
#define XNL 0
#define K1DELAY 40
#define YDELAY 0
#define ODELAY 0
#define WMODE 0
#define YIDLE 8
#define PROBE 1
#define PFDELAY 40
#define YSLEEP 4
#define XSLEEP 1
#define RSLEEP 2
#define AUXB 16

#define STW 0
#define STAMP(k)
#define MARK(i)
#define SARG
#define SPASS
#define SB() __builtin_amdgcn_sched_barrier(0)
typedef __amdgpu_buffer_rsrc_t rsrc_t;
DI rsrc_t mk_rsrc(const void* p, unsigned bytes) { return __builtin_amdgcn_make_buffer_rsrc((void*)p, 0, bytes, 0x00020000); }
template <int AUX> DI void st128(rsrc_t r, unsigned off, u32x4 v) { __builtin_amdgcn_raw_buffer_store_b128(v, r, off, 0, AUX); }
template <int AUX> DI void st64(rsrc_t r, unsigned off, u32x2 v) { __builtin_amdgcn_raw_buffer_store_b64(v, r, off, 0, AUX); }
template <int AUX> DI void st32(rsrc_t r, unsigned off, unsigned v) { __builtin_amdgcn_raw_buffer_store_b32(v, r, off, 0, AUX); }
template <int AUX> DI void st16(rsrc_t r, unsigned off, u16 v) { __builtin_amdgcn_raw_buffer_store_b16(v, r, off, 0, AUX); }
template <int AUX> DI u32x4 gl128(rsrc_t r, unsigned off) { return __builtin_amdgcn_raw_buffer_load_b128(r, off, 0, AUX); }
template <int AUX> DI u32x2 gl64(rsrc_t r, unsigned off) { return __builtin_amdgcn_raw_buffer_load_b64(r, off, 0, AUX); }
template <int AUX> DI unsigned gl32(rsrc_t r, unsigned off) { return __builtin_amdgcn_raw_buffer_load_b32(r, off, 0, AUX); }
DI u32x4 asu(f32x4 v) { return __builtin_bit_cast(u32x4, v); }
DI f32x4 asf(u32x4 v) { return __builtin_bit_cast(f32x4, v); }
DI unsigned pk2(float a, float b) { f32x2 v = {a, b}; return __builtin_bit_cast(unsigned, __builtin_convertvector(v, bf16x2)); }
DI u32x2 pk4(f32x4 a) { return u32x2{pk2(a[0], a[1]), pk2(a[2], a[3])}; }
DI u32x4 pk8(f32x4 a, f32x4 b) { return u32x4{pk2(a[0], a[1]), pk2(a[2], a[3]), pk2(b[0], b[1]), pk2(b[2], b[3])}; }
DI u32x4 clean(u32x4 v) { return __builtin_elementwise_min(v, u32x4{EMPTY - 1, EMPTY - 1, EMPTY - 1, EMPTY - 1}); }
DI unsigned umax4(unsigned m, u32x4 v) { return max(max(max(m, v[0]), v[1]), max(v[2], v[3])); }
DI float bflo(unsigned u) { return __uint_as_float(u << 16); }
DI float bfhi(unsigned u) { return __uint_as_float(u & 0xFFFF0000u); }
DI float dot2(unsigned a, unsigned b, float c) { return __builtin_amdgcn_fdot2_f32_bf16(__builtin_bit_cast(bf16x2, a), __builtin_bit_cast(bf16x2, b), c, false); }
DI int rowof(int p) { return 32 * (((p >> 5) & 0x3C) + (p & 3)) + ((p << 2) & 16) + ((p >> 3) & 15); }
DI int rpos(int p) { return ((p << 8) & 1024) + ((p >> 3) << 2) + (p & 3); }
DI int trow(int T, int r) { return 8 * (r >> 2) + 4 * T + (r & 3); }
template <int CTRL> DI float dpp_add(float v) { return v + __int_as_float(__builtin_amdgcn_update_dpp(0, __float_as_int(v), CTRL, 0xF, 0xF, true)); }
DI float row16_sum(float v) { v = dpp_add<0x128>(v); v = dpp_add<0x124>(v); v = dpp_add<0x122>(v); return dpp_add<0x121>(v); }
DI f32x4 mfma16(u32x4 a, u32x4 b, f32x4 c) {
  return __builtin_amdgcn_mfma_f32_16x16x32_bf16(__builtin_bit_cast(bf16x8, a), __builtin_bit_cast(bf16x8, b), c, 0, 0, 0);
}
DI void mfma16v(f32x4& c, u32x4 a, u32x4 b) { asm volatile("v_mfma_f32_16x16x32_bf16 %0, %1, %2, %0" : "+v"(c) : "v"(a), "v"(b)); }
DI u32x2 ldtr(lptr p) { return __builtin_bit_cast(u32x2, __builtin_amdgcn_ds_read_tr16_b64_v4i16((LDS_AS s16x4*)p)); }
DI u32x4 ld128(lptr p) { return *(LDS_AS u32x4*)p; }
DI u32x2 ld64(lptr p) { return *(LDS_AS u32x2*)p; }
#define GL(v, s, o, x) "global_load_lds_dwordx4 " v ", " s " offset:" #o x "\n\t"
#define M0SET "s_mov_b32 %[sv], m0\n\ts_mov_b32 m0, %[l]\n\ts_nop 2\n\t"
#define M0ADD(x) "s_add_u32 m0, m0, " x "\n\ts_nop 0\n\t"
#define M0RST "s_mov_b32 m0, %[sv]"
DI void dma2(unsigned v, const char* sb, unsigned l) { unsigned sv;
  asm volatile(M0SET GL("%[v]", "%[s]", 0, "") GL("%[v]", "%[s]", 1024, "") M0RST : [sv] "=&s"(sv) : [v] "v"(v), [s] "s"(sb), [l] "s"(l) : "memory"); }
DI void dma4(unsigned v, const char* sb, unsigned l) { unsigned sv;
  asm volatile(M0SET GL("%[v]", "%[s]", 0, "") GL("%[v]", "%[s]", 1024, "") GL("%[v]", "%[s]", 2048, "") GL("%[v]", "%[s]", 3072, "") M0RST
               : [sv] "=&s"(sv) : [v] "v"(v), [s] "s"(sb), [l] "s"(l) : "memory"); }
DI void dma6_sc1(unsigned v, const char* sb, unsigned l) { unsigned sv;
  asm volatile(M0SET GL("%[v]", "%[s]", 0, " sc1") GL("%[v]", "%[s]", 1024, " sc1") GL("%[v]", "%[s]", 2048, " sc1") M0ADD("3072")
               GL("%[w]", "%[s]", 0, " sc1") GL("%[w]", "%[s]", 1024, " sc1") GL("%[w]", "%[s]", 2048, " sc1") M0RST
               : [sv] "=&s"(sv) : [v] "v"(v), [w] "v"(v + 3072), [s] "s"(sb), [l] "s"(l) : "memory", "scc"); }
template <int N> DI void vm_bar() { asm volatile("s_waitcnt vmcnt(%0)\n\ts_barrier" ::"i"(N) : "memory"); }
DI void lds_bar() { asm volatile("s_waitcnt lgkmcnt(0)\n\ts_barrier" ::: "memory"); }
DI void raw_bar() { asm volatile("s_barrier" ::: "memory"); }
template <int N> DI void nap() { __builtin_amdgcn_s_sleep(N); asm volatile("" ::: "memory"); }
DI void drain() { asm volatile("s_waitcnt vmcnt(0)" ::: "memory"); }
#define CODE_ADDR(sym) ({ unsigned lo_, hi_; \
    asm volatile("s_getpc_b64 vcc\n\ts_add_u32 %0, vcc_lo, " sym "@rel32@lo+4\n\ts_addc_u32 %1, vcc_hi, " sym "@rel32@hi+12" : "=s"(lo_), "=s"(hi_) :: "vcc", "scc"); \
    (const unsigned*)(((unsigned long long)hi_ << 32) | lo_); })
#define SYM_PREP "_Z6k_prepPKtS0_S0_S0_S0_Pc"
#define SYM_MAIN "_Z6k_mainPcPt"
DI int wave_id() { return __builtin_amdgcn_readfirstlane(threadIdx.x >> 6); }
DI unsigned lds_addr(lptr p) { return (unsigned)(size_t)p; }

DI int kappa_to_i(int kap) {
  int ks = kap >> 5, g = (kap >> 3) & 3, e = kap & 7;
  if (ks < 16) return (2 * ks + (g >> 1)) * 20 + 8 * (e >> 2) + 4 * (g & 1) + (e & 3);
  return (8 * (ks - 16) + 2 * g + (e >> 2)) * 20 + 16 + (e & 3);
}

DI unsigned wo_frag(int nt, int ks) {
  const int ct = nt / 5, c5 = nt - 5 * ct, kh = ks < 16 ? ks >> 3 : (ks - 16) >> 1, i = ks < 16 ? ks & 7 : 8 + (ks & 1);
  const int a = kh ? (c5 >= 3 ? c5 - 3 : c5 + 2) : c5;
  return ((ct * 2 + kh) * 50 + 5 * i + a) * 1024;
}

constexpr int SLOT32 = 36864;
DI void dma_a(const unsigned* vm, const char* mb, unsigned l) { unsigned sv;
  asm volatile(M0SET GL("%[a0]", "%[m]", 0, " sc1") GL("%[a1]", "%[m]", 1024, " sc1") GL("%[a2]", "%[m]", 2048, " sc1") GL("%[a3]", "%[m]", 3072, " sc1") M0RST
               : [sv] "=&s"(sv) : [a0] "v"(vm[0]), [a1] "v"(vm[1]), [a2] "v"(vm[2]), [a3] "v"(vm[3]), [m] "s"(mb), [l] "s"(l) : "memory"); }
DI void dma_s(unsigned v0, unsigned v1, const char* sb, unsigned l) { unsigned sv;
  asm volatile(M0SET GL("%[v0]", "%[s]", 0, " sc1") GL("%[v0]", "%[s]", 1024, " sc1") GL("%[v1]", "%[s]", 2048, " sc1") GL("%[v1]", "%[s]", 3072, " sc1") M0RST
               : [sv] "=&s"(sv) : [v0] "v"(v0), [v1] "v"(v1), [s] "s"(sb), [l] "s"(l) : "memory"); }
DI void dma1_sc1(unsigned v, const char* sb, unsigned l) { unsigned sv;
  asm volatile(M0SET GL("%[v]", "%[s]", 0, " sc1") M0RST : [sv] "=&s"(sv) : [v] "v"(v), [s] "s"(sb), [l] "s"(l) : "memory"); }
DI void dma1(unsigned v, const char* sb, unsigned l) { unsigned sv;
  asm volatile(M0SET GL("%[v]", "%[s]", 0, "") M0RST : [sv] "=&s"(sv) : [v] "v"(v), [s] "s"(sb), [l] "s"(l) : "memory"); }

constexpr int L_I2 = 40 * 1056, LDS_PREP = L_I2 + 20 * 2064 + 1024;
__global__ void __launch_bounds__(512, 2) k_prep(const u16* __restrict__ x, const u16* __restrict__ wr, const u16* __restrict__ wi,
                                                 const u16* __restrict__ wqkv, const u16* __restrict__ wo, char* __restrict__ ws) {
  STAMP(0);
  extern __shared__ __attribute__((aligned(16))) char smem[];
  const lptr lds = (lptr)smem;
  const int tid = threadIdx.x, lane = tid & 63, w = wave_id(), g = lane >> 4, r16 = lane & 15;
  const int job = blockIdx.x;
  if (__builtin_expect(job < 144, 1)) {
    const bool cols = job >= 64 && job < 124;
    const int cj = job - 64, cb = cj < 48 ? (cj & 48) + 2 * (cj & 7) + ((cj >> 3) & 1) : cj;
    u32x4 a1[3], m2[3][2]; u32x2 a1b[3];
#pragma unroll
    for (int T = 0; T < 3; T++) { a1[T] = *(const u32x4*)g_dt.a1[T][lane]; a1b[T] = *(const u32x2*)g_dt.a1b[T][lane]; }
#pragma unroll
    for (int i = 0; i < 3; i++)
#pragma unroll
      for (int T = 0; T < 2; T++) m2[i][T] = *(const u32x4*)g_dt.m2[(i < 2 || w < 4) ? w + 8 * i : 0][T][lane];
    SB();
    if (cols) {
#pragma unroll
      for (int i = 0; i < 5; i++) dma1((16 * (5 * w + i) + (lane >> 2)) * 3840 + cb * 64 + (lane & 3) * 16, (const char*)wqkv, lds_addr(lds) + (5 * w + i) * 1056);
    } else {
      const char* src = (const char*)(job < 64 ? x : wo);
      const int sl = 8 * (w & 3) + 4 * (w >> 2);
      src += (job < 64 ? rowof(32 * job + sl) : kappa_to_i(32 * (job - 124) + sl)) * 1280;
      const int st = (job < 64 ? 31 : 0) * 1280;
#pragma unroll
      for (int i = 0; i < 5; i++) dma1(1024 * i + lane * 16 + ((i == 4 || lane < 16 * i) ? i - 1 : i) * st, src, lds_addr(lds) + (5 * w + i) * 1024);
    }
    unsigned so[3][2]; float sc[2] = {1.f, 1.f};
    if (cols) {
#pragma unroll
      for (int m = 0; m < 2; m++) {
        const int c = 32 * cb + 16 * m + r16;
        const int which = c / 640, rem = c - which * 640, head = rem / 20, d = rem - head * 20, grp = d >> 2;
        const int T = grp < 4 ? (which == 2 ? 2 : 0) + (grp & 1) : (which == 1 ? 3 : 2), gs = grp < 4 ? (which == 1 ? 2 : 0) + (grp >> 1) : (which == 2 ? 3 : 2);
        if (which == 0) sc[m] = 0.22360679774997896f * 1.4426950408889634f;
#pragma unroll
        for (int i = 0; i < 3; i++) so[i][m] = WS_WQ + (((head >> 1) * 10 + 4 * (head & 1) + T) * 20 + w + 8 * i) * 1024 + (g * 16 + 4 * gs + (d & 3)) * 16;
      }
    } else {
#pragma unroll
      for (int i = 0; i < 3; i++) {
        const int G = w + 8 * i;
        const unsigned f0 = wo_frag(2 * G, job - 124), f1 = wo_frag(2 * G + 1, job - 124);
#pragma unroll
        for (int T = 0; T < 2; T++)
          so[i][T] = job < 64 ? WS_RB0 + (job * 641 + 32 * G + trow(T, r16)) * 64 + (job & 1) * 64 + g * 16 : WS_WOF + (r16 < 8 ? f0 : f1) + (g * 16 + (trow(T, r16) & 15)) * 16;
      }
    }
    if (w >= 4) { so[2][0] = WS_END; so[2][1] = WS_END; }
    asm volatile("" : "+v"(so[0][0]), "+v"(so[0][1]), "+v"(so[1][0]), "+v"(so[1][1]), "+v"(so[2][0]), "+v"(so[2][1]), "+v"(sc[0]), "+v"(sc[1]));
    const f32x4 z4 = {0.f, 0.f, 0.f, 0.f};
    const lptr i2 = lds + L_I2;
    asm volatile("s_waitcnt vmcnt(0)" : "+v"(a1[0]), "+v"(a1[1]), "+v"(a1[2]), "+v"(a1b[0]), "+v"(a1b[1]), "+v"(a1b[2]) :: "memory");
    MARK(0);
    f32x4 t1[4][3];
    if (cols) {
      raw_bar();
      const lptr src = lds + (4 * g + (r16 >> 2)) * 1056 + (w >> 1) * 256 + (w & 1) * 32 + (lane & 3) * 8, srb = src + (32 - 8 * (g >> 1)) * 1056;
#pragma unroll
      for (int i = 0; i < 4; i++) {
        const u32x2 f0 = ldtr(src + i * 64), f1 = ldtr(src + 16 * 1056 + i * 64), f2 = ldtr(srb + i * 64);
#pragma unroll
        for (int T = 0; T < 3; T++) t1[i][T] = mfma16(u32x4{a1b[T][0], a1b[T][1], 0u, 0u}, u32x4{f2[0], f2[1], 0u, 0u}, mfma16(a1[T], u32x4{f0[0], f0[1], f1[0], f1[1]}, z4));
      }
      const int xo = ((w >> 1) * 8) ^ (((r16 >> 1) & 3) * 16);
      const lptr dst = i2 + 2 * g * 2064 + (16 * (w & 1) + r16) * 64;
#pragma unroll
      for (int T = 0; T < 3; T++)
#pragma unroll
        for (int e = 0; e < 4; e++)
          if (T < 2 || g < 2) *(LDS_AS u32x2*)(dst + (8 * T + (e >> 1)) * 2064 + (xo ^ ((e & 1) * 32))) = u32x2{pk2(t1[0][T][e], t1[1][T][e]), pk2(t1[2][T][e], t1[3][T][e])};
    } else {
      const lptr src = lds + w * 5120 + lane * 8, srb = lds + w * 5120 + 1024 + (lane & 31) * 8;
#pragma unroll
      for (int i = 0; i < 4; i++) {
        const u32x2 f0 = ldtr(src + i * 1280), f1 = ldtr(src + i * 1280 + 512), f2 = ldtr(srb + i * 1280);
#pragma unroll
        for (int T = 0; T < 3; T++) t1[i][T] = mfma16(u32x4{f2[0], f2[1], 0u, 0u}, u32x4{a1b[T][0], a1b[T][1], 0u, 0u}, mfma16(u32x4{f0[0], f0[1], f1[0], f1[1]}, a1[T], z4));
      }
      const int xo = ((r16 & 1) * 32 + g * 8) ^ ((w & 1) * 32);
      const lptr dst = i2 + (r16 >> 1) * 2064 + w * 256;
#pragma unroll
      for (int i = 0; i < 4; i++)
#pragma unroll
        for (int T = 0; T < 3; T++)
          if (T < 2 || r16 < 8) *(LDS_AS u32x2*)(dst + T * 8 * 2064 + i * 64 + (xo ^ ((i >> 1) * 16))) = pk4(t1[i][T]);
    }
    lds_bar();
    MARK(1);
    const rsrc_t rw = mk_rsrc(ws, WS_END);
    f32x4 sq[2] = {z4, z4};
    u32x4 f[3][2];
#pragma unroll
    for (int i = 0; i < 3; i++) {
      const lptr fp = i2 + ((i < 2 || w < 4) ? w + 8 * i : 0) * 2064 + r16 * 64 + ((g ^ ((r16 >> 1) & 3)) * 16);
      f[i][0] = ld128(fp); f[i][1] = ld128(fp + 1024);
    }
    if (cols) {
#pragma unroll
      for (int i = 0; i < 3; i++)
#pragma unroll
        for (int m = 0; m < 2; m++) st128<AUXB>(rw, so[i][m], pk8(mfma16(m2[i][0], f[i][m], z4) * sc[m], mfma16(m2[i][1], f[i][m], z4) * sc[m]));
    } else {
#pragma unroll
      for (int i = 0; i < 3; i++)
#pragma unroll
        for (int T = 0; T < 2; T++) {
          const f32x4 d0 = mfma16(f[i][0], m2[i][T], z4), d1 = mfma16(f[i][1], m2[i][T], z4);
          st128<AUXB>(rw, so[i][T], pk8(d0, d1));
          const float k = ((i == 2 && w >= 4) || (i == 0 && T == 0 && tid < 64 && r16 == 1)) ? 0.f : 1.f;
          sq[0] += d0 * d0 * k; sq[1] += d1 * d1 * k;
        }
    }
    LDS_AS float* sl = (LDS_AS float*)(lds + LDS_PREP - 1024);
    if (job < 64) {
#pragma unroll
      for (int m = 0; m < 2; m++) {
#pragma unroll
        for (int e = 0; e < 4; e++) sq[m][e] = row16_sum(sq[m][e]);
        if (r16 == 0) *(LDS_AS f32x4*)(sl + w * 32 + 8 * g + 4 * m) = sq[m];
      }
    }
    MARK(2);
    if (job < 64) {
      lds_bar();
      if (tid < 32) {
        float v = 0.f;
#pragma unroll
        for (int i = 0; i < 8; i++) v += sl[i * 32 + tid];
        st32<AUXB>(rw, WS_RS1 + rpos(32 * job + tid) * 4, __float_as_uint(__builtin_amdgcn_rsqf((v + 640.f * 1e-5f) * (2048.f * 2048.f / 640.f))));
      }
    }
    return;
  }
  const int mi = job - 144;
  const rsrc_t rw = mk_rsrc(ws, WS_END);
  if (mi < 64) {
    const int m = mi >> 3, oc = mi & 7;
    const char* src = (const char*)((w >> 2) ? wi : wr) + (64 * (4 * m + (w & 3)) + 8 * oc) * 642;
    dma4(lane * 16, src, lds_addr(lds) + w * 5152);
    dma1(4096 + lane * 16, src, lds_addr(lds) + w * 5152 + 4096);
    if (lane == 0) dma1(5120, src, lds_addr(lds) + w * 5152 + 5120);
    vm_bar<0>();
    MARK(3);
    LDS_AS u16* L = (LDS_AS u16*)lds + (tid & 7) * 321;
#pragma unroll
    for (int it = 0; it < 5; it++) {
      const int c = (tid >> 3) + 64 * it, col = freq_of(c >> 4, c & 15);
      u32x4 o;
#pragma unroll
      for (int e = 0; e < 4; e++) o[e] = (unsigned)L[e * 2576 + col] | ((unsigned)L[(4 + e) * 2576 + col] << 16);
      st128<AUXB>(rw, WS_WF + c * 8192 + (m * 64 + 8 * oc + (tid & 7)) * 16, o);
    }
    MARK(4);
#pragma unroll
    for (int it = 0; it < 3; it++) st128<AUXB>(rw, WS_RB1 + ((it * 64 + mi) * 512 + tid) * 16, u32x4{EMPTY, EMPTY, EMPTY, EMPTY});
  } else if (mi < 84) {
    const char* src = (const char*)wo + kappa_to_i(32 * (mi - 64) + 8 * (w & 3) + 4 * (w >> 2)) * 1280;
    dma4(lane * 16, src, lds_addr(lds) + w * 5152);
    dma1(4096 + lane * 16, src, lds_addr(lds) + w * 5152 + 4096);
    vm_bar<0>();
#pragma unroll
    for (int i = 0; i < 5; i++) {
      const lptr a = lds + g * 5152 + (r16 >> 2) * 1280 + (5 * w + i) * 32 + (lane & 3) * 8;
      const u32x2 lo = ldtr(a), hi = ldtr(a + 4 * 5152);
      st128<AUXB>(rw, WS_WOP + wo_frag(5 * w + i, mi - 64) + lane * 16, u32x4{lo[0], lo[1], hi[0], hi[1]});
    }
  } else {
    const int i0 = (mi - 84) * 512 + tid;
    unsigned pf0, pf1;
    {
      const unsigned* cp = CODE_ADDR(SYM_PREP); const unsigned* cm = CODE_ADDR(SYM_MAIN);
      const unsigned* p = (const unsigned*)wqkv + min(14336 + i0 - 4096, 19199) * 32;
      if (mi < 92) p = tid < 72 ? cp + tid * 32 : tid < 152 ? (const unsigned*)&g_ct + (tid - 72) * 32 : cm + min(tid - 152, 259) * 32;
      asm volatile("global_load_dword %0, %1, off" : "=v"(pf0) : "v"(p) : "memory");
      asm volatile("global_load_dword %0, %1, off" : "=v"(pf1) : "v"((const unsigned*)wqkv + i0 * 32) : "memory");
    }
    nap<K1DELAY>();
    if (i0 < 6272) st128<AUXB>(rw, WS_FL + i0 * 16, u32x4{0u, 0u, 0u, 0u});
#pragma unroll
    for (int it = 0; it < 6; it++) { const int i = 98304 + i0 + it * 14336; if (i < (int)RBSZ / 16) st128<AUXB>(rw, WS_RB1 + i * 16, u32x4{EMPTY, EMPTY, EMPTY, EMPTY}); }
    asm volatile("s_waitcnt vmcnt(0)" : "+v"(pf0), "+v"(pf1));
  }
}

#define PINF(x) asm volatile("" : "+v"(x))
DI void put_scale(lptr lds, int q, int tid, const u32x2* sp) {
  f32x4 r = {640.f * 1e-5f, 640.f * 1e-5f, 640.f * 1e-5f, 640.f * 1e-5f};
#pragma unroll
  for (int i = 0; i < 8; i++) r += f32x4{bflo(sp[i][0]), bfhi(sp[i][0]), bflo(sp[i][1]), bfhi(sp[i][1])};
#pragma unroll
  for (int e = 0; e < 4; e++) r[e] = __builtin_amdgcn_rsqf(r[e] * (2048.f * 2048.f / 640.f));
  ((LDS_AS f32x4*)(lds + 2 * SLOT32))[q * 256 + tid] = r;
}
template <bool FIRST>
DI void col_phase(rsrc_t rw, lptr lds, int c SARG) {
  int lane = threadIdx.x & 63; PINF(lane);
  const int wv = wave_id(), w = wv & 3, q = wv >> 2, tid = w * 64 + lane, g = lane >> 4, r16 = lane & 15;
  const lptr img = lds + q * SLOT32;
  f32x4 xr[2], xi[2], rs[2]; u32x4 wf[2], xb[2];
  if (FIRST) {
#pragma unroll
    for (int t = 0; t < 2; t++) { xb[t] = gl128<0>(rw, WS_RB0 + ((tid >> 2) * 641 + ((tid >> 2) & 1) + 2 * c + t) * 64 + (tid & 3) * 16); rs[t] = asf(gl128<0>(rw, WS_RS1 + (1024 * t + 4 * tid) * 4)); }
  }
  u32x4 c64[2], s64[2], n64[2], c32[2], s32[2], n32[2];
  const u32x4* ct = (const u32x4*)&g_ct + lane;
  auto ld_const = [&]() {
  c64[0] = ct[(2 * w) * 64]; s64[0] = ct[(2 * w + 1) * 64]; c32[0] = ct[8 * 64]; s32[0] = ct[9 * 64];
#pragma unroll
  for (int t = 0; t < 2; t++) wf[t] = gl128<0>(rw, WS_WF + c * 8192 + ((4 * t + g) * 64 + 16 * w + r16) * 16);
  };
  if (FIRST) ld_const();
  SB();
  f32x4 t1c[2], t1s[2], t2c[2], t2s[2];
  {
    const float f1 = (float)(16 * w + r16) * (1.f / 2048.f), b1 = (float)(4 * g), f2 = (float)(16 * w + 4 * g) * (1.f / 2048.f), b2 = (float)r16;
#pragma unroll
    for (int t = 0; t < 2; t++) {
#pragma unroll
      for (int e = 0; e < 4; e++) {
        const float a1 = (b1 + (float)(16 * t + e)) * f1, a2 = (b2 + (float)(16 * t)) * (f2 + (float)e * (1.f / 2048.f));
        t1c[t][e] = __builtin_amdgcn_cosf(a1); t1s[t][e] = __builtin_amdgcn_sinf(-a1);
        t2c[t][e] = __builtin_amdgcn_cosf(a2); t2s[t][e] = __builtin_amdgcn_sinf(a2);
      }
      PINF(t1c[t]); PINF(t1s[t]); PINF(t2c[t]); PINF(t2s[t]);
    }
  }
  int wo_ = r16 * 128 + (((2 * w + (g >> 1)) ^ (r16 >> 1)) * 16) + (g & 1) * 8, ro_ = r16 * 128 + ((g ^ (r16 >> 1)) * 16), ro2 = ro_ ^ 64;
  PINF(wo_); PINF(ro_); PINF(ro2);
  SB();
  if (FIRST) {
#pragma unroll
    for (int t = 0; t < 2; t++) {
      xr[t] = f32x4{bflo(xb[0][2 * t]), bfhi(xb[0][2 * t]), bflo(xb[0][2 * t + 1]), bfhi(xb[0][2 * t + 1])};
      xi[t] = f32x4{bflo(xb[1][2 * t]), bfhi(xb[1][2 * t]), bflo(xb[1][2 * t + 1]), bfhi(xb[1][2 * t + 1])};
    }
  } else {
    MARK(23);
    drain();
    MARK(11);
    nap<YDELAY>();
    u32x2 sp[8];
    const unsigned so = WS_SSQ + (blockIdx.x & 7) * 32768 + (1024 * q + 4 * tid) * 2, xo = WS_R3 + ((tid >> 2) * 642 + 2 * c) * 64 + (tid & 3) * 16;
    bool bs = true, bx = true;
#pragma unroll 1
    for (int spins = 0; spins < (1 << 18); spins++) {
      if (bx) {
#pragma unroll
        for (int t = 0; t < 2; t++) { xr[t] = asf(gl128<16>(rw, xo + 64 * 642 * 64 * t)); xi[t] = asf(gl128<16>(rw, xo + 64 * 642 * 64 * t + 64)); }
      }
      if (bs) {
#pragma unroll
        for (int i = 0; i < 8; i++) sp[i] = gl64<16>(rw, so + i * 4096);
      }
      unsigned m = 0;
#pragma unroll
      for (int t = 0; t < 2; t++) m = umax4(umax4(m, asu(xr[t])), asu(xi[t]));
      bx = __builtin_amdgcn_ballot_w64(m == EMPTY) != 0;
      m = 0;
#pragma unroll
      for (int i = 0; i < 8; i++) m = max(max(m, sp[i][0]), sp[i][1]);
      bs = __builtin_amdgcn_ballot_w64(m == EMPTY) != 0;
      if (!bs && !bx) break;
      nap<YSLEEP>();
    }
    ld_const();
    SB();
    put_scale(lds, q, tid, sp);
    LDS_AS f32x4* ex = (LDS_AS f32x4*)(lds + 2 * SLOT32) + tid;
    lds_bar();
    rs[0] = ex[0]; rs[1] = ex[256];
  }
  MARK(FIRST ? 9 : 21);
#pragma unroll
  for (int t = 0; t < 2; t++) {
    u32x2 pb = pk4(xi[t] * rs[t]);
    if (c == 0) pb = u32x2{0u, 0u};
    *(LDS_AS u32x2*)(img + wo_ + t * 2048) = pk4(xr[t] * rs[t]);
    *(LDS_AS u32x2*)(img + 4096 + wo_ + t * 2048) = pb;
  }
  lds_bar();
  u32x4 ur[2][2], ui[2][2], br, bi;
  f32x4 ar[2], ai[2];
  const f32x4 z4 = {0.f, 0.f, 0.f, 0.f};
  auto rd_in = [&](int off) {
#pragma unroll
    for (int ks = 0; ks < 2; ks++)
#pragma unroll
      for (int t = 0; t < 2; t++) { const lptr p = img + off + (ks ? ro2 : ro_) + t * 2048; ur[t][ks] = ld128(p); ui[t][ks] = ld128(p + 4096); }
    SB();
  };
  auto cmul = [&](const f32x4* cc, const f32x4* sn) {
    br = pk8(ar[0] * cc[0] - ai[0] * sn[0], ar[1] * cc[1] - ai[1] * sn[1]);
    bi = pk8(ai[0] * cc[0] + ar[0] * sn[0], ai[1] * cc[1] + ar[1] * sn[1]);
  };
  rd_in(0);
  {
    const unsigned par = (lane & 1) ? 0x80008000u : 0u;
    c64[1] = c64[0] ^ par; s64[1] = s64[0] ^ par; n64[0] = s64[0] ^ 0x80008000u; n64[1] = s64[1] ^ 0x80008000u;
    c32[1] = c32[0] ^ 0x80000000u; s32[1] = s32[0] ^ 0x80000000u; n32[0] = s32[0] ^ 0x80008000u; n32[1] = s32[0] ^ 0x00008000u;
    PINF(c64[1]); PINF(s64[1]); PINF(n64[0]); PINF(n64[1]); PINF(c32[1]); PINF(s32[1]); PINF(n32[0]); PINF(n32[1]);
  }
  SB();
#pragma unroll
  for (int ks = 0; ks < 2; ks++) {
#pragma unroll
    for (int t = 0; t < 2; t++) { ar[t] = mfma16(ur[t][ks], c64[ks], ks ? ar[t] : z4); ai[t] = mfma16(ui[t][ks], c64[ks], ks ? ai[t] : z4); }
#pragma unroll
    for (int t = 0; t < 2; t++) { ar[t] = mfma16(ui[t][ks], s64[ks], ar[t]); ai[t] = mfma16(ur[t][ks], n64[ks], ai[t]); }
  }
  const f32x4 fa[2] = {asf(wf[0] << 16), asf(wf[1] << 16)}, fb[2] = {asf(wf[0] & 0xFFFF0000u), asf(wf[1] & 0xFFFF0000u)};
  cmul(t1c, t1s);
#pragma unroll
  for (int t = 0; t < 2; t++) { ar[t] = mfma16(c32[t], br, z4); ai[t] = mfma16(c32[t], bi, z4); }
#pragma unroll
  for (int t = 0; t < 2; t++) { ar[t] = mfma16(s32[t], bi, ar[t]); ai[t] = mfma16(n32[t], br, ai[t]); }
  cmul(fa, fb);
#pragma unroll
  for (int t = 0; t < 2; t++) { ar[t] = mfma16(br, c32[t], z4); ai[t] = mfma16(bi, c32[t], z4); }
#pragma unroll
  for (int t = 0; t < 2; t++) { ar[t] = mfma16(bi, n32[t], ar[t]); ai[t] = mfma16(br, s32[t], ai[t]); }
  cmul(t2c, t2s);
#pragma unroll
  for (int t = 0; t < 2; t++) {
    *(LDS_AS u32x2*)(img + 8192 + wo_ + t * 2048) = u32x2{br[2 * t], br[2 * t + 1]};
    *(LDS_AS u32x2*)(img + 12288 + wo_ + t * 2048) = u32x2{bi[2 * t], bi[2 * t + 1]};
  }
  lds_bar();
  rd_in(8192);
#pragma unroll
  for (int ks = 0; ks < 2; ks++) {
#pragma unroll
    for (int t = 0; t < 2; t++) { ar[t] = mfma16(c64[ks], ur[t][ks], ks ? ar[t] : xr[t]); ai[t] = mfma16(c64[ks], ui[t][ks], ks ? ai[t] : xi[t]); }
#pragma unroll
    for (int t = 0; t < 2; t++) { ar[t] = mfma16(n64[ks], ui[t][ks], ar[t]); ai[t] = mfma16(s64[ks], ur[t][ks], ai[t]); }
  }
  const unsigned ob = (FIRST ? WS_RB1 : WS_RB2) + (2 * c * RBP + 8 * tid) * 2;
  st128<16>(rw, ob, clean(pk8(ar[0], ar[1])));
  st128<16>(rw, ob + RBP * 2, c == 0 ? u32x4{0u, 0u, 0u, 0u} : clean(pk8(ai[0], ai[1])));
}

DI f32x16 mfma32(u32x4 a, u32x4 b, f32x16 c) {
  return __builtin_amdgcn_mfma_f32_32x32x16_bf16(__builtin_bit_cast(bf16x8, a), __builtin_bit_cast(bf16x8, b), c, 0, 0, 0);
}
DI f32x16 mfma32k8(u32x2 a, u32x2 b, f32x16 c) {
  return __builtin_amdgcn_mfma_f32_32x32x8bf16_1k(__builtin_bit_cast(s16x4, a), __builtin_bit_cast(s16x4, b), c, 0, 0, 0);
}
DI f32x16 scores(u32x4 k1, u32x2 k2, u32x4 q1, u32x2 q2) {
  f32x16 z;
#pragma unroll
  for (int i = 0; i < 16; i++) z[i] = 0.f;
  f32x16 s = mfma32(k1, q1, z);
  asm volatile("s_nop 11" : "+v"(s));
  return mfma32k8(k2, q2, s);
}
DI float max16(const f32x16& s) {
  float m = fmaxf(s[0], s[1]);
#pragma unroll
  for (int i = 2; i < 16; i++) m = fmaxf(m, s[i]);
  return m;
}
#define STR_(x) #x
#define STR(x) STR_(x)
#define KOF(t) ((((t)&7)>>1)*11264+((t)&1)*1280)
#define VOF(t) ((((t)&7)>>1)*11264+5120+((t)&1)*1536)
#define EXP(d, s) "v_exp_f32 v" #d ", v" #s "\n\t"
#define CVT(d, s) "v_cvt_pk_bf16_f32 v" #d ", v" #s ", v[" #s "+1]\n\t"
#define STEPA(t) \
  "v_mfma_f32_32x32x16_bf16 v[48:63], v[80:83], v[136:139], v[120:135]\n\t" \
  "ds_read_b128 v[80:83], v100 offset:" STR(KOF(t+1)) "\n\t" \
  EXP(64,32) EXP(65,33) CVT(78,68) CVT(79,70) EXP(66,34) EXP(67,35) \
  "v_mfma_f32_32x32x16_bf16 v[104:119], v[90:93], v[76:79], v[104:119]\n\t" \
  "ds_read_b64_tr_b16 v[90:91], v102 offset:" STR(VOF(t)+768) "\n\t" \
  "ds_read_b64_tr_b16 v[92:93], v102 offset:" STR(VOF(t)+1152) "\n\t" \
  EXP(68,36) EXP(69,37) CVT(72,64) CVT(73,66) EXP(70,38) EXP(71,39) \
  "v_mfma_f32_32x32x8_bf16 v[48:63], v[84:85], v[140:141], v[48:63]\n\t" \
  "ds_read_b64 v[84:85], v101 offset:" STR(KOF(t+1)) "\n\t" \
  EXP(64,40) EXP(65,41) CVT(74,68) CVT(75,70) EXP(66,42) EXP(67,43) \
  "s_waitcnt lgkmcnt(4)\n\t" \
  "v_mfma_f32_32x32x16_bf16 v[0:15], v[86:89], v[72:75], v[0:15]\n\t" \
  EXP(68,44) EXP(69,45) CVT(76,64) CVT(77,66) EXP(70,46) EXP(71,47)
#define STEPB(t) \
  "s_waitcnt lgkmcnt(0)\n\t" \
  "v_mfma_f32_32x32x16_bf16 v[32:47], v[80:83], v[94:97], v[16:31]\n\t" \
  EXP(64,48) EXP(65,49) CVT(78,68) CVT(79,70) EXP(66,50) EXP(67,51) \
  "v_mfma_f32_32x32x16_bf16 v[0:15], v[90:93], v[76:79], v[0:15]\n\t" \
  EXP(68,52) EXP(69,53) CVT(72,64) CVT(73,66) EXP(70,54) EXP(71,55) \
  "v_mfma_f32_32x32x8_bf16 v[32:47], v[84:85], v[98:99], v[32:47]\n\t" \
  EXP(64,56) EXP(65,57) CVT(74,68) CVT(75,70) EXP(66,58) EXP(67,59) \
  "v_mfma_f32_32x32x16_bf16 v[104:119], v[86:89], v[72:75], v[104:119]\n\t" \
  "ds_read_b64_tr_b16 v[86:87], v102 offset:" STR(VOF(t+1)) "\n\t" \
  "ds_read_b64_tr_b16 v[88:89], v102 offset:" STR(VOF(t+1)+384) "\n\t" \
  EXP(68,60) EXP(69,61) CVT(76,64) CVT(77,66) EXP(70,62) EXP(71,63)
#define PR(n) "s_setprio " #n "\n\t"
#define CHUNK(i) PR(1) STEPA(2*(i)) PR(0) STEPB(2*(i)) "s_barrier\n\t" PR(2) STEPA(2*(i)+1) STEPB(2*(i)+1)
#define Z4(a) "v_mov_b64 v[" #a ":" #a "+1], 0\n\tv_mov_b64 v[" #a "+2:" #a "+3], 0\n\t"

#define R4(b) "v[" STR(b) ":" STR(b) "+3]"
#define MM(d, a, b) "v_mfma_f32_16x16x32_bf16 " R4(d) ", " R4(a) ", " R4(b) ", " R4(d) "\n\t"
#define GM(B, m, i) MM(20*m+4*i, B+8+4*i, B+4*m)
#define RT(B, m, hf, k) "ds_read_b64_tr_b16 v[" STR(B+4*m+2*hf) ":" STR(B+4*m+2*hf+1) "], v10" #m " offset:" STR(k*8192+hf*1024) "\n\t"
#define RS(B, i, k) "ds_read_b128 " R4(B+8+4*i) ", v102 offset:" STR(i*2048+k*1024) "\n\t"
#define IF5_4(x) ""
#define IF5_5(x) x
#define GSTEP(N, B, C, k) GM(B,0,0) RT(C,0,0,k) RT(C,0,1,k) GM(B,1,0) RS(C,0,k) RT(C,1,0,k) GM(B,0,1) RT(C,1,1,k) RS(C,1,k) GM(B,1,1) RS(C,2,k) \
  GM(B,0,2) RS(C,3,k) GM(B,1,2) IF5_##N(RS(C,4,k)) GM(B,0,3) GM(B,1,3) IF5_##N(GM(B,0,4) GM(B,1,4)) MM(40,B,B)
#define GEMM_ASM(N) \
  Z4(0) Z4(4) Z4(8) Z4(12) Z4(16) Z4(20) Z4(24) Z4(28) Z4(32) Z4(36) Z4(40) "s_mov_b32 %[c], 0\n\ts_barrier\n\t" \
  RT(44,0,0,0) RT(44,0,1,0) RS(44,0,0) RT(44,1,0,0) RT(44,1,1,0) RS(44,1,0) RS(44,2,0) RS(44,3,0) IF5_##N(RS(44,4,0)) \
  "L_gemm_%=:\n\ts_add_u32 %[c], %[c], 1\n\ts_and_b32 %[t], %[c], 3\n\ts_cselect_b32 %[t], 1, -3\n\ts_mul_i32 %[t], %[t], 36864\n\ts_waitcnt lgkmcnt(0)\n\t" \
  GSTEP(N,44,72,1) \
  "v_add_u32 v100, %[t], v100\n\tv_add_u32 v101, %[t], v101\n\tv_add_u32 v102, %[t], v102\n\ts_waitcnt lgkmcnt(0)\n\ts_barrier\n\t" \
  GSTEP(N,72,44,0) \
  "s_cmp_lg_u32 %[c], 9\n\ts_cbranch_scc1 L_gemm_%=\n\ts_waitcnt lgkmcnt(0)\n\t" \
  GSTEP(N,44,72,1) "s_waitcnt lgkmcnt(0)\n\t" \
  MM(40,72,72) GM(72,0,0) GM(72,1,0) GM(72,0,1) GM(72,1,1) "s_nop 4\n\t" \
  "v_cndmask_b32 v44, v40, v41, %[m1]\n\tv_cndmask_b32 v45, v42, v43, %[m1]\n\tv_mov_b32 v46, 0x3727c5ac\n\tv_cndmask_b32 v44, v44, v45, %[m2]\n\t" \
  "v_fmac_f32 v46, 0x3acccccd, v44\n\tv_cmp_u_f32 vcc, v44, v44\n\tv_rsq_f32 v46, v46\n\ts_cbranch_vccz L_gok_%=\n\tds_write_b32 v106, v106\n\t" \
  "L_gok_%=:\n\tds_write_b32 v103, v46\n\ts_waitcnt lgkmcnt(0)\n\ts_barrier\n\t" \
  "ds_read_b32 v44, v104\n\tds_read_b32 v45, v105\n\tds_read_b32 v46, v106\n\t" \
  GM(72,0,2) GM(72,1,2) GM(72,0,3) GM(72,1,3) IF5_##N(GM(72,0,4) GM(72,1,4)) "s_waitcnt lgkmcnt(0)"
#define GEMM_OPS \
  : "={v[0:3]}"(acc[0][0]), "={v[4:7]}"(acc[0][1]), "={v[8:11]}"(acc[0][2]), "={v[12:15]}"(acc[0][3]), "={v[16:19]}"(acc[0][4]), \
    "={v[20:23]}"(acc[1][0]), "={v[24:27]}"(acc[1][1]), "={v[28:31]}"(acc[1][2]), "={v[32:35]}"(acc[1][3]), "={v[36:39]}"(acc[1][4]), \
    "={v44}"(rs[0]), "={v45}"(rs[1]), "={v46}"(vote), "+{v100}"(am0), "+{v101}"(am1), "+{v102}"(as), [c] "=&s"(c_), [t] "=&s"(t_) \
  : "{v103}"(a_w), "{v104}"(a_r[0]), "{v105}"(a_r[1]), "{v106}"(a_v), [m1] "s"(0xAAAAAAAAAAAAAAAAull), [m2] "s"(0xCCCCCCCCCCCCCCCCull) \
  : "memory", "scc", "vcc", "v40", "v41", "v42", "v43", "v47", "v48", "v49", "v50", "v51", "v52", "v53", "v54", "v55", "v56", "v57", "v58", "v59", "v60", \
    "v61", "v62", "v63", "v64", "v65", "v66", "v67", "v68", "v69", "v70", "v71", "v72", "v73", "v74", "v75", "v76", "v77", "v78", "v79", "v80", "v81", \
    "v82", "v83", "v84", "v85", "v86", "v87", "v88", "v89", "v90", "v91", "v92", "v93", "v94", "v95", "v96", "v97", "v98", "v99"

constexpr int HR = 4 * KVC;
constexpr int L_A = 2 * HR;
constexpr int L_EX = 15360;
constexpr int L_MISC = L_A + 70 * 1024;
constexpr int LDS_FUSED = L_MISC + 2048;

DI bool poll16(rsrc_t rw, unsigned off, unsigned need) {
  const int lane = threadIdx.x & 63;
#pragma unroll 1
  for (int spins = 0; spins < (1 << 20); spins++) {
    const unsigned v = gl32<16>(rw, off + (lane & 15) * 4);
    const unsigned got = (unsigned)__builtin_amdgcn_ballot_w64(v != 0) & 0xFFFFu;
    if ((got & need) == need) return true;
    nap<1>();
  }
  return false;
}

DI void poll_aux(rsrc_t rw, unsigned off) {
#pragma unroll 1
  for (int spins = 0; spins < (1 << 20); spins++) {
    const u32x4 v = gl128<16>(rw, off + (threadIdx.x & 63) * 16);
    if (!__builtin_amdgcn_ballot_w64(min(min(v[0], v[1]), min(v[2], v[3])) == 0u)) break;
    nap<4>();
  }
}
#define LAYER_VARS \
  extern __shared__ __attribute__((aligned(16))) char smem[]; \
  const lptr lds = (lptr)smem; \
  const rsrc_t rw = mk_rsrc(ws, WS_END); \
  int lane = threadIdx.x & 63; asm volatile("" : "+v"(lane)); \
  const int w = wave_id(), g = lane >> 4, r16 = lane & 15; \
  const int xcd = blockIdx.x & 7, bi = blockIdx.x >> 3, u = 8 * (xcd & 1) + (bi & 7), qt = 4 * (xcd >> 1) + (bi >> 3); \
  const int hf = u & 1, ct = u >> 1;     \
  constexpr unsigned FKV = WS_FL + (FINAL ? 32768 : 0), KV = WS_KV + (FINAL ? LAYER2 : 0), OB = WS_O + (FINAL ? LAYER2 : 0); \
  constexpr unsigned AUX = WS_AUX + (FINAL ? 1024 : 0), RB = FINAL ? WS_RB2 : WS_RB1; \
  constexpr int MK = FINAL ? 12 : 0; (void)MK; (void)hf; (void)AUX; (void)RB; (void)FKV; \
  LDS_AS unsigned* misc = (LDS_AS unsigned*)(lds + L_MISC + 1024) + (FINAL ? 8 : 0);

template <bool FINAL>
DI void loader(char* ws SARG) {
  LAYER_VARS
  {
    const int lw = w - 8;
    {
      unsigned vm[4];
#pragma unroll
      for (int r = 0; r < 4; r++) vm[r] = (4 * (4 * lw + r) + g) * (RBP * 2) + 256 * qt + (((r16 >> 1) ^ (g | ((r & 2) << 1))) * 32) + (r16 & 1) * 16 + 3072 - 1024 * r;
      const unsigned l0 = lds_addr(lds) + lw * 4096, v0 = 2 * lw * 20480 + lane * 16, v2 = ((8 + (lw >> 1)) * 20 + (lw & 1)) * 1024 + lane * 16;
      constexpr int CNT = FINAL ? 9 : 8;
      auto lds_w = [&](int s) {
        const char* sb = ws + WS_WQ + u * 204800 + s * 2048;
        dma_s(v0, v0 + 20480 - 2048, sb, l0 + (s & 3) * SLOT32 + 16384 + (lw >> 1) * 2048);
        if (FINAL) dma1_sc1(v2, sb, lds_addr(lds) + (s & 3) * SLOT32 + 24576 + (lw >> 1) * 10240 + (lw & 1) * 1024);
      };
      auto lds_a = [&](int s) {
        dma_a(vm, ws + RB - 3072 + s * (64 * RBP * 2), l0 + (s & 3) * SLOT32); };
#pragma unroll 1
      for (int att = 0;; att++) {
        if (att == 0) { raw_bar(); if (FINAL) raw_bar(); }
        lds_w(0); lds_w(1);
        if (att == 0) raw_bar();
        lds_w(2); lds_w(3);
        auto probe = [&](int s0, int n) {
          const int pl = min(lane, n - 1);
          const unsigned po = RB + ((64 * (s0 + (pl >> 3)) + 16 * lw + 2 * (pl & 7) + 1) * RBP + 128 * qt + 126) * 2;
#pragma unroll 1
          for (int spins = 0; spins < (1 << 20); spins++) {
            if (!__builtin_amdgcn_ballot_w64(gl32<16>(rw, po) == EMPTY)) break;
            nap<XSLEEP>();
          }
        };
        if (att == 0) {
          raw_bar();
          if (blockIdx.x >= 64) nap<XDELAY>();
          MARK(MK + 7);
          probe(0, 48);
        }
        drain();
        MARK(MK + 8);
#pragma unroll 1
        for (int s = 0; s < 4; s++) lds_a(s);
        vm_bar<12>(); vm_bar<8>();
        if (att == 0) probe(6, 32);
        lds_a(4); lds_w(4);
        vm_bar<4 + CNT>();
#pragma unroll 1
        for (int s = 5; s < 10; s++) { lds_a(s); lds_w(s); vm_bar<2 * CNT>(); }
        vm_bar<CNT>();
        poll_aux(rw, AUX);
        vm_bar<0>();
        raw_bar();
        if (XNL || att || !*(volatile LDS_AS unsigned*)(misc + 2)) break;
      }
    }
    MARK(MK + 0);
    const int hh = lw >> 1, hp = lw & 1;
    const unsigned kvv = KV + (2 * u + hh) * 16 * KVC + hp * 5120 + lane * 16;
    const unsigned lkv = lds_addr(lds) + hh * HR + hp * 5120;
    const unsigned wv = (FINAL ? WS_WOP : WS_WOF) + (ct * 2 + hh) * 51200 + lane * 16;
    const unsigned myfl = FKV + (u * 16 + qt) * 128;
    raw_bar();
    MARK(MK + 1);
    {
      u32x4 v[6];
#pragma unroll
      for (int i = 0; i < 6; i++) v[i] = ld128(lds + hh * HR + hp * 5120 + lane * 16 + i * 1024);
#pragma unroll
      for (int i = 0; i < 6; i++) st128<16>(rw, kvv + qt * KVC + i * 1024, clean(v[i]));
    }
    drain();
    unsigned cnt = 0;
    if (lane == 0) cnt = __hip_atomic_fetch_add(misc, 1u, __ATOMIC_RELAXED, __HIP_MEMORY_SCOPE_WORKGROUP);
    if (__builtin_amdgcn_readfirstlane(cnt) == 3 && lane < 16) st32<16>(rw, FKV + (u * 16 + lane) * 128 + qt * 4, 1u);
    MARK(MK + 2);
    auto ldkv = [&](int seq) {
      const int ch = seq == 0 ? qt : seq < 13 ? ((qt & 12) + 3 + seq) & 15 : (qt & 12) | ((qt + seq) & 3);
      dma6_sc1(kvv + ch * KVC, ws, lkv + (seq & 3) * KVC);
    };
    auto fetch = [&](int seq) {
      const unsigned src = kvv + ((((qt & 12) + 3 + seq) & 15)) * KVC;
      u32x4 v[6];
#pragma unroll 1
      for (int spins = 0; spins < (1 << 18); spins++) {
#pragma unroll
        for (int i = 0; i < 6; i++) v[i] = gl128<16>(rw, src + i * 1024);
        unsigned m = 0;
#pragma unroll
        for (int i = 0; i < 6; i++) m = umax4(m, v[i]);
        if (!__builtin_amdgcn_ballot_w64(m == EMPTY)) break;
        nap<2>();
      }
#pragma unroll
      for (int i = 0; i < 6; i++) *(LDS_AS u32x4*)(lds + hh * HR + hp * 5120 + seq * KVC + lane * 16 + i * 1024) = v[i];
    };
#pragma unroll 1
    for (int attempt = 0;; attempt++) {
      int c = -2;
      if (attempt) { ldkv(0); vm_bar<0>(); }
      else {
        fetch(1); MARK(MK + 3); lds_bar();
        MARK(MK + 4);
        fetch(2); poll16(rw, myfl, 12u << (((qt & 12) + 4) & 15)); ldkv(3); MARK(MK + 5); lds_bar();
        c = 1;
      }
#pragma unroll 1
      for (; c < 13; c++) {
        if (c == 2 && !attempt) poll16(rw, myfl, 0xFFFFu);
        if (!FINAL && !attempt) {
          if (c == 10) {
            unsigned n = 0;
            if (lane == 0) n = __hip_atomic_fetch_add(misc + 3, 1u, __ATOMIC_RELAXED, __HIP_MEMORY_SCOPE_WORKGROUP);
            if (__builtin_amdgcn_readfirstlane(n) == 3 && lane < 32) st32<16>(rw, WS_FL + 65536 + (lane < 16 ? (u * 16 + lane) * 128 + qt * 4 : (r16 * 16 + qt) * 128 + 64 + u * 4), 1u);
          }
        }
        if (c >= 2) { const int e = min(4 * (c - 2) + 2 * hp, 33); dma2(wv + e * 1024, ws, lds_addr(lds) + L_A + (hh * 35 + e) * 1024); }
        ldkv(c + 3);
        if (c == 0) vm_bar<12>();
        if (!FINAL && !attempt && c >= 2 && c < 9) {
          const u32x4 e = {EMPTY, EMPTY, EMPTY, EMPTY};
          const unsigned o = OB + LAYER2 + (((c - 2) * 256 + blockIdx.x) * 4 + lw) * 2048 + lane * 16;
          st128<16>(rw, o, e); st128<16>(rw, o + 1024, e);
          st128<16>(rw, ((qt & 3) < 2 && c < 8) ? kvv + LAYER2 + qt * KVC + (c - 2) * 1024 : WS_END, e);
          st128<16>(rw, c < 5 ? OB + ((4 * qt + lw) * 32 + 2 * u) * 1280 + min((c - 2) * 1024, 1536) + lane * 16 : WS_END, e);
          if (c == 2) vm_bar<12>(); else vm_bar<16>();
        } else if (c >= 2) vm_bar<8>(); else if (c >= 0) vm_bar<6>();
      }
      if (!FINAL && !attempt) poll16(rw, myfl + 65536 + hp * 64, 0xFFFFu);
      vm_bar<0>();
      dma4(wv + (35 + 7 * hp) * 1024, ws, lds_addr(lds) + hh * HR + 7 * hp * 1024);
      dma4(wv + (39 + 7 * hp) * 1024, ws, lds_addr(lds) + hh * HR + (4 + 7 * hp) * 1024);
      raw_bar();
      vm_bar<0>();
      if (attempt || !*(volatile LDS_AS unsigned*)(misc + 1)) break;
      raw_bar();
    }
    if (!FINAL) { raw_bar(); raw_bar(); }
  }
}

template <bool FINAL>
DI void compute(char* ws, u16* out SARG) {
  LAYER_VARS
  const int wm = w & 3, wn = w >> 2, h = lane >> 5;
  if (blockIdx.x < 64 || wn == 0) col_phase<!FINAL>(rw, lds, blockIdx.x < 64 ? 192 + 2 * blockIdx.x + wn : blockIdx.x - 64 SPASS);
  else if (FINAL) {
    drain();
    nap<YIDLE>();
    u32x2 sp[8];
#pragma unroll 1
    for (int spins = 0; spins < (1 << 18); spins++) {
      unsigned m = 0;
#pragma unroll
      for (int i = 0; i < 8; i++) sp[i] = gl64<16>(rw, WS_SSQ + (blockIdx.x & 7) * 32768 + (1024 + 4 * (wm * 64 + lane)) * 2 + i * 4096);
#pragma unroll
      for (int i = 0; i < 8; i++) m = max(max(m, sp[i][0]), sp[i][1]);
      if (!__builtin_amdgcn_ballot_w64(m == EMPTY)) break;
      nap<YSLEEP>();
    }
    put_scale(lds, 1, wm * 64 + lane, sp);
    lds_bar(); raw_bar(); raw_bar();
  } else {
    const int i0 = (blockIdx.x - 64) * 256 + wm * 64 + lane;
    for (int pc = i0; pc < 640 * 80; pc += 192 * 256) {
      const int n = pc % 640, kq = pc / 640;
      const int u_ = n / 40, rn = n - 40 * u_, hn = rn / 20, dn = rn - 20 * hn, gq = dn >> 2;
      const int T = gq < 4 ? 8 + hn : 4 * hn + 3, gs = gq < 4 ? gq : 3;
      float v[8];
#pragma unroll
      for (int e = 0; e < 4; e++) {
        const float a = (float)((freq_of(kq >> 2, 4 * (kq & 3) + e) * n) % 640) * (1.f / 640.f);
        v[2 * e] = 0.055901699437494742f * __builtin_amdgcn_cosf(a);
        v[2 * e + 1] = -0.055901699437494742f * __builtin_amdgcn_sinf(a);
      }
      if (kq == 0) { v[0] = (n & 1) ? -0.039528470752104741f : 0.039528470752104741f; v[1] = 0.039528470752104741f; }
      st128<16>(rw, WS_WQ + ((u_ * 10 + T) * 20 + (kq >> 2)) * 1024 + ((kq & 3) * 16 + 4 * gs + (dn & 3)) * 16,
                  u32x4{pk2(v[0], v[1]), pk2(v[2], v[3]), pk2(v[4], v[5]), pk2(v[6], v[7])});
    }
    raw_bar();
#pragma unroll
    for (int it = 0; it < 4; it++) {
      const int k = it * 49152 + i0, b = k / 1408;
      st128<16>(rw, k < 128 * 1408 ? WS_KV + b * 4 * KVC + (k - b * 1408) * 16 : WS_END, u32x4{EMPTY, EMPTY, EMPTY, EMPTY});
    }
    raw_bar();
  }
  MARK(MK + 10);
  lds_bar();
  {
    drain();
    unsigned n = 0;
    if (lane == 0) n = __hip_atomic_fetch_add(misc + 4, 1u, __ATOMIC_RELAXED, __HIP_MEMORY_SCOPE_WORKGROUP);
    if (n == 7) st32<16>(rw, AUX + blockIdx.x * 4, 1u);
  }

  {
    constexpr int NT = FINAL ? 5 : 4;
    f32x4 acc[2][5]; float rs[2];
    unsigned mo[2], a0[2], a1[2], a2[2], a_r[2];
#pragma unroll
    for (int m = 0; m < 2; m++) {
      const int q = r16 >> 2, pr = 16 * (m ^ wn) + r16;
      mo[m] = lds_addr(lds) + (8 * g + q) * 256 + (((2 * wm + (m ^ wn)) ^ (q | ((g & 1) << 2))) * 32) + (lane & 3) * 8;
      const unsigned qb = L_A + (wn * 4 + wm) * 1280, kb = wn * HR + wm * 1280, vr = wn * HR + 5120 + (32 * wm + pr) * 48 + (g == 3 ? 32 : g * 16);
      a0[m] = ((g >> 1) ? kb : qb) + (g & 1) * 512 + pr * 16;
      a1[m] = g == 2 ? qb + 1024 + pr * 8 : vr; a2[m] = g == 2 ? kb + 1024 + pr * 8 : vr + 8;
      a_r[m] = lds_addr(lds) + L_MISC + (32 * wm + pr) * 4;
      asm volatile("" : "+v"(a0[m]), "+v"(a1[m]), "+v"(a2[m]));
    }
    const unsigned a_w = a_r[0] + (g == (r16 >> 2) ? 0 : 512), a_v = lds_addr((lptr)(misc + 2));
#pragma unroll 1
    for (int att = 0;; att++) {
      unsigned am0 = mo[0], am1 = mo[1], as = lds_addr(lds) + 16384 + wn * 10240 + lane * 16, vote; int c_, t_;
      if (FINAL) asm volatile(GEMM_ASM(5) GEMM_OPS); else asm volatile(GEMM_ASM(4) GEMM_OPS);
      if (XNL || att || !__builtin_amdgcn_readfirstlane(vote)) break;
    }
    SB();
    MARK(MK + 0);
#pragma unroll
    for (int m = 0; m < 2; m++) *(LDS_AS u32x4*)(lds + a0[m]) = pk8(acc[m][0] * rs[m], acc[m][1] * rs[m]);
    asm volatile("s_nop 1" : "+v"(acc[0][2]), "+v"(acc[1][2]), "+v"(acc[0][3]), "+v"(acc[1][3]), "+v"(acc[0][NT - 1]), "+v"(acc[1][NT - 1]) :: "memory");
#pragma unroll
    for (int m = 0; m < 2; m++) {
      u32x2 hi = pk4(acc[m][3] * rs[m]);
      if (g == 3) hi = u32x2{0x3F80u, 0u};
      *(LDS_AS u32x2*)(lds + a1[m]) = pk4(acc[m][2] * rs[m]);
      *(LDS_AS u32x2*)(lds + a2[m]) = hi;
    }
    lds_bar();
    if (FINAL) {
      const unsigned x4 = WS_X4 + (((u * 16 + qt) * 4 + wm) * 2 + wn) * 1280;
      const u32x2 p0 = pk4(acc[0][NT - 1]), p1 = pk4(acc[1][NT - 1]), q0 = pk4(acc[0][3]), q1 = pk4(acc[1][3]);
      st128<16>(rw, x4 + lane * 16, clean(u32x4{p0[0], p0[1], p1[0], p1[1]}));
      if (g == 3) st128<16>(rw, x4 + 1024 + r16 * 16, clean(u32x4{q0[0], q0[1], q1[0], q1[1]}));
    }
  }
  MARK(MK + 1);
  const int head = 2 * u + wn, kh = w & 1;
  const lptr ring = lds + wn * HR;
  const int pblk = 128 * qt + 64 * hf + 32 * (wm >> 1);
  const unsigned r3t = WS_R3 + (64 * (wm & 1) + (pblk >> 5)) * (642 * 64);
  u32x2 x4v[3]; float dc = 0.f;
  unsigned os = OB + ((4 * qt + wm) * 32 + head) * 1280 + (((lane << 2) & 16) + ((lane >> 1) & 12) + (lane & 3)) * 8;
  unsigned o1 = OB + ((pblk >> 5) * 32 + 16 * wn) * 1280 + (g >> 1) * 1280 + (g & 1) * 512 + (wm & 1) * 256 + r16 * 16;
  unsigned o2 = OB + ((pblk >> 5) * 32 + 16 * wn) * 1280 + 2 * g * 1280 + 1024 + (wm & 1) * 128 + r16 * 8;
  u32x4 o3 = {0u, 0u, 0u, 0u};
  if (!FINAL) {
#pragma unroll
    for (int i = 0; i < 3; i++) {
      const int j = 80 * ct + 48 * wn + 16 * (wn ? (i & 1) : i) + r16;
      o3[i] = (j == 1 ? WS_RB0 + ((pblk >> 5) * 641 + ((pblk >> 5) & 1) + 1) * 64 : WS_RB1 + (j * RBP + pblk) * 2) + 16 * g + 8 * (wm & 1);
    }
  } else {
    o3[3] = r3t + 64 + r16 * 4;
#pragma unroll
    for (int i = 0; i < 3; i++) {
      const int cg = 12 * wn + 4 * ((i == 2 && wn) ? 0 : i) + g, pw = cg / 5, gq = cg - 5 * pw;
      o3[i] = WS_X4 + ((((2 * ct + (pw >> 1)) * 16 + qt) * 4 + 2 * hf + (wm >> 1)) * 2 + (pw & 1)) * 1280 + (gq * 16 + (trow(wm & 1, r16) & 15)) * 16 + ((((r16 >> 3) ^ pw) & 1) * 8);
    }
  }
  asm volatile("" : "+v"(os), "+v"(o1), "+v"(o2), "+v"(o3));
  f32x4 r[3];
  {
    const lptr qpa = lds + L_A + (wn * 4 + wm) * 1280, qpb = lds + L_A + (wn * 4 + (wm ^ 1)) * 1280;
    const u32x4 qa1 = ld128(qpa + lane * 16), qb1 = ld128(qpb + lane * 16);
    u32x2 qa2 = ld64(qpa + 1024 + (lane & 31) * 8), qb2 = ld64(qpb + 1024 + (lane & 31) * 8);
    if (h) { qa2 = u32x2{0u, 0u}; qb2 = qa2; }
    const unsigned ka = lds_addr(ring) + kh * 2560 + lane * 16, kb = lds_addr(ring) + kh * 2560 + 1024 + (lane & 31) * 8;
    const unsigned va = lds_addr(ring) + kh * 3072 + (4 * h + (r16 >> 2)) * 48 + ((g & 1) ? ((lane & 3) ? 40 : 32) : (lane & 3) * 8);
    float ma, mb;
    {
      const u32x4 k1 = ld128(ring + lane * 16); const u32x2 k2 = ld64(ring + 1024 + (lane & 31) * 8);
      ma = max16(scores(k1, k2, qa1, qa2)); mb = max16(scores(k1, k2, qb1, qb2));
    }
    for (int attempt = 0;; attempt++) {
      ma = fmaxf(ma, __shfl_xor(ma, 32)); mb = fmaxf(mb, __shfl_xor(mb, 32));
      f32x16 cia, cib, oa, ob;
#pragma unroll
      for (int i = 0; i < 16; i++) { cia[i] = -ma; cib[i] = -mb; }
      int cnt = 4;
      MARK(MK + 2);
      asm volatile(
          "ds_read_b128 v[80:83], v100\n\t"
          "ds_read_b64 v[84:85], v101\n\t"
          "ds_read_b64_tr_b16 v[86:87], v102 offset:5120\n\t"
          "ds_read_b64_tr_b16 v[88:89], v102 offset:5504\n\t"
          Z4(0) Z4(68) Z4(76) Z4(90)
          "s_waitcnt lgkmcnt(0)\n\t"
          "v_mfma_f32_32x32x16_bf16 v[32:47], v[80:83], v[94:97], v[16:31]\n\t"
          Z4(4) Z4(8) Z4(12) Z4(104) Z4(108) Z4(112) Z4(116)
          "v_mfma_f32_32x32x8_bf16 v[32:47], v[84:85], v[98:99], v[32:47]\n\ts_nop 9\n\t"
          "L_attn_%=:\n\t"
          CHUNK(0) CHUNK(1) CHUNK(2) CHUNK(3)
          "s_sub_u32 %[cnt], %[cnt], 1\n\ts_cmp_lg_u32 %[cnt], 0\n\ts_cbranch_scc1 L_attn_%=\n\t"
          "s_setprio 0\n\t" CVT(78,68) CVT(79,70) "s_waitcnt lgkmcnt(0)\n\ts_nop 1\n\t"
          "v_mfma_f32_32x32x16_bf16 v[104:119], v[90:93], v[76:79], v[104:119]\n\ts_nop 15\n\t"
          : "={v[0:15]}"(oa), "={v[104:119]}"(ob), [cnt] "+s"(cnt)
          : "{v[16:31]}"(cia), "{v[120:135]}"(cib), "{v[94:97]}"(qa1), "{v[98:99]}"(qa2), "{v[136:139]}"(qb1), "{v[140:141]}"(qb2),
            "{v100}"(ka), "{v101}"(kb), "{v102}"(va)
          : "memory", "scc", "v32", "v33", "v34", "v35", "v36", "v37", "v38", "v39", "v40", "v41", "v42", "v43", "v44", "v45", "v46", "v47",
            "v48", "v49", "v50", "v51", "v52", "v53", "v54", "v55", "v56", "v57", "v58", "v59", "v60", "v61", "v62", "v63", "v64", "v65",
            "v66", "v67", "v68", "v69", "v70", "v71", "v72", "v73", "v74", "v75", "v76", "v77", "v78", "v79", "v80", "v81", "v82", "v83",
            "v84", "v85", "v86", "v87", "v88", "v89", "v90", "v91", "v92", "v93");
      MARK(MK + 3);
      LDS_AS f32x4* ex = (LDS_AS f32x4*)(ring + L_EX + wm * 3072) + lane;
#pragma unroll
      for (int i = 0; i < 3; i++) ex[i * 64] = f32x4{ob[4 * i], ob[4 * i + 1], ob[4 * i + 2], ob[4 * i + 3]};
      f32x4 cs = {0.f, 0.f, 0.f, 0.f};
#pragma unroll
      for (int i = 0; i < 3; i++) cs += f32x4{oa[4 * i], oa[4 * i + 1], oa[4 * i + 2], oa[4 * i + 3]} + f32x4{ob[4 * i], ob[4 * i + 1], ob[4 * i + 2], ob[4 * i + 3]};
      if (!(fabsf(cs[0] + cs[1] + cs[2] + cs[3]) < INFINITY) || (h && !(oa[8] + ob[8] < 0x1p60f))) misc[1] = 1u;
      lds_bar();
      const unsigned vote = *(volatile LDS_AS unsigned*)(misc + 1);
      LDS_AS f32x4* ey = (LDS_AS f32x4*)(ring + L_EX + (wm ^ 1) * 3072) + lane;
#pragma unroll
      for (int i = 0; i < 3; i++) r[i] = ey[i * 64] + f32x4{oa[4 * i], oa[4 * i + 1], oa[4 * i + 2], oa[4 * i + 3]};
      if (attempt || !vote) break;
      lds_bar();
      ma = -INFINITY; mb = -INFINITY;
      for (int t = 0; t < 64; t++) {
        const unsigned kb_ = KV + (head * 16 + (t >> 2)) * KVC + (t & 3) * 1280;
        const u32x4 k1 = gl128<16>(rw, kb_ + lane * 16); const u32x2 k2 = gl64<16>(rw, kb_ + 1024 + (lane & 31) * 8);
        ma = fmaxf(ma, max16(scores(k1, k2, qa1, qa2))); mb = fmaxf(mb, max16(scores(k1, k2, qb1, qb2)));
      }
      raw_bar();
    }
  }
  {
    const unsigned lb = __float_as_uint(r[2][0]);
    const float il = __builtin_amdgcn_rcpf(__uint_as_float(__builtin_amdgcn_permlane32_swap(lb, lb, false, false)[1]));
    const unsigned sl8 = os & 255;
    st128<16>(rw, os + sl8 + h * 512, clean(pk8(r[0] * il, r[1] * il)));
    const u32x4 t = clean(pk8(r[2] * il, r[2] * il));
    st64<16>(rw, h ? WS_END : os + 1024, u32x2{t[0], t[1]});
  }
  MARK(MK + 4);
  const lptr wa = lds + L_A + wn * 35 * 1024 + lane * 16, wb = ring + lane * 16;
  constexpr int NPRE = 15;
  u32x4 bf[50];
  f32x4 acc[5] = {};
#pragma unroll
  for (int j = 0; j < NPRE; j++) bf[j] = ld128(wa + j * 1024);
#pragma unroll
  for (int j = 0; j < 5; j++) asm volatile("" : "+v"(acc[j]));
  if (FINAL) dc = __uint_as_float(gl32<16>(rw, o3[3]));
#pragma unroll 1
  for (int spins = 0; spins < (1 << 18); spins++) {
#pragma unroll
    for (int i = 0; i < 3; i++) x4v[i] = gl64<16>(rw, o3[i]);
    if (!FINAL || !__builtin_amdgcn_ballot_w64(umax4(max(x4v[0][0], x4v[0][1]), u32x4{x4v[1][0], x4v[1][1], x4v[2][0], x4v[2][1]}) == EMPTY)) break;
    nap<8>();
  }
  drain();
  nap<ODELAY>();
  MARK(MK + 5);
  {
    u32x4 af[10];
    auto ldf = [&](int i) {
      if (i < 8) af[i] = __builtin_amdgcn_raw_buffer_load_b128(rw, o1, i * 2 * 1280, 16);
      else { const u32x2 a = __builtin_amdgcn_raw_buffer_load_b64(rw, o2, (i - 8) * 8 * 1280, 16), b = __builtin_amdgcn_raw_buffer_load_b64(rw, o2, ((i - 8) * 8 + 1) * 1280, 16);
             af[i] = u32x4{a[0], a[1], b[0], b[1]}; }
    };
#pragma unroll
    for (int i = 0; i < 10; i++) ldf(i);
#pragma unroll 1
    for (int spins = 0; spins < (1 << 18); spins++) {
      unsigned m = 0;
#pragma unroll
      for (int i = 0; i < 10; i++) m = umax4(m, af[i]);
      if (!__builtin_amdgcn_ballot_w64(m == EMPTY)) break;
      nap<RSLEEP>();
#pragma unroll
      for (int i = 0; i < 10; i++) if (__builtin_amdgcn_ballot_w64(umax4(0, af[i]) == EMPTY)) ldf(i);
    }
    MARK(MK + 8);
#pragma unroll
    for (int i = 0; i < 10; i++) asm volatile("" : "+v"(af[i]));
    asm volatile("s_nop 1" : "+v"(acc[0]), "+v"(acc[1]), "+v"(acc[2]), "+v"(acc[3]), "+v"(acc[4]));
    SB();
#pragma unroll
    for (int i = 0; i < 10; i++) {
#pragma unroll
      for (int a = 0; a < 5; a++) {
        if (FINAL) mfma16v(acc[a], bf[5 * i + a], af[i]); else mfma16v(acc[a], af[i], bf[5 * i + a]);
        const int j = 5 * i + a + NPRE; if (j < 50) bf[j] = ld128(j < 35 ? wa + j * 1024 : wb + (j - 35) * 1024);
        SB();
      }
    }
  }
  asm volatile("s_nop 7\n\ts_nop 1" : "+v"(acc[0]), "+v"(acc[1]), "+v"(acc[2]), "+v"(acc[3]), "+v"(acc[4]) :: "memory");
  MARK(MK + 6);
  {
    LDS_AS f32x4* ex = (LDS_AS f32x4*)(ring + L_EX + 12288 + wm * 3072) + lane;
    LDS_AS f32x4* ey = (LDS_AS f32x4*)(lds + (wn ^ 1) * HR + L_EX + 12288 + wm * 3072) + lane + 64 * wn;
    ex[0] = acc[2]; ex[64] = acc[3]; ex[128] = acc[4];
    dc *= 0.039528470752104741f;
#pragma unroll
    for (int i = 0; i < 3; i++) { acc[i] += f32x4{bflo(x4v[i][0]), bfhi(x4v[i][0]), bflo(x4v[i][1]), bfhi(x4v[i][1])}; if (FINAL) acc[i] += dc; }
    lds_bar();
#pragma unroll
    for (int i = 0; i < 3; i++) acc[i] += ey[64 * i];
  }
  if (FINAL) {
    const rsrc_t ro = mk_rsrc(out, 2048 * 640 * 2);
    const unsigned op = (rowof(pblk + trow(wm & 1, r16)) * 640 + 80 * ct + 48 * wn + 4 * g) * 2;
#pragma unroll
    for (int i = 0; i < 3; i++)
      if (i < 2 || wn == 0) st64<OUTAUX>(ro, op + 32 * i, pk4(acc[i]));
  } else {
    f32x4 sq = {0.f, 0.f, 0.f, 0.f};
#pragma unroll
    for (int i = 0; i < 3; i++)
      if (i < 2 || wn == 0) {
        const int j = 80 * ct + 48 * wn + 16 * i + r16;
        st128<16>(rw, r3t + j * 64 + g * 16, clean(asu(acc[i])));
        if (j != 1) sq += acc[i] * acc[i];
      }
#pragma unroll
    for (int e = 0; e < 4; e++) sq[e] = row16_sum(sq[e]);
    LDS_AS float* sl = (LDS_AS float*)(lds + L_MISC);
    if (r16 == 0) *(LDS_AS f32x4*)(sl + wn * 64 + 32 * (wm & 1) + 16 * (wm >> 1) + 4 * g) = sq;
    lds_bar();
    if (w == 0) {
      LDS_AS f32x4* s4 = (LDS_AS f32x4*)sl + 2 * (lane & 7);
      st128<16>(rw, WS_SSQ + (lane >> 3) * 32768 + (ct * 2048 + 1024 * ((lane >> 2) & 1) + 64 * qt + 32 * hf + 8 * (lane & 3)) * 2, clean(pk8(s4[0] + s4[16], s4[1] + s4[17])));
    }
  }
  MARK(MK + 7);
}

__global__ void __launch_bounds__(768) k_main(char* ws, u16* out) {
  STAMP(1);
  if (__builtin_expect(wave_id() >= 8, 1)) {
    extern __shared__ __attribute__((aligned(16))) char smem[];
    __builtin_amdgcn_s_setprio(3);
    if (threadIdx.x < 528) ((LDS_AS unsigned*)((lptr)smem + L_MISC + 1024))[threadIdx.x - 512] = 0u;
    asm volatile("s_waitcnt lgkmcnt(0)" ::: "memory");
    loader<false>(ws SPASS);
    loader<true>(ws SPASS);
  } else {
    compute<false>(ws, out SPASS);
    compute<true>(ws, out SPASS);
  }
}

#define STOPAT(n)
extern "C" void kernel_launch(void* const* d_in, const int* in_sizes, int n_in, void* d_out, int out_size, void* d_ws, size_t ws_size,
                              hipStream_t stream) {
  const u16* x = (const u16*)d_in[0];
  const u16* wr = (const u16*)d_in[1];
  const u16* wi = (const u16*)d_in[2];
  const u16* wqkv = (const u16*)d_in[4];
  const u16* wo = (const u16*)d_in[5];
  char* ws = (char*)d_ws;
  (void)hipFuncSetAttribute((const void*)k_prep, hipFuncAttributeMaxDynamicSharedMemorySize, LDS_PREP);
  (void)hipFuncSetAttribute((const void*)k_main, hipFuncAttributeMaxDynamicSharedMemorySize, LDS_FUSED);
  k_prep<<<256, 512, LDS_PREP, stream>>>(x, wr, wi, wqkv, wo, ws);
  STOPAT(1)
  k_main<<<256, 768, LDS_FUSED, stream>>>(ws, (u16*)d_out);
}
```

```cpp
#include <hip/hip_runtime.h>
#include <hip/hip_bf16.h>
#include <utility>
#include <type_traits>

typedef unsigned short u16;
typedef __attribute__((ext_vector_type(16))) float f32x16;
typedef __attribute__((ext_vector_type(4))) float f32x4;
typedef __attribute__((ext_vector_type(2))) float f32x2;
typedef __attribute__((ext_vector_type(8))) __bf16 bf16x8;
typedef __attribute__((ext_vector_type(2))) __bf16 bf16x2;
typedef __attribute__((ext_vector_type(4))) short s16x4;
typedef __attribute__((ext_vector_type(4))) unsigned u32x4;
typedef __attribute__((ext_vector_type(2))) unsigned u32x2;
#define DI __device__ __forceinline__
#define LDS_AS __attribute__((address_space(3)))
typedef LDS_AS char* lptr;

constexpr double cx_cos_small(double x) { double x2 = x * x, t = 1, s = 1; for (int i = 1; i < 12; i++) { t *= -x2 / ((2 * i - 1) * (2 * i)); s += t; } return s; }
constexpr double cx_sin_small(double x) { double x2 = x * x, t = x, s = x; for (int i = 1; i < 12; i++) { t *= -x2 / ((2 * i) * (2 * i + 1)); s += t; } return s; }
constexpr double cx_cos640(int m) {
  m %= 640; if (m > 320) m = 640 - m; double sg = 1; if (m > 160) { m = 320 - m; sg = -1; }
  const double w = 6.283185307179586476925 / 640; return sg * (m <= 80 ? cx_cos_small(w * m) : cx_sin_small(w * (160 - m)));
}
constexpr u16 cx_bf16(double d) { float f = (float)d; unsigned u = __builtin_bit_cast(unsigned, f); u += 0x7FFFu + ((u >> 16) & 1u); return (u16)(u >> 16); }
struct Cos640 { double v[640]; };
constexpr Cos640 mk_cos() { Cos640 c{}; for (int m = 0; m < 640; m++) c.v[m] = cx_cos640(m); return c; }
constexpr Cos640 COS = mk_cos();
__host__ __device__ constexpr int kf_of(int G, int i) { return G ? G + 40 * i : (i == 0 ? 320 : i < 8 ? 40 * i : 40 * i - 300); }
__host__ __device__ constexpr int freq_of(int G, int i) { return kf_of(G, i) > 320 ? 640 - kf_of(G, i) : kf_of(G, i); }
constexpr double a1val(int out, int a) {
  if (out >= 40 || a >= 40) return 0;
  const int G = out >> 1, part = out & 1;
  if (G == 0) return (part && (a & 1)) ? -1 : 1;
  return COS.v[(16 * a * G + 160 * part) % 640];
}
constexpr double m2val(int G, int o, int k) {
  const int i = o >> 1, po = o & 1, part = k >> 4, b = k & 15, kf = kf_of(G, i);
  const double c = COS.v[(b * kf) % 640], sn = COS.v[(b * kf + 480) % 640];
  if (G == 0) {
    if (part != (i >= 8)) return 0;
    if (i == 0) return 0.039528470752104741 * (po ? 1.0 : c);
    return 0.055901699437494742 * (po ? -sn : c);
  }
  const double v = po ? (part ? c : -sn) : (part ? sn : c);
  return 0.055901699437494742 * ((po && kf > 320) ? -v : v);
}
struct DTab { u16 a1[3][64][8]; u16 a1b[3][64][4]; u16 pad[256]; u16 m2[20][2][64][8]; };
constexpr DTab mk_dtab() {
  DTab t{};
  for (int l = 0; l < 64; l++) {
    const int r = l & 15, g = l >> 4;
    for (int T = 0; T < 3; T++) {
      for (int e = 0; e < 8; e++) t.a1[T][l][e] = cx_bf16(a1val(16 * T + r, 16 * (e >> 2) + 4 * g + (e & 3)));
      for (int e = 0; e < 4; e++) t.a1b[T][l][e] = cx_bf16(a1val(16 * T + r, 32 + 4 * g + e));
    }
    for (int G = 0; G < 20; G++) for (int T = 0; T < 2; T++) for (int e = 0; e < 8; e++)
      t.m2[G][T][l][e] = cx_bf16(m2val(G, 8 * (r >> 2) + 4 * T + (r & 3), 8 * g + e));
  }
  return t;
}
static __device__ const __attribute__((aligned(1024))) DTab g_dt = mk_dtab();

struct CTab { u16 v[10 * 512]; };
constexpr CTab mk_ctab() {
  CTab c{}; u16 cs[64] = {};
  for (int m = 0; m < 64; m++) cs[m] = cx_bf16(cx_cos640(10 * m));
  for (int f = 0; f < 10; f++) for (int l = 0; l < 64; l++) for (int e = 0; e < 8; e++) {
    const int r = l & 15, g = l >> 4;
    const int m = f < 8 ? (8 * g + e) * (16 * (f >> 1) + r) : 2 * r * (16 * (e >> 2) + 4 * g + (e & 3));
    c.v[(f * 64 + l) * 8 + e] = cs[(m + (f & 1) * 48) & 63];
  }
  return c;
}
static __device__ const __attribute__((aligned(1024))) CTab g_ct = mk_ctab();

constexpr int RP = 2048 + 64, RBP = 2048 + 128;
constexpr unsigned RBSZ = 640 * RBP * 2;
constexpr unsigned WS_FL = 0;
constexpr unsigned WS_AUX = WS_FL + 3 * 32768;
constexpr unsigned WS_F0 = WS_AUX + 2048;
constexpr unsigned WS_GO = WS_F0 + 8 * 2048;
constexpr unsigned WS_EP = WS_GO + 256 * 64;
constexpr unsigned WS_RS1 = WS_EP + 256;
constexpr unsigned WS_RB0 = WS_RS1 + 8192;
constexpr unsigned WS_RB1 = WS_RB0 + RBSZ;
constexpr unsigned WS_WF = WS_RB1 + RBSZ;
constexpr unsigned WS_WQ = WS_WF + 320 * 8192;
constexpr unsigned WS_WOF = WS_WQ + 16 * 10 * 20 * 1024;
constexpr unsigned WS_WOP = WS_WOF + 40 * 20 * 1024;
constexpr int KVC = 11264;
constexpr unsigned WS_KV = WS_WOP + 40 * 20 * 1024;
constexpr unsigned WS_O = WS_KV + 32 * 16 * KVC;
constexpr unsigned LAYER2 = 32 * 16 * KVC + 32 * 64 * 1280;
constexpr unsigned WS_X4 = WS_KV + 2 * LAYER2;
constexpr unsigned WS_R3 = WS_X4 + 2048 * 640 * 2;
constexpr unsigned WS_RB2 = WS_R3 + 640 * RP * 4;
constexpr unsigned WS_SSQ = WS_RB2 + RBSZ;
constexpr unsigned EMPTY = 0xFFFFFFFFu;
constexpr unsigned WS_END = WS_SSQ + 8 * 2048 * 16;
#define FILLDELAY 10
#define OUTAUX 0
#define XDELAY 20
#define SW1 50
#define SW2 46
#define XNL 0
#define K1DELAY 40
#define YDELAY 0
#define ODELAY 0
#define WMODE 0
#define YIDLE 8
#define PROBE 1
#define TPOLL 300
#define TSLEEP 1
#define GSLEEP 2
#define LDELAY 30
#define TFILL 450
#define PFDELAY 40
#define YSLEEP 4
#define XSLEEP 1
#define RSLEEP 2
#define AUXB 16

#define STW 0
#define STAMP(k)
#define MARK(i)
#define SARG
#define SPASS
#define SB() __builtin_amdgcn_sched_barrier(0)
typedef __amdgpu_buffer_rsrc_t rsrc_t;
DI rsrc_t mk_rsrc(const void* p, unsigned bytes) { return __builtin_amdgcn_make_buffer_rsrc((void*)p, 0, bytes, 0x00020000); }
template <int AUX> DI void st128(rsrc_t r, unsigned off, u32x4 v) { __builtin_amdgcn_raw_buffer_store_b128(v, r, off, 0, AUX); }
template <int AUX> DI void st64(rsrc_t r, unsigned off, u32x2 v) { __builtin_amdgcn_raw_buffer_store_b64(v, r, off, 0, AUX); }
template <int AUX> DI void st32(rsrc_t r, unsigned off, unsigned v) { __builtin_amdgcn_raw_buffer_store_b32(v, r, off, 0, AUX); }
template <int AUX> DI void st16(rsrc_t r, unsigned off, u16 v) { __builtin_amdgcn_raw_buffer_store_b16(v, r, off, 0, AUX); }
template <int AUX> DI u32x4 gl128(rsrc_t r, unsigned off) { return __builtin_amdgcn_raw_buffer_load_b128(r, off, 0, AUX); }
template <int AUX> DI u32x2 gl64(rsrc_t r, unsigned off) { return __builtin_amdgcn_raw_buffer_load_b64(r, off, 0, AUX); }
template <int AUX> DI unsigned gl32(rsrc_t r, unsigned off) { return __builtin_amdgcn_raw_buffer_load_b32(r, off, 0, AUX); }
DI u32x4 asu(f32x4 v) { return __builtin_bit_cast(u32x4, v); }
DI f32x4 asf(u32x4 v) { return __builtin_bit_cast(f32x4, v); }
DI unsigned pk2(float a, float b) { f32x2 v = {a, b}; return __builtin_bit_cast(unsigned, __builtin_convertvector(v, bf16x2)); }
DI u32x2 pk4(f32x4 a) { return u32x2{pk2(a[0], a[1]), pk2(a[2], a[3])}; }
DI u32x4 pk8(f32x4 a, f32x4 b) { return u32x4{pk2(a[0], a[1]), pk2(a[2], a[3]), pk2(b[0], b[1]), pk2(b[2], b[3])}; }
DI u32x4 clean(u32x4 v) { return __builtin_elementwise_min(v, u32x4{EMPTY - 1, EMPTY - 1, EMPTY - 1, EMPTY - 1}); }
DI unsigned umax4(unsigned m, u32x4 v) { return max(max(max(m, v[0]), v[1]), max(v[2], v[3])); }
DI float bflo(unsigned u) { return __uint_as_float(u << 16); }
DI float bfhi(unsigned u) { return __uint_as_float(u & 0xFFFF0000u); }
DI float dot2(unsigned a, unsigned b, float c) { return __builtin_amdgcn_fdot2_f32_bf16(__builtin_bit_cast(bf16x2, a), __builtin_bit_cast(bf16x2, b), c, false); }
DI int rowof(int p) { return 32 * (((p >> 5) & 0x3C) + (p & 3)) + ((p << 2) & 16) + ((p >> 3) & 15); }
DI int rpos(int p) { return ((p << 8) & 1024) + ((p >> 3) << 2) + (p & 3); }
DI int trow(int T, int r) { return 8 * (r >> 2) + 4 * T + (r & 3); }
template <int CTRL> DI float dpp_add(float v) { return v + __int_as_float(__builtin_amdgcn_update_dpp(0, __float_as_int(v), CTRL, 0xF, 0xF, true)); }
DI float row16_sum(float v) { v = dpp_add<0x128>(v); v = dpp_add<0x124>(v); v = dpp_add<0x122>(v); return dpp_add<0x121>(v); }
DI f32x4 mfma16(u32x4 a, u32x4 b, f32x4 c) {
  return __builtin_amdgcn_mfma_f32_16x16x32_bf16(__builtin_bit_cast(bf16x8, a), __builtin_bit_cast(bf16x8, b), c, 0, 0, 0);
}
DI void mfma16v(f32x4& c, u32x4 a, u32x4 b) { asm volatile("v_mfma_f32_16x16x32_bf16 %0, %1, %2, %0" : "+v"(c) : "v"(a), "v"(b)); }
DI u32x2 ldtr(lptr p) { return __builtin_bit_cast(u32x2, __builtin_amdgcn_ds_read_tr16_b64_v4i16((LDS_AS s16x4*)p)); }
DI u32x4 ld128(lptr p) { return *(LDS_AS u32x4*)p; }
DI u32x2 ld64(lptr p) { return *(LDS_AS u32x2*)p; }
#define GL(v, s, o, x) "global_load_lds_dwordx4 " v ", " s " offset:" #o x "\n\t"
#define M0SET "s_mov_b32 %[sv], m0\n\ts_mov_b32 m0, %[l]\n\ts_nop 2\n\t"
#define M0ADD(x) "s_add_u32 m0, m0, " x "\n\ts_nop 0\n\t"
#define M0RST "s_mov_b32 m0, %[sv]"
DI void dma2(unsigned v, const char* sb, unsigned l) { unsigned sv;
  asm volatile(M0SET GL("%[v]", "%[s]", 0, " sc1") GL("%[v]", "%[s]", 1024, " sc1") M0RST : [sv] "=&s"(sv) : [v] "v"(v), [s] "s"(sb), [l] "s"(l) : "memory"); }
DI void dma4(unsigned v, const char* sb, unsigned l) { unsigned sv;
  asm volatile(M0SET GL("%[v]", "%[s]", 0, "") GL("%[v]", "%[s]", 1024, "") GL("%[v]", "%[s]", 2048, "") GL("%[v]", "%[s]", 3072, "") M0RST
               : [sv] "=&s"(sv) : [v] "v"(v), [s] "s"(sb), [l] "s"(l) : "memory"); }
DI void dma6_sc1(unsigned v, const char* sb, unsigned l) { unsigned sv;
  asm volatile(M0SET GL("%[v]", "%[s]", 0, " sc1") GL("%[v]", "%[s]", 1024, " sc1") GL("%[v]", "%[s]", 2048, " sc1") M0ADD("3072")
               GL("%[w]", "%[s]", 0, " sc1") GL("%[w]", "%[s]", 1024, " sc1") GL("%[w]", "%[s]", 2048, " sc1") M0RST
               : [sv] "=&s"(sv) : [v] "v"(v), [w] "v"(v + 3072), [s] "s"(sb), [l] "s"(l) : "memory", "scc"); }
template <int N> DI void vm_bar() { asm volatile("s_waitcnt vmcnt(%0)\n\ts_barrier" ::"i"(N) : "memory"); }
DI void lds_bar() { asm volatile("s_waitcnt lgkmcnt(0)\n\ts_barrier" ::: "memory"); }
DI void raw_bar() { asm volatile("s_barrier" ::: "memory"); }
template <int N> DI void nap() { __builtin_amdgcn_s_sleep(N); asm volatile("" ::: "memory"); }
DI void drain() { asm volatile("s_waitcnt vmcnt(0)" ::: "memory"); }
#define CODE_ADDR(sym) ({ unsigned lo_, hi_; \
    asm volatile("s_getpc_b64 vcc\n\ts_add_u32 %0, vcc_lo, " sym "@rel32@lo+4\n\ts_addc_u32 %1, vcc_hi, " sym "@rel32@hi+12" : "=s"(lo_), "=s"(hi_) :: "vcc", "scc"); \
    (const unsigned*)(((unsigned long long)hi_ << 32) | lo_); })
#define SYM_MAIN "_Z6k_mainPKtS0_S0_S0_S0_PcPt"
constexpr int CODE_LINES = 368;
DI int wave_id() { return __builtin_amdgcn_readfirstlane(threadIdx.x >> 6); }
DI unsigned lds_addr(lptr p) { return (unsigned)(size_t)p; }

DI int kappa_to_i(int kap) {
  int ks = kap >> 5, g = (kap >> 3) & 3, e = kap & 7;
  if (ks < 16) return (2 * ks + (g >> 1)) * 20 + 8 * (e >> 2) + 4 * (g & 1) + (e & 3);
  return (8 * (ks - 16) + 2 * g + (e >> 2)) * 20 + 16 + (e & 3);
}

DI unsigned wo_frag(int nt, int ks) {
  const int ct = nt / 5, c5 = nt - 5 * ct, kh = ks < 16 ? ks >> 3 : (ks - 16) >> 1, i = ks < 16 ? ks & 7 : 8 + (ks & 1);
  const int a = kh ? (c5 >= 3 ? c5 - 3 : c5 + 2) : c5;
  return ((ct * 2 + kh) * 50 + 5 * i + a) * 1024;
}

constexpr int SLOT32 = 36864;
DI void dma_a(const unsigned* vm, const char* mb, unsigned l) { unsigned sv;
  asm volatile(M0SET GL("%[a0]", "%[m]", 0, " sc1") GL("%[a1]", "%[m]", 1024, " sc1") GL("%[a2]", "%[m]", 2048, " sc1") GL("%[a3]", "%[m]", 3072, " sc1") M0RST
               : [sv] "=&s"(sv) : [a0] "v"(vm[0]), [a1] "v"(vm[1]), [a2] "v"(vm[2]), [a3] "v"(vm[3]), [m] "s"(mb), [l] "s"(l) : "memory"); }
DI void dma_s(unsigned v0, unsigned v1, const char* sb, unsigned l) { unsigned sv;
  asm volatile(M0SET GL("%[v0]", "%[s]", 0, " sc1") GL("%[v0]", "%[s]", 1024, " sc1") GL("%[v1]", "%[s]", 2048, " sc1") GL("%[v1]", "%[s]", 3072, " sc1") M0RST
               : [sv] "=&s"(sv) : [v0] "v"(v0), [v1] "v"(v1), [s] "s"(sb), [l] "s"(l) : "memory"); }
DI void dma1_sc1(unsigned v, const char* sb, unsigned l) { unsigned sv;
  asm volatile(M0SET GL("%[v]", "%[s]", 0, " sc1") M0RST : [sv] "=&s"(sv) : [v] "v"(v), [s] "s"(sb), [l] "s"(l) : "memory"); }
DI void dma1(unsigned v, const char* sb, unsigned l) { unsigned sv;
  asm volatile(M0SET GL("%[v]", "%[s]", 0, "") M0RST : [sv] "=&s"(sv) : [v] "v"(v), [s] "s"(sb), [l] "s"(l) : "memory"); }

constexpr int L_I2 = 40 * 1056, LDS_PREP = L_I2 + 20 * 2064 + 1024;
DI void prep(const u16* x, const u16* wr, const u16* wi, const u16* wqkv, const u16* wo, char* ws) {
  extern __shared__ __attribute__((aligned(16))) char smem[];
  const lptr lds = (lptr)smem;
  const int tid = threadIdx.x, lane = tid & 63, w = wave_id(), g = lane >> 4, r16 = lane & 15;
  const int job = blockIdx.x;
  if (__builtin_expect(job < 144, 1)) {
    const bool cols = job >= 64 && job < 124;
    const int cj = job - 64, cb = cj < 48 ? (cj & 48) + 2 * (cj & 7) + ((cj >> 3) & 1) : cj;
    u32x4 a1[3], m2[3][2]; u32x2 a1b[3];
#pragma unroll
    for (int T = 0; T < 3; T++) {
      asm volatile("global_load_dwordx4 %0, %1, %2" : "=v"(a1[T]) : "v"((T * 64 + lane) * 16), "s"(&g_dt.a1[0][0][0]) : "memory");
      asm volatile("global_load_dwordx2 %0, %1, %2" : "=v"(a1b[T]) : "v"((T * 64 + lane) * 8), "s"(&g_dt.a1b[0][0][0]) : "memory");
    }
    SB();
    if (cols) {
#pragma unroll
      for (int i = 0; i < 5; i++) dma1((16 * (5 * w + i) + (lane >> 2)) * 3840 + cb * 64 + (lane & 3) * 16, (const char*)wqkv, lds_addr(lds) + (5 * w + i) * 1056);
    } else {
      const char* src = (const char*)(job < 64 ? x : wo);
      const int sl = 8 * (w & 3) + 4 * (w >> 2);
      src += (job < 64 ? rowof(32 * job + sl) : kappa_to_i(32 * (job - 124) + sl)) * 1280;
      const int st = (job < 64 ? 31 : 0) * 1280;
#pragma unroll
      for (int i = 0; i < 5; i++) dma1(1024 * i + lane * 16 + ((i == 4 || lane < 16 * i) ? i - 1 : i) * st, src, lds_addr(lds) + (5 * w + i) * 1024);
    }
#pragma unroll
    for (int i = 0; i < 3; i++)
#pragma unroll
      for (int T = 0; T < 2; T++)
        asm volatile("global_load_dwordx4 %0, %1, %2" : "=v"(m2[i][T]) : "v"(((((i < 2 || w < 4) ? w + 8 * i : w) * 2 + T) * 64 + lane) * 16), "s"(&g_dt.m2[0][0][0][0]) : "memory");
    unsigned so[3][2]; float sc[2] = {1.f, 1.f};
    if (cols) {
#pragma unroll
      for (int m = 0; m < 2; m++) {
        const int c = 32 * cb + 16 * m + r16;
        const int which = c / 640, rem = c - which * 640, head = rem / 20, d = rem - head * 20, grp = d >> 2;
        const int T = grp < 4 ? (which == 2 ? 2 : 0) + (grp & 1) : (which == 1 ? 3 : 2), gs = grp < 4 ? (which == 1 ? 2 : 0) + (grp >> 1) : (which == 2 ? 3 : 2);
        if (which == 0) sc[m] = 0.22360679774997896f * 1.4426950408889634f;
#pragma unroll
        for (int i = 0; i < 3; i++) so[i][m] = WS_WQ + (((head >> 1) * 10 + 4 * (head & 1) + T) * 20 + w + 8 * i) * 1024 + (g * 16 + 4 * gs + (d & 3)) * 16;
      }
    } else if (job < 64) {
#pragma unroll
      for (int i = 0; i < 3; i++)
#pragma unroll
        for (int T = 0; T < 2; T++) so[i][T] = WS_RB0 + (job * 641 + (job & 1) + 32 * (w + 8 * i) + trow(T, r16)) * 64 + g * 16;
    } else {
#pragma unroll
      for (int i = 0; i < 3; i++) {
        const unsigned f = wo_frag(2 * (w + 8 * i) + (r16 >> 3), job - 124);
#pragma unroll
        for (int T = 0; T < 2; T++) so[i][T] = WS_WOF + f + (g * 16 + (trow(T, r16) & 15)) * 16;
      }
    }
    if (w >= 4) { so[2][0] = WS_END; so[2][1] = WS_END; }
    asm volatile("" : "+v"(so[0][0]), "+v"(so[0][1]), "+v"(so[1][0]), "+v"(so[1][1]), "+v"(so[2][0]), "+v"(so[2][1]), "+v"(sc[0]), "+v"(sc[1]));
    const f32x4 z4 = {0.f, 0.f, 0.f, 0.f};
    const lptr i2 = lds + L_I2;
    asm volatile("s_waitcnt vmcnt(6)" : "+v"(a1[0]), "+v"(a1[1]), "+v"(a1[2]), "+v"(a1b[0]), "+v"(a1b[1]), "+v"(a1b[2]) :: "memory");
    f32x4 t1[4][3];
    if (cols) {
      raw_bar();
      const lptr src = lds + (4 * g + (r16 >> 2)) * 1056 + (w >> 1) * 256 + (w & 1) * 32 + (lane & 3) * 8, srb = src + (32 - 8 * (g >> 1)) * 1056;
#pragma unroll
      for (int i = 0; i < 4; i++) {
        const u32x2 f0 = ldtr(src + i * 64), f1 = ldtr(src + 16 * 1056 + i * 64), f2 = ldtr(srb + i * 64);
#pragma unroll
        for (int T = 0; T < 3; T++) t1[i][T] = mfma16(u32x4{a1b[T][0], a1b[T][1], 0u, 0u}, u32x4{f2[0], f2[1], 0u, 0u}, mfma16(a1[T], u32x4{f0[0], f0[1], f1[0], f1[1]}, z4));
      }
      const int xo = ((w >> 1) * 8) ^ (((r16 >> 1) & 3) * 16);
      const lptr dst = i2 + 2 * g * 2064 + (16 * (w & 1) + r16) * 64;
#pragma unroll
      for (int T = 0; T < 3; T++)
#pragma unroll
        for (int e = 0; e < 4; e++)
          if (T < 2 || g < 2) *(LDS_AS u32x2*)(dst + (8 * T + (e >> 1)) * 2064 + (xo ^ ((e & 1) * 32))) = u32x2{pk2(t1[0][T][e], t1[1][T][e]), pk2(t1[2][T][e], t1[3][T][e])};
    } else {
      const lptr src = lds + w * 5120 + lane * 8, srb = lds + w * 5120 + 1024 + (lane & 31) * 8;
#pragma unroll
      for (int i = 0; i < 4; i++) {
        const u32x2 f0 = ldtr(src + i * 1280), f1 = ldtr(src + i * 1280 + 512), f2 = ldtr(srb + i * 1280);
#pragma unroll
        for (int T = 0; T < 3; T++) t1[i][T] = mfma16(u32x4{f2[0], f2[1], 0u, 0u}, u32x4{a1b[T][0], a1b[T][1], 0u, 0u}, mfma16(u32x4{f0[0], f0[1], f1[0], f1[1]}, a1[T], z4));
      }
      const int xo = ((r16 & 1) * 32 + g * 8) ^ ((w & 1) * 32);
      const lptr dst = i2 + (r16 >> 1) * 2064 + w * 256;
#pragma unroll
      for (int i = 0; i < 4; i++)
#pragma unroll
        for (int T = 0; T < 3; T++)
          if (T < 2 || r16 < 8) *(LDS_AS u32x2*)(dst + T * 8 * 2064 + i * 64 + (xo ^ ((i >> 1) * 16))) = pk4(t1[i][T]);
    }
    lds_bar();
    const rsrc_t rw = mk_rsrc(ws, WS_END);
    asm volatile("s_waitcnt vmcnt(0)" : "+v"(m2[0][0]), "+v"(m2[0][1]), "+v"(m2[1][0]), "+v"(m2[1][1]), "+v"(m2[2][0]), "+v"(m2[2][1]));
    f32x4 sq[2] = {z4, z4};
    u32x4 f[3][2];
#pragma unroll
    for (int i = 0; i < 3; i++) {
      const lptr fp = i2 + ((i < 2 || w < 4) ? w + 8 * i : 0) * 2064 + r16 * 64 + ((g ^ ((r16 >> 1) & 3)) * 16);
      f[i][0] = ld128(fp); f[i][1] = ld128(fp + 1024);
    }
    if (cols) {
#pragma unroll
      for (int i = 0; i < 3; i++)
#pragma unroll
        for (int m = 0; m < 2; m++) st128<16>(rw, so[i][m], pk8(mfma16(m2[i][0], f[i][m], z4) * sc[m], mfma16(m2[i][1], f[i][m], z4) * sc[m]));
    } else {
#pragma unroll
      for (int i = 0; i < 3; i++)
#pragma unroll
        for (int T = 0; T < 2; T++) {
          const f32x4 d0 = mfma16(f[i][0], m2[i][T], z4), d1 = mfma16(f[i][1], m2[i][T], z4);
          st128<16>(rw, so[i][T], pk8(d0, d1));
          const float k = ((i == 2 && w >= 4) || (i == 0 && T == 0 && tid < 64 && r16 == 1)) ? 0.f : 1.f;
          sq[0] += d0 * d0 * k; sq[1] += d1 * d1 * k;
        }
    }
    LDS_AS float* sl = (LDS_AS float*)(lds + LDS_PREP - 1024);
    if (job < 64) {
#pragma unroll
      for (int m = 0; m < 2; m++) {
#pragma unroll
        for (int e = 0; e < 4; e++) sq[m][e] = row16_sum(sq[m][e]);
        if (r16 == 0) *(LDS_AS f32x4*)(sl + w * 32 + 8 * g + 4 * m) = sq[m];
      }
    }
    if (job < 64) {
      lds_bar();
      if (tid < 32) {
        float v = 0.f;
#pragma unroll
        for (int i = 0; i < 8; i++) v += sl[i * 32 + tid];
        st32<16>(rw, WS_RS1 + rpos(32 * job + tid) * 4, __float_as_uint(__builtin_amdgcn_rsqf((v + 640.f * 1e-5f) * (2048.f * 2048.f / 640.f))));
      }
    }
    return;
  }
  const int mi = job - 144;
  const rsrc_t rw = mk_rsrc(ws, WS_END);
  if (mi < 64) {
    const int m = mi >> 3, oc = mi & 7;
    const char* src = (const char*)((w >> 2) ? wi : wr) + (64 * (4 * m + (w & 3)) + 8 * oc) * 642;
    dma4(lane * 16, src, lds_addr(lds) + w * 5152);
    dma1(4096 + lane * 16, src, lds_addr(lds) + w * 5152 + 4096);
    if (lane == 0) dma1(5120, src, lds_addr(lds) + w * 5152 + 5120);
    vm_bar<0>();
    LDS_AS u16* L = (LDS_AS u16*)lds + (tid & 7) * 321;
#pragma unroll
    for (int it = 0; it < 5; it++) {
      const int c = (tid >> 3) + 64 * it, col = freq_of(c >> 4, c & 15);
      u32x4 o;
#pragma unroll
      for (int e = 0; e < 4; e++) o[e] = (unsigned)L[e * 2576 + col] | ((unsigned)L[(4 + e) * 2576 + col] << 16);
      st128<16>(rw, WS_WF + c * 8192 + (m * 64 + 8 * oc + (tid & 7)) * 16, o);
    }
#pragma unroll
    for (int it = 0; it < 3; it++) st128<16>(rw, WS_RB1 + ((it * 64 + mi) * 512 + tid) * 16, u32x4{EMPTY, EMPTY, EMPTY, EMPTY});
  } else if (mi < 84) {
    const char* src = (const char*)wo + kappa_to_i(32 * (mi - 64) + 8 * (w & 3) + 4 * (w >> 2)) * 1280;
    dma4(lane * 16, src, lds_addr(lds) + w * 5152);
    dma1(4096 + lane * 16, src, lds_addr(lds) + w * 5152 + 4096);
    vm_bar<0>();
#pragma unroll
    for (int i = 0; i < 5; i++) {
      const lptr a = lds + g * 5152 + (r16 >> 2) * 1280 + (5 * w + i) * 32 + (lane & 3) * 8;
      const u32x2 lo = ldtr(a), hi = ldtr(a + 4 * 5152);
      st128<16>(rw, WS_WOP + wo_frag(5 * w + i, mi - 64) + lane * 16, u32x4{lo[0], lo[1], hi[0], hi[1]});
    }
  } else {
    const int i0 = (mi - 84) * 512 + tid;
    unsigned pf0, pf1;
    {
      const unsigned* cm = CODE_ADDR(SYM_MAIN);
      const unsigned* p = (const unsigned*)wqkv + min(14336 + i0 - 4096, 19199) * 32;
      if (mi < 92) p = tid < 80 ? (const unsigned*)&g_ct + tid * 32 : cm + min(tid - 80, CODE_LINES - 1) * 32;
      asm volatile("global_load_dword %0, %1, off" : "=v"(pf0) : "v"(p) : "memory");
      asm volatile("global_load_dword %0, %1, off" : "=v"(pf1) : "v"((const unsigned*)wqkv + i0 * 32) : "memory");
    }
    nap<K1DELAY>();
    if (i0 < 6272) st128<16>(rw, WS_FL + i0 * 16, u32x4{0u, 0u, 0u, 0u});
#pragma unroll
    for (int it = 0; it < 6; it++) { const int i = 98304 + i0 + it * 14336; if (i < (int)RBSZ / 16) st128<16>(rw, WS_RB1 + i * 16, u32x4{EMPTY, EMPTY, EMPTY, EMPTY}); }
    asm volatile("s_waitcnt vmcnt(0)" : "+v"(pf0), "+v"(pf1));
  }
}

#define PINF(x) asm volatile("" : "+v"(x))
DI void put_scale(lptr lds, int q, int tid, const u32x2* sp) {
  f32x4 r = {640.f * 1e-5f, 640.f * 1e-5f, 640.f * 1e-5f, 640.f * 1e-5f};
#pragma unroll
  for (int i = 0; i < 8; i++) r += f32x4{bflo(sp[i][0]), bfhi(sp[i][0]), bflo(sp[i][1]), bfhi(sp[i][1])};
#pragma unroll
  for (int e = 0; e < 4; e++) r[e] = __builtin_amdgcn_rsqf(r[e] * (2048.f * 2048.f / 640.f));
  ((LDS_AS f32x4*)(lds + 2 * SLOT32))[q * 256 + tid] = r;
}
template <bool FIRST>
DI void col_phase(rsrc_t rw, lptr lds, int c SARG) {
  int lane = threadIdx.x & 63; PINF(lane);
  const int wv = wave_id(), w = wv & 3, q = wv >> 2, tid = w * 64 + lane, g = lane >> 4, r16 = lane & 15;
  const lptr img = lds + q * SLOT32;
  f32x4 xr[2], xi[2], rs[2]; u32x4 wf[2], xb[2];
  u32x4 c64[2], s64[2], n64[2], c32[2], s32[2], n32[2];
  const u32x4* ct = (const u32x4*)&g_ct + lane;
  auto ld_const = [&]() { c64[0] = ct[(2 * w) * 64]; s64[0] = ct[(2 * w + 1) * 64]; c32[0] = ct[8 * 64]; s32[0] = ct[9 * 64]; };
  auto ld_filter = [&]() {
#pragma unroll
  for (int t = 0; t < 2; t++) wf[t] = gl128<16>(rw, WS_WF + c * 8192 + ((4 * t + g) * 64 + 16 * w + r16) * 16);
  };
  if (FIRST) ld_const();
  SB();
  f32x4 t1c[2], t1s[2], t2c[2], t2s[2];
  {
    const float f1 = (float)(16 * w + r16) * (1.f / 2048.f), b1 = (float)(4 * g), f2 = (float)(16 * w + 4 * g) * (1.f / 2048.f), b2 = (float)r16;
#pragma unroll
    for (int t = 0; t < 2; t++) {
#pragma unroll
      for (int e = 0; e < 4; e++) {
        const float a1 = (b1 + (float)(16 * t + e)) * f1, a2 = (b2 + (float)(16 * t)) * (f2 + (float)e * (1.f / 2048.f));
        t1c[t][e] = __builtin_amdgcn_cosf(a1); t1s[t][e] = __builtin_amdgcn_sinf(-a1);
        t2c[t][e] = __builtin_amdgcn_cosf(a2); t2s[t][e] = __builtin_amdgcn_sinf(a2);
      }
      PINF(t1c[t]); PINF(t1s[t]); PINF(t2c[t]); PINF(t2s[t]);
    }
  }
  int wo_ = r16 * 128 + (((2 * w + (g >> 1)) ^ (r16 >> 1)) * 16) + (g & 1) * 8, ro_ = r16 * 128 + ((g ^ (r16 >> 1)) * 16), ro2 = ro_ ^ 64;
  PINF(wo_); PINF(ro_); PINF(ro2);
  SB();
  if (FIRST) {
    raw_bar();
#pragma unroll
    for (int t = 0; t < 2; t++) { xb[t] = gl128<16>(rw, WS_RB0 + ((tid >> 2) * 641 + ((tid >> 2) & 1) + 2 * c + t) * 64 + (tid & 3) * 16); rs[t] = asf(gl128<16>(rw, WS_RS1 + (1024 * t + 4 * tid) * 4)); }
    ld_filter();
    SB();
#pragma unroll
    for (int t = 0; t < 2; t++) {
      xr[t] = f32x4{bflo(xb[0][2 * t]), bfhi(xb[0][2 * t]), bflo(xb[0][2 * t + 1]), bfhi(xb[0][2 * t + 1])};
      xi[t] = f32x4{bflo(xb[1][2 * t]), bfhi(xb[1][2 * t]), bflo(xb[1][2 * t + 1]), bfhi(xb[1][2 * t + 1])};
    }
  } else {
    MARK(23);
    drain();
    MARK(11);
    nap<YDELAY>();
    u32x2 sp[8];
    const unsigned so = WS_SSQ + (blockIdx.x & 7) * 32768 + (1024 * q + 4 * tid) * 2, xo = WS_R3 + ((tid >> 2) * 642 + 2 * c) * 64 + (tid & 3) * 16;
    bool bs = true, bx = true;
#pragma unroll 1
    for (int spins = 0; spins < (1 << 18); spins++) {
      if (bx) {
#pragma unroll
        for (int t = 0; t < 2; t++) { xr[t] = asf(gl128<16>(rw, xo + 64 * 642 * 64 * t)); xi[t] = asf(gl128<16>(rw, xo + 64 * 642 * 64 * t + 64)); }
      }
      if (bs) {
#pragma unroll
        for (int i = 0; i < 8; i++) sp[i] = gl64<16>(rw, so + i * 4096);
      }
      unsigned m = 0;
#pragma unroll
      for (int t = 0; t < 2; t++) m = umax4(umax4(m, asu(xr[t])), asu(xi[t]));
      bx = __builtin_amdgcn_ballot_w64(m == EMPTY) != 0;
      m = 0;
#pragma unroll
      for (int i = 0; i < 8; i++) m = max(max(m, sp[i][0]), sp[i][1]);
      bs = __builtin_amdgcn_ballot_w64(m == EMPTY) != 0;
      if (!bs && !bx) break;
      nap<YSLEEP>();
    }
    ld_const(); ld_filter();
    SB();
    put_scale(lds, q, tid, sp);
    LDS_AS f32x4* ex = (LDS_AS f32x4*)(lds + 2 * SLOT32) + tid;
    lds_bar();
    rs[0] = ex[0]; rs[1] = ex[256];
  }
  MARK(FIRST ? 9 : 21);
#pragma unroll
  for (int t = 0; t < 2; t++) {
    u32x2 pb = pk4(xi[t] * rs[t]);
    if (c == 0) pb = u32x2{0u, 0u};
    *(LDS_AS u32x2*)(img + wo_ + t * 2048) = pk4(xr[t] * rs[t]);
    *(LDS_AS u32x2*)(img + 4096 + wo_ + t * 2048) = pb;
  }
  lds_bar();
  u32x4 ur[2][2], ui[2][2], br, bi;
  f32x4 ar[2], ai[2];
  const f32x4 z4 = {0.f, 0.f, 0.f, 0.f};
  auto rd_in = [&](int off) {
#pragma unroll
    for (int ks = 0; ks < 2; ks++)
#pragma unroll
      for (int t = 0; t < 2; t++) { const lptr p = img + off + (ks ? ro2 : ro_) + t * 2048; ur[t][ks] = ld128(p); ui[t][ks] = ld128(p + 4096); }
    SB();
  };
  auto cmul = [&](const f32x4* cc, const f32x4* sn) {
    br = pk8(ar[0] * cc[0] - ai[0] * sn[0], ar[1] * cc[1] - ai[1] * sn[1]);
    bi = pk8(ai[0] * cc[0] + ar[0] * sn[0], ai[1] * cc[1] + ar[1] * sn[1]);
  };
  rd_in(0);
  {
    const unsigned par = (lane & 1) ? 0x80008000u : 0u;
    c64[1] = c64[0] ^ par; s64[1] = s64[0] ^ par; n64[0] = s64[0] ^ 0x80008000u; n64[1] = s64[1] ^ 0x80008000u;
    c32[1] = c32[0] ^ 0x80000000u; s32[1] = s32[0] ^ 0x80000000u; n32[0] = s32[0] ^ 0x80008000u; n32[1] = s32[0] ^ 0x00008000u;
    PINF(c64[1]); PINF(s64[1]); PINF(n64[0]); PINF(n64[1]); PINF(c32[1]); PINF(s32[1]); PINF(n32[0]); PINF(n32[1]);
  }
  SB();
#pragma unroll
  for (int ks = 0; ks < 2; ks++) {
#pragma unroll
    for (int t = 0; t < 2; t++) { ar[t] = mfma16(ur[t][ks], c64[ks], ks ? ar[t] : z4); ai[t] = mfma16(ui[t][ks], c64[ks], ks ? ai[t] : z4); }
#pragma unroll
    for (int t = 0; t < 2; t++) { ar[t] = mfma16(ui[t][ks], s64[ks], ar[t]); ai[t] = mfma16(ur[t][ks], n64[ks], ai[t]); }
  }
  const f32x4 fa[2] = {asf(wf[0] << 16), asf(wf[1] << 16)}, fb[2] = {asf(wf[0] & 0xFFFF0000u), asf(wf[1] & 0xFFFF0000u)};
  cmul(t1c, t1s);
#pragma unroll
  for (int t = 0; t < 2; t++) { ar[t] = mfma16(c32[t], br, z4); ai[t] = mfma16(c32[t], bi, z4); }
#pragma unroll
  for (int t = 0; t < 2; t++) { ar[t] = mfma16(s32[t], bi, ar[t]); ai[t] = mfma16(n32[t], br, ai[t]); }
  cmul(fa, fb);
#pragma unroll
  for (int t = 0; t < 2; t++) { ar[t] = mfma16(br, c32[t], z4); ai[t] = mfma16(bi, c32[t], z4); }
#pragma unroll
  for (int t = 0; t < 2; t++) { ar[t] = mfma16(bi, n32[t], ar[t]); ai[t] = mfma16(br, s32[t], ai[t]); }
  cmul(t2c, t2s);
#pragma unroll
  for (int t = 0; t < 2; t++) {
    *(LDS_AS u32x2*)(img + 8192 + wo_ + t * 2048) = u32x2{br[2 * t], br[2 * t + 1]};
    *(LDS_AS u32x2*)(img + 12288 + wo_ + t * 2048) = u32x2{bi[2 * t], bi[2 * t + 1]};
  }
  lds_bar();
  rd_in(8192);
#pragma unroll
  for (int ks = 0; ks < 2; ks++) {
#pragma unroll
    for (int t = 0; t < 2; t++) { ar[t] = mfma16(c64[ks], ur[t][ks], ks ? ar[t] : xr[t]); ai[t] = mfma16(c64[ks], ui[t][ks], ks ? ai[t] : xi[t]); }
#pragma unroll
    for (int t = 0; t < 2; t++) { ar[t] = mfma16(n64[ks], ui[t][ks], ar[t]); ai[t] = mfma16(s64[ks], ur[t][ks], ai[t]); }
  }
  const unsigned ob = (FIRST ? WS_RB1 : WS_RB2) + (2 * c * RBP + 8 * tid) * 2;
  st128<16>(rw, ob, clean(pk8(ar[0], ar[1])));
  st128<16>(rw, ob + RBP * 2, c == 0 ? u32x4{0u, 0u, 0u, 0u} : clean(pk8(ai[0], ai[1])));
}

DI f32x16 mfma32(u32x4 a, u32x4 b, f32x16 c) {
  return __builtin_amdgcn_mfma_f32_32x32x16_bf16(__builtin_bit_cast(bf16x8, a), __builtin_bit_cast(bf16x8, b), c, 0, 0, 0);
}
DI f32x16 mfma32k8(u32x2 a, u32x2 b, f32x16 c) {
  return __builtin_amdgcn_mfma_f32_32x32x8bf16_1k(__builtin_bit_cast(s16x4, a), __builtin_bit_cast(s16x4, b), c, 0, 0, 0);
}
DI f32x16 scores(u32x4 k1, u32x2 k2, u32x4 q1, u32x2 q2) {
  f32x16 z;
#pragma unroll
  for (int i = 0; i < 16; i++) z[i] = 0.f;
  f32x16 s = mfma32(k1, q1, z);
  asm volatile("s_nop 11" : "+v"(s));
  return mfma32k8(k2, q2, s);
}
DI float max16(const f32x16& s) {
  float m = fmaxf(s[0], s[1]);
#pragma unroll
  for (int i = 2; i < 16; i++) m = fmaxf(m, s[i]);
  return m;
}
#define STR_(x) #x
#define STR(x) STR_(x)
#define KOF(t) ((((t)&7)>>1)*11264+((t)&1)*1280)
#define VOF(t) ((((t)&7)>>1)*11264+5120+((t)&1)*1536)
#define EXP(d, s) "v_exp_f32 v" #d ", v" #s "\n\t"
#define CVT(d, s) "v_cvt_pk_bf16_f32 v" #d ", v" #s ", v[" #s "+1]\n\t"
#define STEPA(t) \
  "v_mfma_f32_32x32x16_bf16 v[48:63], v[80:83], v[136:139], v[120:135]\n\t" \
  "ds_read_b128 v[80:83], v100 offset:" STR(KOF(t+1)) "\n\t" \
  EXP(64,32) EXP(65,33) CVT(78,68) CVT(79,70) EXP(66,34) EXP(67,35) \
  "v_mfma_f32_32x32x16_bf16 v[104:119], v[90:93], v[76:79], v[104:119]\n\t" \
  "ds_read_b64_tr_b16 v[90:91], v102 offset:" STR(VOF(t)+768) "\n\t" \
  "ds_read_b64_tr_b16 v[92:93], v102 offset:" STR(VOF(t)+1152) "\n\t" \
  EXP(68,36) EXP(69,37) CVT(72,64) CVT(73,66) EXP(70,38) EXP(71,39) \
  "v_mfma_f32_32x32x8_bf16 v[48:63], v[84:85], v[140:141], v[48:63]\n\t" \
  "ds_read_b64 v[84:85], v101 offset:" STR(KOF(t+1)) "\n\t" \
  EXP(64,40) EXP(65,41) CVT(74,68) CVT(75,70) EXP(66,42) EXP(67,43) \
  "s_waitcnt lgkmcnt(4)\n\t" \
  "v_mfma_f32_32x32x16_bf16 v[0:15], v[86:89], v[72:75], v[0:15]\n\t" \
  EXP(68,44) EXP(69,45) CVT(76,64) CVT(77,66) EXP(70,46) EXP(71,47)
#define STEPB(t) \
  "s_waitcnt lgkmcnt(0)\n\t" \
  "v_mfma_f32_32x32x16_bf16 v[32:47], v[80:83], v[94:97], v[16:31]\n\t" \
  EXP(64,48) EXP(65,49) CVT(78,68) CVT(79,70) EXP(66,50) EXP(67,51) \
  "v_mfma_f32_32x32x16_bf16 v[0:15], v[90:93], v[76:79], v[0:15]\n\t" \
  EXP(68,52) EXP(69,53) CVT(72,64) CVT(73,66) EXP(70,54) EXP(71,55) \
  "v_mfma_f32_32x32x8_bf16 v[32:47], v[84:85], v[98:99], v[32:47]\n\t" \
  EXP(64,56) EXP(65,57) CVT(74,68) CVT(75,70) EXP(66,58) EXP(67,59) \
  "v_mfma_f32_32x32x16_bf16 v[104:119], v[86:89], v[72:75], v[104:119]\n\t" \
  "ds_read_b64_tr_b16 v[86:87], v102 offset:" STR(VOF(t+1)) "\n\t" \
  "ds_read_b64_tr_b16 v[88:89], v102 offset:" STR(VOF(t+1)+384) "\n\t" \
  EXP(68,60) EXP(69,61) CVT(76,64) CVT(77,66) EXP(70,62) EXP(71,63)
#define PR(n) "s_setprio " #n "\n\t"
#define CHUNK(i) PR(1) STEPA(2*(i)) PR(0) STEPB(2*(i)) "s_barrier\n\t" PR(2) STEPA(2*(i)+1) STEPB(2*(i)+1)
#define Z4(a) "v_mov_b64 v[" #a ":" #a "+1], 0\n\tv_mov_b64 v[" #a "+2:" #a "+3], 0\n\t"

#define R4(b) "v[" STR(b) ":" STR(b) "+3]"
#define MM(d, a, b) "v_mfma_f32_16x16x32_bf16 " R4(d) ", " R4(a) ", " R4(b) ", " R4(d) "\n\t"
#define GM(B, m, i) MM(20*m+4*i, B+8+4*i, B+4*m)
#define RT(B, m, hf, k) "ds_read_b64_tr_b16 v[" STR(B+4*m+2*hf) ":" STR(B+4*m+2*hf+1) "], v10" #m " offset:" STR(k*8192+hf*1024) "\n\t"
#define RS(B, i, k) "ds_read_b128 " R4(B+8+4*i) ", v102 offset:" STR(i*2048+k*1024) "\n\t"
#define IF5_4(x) ""
#define IF5_5(x) x
#define GSTEP(N, B, C, k) GM(B,0,0) RT(C,0,0,k) RT(C,0,1,k) GM(B,1,0) RS(C,0,k) RT(C,1,0,k) GM(B,0,1) RT(C,1,1,k) RS(C,1,k) GM(B,1,1) RS(C,2,k) \
  GM(B,0,2) RS(C,3,k) GM(B,1,2) IF5_##N(RS(C,4,k)) GM(B,0,3) GM(B,1,3) IF5_##N(GM(B,0,4) GM(B,1,4)) MM(40,B,B)
#define GEMM_ASM(N) \
  Z4(0) Z4(4) Z4(8) Z4(12) Z4(16) Z4(20) Z4(24) Z4(28) Z4(32) Z4(36) Z4(40) "s_mov_b32 %[c], 0\n\ts_barrier\n\t" \
  RT(44,0,0,0) RT(44,0,1,0) RS(44,0,0) RT(44,1,0,0) RT(44,1,1,0) RS(44,1,0) RS(44,2,0) RS(44,3,0) IF5_##N(RS(44,4,0)) \
  "L_gemm_%=:\n\ts_add_u32 %[c], %[c], 1\n\ts_and_b32 %[t], %[c], 3\n\ts_cselect_b32 %[t], 1, -3\n\ts_mul_i32 %[t], %[t], 36864\n\ts_waitcnt lgkmcnt(0)\n\t" \
  GSTEP(N,44,72,1) \
  "v_add_u32 v100, %[t], v100\n\tv_add_u32 v101, %[t], v101\n\tv_add_u32 v102, %[t], v102\n\ts_waitcnt lgkmcnt(0)\n\ts_barrier\n\t" \
  GSTEP(N,72,44,0) \
  "s_cmp_lg_u32 %[c], 9\n\ts_cbranch_scc1 L_gemm_%=\n\ts_waitcnt lgkmcnt(0)\n\t" \
  GSTEP(N,44,72,1) "s_waitcnt lgkmcnt(0)\n\t" \
  MM(40,72,72) GM(72,0,0) GM(72,1,0) GM(72,0,1) GM(72,1,1) "s_nop 4\n\t" \
  "v_cndmask_b32 v44, v40, v41, %[m1]\n\tv_cndmask_b32 v45, v42, v43, %[m1]\n\tv_mov_b32 v46, 0x3727c5ac\n\tv_cndmask_b32 v44, v44, v45, %[m2]\n\t" \
  "v_fmac_f32 v46, 0x3acccccd, v44\n\tv_cmp_u_f32 vcc, v44, v44\n\tv_rsq_f32 v46, v46\n\ts_cbranch_vccz L_gok_%=\n\tds_write_b32 v106, v106\n\t" \
  "L_gok_%=:\n\tds_write_b32 v103, v46\n\ts_waitcnt lgkmcnt(0)\n\ts_barrier\n\t" \
  "ds_read_b32 v44, v104\n\tds_read_b32 v45, v105\n\tds_read_b32 v46, v106\n\t" \
  GM(72,0,2) GM(72,1,2) GM(72,0,3) GM(72,1,3) IF5_##N(GM(72,0,4) GM(72,1,4)) "s_waitcnt lgkmcnt(0)"
#define GEMM_OPS \
  : "={v[0:3]}"(acc[0][0]), "={v[4:7]}"(acc[0][1]), "={v[8:11]}"(acc[0][2]), "={v[12:15]}"(acc[0][3]), "={v[16:19]}"(acc[0][4]), \
    "={v[20:23]}"(acc[1][0]), "={v[24:27]}"(acc[1][1]), "={v[28:31]}"(acc[1][2]), "={v[32:35]}"(acc[1][3]), "={v[36:39]}"(acc[1][4]), \
    "={v44}"(rs[0]), "={v45}"(rs[1]), "={v46}"(vote), "+{v100}"(am0), "+{v101}"(am1), "+{v102}"(as), [c] "=&s"(c_), [t] "=&s"(t_) \
  : "{v103}"(a_w), "{v104}"(a_r[0]), "{v105}"(a_r[1]), "{v106}"(a_v), [m1] "s"(0xAAAAAAAAAAAAAAAAull), [m2] "s"(0xCCCCCCCCCCCCCCCCull) \
  : "memory", "scc", "vcc", "v40", "v41", "v42", "v43", "v47", "v48", "v49", "v50", "v51", "v52", "v53", "v54", "v55", "v56", "v57", "v58", "v59", "v60", \
    "v61", "v62", "v63", "v64", "v65", "v66", "v67", "v68", "v69", "v70", "v71", "v72", "v73", "v74", "v75", "v76", "v77", "v78", "v79", "v80", "v81", \
    "v82", "v83", "v84", "v85", "v86", "v87", "v88", "v89", "v90", "v91", "v92", "v93", "v94", "v95", "v96", "v97", "v98", "v99"

constexpr int HR = 4 * KVC;
constexpr int L_A = 2 * HR;
constexpr int L_EX = 15360;
constexpr int L_MISC = L_A + 70 * 1024;
constexpr int LDS_FUSED = L_MISC + 2048;

DI bool poll16(rsrc_t rw, unsigned off, unsigned need) {
  const int lane = threadIdx.x & 63;
#pragma unroll 1
  for (int spins = 0; spins < (1 << 20); spins++) {
    const unsigned v = gl32<16>(rw, off + (lane & 15) * 4);
    const unsigned got = (unsigned)__builtin_amdgcn_ballot_w64(v != 0) & 0xFFFFu;
    if ((got & need) == need) return true;
    nap<1>();
  }
  return false;
}

DI void poll_aux(rsrc_t rw, unsigned off) {
#pragma unroll 1
  for (int spins = 0; spins < (1 << 20); spins++) {
    const u32x4 v = gl128<16>(rw, off + (threadIdx.x & 63) * 16);
    if (!__builtin_amdgcn_ballot_w64(min(min(v[0], v[1]), min(v[2], v[3])) == 0u)) break;
    nap<4>();
  }
}
DI void poll_tag(rsrc_t rw, u32x2 tag, unsigned long long need) {
  const unsigned o = WS_F0 + (blockIdx.x & 7) * 2048 + (threadIdx.x & 63) * 32;
#pragma unroll 1
  for (int spins = 0; spins < (1 << 20); spins++) {
    const u32x4 a = gl128<16>(rw, o), b = gl128<16>(rw, o + 16);
    const unsigned d = (a[0] ^ tag[0]) | (a[1] ^ tag[1]) | (a[2] ^ tag[0]) | (a[3] ^ tag[1]) | (b[0] ^ tag[0]) | (b[1] ^ tag[1]) | (b[2] ^ tag[0]) | (b[3] ^ tag[1]);
    if (!(__builtin_amdgcn_ballot_w64(d != 0u) & need)) break;
    nap<TSLEEP>();
  }
}
#define LAYER_VARS \
  extern __shared__ __attribute__((aligned(16))) char smem[]; \
  const lptr lds = (lptr)smem; \
  const rsrc_t rw = mk_rsrc(ws, WS_END); \
  int lane = threadIdx.x & 63; asm volatile("" : "+v"(lane)); \
  const int w = wave_id(), g = lane >> 4, r16 = lane & 15; \
  const int xcd = blockIdx.x & 7, bi = blockIdx.x >> 3, u = 8 * (xcd & 1) + (bi & 7), qt = 4 * (xcd >> 1) + (bi >> 3); \
  const int hf = u & 1, ct = u >> 1;     \
  constexpr unsigned FKV = WS_FL + (FINAL ? 32768 : 0), KV = WS_KV + (FINAL ? LAYER2 : 0), OB = WS_O + (FINAL ? LAYER2 : 0); \
  constexpr unsigned AUX = WS_AUX + (FINAL ? 1024 : 0), RB = FINAL ? WS_RB2 : WS_RB1; \
  constexpr int MK = FINAL ? 12 : 0; (void)MK; (void)hf; (void)AUX; (void)RB; (void)FKV; \
  LDS_AS unsigned* misc = (LDS_AS unsigned*)(lds + L_MISC + 1024) + (FINAL ? 8 : 0);

template <bool FINAL>
DI void loader(char* ws, u32x2 tag SARG) {
  LAYER_VARS
  {
    const int lw = w - 8;
    {
      unsigned vm[4];
#pragma unroll
      for (int r = 0; r < 4; r++) vm[r] = (4 * (4 * lw + r) + g) * (RBP * 2) + 256 * qt + (((r16 >> 1) ^ (g | ((r & 2) << 1))) * 32) + (r16 & 1) * 16 + 3072 - 1024 * r;
      const unsigned l0 = lds_addr(lds) + lw * 4096, v0 = 2 * lw * 20480 + lane * 16, v2 = ((8 + (lw >> 1)) * 20 + (lw & 1)) * 1024 + lane * 16;
      constexpr int CNT = FINAL ? 9 : 8;
      auto lds_w = [&](int s) {
        const char* sb = ws + WS_WQ + u * 204800 + s * 2048;
        dma_s(v0, v0 + 20480 - 2048, sb, l0 + (s & 3) * SLOT32 + 16384 + (lw >> 1) * 2048);
        if (FINAL) dma1_sc1(v2, sb, lds_addr(lds) + (s & 3) * SLOT32 + 24576 + (lw >> 1) * 10240 + (lw & 1) * 1024);
      };
      auto lds_a = [&](int s) {
        dma_a(vm, ws + RB - 3072 + s * (64 * RBP * 2), l0 + (s & 3) * SLOT32); };
#pragma unroll 1
      for (int att = 0;; att++) {
        if (att == 0) { raw_bar(); if (FINAL) raw_bar(); else poll_tag(rw, tag, 0x7FFF0000ull); }
        lds_w(0); lds_w(1);
        if (att == 0) raw_bar();
        lds_w(2); lds_w(3);
        auto probe = [&](int s0, int n) {
          const int pl = min(lane, n - 1);
          const unsigned po = RB + ((64 * (s0 + (pl >> 3)) + 16 * lw + 2 * (pl & 7) + 1) * RBP + 128 * qt + 126) * 2;
#pragma unroll 1
          for (int spins = 0; spins < (1 << 20); spins++) {
            if (!__builtin_amdgcn_ballot_w64(gl32<16>(rw, po) == EMPTY)) break;
            nap<XSLEEP>();
          }
        };
        if (att == 0) {
          raw_bar();
          if (blockIdx.x >= 64) nap<XDELAY>();
          MARK(MK + 7);
          probe(0, 48);
        }
        drain();
        MARK(MK + 8);
        unsigned nd = 0;
        if (att == 0 && lane == 0) nd = __hip_atomic_fetch_add(misc + 4, 1u, __ATOMIC_RELAXED, __HIP_MEMORY_SCOPE_WORKGROUP);
        if (nd == 11) st32<16>(rw, AUX + blockIdx.x * 4, 1u);
#pragma unroll 1
        for (int s = 0; s < 4; s++) lds_a(s);
        vm_bar<12>(); vm_bar<8>();
        if (att == 0) probe(6, 32);
        lds_a(4); lds_w(4);
        vm_bar<4 + CNT>();
#pragma unroll 1
        for (int s = 5; s < 10; s++) { lds_a(s); lds_w(s); vm_bar<2 * CNT>(); }
        vm_bar<CNT>();
        poll_aux(rw, AUX);
        if (!FINAL) { poll_tag(rw, tag, ~0ull); if (blockIdx.x == 0 && threadIdx.x == 512) st64<16>(rw, WS_EP, tag); }
        vm_bar<0>();
        raw_bar();
        if (XNL || att || !*(volatile LDS_AS unsigned*)(misc + 2)) break;
      }
    }
    MARK(MK + 0);
    const int hh = lw >> 1, hp = lw & 1;
    const unsigned kvv = KV + (2 * u + hh) * 16 * KVC + hp * 5120 + lane * 16;
    const unsigned lkv = lds_addr(lds) + hh * HR + hp * 5120;
    const unsigned wv = (FINAL ? WS_WOP : WS_WOF) + (ct * 2 + hh) * 51200 + lane * 16;
    const unsigned myfl = FKV + (u * 16 + qt) * 128;
    raw_bar();
    MARK(MK + 1);
    {
      u32x4 v[6];
#pragma unroll
      for (int i = 0; i < 6; i++) v[i] = ld128(lds + hh * HR + hp * 5120 + lane * 16 + i * 1024);
#pragma unroll
      for (int i = 0; i < 6; i++) st128<16>(rw, kvv + qt * KVC + i * 1024, clean(v[i]));
    }
    drain();
    unsigned cnt = 0;
    if (lane == 0) cnt = __hip_atomic_fetch_add(misc, 1u, __ATOMIC_RELAXED, __HIP_MEMORY_SCOPE_WORKGROUP);
    if (__builtin_amdgcn_readfirstlane(cnt) == 3 && lane < 16) st32<16>(rw, FKV + (u * 16 + lane) * 128 + qt * 4, 1u);
    MARK(MK + 2);
    auto ldkv = [&](int seq) {
      const int ch = seq == 0 ? qt : seq < 13 ? ((qt & 12) + 3 + seq) & 15 : (qt & 12) | ((qt + seq) & 3);
      dma6_sc1(kvv + ch * KVC, ws, lkv + (seq & 3) * KVC);
    };
    auto fetch = [&](int seq) {
      const unsigned src = kvv + ((((qt & 12) + 3 + seq) & 15)) * KVC;
      u32x4 v[6];
#pragma unroll 1
      for (int spins = 0; spins < (1 << 18); spins++) {
#pragma unroll
        for (int i = 0; i < 6; i++) v[i] = gl128<16>(rw, src + i * 1024);
        unsigned m = 0;
#pragma unroll
        for (int i = 0; i < 6; i++) m = umax4(m, v[i]);
        if (!__builtin_amdgcn_ballot_w64(m == EMPTY)) break;
        nap<2>();
      }
#pragma unroll
      for (int i = 0; i < 6; i++) *(LDS_AS u32x4*)(lds + hh * HR + hp * 5120 + seq * KVC + lane * 16 + i * 1024) = v[i];
    };
#pragma unroll 1
    for (int attempt = 0;; attempt++) {
      int c = -2;
      if (attempt) { ldkv(0); vm_bar<0>(); }
      else {
        fetch(1); MARK(MK + 3); lds_bar();
        MARK(MK + 4);
        fetch(2); poll16(rw, myfl, 12u << (((qt & 12) + 4) & 15)); ldkv(3); MARK(MK + 5); lds_bar();
        c = 1;
      }
#pragma unroll 1
      for (; c < 13; c++) {
        if (c == 2 && !attempt) poll16(rw, myfl, 0xFFFFu);
        if (!FINAL && !attempt) {
          if (c == 10) {
            unsigned n = 0;
            if (lane == 0) n = __hip_atomic_fetch_add(misc + 3, 1u, __ATOMIC_RELAXED, __HIP_MEMORY_SCOPE_WORKGROUP);
            if (__builtin_amdgcn_readfirstlane(n) == 3 && lane < 32) st32<16>(rw, WS_FL + 65536 + (lane < 16 ? (u * 16 + lane) * 128 + qt * 4 : (r16 * 16 + qt) * 128 + 64 + u * 4), 1u);
          }
        }
        if (c >= 2) { const int e = min(4 * (c - 2) + 2 * hp, 33); dma2(wv + e * 1024, ws, lds_addr(lds) + L_A + (hh * 35 + e) * 1024); }
        ldkv(c + 3);
        if (c == 0) vm_bar<12>();
        if (!FINAL && !attempt && c >= 2 && c < 9) {
          const u32x4 e = {EMPTY, EMPTY, EMPTY, EMPTY};
          const unsigned o = OB + LAYER2 + (((c - 2) * 256 + blockIdx.x) * 4 + lw) * 2048 + lane * 16;
          st128<16>(rw, o, e); st128<16>(rw, o + 1024, e);
          st128<16>(rw, ((qt & 3) < 2 && c < 8) ? kvv + LAYER2 + qt * KVC + (c - 2) * 1024 : WS_END, e);
          st128<16>(rw, c < 5 ? OB + ((4 * qt + lw) * 32 + 2 * u) * 1280 + min((c - 2) * 1024, 1536) + lane * 16 : WS_END, e);
          if (c == 2) vm_bar<12>(); else vm_bar<16>();
        } else if (c >= 2) vm_bar<8>(); else if (c >= 0) vm_bar<6>();
      }
      if (!FINAL && !attempt) poll16(rw, myfl + 65536 + hp * 64, 0xFFFFu);
      vm_bar<0>();
      dma_s(wv + (35 + 7 * hp) * 1024, wv + (35 + 7 * hp) * 1024, ws, lds_addr(lds) + hh * HR + 7 * hp * 1024);
      dma_s(wv + (39 + 7 * hp) * 1024, wv + (39 + 7 * hp) * 1024, ws, lds_addr(lds) + hh * HR + (4 + 7 * hp) * 1024);
      raw_bar();
      vm_bar<0>();
      if (attempt || !*(volatile LDS_AS unsigned*)(misc + 1)) break;
      raw_bar();
    }
    if (!FINAL) { raw_bar(); raw_bar(); }
  }
}

template <bool FINAL>
DI void compute(char* ws, u16* out SARG) {
  LAYER_VARS
  const int wm = w & 3, wn = w >> 2, h = lane >> 5;
  if (blockIdx.x < 64 || wn == 0) col_phase<!FINAL>(rw, lds, blockIdx.x < 64 ? 192 + 2 * blockIdx.x + wn : blockIdx.x - 64 SPASS);
  else if (FINAL) {
    drain();
    nap<YIDLE>();
    u32x2 sp[8];
#pragma unroll 1
    for (int spins = 0; spins < (1 << 18); spins++) {
      unsigned m = 0;
#pragma unroll
      for (int i = 0; i < 8; i++) sp[i] = gl64<16>(rw, WS_SSQ + (blockIdx.x & 7) * 32768 + (1024 + 4 * (wm * 64 + lane)) * 2 + i * 4096);
#pragma unroll
      for (int i = 0; i < 8; i++) m = max(max(m, sp[i][0]), sp[i][1]);
      if (!__builtin_amdgcn_ballot_w64(m == EMPTY)) break;
      nap<YSLEEP>();
    }
    put_scale(lds, 1, wm * 64 + lane, sp);
    lds_bar(); raw_bar(); raw_bar();
  } else {
    raw_bar(); raw_bar(); raw_bar();
  }
  MARK(MK + 10);
  lds_bar();
  {
    drain();
    unsigned n = 0;
    if (lane == 0) n = __hip_atomic_fetch_add(misc + 4, 1u, __ATOMIC_RELAXED, __HIP_MEMORY_SCOPE_WORKGROUP);
    if (n == 11) st32<16>(rw, AUX + blockIdx.x * 4, 1u);
  }

  {
    constexpr int NT = FINAL ? 5 : 4;
    f32x4 acc[2][5]; float rs[2];
    unsigned mo[2], a0[2], a1[2], a2[2], a_r[2];
#pragma unroll
    for (int m = 0; m < 2; m++) {
      const int q = r16 >> 2, pr = 16 * (m ^ wn) + r16;
      mo[m] = lds_addr(lds) + (8 * g + q) * 256 + (((2 * wm + (m ^ wn)) ^ (q | ((g & 1) << 2))) * 32) + (lane & 3) * 8;
      const unsigned qb = L_A + (wn * 4 + wm) * 1280, kb = wn * HR + wm * 1280, vr = wn * HR + 5120 + (32 * wm + pr) * 48 + (g == 3 ? 32 : g * 16);
      a0[m] = ((g >> 1) ? kb : qb) + (g & 1) * 512 + pr * 16;
      a1[m] = g == 2 ? qb + 1024 + pr * 8 : vr; a2[m] = g == 2 ? kb + 1024 + pr * 8 : vr + 8;
      a_r[m] = lds_addr(lds) + L_MISC + (32 * wm + pr) * 4;
      asm volatile("" : "+v"(a0[m]), "+v"(a1[m]), "+v"(a2[m]));
    }
    const unsigned a_w = a_r[0] + (g == (r16 >> 2) ? 0 : 512), a_v = lds_addr((lptr)(misc + 2));
#pragma unroll 1
    for (int att = 0;; att++) {
      unsigned am0 = mo[0], am1 = mo[1], as = lds_addr(lds) + 16384 + wn * 10240 + lane * 16, vote; int c_, t_;
      if (FINAL) asm volatile(GEMM_ASM(5) GEMM_OPS); else asm volatile(GEMM_ASM(4) GEMM_OPS);
      if (XNL || att || !__builtin_amdgcn_readfirstlane(vote)) break;
    }
    SB();
    MARK(MK + 0);
#pragma unroll
    for (int m = 0; m < 2; m++) *(LDS_AS u32x4*)(lds + a0[m]) = pk8(acc[m][0] * rs[m], acc[m][1] * rs[m]);
    asm volatile("s_nop 1" : "+v"(acc[0][2]), "+v"(acc[1][2]), "+v"(acc[0][3]), "+v"(acc[1][3]), "+v"(acc[0][NT - 1]), "+v"(acc[1][NT - 1]) :: "memory");
#pragma unroll
    for (int m = 0; m < 2; m++) {
      u32x2 hi = pk4(acc[m][3] * rs[m]);
      if (g == 3) hi = u32x2{0x3F80u, 0u};
      *(LDS_AS u32x2*)(lds + a1[m]) = pk4(acc[m][2] * rs[m]);
      *(LDS_AS u32x2*)(lds + a2[m]) = hi;
    }
    lds_bar();
    if (FINAL) {
      const unsigned x4 = WS_X4 + (((u * 16 + qt) * 4 + wm) * 2 + wn) * 1280;
      const u32x2 p0 = pk4(acc[0][NT - 1]), p1 = pk4(acc[1][NT - 1]), q0 = pk4(acc[0][3]), q1 = pk4(acc[1][3]);
      st128<16>(rw, x4 + lane * 16, clean(u32x4{p0[0], p0[1], p1[0], p1[1]}));
      if (g == 3) st128<16>(rw, x4 + 1024 + r16 * 16, clean(u32x4{q0[0], q0[1], q1[0], q1[1]}));
    }
  }
  MARK(MK + 1);
  const int head = 2 * u + wn, kh = w & 1;
  const lptr ring = lds + wn * HR;
  const int pblk = 128 * qt + 64 * hf + 32 * (wm >> 1);
  const unsigned r3t = WS_R3 + (64 * (wm & 1) + (pblk >> 5)) * (642 * 64);
  u32x2 x4v[3]; float dc = 0.f;
  unsigned os = OB + ((4 * qt + wm) * 32 + head) * 1280 + (((lane << 2) & 16) + ((lane >> 1) & 12) + (lane & 3)) * 8;
  unsigned o1 = OB + ((pblk >> 5) * 32 + 16 * wn) * 1280 + (g >> 1) * 1280 + (g & 1) * 512 + (wm & 1) * 256 + r16 * 16;
  unsigned o2 = OB + ((pblk >> 5) * 32 + 16 * wn) * 1280 + 2 * g * 1280 + 1024 + (wm & 1) * 128 + r16 * 8;
  u32x4 o3 = {0u, 0u, 0u, 0u};
  if (!FINAL) {
#pragma unroll
    for (int i = 0; i < 3; i++) {
      const int j = 80 * ct + 48 * wn + 16 * (wn ? (i & 1) : i) + r16;
      o3[i] = (j == 1 ? WS_RB0 + ((pblk >> 5) * 641 + ((pblk >> 5) & 1) + 1) * 64 : WS_RB1 + (j * RBP + pblk) * 2) + 16 * g + 8 * (wm & 1);
    }
  } else {
    o3[3] = r3t + 64 + r16 * 4;
#pragma unroll
    for (int i = 0; i < 3; i++) {
      const int cg = 12 * wn + 4 * ((i == 2 && wn) ? 0 : i) + g, pw = cg / 5, gq = cg - 5 * pw;
      o3[i] = WS_X4 + ((((2 * ct + (pw >> 1)) * 16 + qt) * 4 + 2 * hf + (wm >> 1)) * 2 + (pw & 1)) * 1280 + (gq * 16 + (trow(wm & 1, r16) & 15)) * 16 + ((((r16 >> 3) ^ pw) & 1) * 8);
    }
  }
  asm volatile("" : "+v"(os), "+v"(o1), "+v"(o2), "+v"(o3));
  f32x4 r[3];
  {
    const lptr qpa = lds + L_A + (wn * 4 + wm) * 1280, qpb = lds + L_A + (wn * 4 + (wm ^ 1)) * 1280;
    const u32x4 qa1 = ld128(qpa + lane * 16), qb1 = ld128(qpb + lane * 16);
    u32x2 qa2 = ld64(qpa + 1024 + (lane & 31) * 8), qb2 = ld64(qpb + 1024 + (lane & 31) * 8);
    if (h) { qa2 = u32x2{0u, 0u}; qb2 = qa2; }
    const unsigned ka = lds_addr(ring) + kh * 2560 + lane * 16, kb = lds_addr(ring) + kh * 2560 + 1024 + (lane & 31) * 8;
    const unsigned va = lds_addr(ring) + kh * 3072 + (4 * h + (r16 >> 2)) * 48 + ((g & 1) ? ((lane & 3) ? 40 : 32) : (lane & 3) * 8);
    float ma, mb;
    {
      const u32x4 k1 = ld128(ring + lane * 16); const u32x2 k2 = ld64(ring + 1024 + (lane & 31) * 8);
      ma = max16(scores(k1, k2, qa1, qa2)); mb = max16(scores(k1, k2, qb1, qb2));
    }
    for (int attempt = 0;; attempt++) {
      ma = fmaxf(ma, __shfl_xor(ma, 32)); mb = fmaxf(mb, __shfl_xor(mb, 32));
      f32x16 cia, cib, oa, ob;
#pragma unroll
      for (int i = 0; i < 16; i++) { cia[i] = -ma; cib[i] = -mb; }
      int cnt = 4;
      MARK(MK + 2);
      asm volatile(
          "ds_read_b128 v[80:83], v100\n\t"
          "ds_read_b64 v[84:85], v101\n\t"
          "ds_read_b64_tr_b16 v[86:87], v102 offset:5120\n\t"
          "ds_read_b64_tr_b16 v[88:89], v102 offset:5504\n\t"
          Z4(0) Z4(68) Z4(76) Z4(90)
          "s_waitcnt lgkmcnt(0)\n\t"
          "v_mfma_f32_32x32x16_bf16 v[32:47], v[80:83], v[94:97], v[16:31]\n\t"
          Z4(4) Z4(8) Z4(12) Z4(104) Z4(108) Z4(112) Z4(116)
          "v_mfma_f32_32x32x8_bf16 v[32:47], v[84:85], v[98:99], v[32:47]\n\ts_nop 9\n\t"
          "L_attn_%=:\n\t"
          CHUNK(0) CHUNK(1) CHUNK(2) CHUNK(3)
          "s_sub_u32 %[cnt], %[cnt], 1\n\ts_cmp_lg_u32 %[cnt], 0\n\ts_cbranch_scc1 L_attn_%=\n\t"
          "s_setprio 0\n\t" CVT(78,68) CVT(79,70) "s_waitcnt lgkmcnt(0)\n\ts_nop 1\n\t"
          "v_mfma_f32_32x32x16_bf16 v[104:119], v[90:93], v[76:79], v[104:119]\n\ts_nop 15\n\t"
          : "={v[0:15]}"(oa), "={v[104:119]}"(ob), [cnt] "+s"(cnt)
          : "{v[16:31]}"(cia), "{v[120:135]}"(cib), "{v[94:97]}"(qa1), "{v[98:99]}"(qa2), "{v[136:139]}"(qb1), "{v[140:141]}"(qb2),
            "{v100}"(ka), "{v101}"(kb), "{v102}"(va)
          : "memory", "scc", "v32", "v33", "v34", "v35", "v36", "v37", "v38", "v39", "v40", "v41", "v42", "v43", "v44", "v45", "v46", "v47",
            "v48", "v49", "v50", "v51", "v52", "v53", "v54", "v55", "v56", "v57", "v58", "v59", "v60", "v61", "v62", "v63", "v64", "v65",
            "v66", "v67", "v68", "v69", "v70", "v71", "v72", "v73", "v74", "v75", "v76", "v77", "v78", "v79", "v80", "v81", "v82", "v83",
            "v84", "v85", "v86", "v87", "v88", "v89", "v90", "v91", "v92", "v93");
      MARK(MK + 3);
      LDS_AS f32x4* ex = (LDS_AS f32x4*)(ring + L_EX + wm * 3072) + lane;
#pragma unroll
      for (int i = 0; i < 3; i++) ex[i * 64] = f32x4{ob[4 * i], ob[4 * i + 1], ob[4 * i + 2], ob[4 * i + 3]};
      f32x4 cs = {0.f, 0.f, 0.f, 0.f};
#pragma unroll
      for (int i = 0; i < 3; i++) cs += f32x4{oa[4 * i], oa[4 * i + 1], oa[4 * i + 2], oa[4 * i + 3]} + f32x4{ob[4 * i], ob[4 * i + 1], ob[4 * i + 2], ob[4 * i + 3]};
      if (!(fabsf(cs[0] + cs[1] + cs[2] + cs[3]) < INFINITY) || (h && !(oa[8] + ob[8] < 0x1p60f))) misc[1] = 1u;
      lds_bar();
      const unsigned vote = *(volatile LDS_AS unsigned*)(misc + 1);
      LDS_AS f32x4* ey = (LDS_AS f32x4*)(ring + L_EX + (wm ^ 1) * 3072) + lane;
#pragma unroll
      for (int i = 0; i < 3; i++) r[i] = ey[i * 64] + f32x4{oa[4 * i], oa[4 * i + 1], oa[4 * i + 2], oa[4 * i + 3]};
      if (attempt || !vote) break;
      lds_bar();
      ma = -INFINITY; mb = -INFINITY;
      for (int t = 0; t < 64; t++) {
        const unsigned kb_ = KV + (head * 16 + (t >> 2)) * KVC + (t & 3) * 1280;
        const u32x4 k1 = gl128<16>(rw, kb_ + lane * 16); const u32x2 k2 = gl64<16>(rw, kb_ + 1024 + (lane & 31) * 8);
        ma = fmaxf(ma, max16(scores(k1, k2, qa1, qa2))); mb = fmaxf(mb, max16(scores(k1, k2, qb1, qb2)));
      }
      raw_bar();
    }
  }
  {
    const unsigned lb = __float_as_uint(r[2][0]);
    const float il = __builtin_amdgcn_rcpf(__uint_as_float(__builtin_amdgcn_permlane32_swap(lb, lb, false, false)[1]));
    const unsigned sl8 = os & 255;
    st128<16>(rw, os + sl8 + h * 512, clean(pk8(r[0] * il, r[1] * il)));
    const u32x4 t = clean(pk8(r[2] * il, r[2] * il));
    st64<16>(rw, h ? WS_END : os + 1024, u32x2{t[0], t[1]});
  }
  MARK(MK + 4);
  const lptr wa = lds + L_A + wn * 35 * 1024 + lane * 16, wb = ring + lane * 16;
  constexpr int NPRE = 15;
  u32x4 bf[50];
  f32x4 acc[5] = {};
#pragma unroll
  for (int j = 0; j < NPRE; j++) bf[j] = ld128(wa + j * 1024);
#pragma unroll
  for (int j = 0; j < 5; j++) asm volatile("" : "+v"(acc[j]));
  if (FINAL) dc = __uint_as_float(gl32<16>(rw, o3[3]));
#pragma unroll 1
  for (int spins = 0; spins < (1 << 18); spins++) {
#pragma unroll
    for (int i = 0; i < 3; i++) x4v[i] = gl64<16>(rw, o3[i]);
    if (!FINAL || !__builtin_amdgcn_ballot_w64(umax4(max(x4v[0][0], x4v[0][1]), u32x4{x4v[1][0], x4v[1][1], x4v[2][0], x4v[2][1]}) == EMPTY)) break;
    nap<8>();
  }
  drain();
  nap<ODELAY>();
  MARK(MK + 5);
  {
    u32x4 af[10];
    auto ldf = [&](int i) {
      if (i < 8) af[i] = __builtin_amdgcn_raw_buffer_load_b128(rw, o1, i * 2 * 1280, 16);
      else { const u32x2 a = __builtin_amdgcn_raw_buffer_load_b64(rw, o2, (i - 8) * 8 * 1280, 16), b = __builtin_amdgcn_raw_buffer_load_b64(rw, o2, ((i - 8) * 8 + 1) * 1280, 16);
             af[i] = u32x4{a[0], a[1], b[0], b[1]}; }
    };
#pragma unroll
    for (int i = 0; i < 10; i++) ldf(i);
#pragma unroll 1
    for (int spins = 0; spins < (1 << 18); spins++) {
      unsigned m = 0;
#pragma unroll
      for (int i = 0; i < 10; i++) m = umax4(m, af[i]);
      if (!__builtin_amdgcn_ballot_w64(m == EMPTY)) break;
      nap<RSLEEP>();
#pragma unroll
      for (int i = 0; i < 10; i++) if (__builtin_amdgcn_ballot_w64(umax4(0, af[i]) == EMPTY)) ldf(i);
    }
    MARK(MK + 8);
#pragma unroll
    for (int i = 0; i < 10; i++) asm volatile("" : "+v"(af[i]));
    asm volatile("s_nop 1" : "+v"(acc[0]), "+v"(acc[1]), "+v"(acc[2]), "+v"(acc[3]), "+v"(acc[4]));
    SB();
#pragma unroll
    for (int i = 0; i < 10; i++) {
#pragma unroll
      for (int a = 0; a < 5; a++) {
        if (FINAL) mfma16v(acc[a], bf[5 * i + a], af[i]); else mfma16v(acc[a], af[i], bf[5 * i + a]);
        const int j = 5 * i + a + NPRE; if (j < 50) bf[j] = ld128(j < 35 ? wa + j * 1024 : wb + (j - 35) * 1024);
        SB();
      }
    }
  }
  asm volatile("s_nop 7\n\ts_nop 1" : "+v"(acc[0]), "+v"(acc[1]), "+v"(acc[2]), "+v"(acc[3]), "+v"(acc[4]) :: "memory");
  MARK(MK + 6);
  {
    LDS_AS f32x4* ex = (LDS_AS f32x4*)(ring + L_EX + 12288 + wm * 3072) + lane;
    LDS_AS f32x4* ey = (LDS_AS f32x4*)(lds + (wn ^ 1) * HR + L_EX + 12288 + wm * 3072) + lane + 64 * wn;
    ex[0] = acc[2]; ex[64] = acc[3]; ex[128] = acc[4];
    dc *= 0.039528470752104741f;
#pragma unroll
    for (int i = 0; i < 3; i++) { acc[i] += f32x4{bflo(x4v[i][0]), bfhi(x4v[i][0]), bflo(x4v[i][1]), bfhi(x4v[i][1])}; if (FINAL) acc[i] += dc; }
    lds_bar();
#pragma unroll
    for (int i = 0; i < 3; i++) acc[i] += ey[64 * i];
  }
  if (FINAL) {
    const rsrc_t ro = mk_rsrc(out, 2048 * 640 * 2);
    const unsigned op = (rowof(pblk + trow(wm & 1, r16)) * 640 + 80 * ct + 48 * wn + 4 * g) * 2;
#pragma unroll
    for (int i = 0; i < 3; i++)
      if (i < 2 || wn == 0) st64<OUTAUX>(ro, op + 32 * i, pk4(acc[i]));
  } else {
    f32x4 sq = {0.f, 0.f, 0.f, 0.f};
#pragma unroll
    for (int i = 0; i < 3; i++)
      if (i < 2 || wn == 0) {
        const int j = 80 * ct + 48 * wn + 16 * i + r16;
        st128<16>(rw, r3t + j * 64 + g * 16, clean(asu(acc[i])));
        if (j != 1) sq += acc[i] * acc[i];
      }
#pragma unroll
    for (int e = 0; e < 4; e++) sq[e] = row16_sum(sq[e]);
    LDS_AS float* sl = (LDS_AS float*)(lds + L_MISC);
    if (r16 == 0) *(LDS_AS f32x4*)(sl + wn * 64 + 32 * (wm & 1) + 16 * (wm >> 1) + 4 * g) = sq;
    lds_bar();
    if (w == 0) {
      LDS_AS f32x4* s4 = (LDS_AS f32x4*)sl + 2 * (lane & 7);
      st128<16>(rw, WS_SSQ + (lane >> 3) * 32768 + (ct * 2048 + 1024 * ((lane >> 2) & 1) + 64 * qt + 32 * hf + 8 * (lane & 3)) * 2, clean(pk8(s4[0] + s4[16], s4[1] + s4[17])));
    }
  }
  MARK(MK + 7);
}

__global__ void __launch_bounds__(768) k_main(const u16* x, const u16* wr, const u16* wi, const u16* wqkv, const u16* wo, char* ws, u16* out) {
  STAMP(1);
  if (wave_id() >= 8) {
    extern __shared__ __attribute__((aligned(16))) char smem[];
    const unsigned long long t0 = __builtin_amdgcn_s_memrealtime();
    const rsrc_t rw = mk_rsrc(ws, WS_END);
    __builtin_amdgcn_s_setprio(3);
    if (threadIdx.x < 528) ((LDS_AS unsigned*)((lptr)smem + L_MISC + 1024))[threadIdx.x - 512] = 0u;
    u32x2 tag = gl64<16>(rw, WS_EP);
    tag[0] += 1u;
    const int job = blockIdx.x;
    if (job < 228) raw_bar();
    if (job < 124) raw_bar();
    asm volatile("s_waitcnt vmcnt(0) lgkmcnt(0)\n\ts_barrier" : "+v"(tag) :: "memory");
    if (wave_id() == 8) {
      if (threadIdx.x < 520) st64<16>(rw, WS_F0 + (threadIdx.x - 512) * 2048 + job * 8, tag);
      MARK(9);
      if (job >= 248) {
        while (__builtin_amdgcn_s_memrealtime() - t0 < TPOLL) nap<2>();
        poll_tag(rw, tag, 0xFE0FFFF00000FFFFull);
        if (threadIdx.x < 544) st64<16>(rw, WS_GO + ((threadIdx.x - 512) * 8 + (job & 7)) * 64, tag);
      }
#pragma unroll 1
      for (int spins = 0; spins < (1 << 20); spins++) {
        const u32x2 v = gl64<16>(rw, WS_GO + job * 64);
        if (__builtin_amdgcn_readfirstlane((v[0] ^ tag[0]) | (v[1] ^ tag[1])) == 0) break;
        nap<GSLEEP>();
      }
      MARK(10);
    }
    if (wave_id() > 8) {
    while (__builtin_amdgcn_s_memrealtime() - t0 < TFILL) nap<2>();
    const int i0 = blockIdx.x * 192 + threadIdx.x - 576;
    for (int pc = i0; pc < 640 * 80; pc += 192 * 256) {
      const int n = pc % 640, kq = pc / 640;
      const int u_ = n / 40, rn = n - 40 * u_, hn = rn / 20, dn = rn - 20 * hn, gq = dn >> 2;
      const int T = gq < 4 ? 8 + hn : 4 * hn + 3, gs = gq < 4 ? gq : 3;
      float v[8];
#pragma unroll
      for (int e = 0; e < 4; e++) {
        const float a = (float)((freq_of(kq >> 2, 4 * (kq & 3) + e) * n) % 640) * (1.f / 640.f);
        v[2 * e] = 0.055901699437494742f * __builtin_amdgcn_cosf(a);
        v[2 * e + 1] = -0.055901699437494742f * __builtin_amdgcn_sinf(a);
      }
      if (kq == 0) { v[0] = (n & 1) ? -0.039528470752104741f : 0.039528470752104741f; v[1] = 0.039528470752104741f; }
      st128<16>(rw, WS_WQ + ((u_ * 10 + T) * 20 + (kq >> 2)) * 1024 + ((kq & 3) * 16 + 4 * gs + (dn & 3)) * 16,
                  u32x4{pk2(v[0], v[1]), pk2(v[2], v[3]), pk2(v[4], v[5]), pk2(v[6], v[7])});
    }
#pragma unroll
    for (int it = 0; it < 4; it++) {
      const int k = it * 49152 + i0, b = k / 1408;
      st128<16>(rw, k < 128 * 1408 ? WS_KV + b * 4 * KVC + (k - b * 1408) * 16 : WS_END, u32x4{EMPTY, EMPTY, EMPTY, EMPTY});
    }
    }
    raw_bar();
    loader<false>(ws, tag SPASS);
    loader<true>(ws, tag SPASS);
  } else {
    prep(x, wr, wi, wqkv, wo, ws);
    asm volatile("s_waitcnt vmcnt(0) lgkmcnt(0)\n\ts_barrier" ::: "memory");
    compute<false>(ws, out SPASS);
    compute<true>(ws, out SPASS);
  }
}

#define STOPAT(n)
extern "C" void kernel_launch(void* const* d_in, const int* in_sizes, int n_in, void* d_out, int out_size, void* d_ws, size_t ws_size,
                              hipStream_t stream) {
  const u16* x = (const u16*)d_in[0];
  const u16* wr = (const u16*)d_in[1];
  const u16* wi = (const u16*)d_in[2];
  const u16* wqkv = (const u16*)d_in[4];
  const u16* wo = (const u16*)d_in[5];
  char* ws = (char*)d_ws;
  (void)hipFuncSetAttribute((const void*)k_main, hipFuncAttributeMaxDynamicSharedMemorySize, LDS_FUSED);
  k_main<<<256, 768, LDS_FUSED, stream>>>(x, wr, wi, wqkv, wo, ws, (u16*)d_out);
}
```

```cpp
#include <hip/hip_runtime.h>
#include <hip/hip_bf16.h>
#include <utility>
#include <type_traits>

typedef unsigned short u16;
typedef __attribute__((ext_vector_type(16))) float f32x16;
typedef __attribute__((ext_vector_type(4))) float f32x4;
typedef __attribute__((ext_vector_type(2))) float f32x2;
typedef __attribute__((ext_vector_type(8))) __bf16 bf16x8;
typedef __attribute__((ext_vector_type(2))) __bf16 bf16x2;
typedef __attribute__((ext_vector_type(4))) short s16x4;
typedef __attribute__((ext_vector_type(4))) unsigned u32x4;
typedef __attribute__((ext_vector_type(2))) unsigned u32x2;
#define DI __device__ __forceinline__
#define LDS_AS __attribute__((address_space(3)))
typedef LDS_AS char* lptr;

constexpr double cx_cos_small(double x) { double x2 = x * x, t = 1, s = 1; for (int i = 1; i < 12; i++) { t *= -x2 / ((2 * i - 1) * (2 * i)); s += t; } return s; }
constexpr double cx_sin_small(double x) { double x2 = x * x, t = x, s = x; for (int i = 1; i < 12; i++) { t *= -x2 / ((2 * i) * (2 * i + 1)); s += t; } return s; }
constexpr double cx_cos640(int m) {
  m %= 640; if (m > 320) m = 640 - m; double sg = 1; if (m > 160) { m = 320 - m; sg = -1; }
  const double w = 6.283185307179586476925 / 640; return sg * (m <= 80 ? cx_cos_small(w * m) : cx_sin_small(w * (160 - m)));
}
constexpr u16 cx_bf16(double d) { float f = (float)d; unsigned u = __builtin_bit_cast(unsigned, f); u += 0x7FFFu + ((u >> 16) & 1u); return (u16)(u >> 16); }
struct Cos640 { double v[640]; };
constexpr Cos640 mk_cos() { Cos640 c{}; for (int m = 0; m < 640; m++) c.v[m] = cx_cos640(m); return c; }
constexpr Cos640 COS = mk_cos();
__host__ __device__ constexpr int kf_of(int G, int i) { return G ? G + 40 * i : (i == 0 ? 320 : i < 8 ? 40 * i : 40 * i - 300); }
__host__ __device__ constexpr int freq_of(int G, int i) { return kf_of(G, i) > 320 ? 640 - kf_of(G, i) : kf_of(G, i); }
constexpr double a1val(int out, int a) {
  if (out >= 40 || a >= 40) return 0;
  const int G = out >> 1, part = out & 1;
  if (G == 0) return (part && (a & 1)) ? -1 : 1;
  return COS.v[(16 * a * G + 160 * part) % 640];
}
constexpr double m2val(int G, int o, int k) {
  const int i = o >> 1, po = o & 1, part = k >> 4, b = k & 15, kf = kf_of(G, i);
  const double c = COS.v[(b * kf) % 640], sn = COS.v[(b * kf + 480) % 640];
  if (G == 0) {
    if (part != (i >= 8)) return 0;
    if (i == 0) return 0.039528470752104741 * (po ? 1.0 : c);
    return 0.055901699437494742 * (po ? -sn : c);
  }
  const double v = po ? (part ? c : -sn) : (part ? sn : c);
  return 0.055901699437494742 * ((po && kf > 320) ? -v : v);
}
struct DTab { u16 a1[3][64][8]; u16 a1b[3][64][4]; u16 pad[256]; u16 m2[20][2][64][8]; };
constexpr DTab mk_dtab() {
  DTab t{};
  for (int l = 0; l < 64; l++) {
    const int r = l & 15, g = l >> 4;
    for (int T = 0; T < 3; T++) {
      for (int e = 0; e < 8; e++) t.a1[T][l][e] = cx_bf16(a1val(16 * T + r, 16 * (e >> 2) + 4 * g + (e & 3)));
      for (int e = 0; e < 4; e++) t.a1b[T][l][e] = cx_bf16(a1val(16 * T + r, 32 + 4 * g + e));
    }
    for (int G = 0; G < 20; G++) for (int T = 0; T < 2; T++) for (int e = 0; e < 8; e++)
      t.m2[G][T][l][e] = cx_bf16(m2val(G, 8 * (r >> 2) + 4 * T + (r & 3), 8 * g + e));
  }
  return t;
}
static __device__ const __attribute__((aligned(1024))) DTab g_dt = mk_dtab();

struct CTab { u16 v[10 * 512]; };
constexpr CTab mk_ctab() {
  CTab c{}; u16 cs[64] = {};
  for (int m = 0; m < 64; m++) cs[m] = cx_bf16(cx_cos640(10 * m));
  for (int f = 0; f < 10; f++) for (int l = 0; l < 64; l++) for (int e = 0; e < 8; e++) {
    const int r = l & 15, g = l >> 4;
    const int m = f < 8 ? (8 * g + e) * (16 * (f >> 1) + r) : 2 * r * (16 * (e >> 2) + 4 * g + (e & 3));
    c.v[(f * 64 + l) * 8 + e] = cs[(m + (f & 1) * 48) & 63];
  }
  return c;
}
static __device__ const __attribute__((aligned(1024))) CTab g_ct = mk_ctab();

constexpr int RP = 2048 + 64, RBP = 2048 + 128;
constexpr unsigned RBSZ = 640 * RBP * 2;
constexpr unsigned WS_FL = 0;
constexpr unsigned WS_AUX = WS_FL + 3 * 32768;
constexpr unsigned WS_F0 = WS_AUX + 2048;
constexpr unsigned WS_GO = WS_F0 + 8 * 2048;
constexpr unsigned WS_EP = WS_GO + 256 * 64;
constexpr unsigned WS_RS1 = WS_EP + 256;
constexpr unsigned WS_RB0 = WS_RS1 + 8192;
constexpr unsigned WS_RB1 = WS_RB0 + RBSZ;
constexpr unsigned WS_WF = WS_RB1 + RBSZ;
constexpr unsigned WS_WQ = WS_WF + 320 * 8192;
constexpr unsigned WS_WOF = WS_WQ + 16 * 10 * 20 * 1024;
constexpr unsigned WS_WOP = WS_WOF + 40 * 20 * 1024;
constexpr int KVC = 11264;
constexpr unsigned WS_KV = WS_WOP + 40 * 20 * 1024;
constexpr unsigned WS_O = WS_KV + 32 * 16 * KVC;
constexpr unsigned LAYER2 = 32 * 16 * KVC + 32 * 64 * 1280;
constexpr unsigned WS_X4 = WS_KV + 2 * LAYER2;
constexpr unsigned WS_R3 = WS_X4 + 2048 * 640 * 2;
constexpr unsigned WS_RB2 = WS_R3 + 640 * RP * 4;
constexpr unsigned WS_SSQ = WS_RB2 + RBSZ;
constexpr unsigned EMPTY = 0xFFFFFFFFu;
constexpr unsigned WS_END = WS_SSQ + 8 * 2048 * 16;
#define FILLDELAY 10
#define OUTAUX 0
#define XDELAY 20
#define SW1 50
#define SW2 46
#define XNL 0
#define K1DELAY 40
#define YDELAY 0
#define ODELAY 0
#define WMODE 0
#define YIDLE 8
#define PROBE 1
#define TPOLL 300
#define TSLEEP 1
#define GSLEEP 2
#define LDELAY 30
#define TFILL 450
#define PFDELAY 40
#define YSLEEP 4
#define XSLEEP 1
#define RSLEEP 2
#define AUXB 16

#define STW 0
#define STAMP(k)
#define MARK(i)
#define SARG
#define SPASS
#define SB() __builtin_amdgcn_sched_barrier(0)
typedef __amdgpu_buffer_rsrc_t rsrc_t;
DI rsrc_t mk_rsrc(const void* p, unsigned bytes) { return __builtin_amdgcn_make_buffer_rsrc((void*)p, 0, bytes, 0x00020000); }
template <int AUX> DI void st128(rsrc_t r, unsigned off, u32x4 v) { __builtin_amdgcn_raw_buffer_store_b128(v, r, off, 0, AUX); }
template <int AUX> DI void st64(rsrc_t r, unsigned off, u32x2 v) { __builtin_amdgcn_raw_buffer_store_b64(v, r, off, 0, AUX); }
template <int AUX> DI void st32(rsrc_t r, unsigned off, unsigned v) { __builtin_amdgcn_raw_buffer_store_b32(v, r, off, 0, AUX); }
template <int AUX> DI void st16(rsrc_t r, unsigned off, u16 v) { __builtin_amdgcn_raw_buffer_store_b16(v, r, off, 0, AUX); }
template <int AUX> DI u32x4 gl128(rsrc_t r, unsigned off) { return __builtin_amdgcn_raw_buffer_load_b128(r, off, 0, AUX); }
template <int AUX> DI u32x2 gl64(rsrc_t r, unsigned off) { return __builtin_amdgcn_raw_buffer_load_b64(r, off, 0, AUX); }
template <int AUX> DI unsigned gl32(rsrc_t r, unsigned off) { return __builtin_amdgcn_raw_buffer_load_b32(r, off, 0, AUX); }
DI u32x4 asu(f32x4 v) { return __builtin_bit_cast(u32x4, v); }
DI f32x4 asf(u32x4 v) { return __builtin_bit_cast(f32x4, v); }
DI unsigned pk2(float a, float b) { f32x2 v = {a, b}; return __builtin_bit_cast(unsigned, __builtin_convertvector(v, bf16x2)); }
DI u32x2 pk4(f32x4 a) { return u32x2{pk2(a[0], a[1]), pk2(a[2], a[3])}; }
DI u32x4 pk8(f32x4 a, f32x4 b) { return u32x4{pk2(a[0], a[1]), pk2(a[2], a[3]), pk2(b[0], b[1]), pk2(b[2], b[3])}; }
DI u32x4 clean(u32x4 v) { return __builtin_elementwise_min(v, u32x4{EMPTY - 1, EMPTY - 1, EMPTY - 1, EMPTY - 1}); }
DI unsigned umax4(unsigned m, u32x4 v) { return max(max(max(m, v[0]), v[1]), max(v[2], v[3])); }
DI float bflo(unsigned u) { return __uint_as_float(u << 16); }
DI float bfhi(unsigned u) { return __uint_as_float(u & 0xFFFF0000u); }
DI float dot2(unsigned a, unsigned b, float c) { return __builtin_amdgcn_fdot2_f32_bf16(__builtin_bit_cast(bf16x2, a), __builtin_bit_cast(bf16x2, b), c, false); }
DI int rowof(int p) { return 32 * (((p >> 5) & 0x3C) + (p & 3)) + ((p << 2) & 16) + ((p >> 3) & 15); }
DI int rpos(int p) { return ((p << 8) & 1024) + ((p >> 3) << 2) + (p & 3); }
DI int trow(int T, int r) { return 8 * (r >> 2) + 4 * T + (r & 3); }
template <int CTRL> DI float dpp_add(float v) { return v + __int_as_float(__builtin_amdgcn_update_dpp(0, __float_as_int(v), CTRL, 0xF, 0xF, true)); }
DI float row16_sum(float v) { v = dpp_add<0x128>(v); v = dpp_add<0x124>(v); v = dpp_add<0x122>(v); return dpp_add<0x121>(v); }
DI f32x4 mfma16(u32x4 a, u32x4 b, f32x4 c) {
  return __builtin_amdgcn_mfma_f32_16x16x32_bf16(__builtin_bit_cast(bf16x8, a), __builtin_bit_cast(bf16x8, b), c, 0, 0, 0);
}
DI void mfma16v(f32x4& c, u32x4 a, u32x4 b) { asm volatile("v_mfma_f32_16x16x32_bf16 %0, %1, %2, %0" : "+v"(c) : "v"(a), "v"(b)); }
DI u32x2 ldtr(lptr p) { return __builtin_bit_cast(u32x2, __builtin_amdgcn_ds_read_tr16_b64_v4i16((LDS_AS s16x4*)p)); }
DI u32x4 ld128(lptr p) { return *(LDS_AS u32x4*)p; }
DI u32x2 ld64(lptr p) { return *(LDS_AS u32x2*)p; }
#define GL(v, s, o, x) "global_load_lds_dwordx4 " v ", " s " offset:" #o x "\n\t"
#define M0SET "s_mov_b32 %[sv], m0\n\ts_mov_b32 m0, %[l]\n\ts_nop 2\n\t"
#define M0ADD(x) "s_add_u32 m0, m0, " x "\n\ts_nop 0\n\t"
#define M0RST "s_mov_b32 m0, %[sv]"
DI void dma2(unsigned v, const char* sb, unsigned l) { unsigned sv;
  asm volatile(M0SET GL("%[v]", "%[s]", 0, " sc1") GL("%[v]", "%[s]", 1024, " sc1") M0RST : [sv] "=&s"(sv) : [v] "v"(v), [s] "s"(sb), [l] "s"(l) : "memory"); }
DI void dma4(unsigned v, const char* sb, unsigned l) { unsigned sv;
  asm volatile(M0SET GL("%[v]", "%[s]", 0, "") GL("%[v]", "%[s]", 1024, "") GL("%[v]", "%[s]", 2048, "") GL("%[v]", "%[s]", 3072, "") M0RST
               : [sv] "=&s"(sv) : [v] "v"(v), [s] "s"(sb), [l] "s"(l) : "memory"); }
DI void dma6_sc1(unsigned v, const char* sb, unsigned l) { unsigned sv;
  asm volatile(M0SET GL("%[v]", "%[s]", 0, " sc1") GL("%[v]", "%[s]", 1024, " sc1") GL("%[v]", "%[s]", 2048, " sc1") M0ADD("3072")
               GL("%[w]", "%[s]", 0, " sc1") GL("%[w]", "%[s]", 1024, " sc1") GL("%[w]", "%[s]", 2048, " sc1") M0RST
               : [sv] "=&s"(sv) : [v] "v"(v), [w] "v"(v + 3072), [s] "s"(sb), [l] "s"(l) : "memory", "scc"); }
template <int N> DI void vm_bar() { asm volatile("s_waitcnt vmcnt(%0)\n\ts_barrier" ::"i"(N) : "memory"); }
DI void lds_bar() { asm volatile("s_waitcnt lgkmcnt(0)\n\ts_barrier" ::: "memory"); }
DI void raw_bar() { asm volatile("s_barrier" ::: "memory"); }
template <int N> DI void nap() { __builtin_amdgcn_s_sleep(N); asm volatile("" ::: "memory"); }
DI void drain() { asm volatile("s_waitcnt vmcnt(0)" ::: "memory"); }
#define CODE_ADDR(sym) ({ unsigned lo_, hi_; \
    asm volatile("s_getpc_b64 vcc\n\ts_add_u32 %0, vcc_lo, " sym "@rel32@lo+4\n\ts_addc_u32 %1, vcc_hi, " sym "@rel32@hi+12" : "=s"(lo_), "=s"(hi_) :: "vcc", "scc"); \
    (const unsigned*)(((unsigned long long)hi_ << 32) | lo_); })
#define SYM_MAIN "_Z6k_mainPKtS0_S0_S0_S0_PcPt"
constexpr int CODE_LINES = 368;
DI int wave_id() { return __builtin_amdgcn_readfirstlane(threadIdx.x >> 6); }
DI unsigned lds_addr(lptr p) { return (unsigned)(size_t)p; }

DI int kappa_to_i(int kap) {
  int ks = kap >> 5, g = (kap >> 3) & 3, e = kap & 7;
  if (ks < 16) return (2 * ks + (g >> 1)) * 20 + 8 * (e >> 2) + 4 * (g & 1) + (e & 3);
  return (8 * (ks - 16) + 2 * g + (e >> 2)) * 20 + 16 + (e & 3);
}

DI unsigned wo_frag(int nt, int ks) {
  const int ct = nt / 5, c5 = nt - 5 * ct, kh = ks < 16 ? ks >> 3 : (ks - 16) >> 1, i = ks < 16 ? ks & 7 : 8 + (ks & 1);
  const int a = kh ? (c5 >= 3 ? c5 - 3 : c5 + 2) : c5;
  return ((ct * 2 + kh) * 50 + 5 * i + a) * 1024;
}

constexpr int SLOT32 = 36864;
DI void dma_a(const unsigned* vm, const char* mb, unsigned l) { unsigned sv;
  asm volatile(M0SET GL("%[a0]", "%[m]", 0, " sc1") GL("%[a1]", "%[m]", 1024, " sc1") GL("%[a2]", "%[m]", 2048, " sc1") GL("%[a3]", "%[m]", 3072, " sc1") M0RST
               : [sv] "=&s"(sv) : [a0] "v"(vm[0]), [a1] "v"(vm[1]), [a2] "v"(vm[2]), [a3] "v"(vm[3]), [m] "s"(mb), [l] "s"(l) : "memory"); }
DI void dma_s(unsigned v0, unsigned v1, const char* sb, unsigned l) { unsigned sv;
  asm volatile(M0SET GL("%[v0]", "%[s]", 0, " sc1") GL("%[v0]", "%[s]", 1024, " sc1") GL("%[v1]", "%[s]", 2048, " sc1") GL("%[v1]", "%[s]", 3072, " sc1") M0RST
               : [sv] "=&s"(sv) : [v0] "v"(v0), [v1] "v"(v1), [s] "s"(sb), [l] "s"(l) : "memory"); }
DI void dma1_sc1(unsigned v, const char* sb, unsigned l) { unsigned sv;
  asm volatile(M0SET GL("%[v]", "%[s]", 0, " sc1") M0RST : [sv] "=&s"(sv) : [v] "v"(v), [s] "s"(sb), [l] "s"(l) : "memory"); }
DI void dma1(unsigned v, const char* sb, unsigned l) { unsigned sv;
  asm volatile(M0SET GL("%[v]", "%[s]", 0, "") M0RST : [sv] "=&s"(sv) : [v] "v"(v), [s] "s"(sb), [l] "s"(l) : "memory"); }

constexpr int L_I2 = 40 * 1056, LDS_PREP = L_I2 + 20 * 2064 + 1024;
DI void prep(const u16* x, const u16* wr, const u16* wi, const u16* wqkv, const u16* wo, char* ws) {
  extern __shared__ __attribute__((aligned(16))) char smem[];
  const lptr lds = (lptr)smem;
  const int tid = threadIdx.x, lane = tid & 63, w = wave_id(), g = lane >> 4, r16 = lane & 15;
  const int job = blockIdx.x;
  if (__builtin_expect(job < 144, 1)) {
    const bool cols = job >= 64 && job < 124;
    const int cj = job - 64, cb = cj < 48 ? (cj & 48) + 2 * (cj & 7) + ((cj >> 3) & 1) : cj;
    u32x4 a1[3], m2[3][2]; u32x2 a1b[3];
#pragma unroll
    for (int T = 0; T < 3; T++) {
      asm volatile("global_load_dwordx4 %0, %1, %2" : "=v"(a1[T]) : "v"((T * 64 + lane) * 16), "s"(&g_dt.a1[0][0][0]) : "memory");
      asm volatile("global_load_dwordx2 %0, %1, %2" : "=v"(a1b[T]) : "v"((T * 64 + lane) * 8), "s"(&g_dt.a1b[0][0][0]) : "memory");
    }
    SB();
    if (cols) {
#pragma unroll
      for (int i = 0; i < 5; i++) dma1((16 * (5 * w + i) + (lane >> 2)) * 3840 + cb * 64 + (lane & 3) * 16, (const char*)wqkv, lds_addr(lds) + (5 * w + i) * 1056);
    } else {
      const char* src = (const char*)(job < 64 ? x : wo);
      const int sl = 8 * (w & 3) + 4 * (w >> 2);
      src += (job < 64 ? rowof(32 * job + sl) : kappa_to_i(32 * (job - 124) + sl)) * 1280;
      const int st = (job < 64 ? 31 : 0) * 1280;
#pragma unroll
      for (int i = 0; i < 5; i++) dma1(1024 * i + lane * 16 + ((i == 4 || lane < 16 * i) ? i - 1 : i) * st, src, lds_addr(lds) + (5 * w + i) * 1024);
    }
#pragma unroll
    for (int i = 0; i < 3; i++)
#pragma unroll
      for (int T = 0; T < 2; T++)
        asm volatile("global_load_dwordx4 %0, %1, %2" : "=v"(m2[i][T]) : "v"(((((i < 2 || w < 4) ? w + 8 * i : w) * 2 + T) * 64 + lane) * 16), "s"(&g_dt.m2[0][0][0][0]) : "memory");
    unsigned so[3][2]; float sc[2] = {1.f, 1.f};
    if (cols) {
#pragma unroll
      for (int m = 0; m < 2; m++) {
        const int c = 32 * cb + 16 * m + r16;
        const int which = c / 640, rem = c - which * 640, head = rem / 20, d = rem - head * 20, grp = d >> 2;
        const int T = grp < 4 ? (which == 2 ? 2 : 0) + (grp & 1) : (which == 1 ? 3 : 2), gs = grp < 4 ? (which == 1 ? 2 : 0) + (grp >> 1) : (which == 2 ? 3 : 2);
        if (which == 0) sc[m] = 0.22360679774997896f * 1.4426950408889634f;
#pragma unroll
        for (int i = 0; i < 3; i++) so[i][m] = WS_WQ + (((head >> 1) * 10 + 4 * (head & 1) + T) * 20 + w + 8 * i) * 1024 + (g * 16 + 4 * gs + (d & 3)) * 16;
      }
    } else if (job < 64) {
#pragma unroll
      for (int i = 0; i < 3; i++)
#pragma unroll
        for (int T = 0; T < 2; T++) so[i][T] = WS_RB0 + (job * 641 + (job & 1) + 32 * (w + 8 * i) + trow(T, r16)) * 64 + g * 16;
    } else {
#pragma unroll
      for (int i = 0; i < 3; i++) {
        const unsigned f = wo_frag(2 * (w + 8 * i) + (r16 >> 3), job - 124);
#pragma unroll
        for (int T = 0; T < 2; T++) so[i][T] = WS_WOF + f + (g * 16 + (trow(T, r16) & 15)) * 16;
      }
    }
    if (w >= 4) { so[2][0] = WS_END; so[2][1] = WS_END; }
    asm volatile("" : "+v"(so[0][0]), "+v"(so[0][1]), "+v"(so[1][0]), "+v"(so[1][1]), "+v"(so[2][0]), "+v"(so[2][1]), "+v"(sc[0]), "+v"(sc[1]));
    const f32x4 z4 = {0.f, 0.f, 0.f, 0.f};
    const lptr i2 = lds + L_I2;
    asm volatile("s_waitcnt vmcnt(6)" : "+v"(a1[0]), "+v"(a1[1]), "+v"(a1[2]), "+v"(a1b[0]), "+v"(a1b[1]), "+v"(a1b[2]) :: "memory");
    f32x4 t1[4][3];
    if (cols) {
      raw_bar();
      const lptr src = lds + (4 * g + (r16 >> 2)) * 1056 + (w >> 1) * 256 + (w & 1) * 32 + (lane & 3) * 8, srb = src + (32 - 8 * (g >> 1)) * 1056;
#pragma unroll
      for (int i = 0; i < 4; i++) {
        const u32x2 f0 = ldtr(src + i * 64), f1 = ldtr(src + 16 * 1056 + i * 64), f2 = ldtr(srb + i * 64);
#pragma unroll
        for (int T = 0; T < 3; T++) t1[i][T] = mfma16(u32x4{a1b[T][0], a1b[T][1], 0u, 0u}, u32x4{f2[0], f2[1], 0u, 0u}, mfma16(a1[T], u32x4{f0[0], f0[1], f1[0], f1[1]}, z4));
      }
      const int xo = ((w >> 1) * 8) ^ (((r16 >> 1) & 3) * 16);
      const lptr dst = i2 + 2 * g * 2064 + (16 * (w & 1) + r16) * 64;
#pragma unroll
      for (int T = 0; T < 3; T++)
#pragma unroll
        for (int e = 0; e < 4; e++)
          if (T < 2 || g < 2) *(LDS_AS u32x2*)(dst + (8 * T + (e >> 1)) * 2064 + (xo ^ ((e & 1) * 32))) = u32x2{pk2(t1[0][T][e], t1[1][T][e]), pk2(t1[2][T][e], t1[3][T][e])};
    } else {
      const lptr src = lds + w * 5120 + lane * 8, srb = lds + w * 5120 + 1024 + (lane & 31) * 8;
#pragma unroll
      for (int i = 0; i < 4; i++) {
        const u32x2 f0 = ldtr(src + i * 1280), f1 = ldtr(src + i * 1280 + 512), f2 = ldtr(srb + i * 1280);
#pragma unroll
        for (int T = 0; T < 3; T++) t1[i][T] = mfma16(u32x4{f2[0], f2[1], 0u, 0u}, u32x4{a1b[T][0], a1b[T][1], 0u, 0u}, mfma16(u32x4{f0[0], f0[1], f1[0], f1[1]}, a1[T], z4));
      }
      const int xo = ((r16 & 1) * 32 + g * 8) ^ ((w & 1) * 32);
      const lptr dst = i2 + (r16 >> 1) * 2064 + w * 256;
#pragma unroll
      for (int i = 0; i < 4; i++)
#pragma unroll
        for (int T = 0; T < 3; T++)
          if (T < 2 || r16 < 8) *(LDS_AS u32x2*)(dst + T * 8 * 2064 + i * 64 + (xo ^ ((i >> 1) * 16))) = pk4(t1[i][T]);
    }
    lds_bar();
    const rsrc_t rw = mk_rsrc(ws, WS_END);
    asm volatile("s_waitcnt vmcnt(0)" : "+v"(m2[0][0]), "+v"(m2[0][1]), "+v"(m2[1][0]), "+v"(m2[1][1]), "+v"(m2[2][0]), "+v"(m2[2][1]));
    f32x4 sq[2] = {z4, z4};
    u32x4 f[3][2];
#pragma unroll
    for (int i = 0; i < 3; i++) {
      const lptr fp = i2 + ((i < 2 || w < 4) ? w + 8 * i : 0) * 2064 + r16 * 64 + ((g ^ ((r16 >> 1) & 3)) * 16);
      f[i][0] = ld128(fp); f[i][1] = ld128(fp + 1024);
    }
    if (cols) {
#pragma unroll
      for (int i = 0; i < 3; i++)
#pragma unroll
        for (int m = 0; m < 2; m++) st128<16>(rw, so[i][m], pk8(mfma16(m2[i][0], f[i][m], z4) * sc[m], mfma16(m2[i][1], f[i][m], z4) * sc[m]));
    } else {
#pragma unroll
      for (int i = 0; i < 3; i++)
#pragma unroll
        for (int T = 0; T < 2; T++) {
          const f32x4 d0 = mfma16(f[i][0], m2[i][T], z4), d1 = mfma16(f[i][1], m2[i][T], z4);
          st128<16>(rw, so[i][T], pk8(d0, d1));
          const float k = ((i == 2 && w >= 4) || (i == 0 && T == 0 && tid < 64 && r16 == 1)) ? 0.f : 1.f;
          sq[0] += d0 * d0 * k; sq[1] += d1 * d1 * k;
        }
    }
    LDS_AS float* sl = (LDS_AS float*)(lds + LDS_PREP - 1024);
    if (job < 64) {
#pragma unroll
      for (int m = 0; m < 2; m++) {
#pragma unroll
        for (int e = 0; e < 4; e++) sq[m][e] = row16_sum(sq[m][e]);
        if (r16 == 0) *(LDS_AS f32x4*)(sl + w * 32 + 8 * g + 4 * m) = sq[m];
      }
    }
    if (job < 64) {
      lds_bar();
      if (tid < 32) {
        float v = 0.f;
#pragma unroll
        for (int i = 0; i < 8; i++) v += sl[i * 32 + tid];
        st32<16>(rw, WS_RS1 + rpos(32 * job + tid) * 4, __float_as_uint(__builtin_amdgcn_rsqf((v + 640.f * 1e-5f) * (2048.f * 2048.f / 640.f))));
      }
    }
    return;
  }
  const int mi = job - 144;
  const rsrc_t rw = mk_rsrc(ws, WS_END);
  if (mi < 64) {
    const int m = mi >> 3, oc = mi & 7;
    const char* src = (const char*)((w >> 2) ? wi : wr) + (64 * (4 * m + (w & 3)) + 8 * oc) * 642;
    dma4(lane * 16, src, lds_addr(lds) + w * 5152);
    dma1(4096 + lane * 16, src, lds_addr(lds) + w * 5152 + 4096);
    if (lane == 0) dma1(5120, src, lds_addr(lds) + w * 5152 + 5120);
    vm_bar<0>();
    LDS_AS u16* L = (LDS_AS u16*)lds + (tid & 7) * 321;
#pragma unroll
    for (int it = 0; it < 5; it++) {
      const int c = (tid >> 3) + 64 * it, col = freq_of(c >> 4, c & 15);
      u32x4 o;
#pragma unroll
      for (int e = 0; e < 4; e++) o[e] = (unsigned)L[e * 2576 + col] | ((unsigned)L[(4 + e) * 2576 + col] << 16);
      st128<16>(rw, WS_WF + c * 8192 + (m * 64 + 8 * oc + (tid & 7)) * 16, o);
    }
#pragma unroll
    for (int it = 0; it < 3; it++) st128<16>(rw, WS_RB1 + ((it * 64 + mi) * 512 + tid) * 16, u32x4{EMPTY, EMPTY, EMPTY, EMPTY});
  } else if (mi < 84) {
    const char* src = (const char*)wo + kappa_to_i(32 * (mi - 64) + 8 * (w & 3) + 4 * (w >> 2)) * 1280;
    dma4(lane * 16, src, lds_addr(lds) + w * 5152);
    dma1(4096 + lane * 16, src, lds_addr(lds) + w * 5152 + 4096);
    vm_bar<0>();
#pragma unroll
    for (int i = 0; i < 5; i++) {
      const lptr a = lds + g * 5152 + (r16 >> 2) * 1280 + (5 * w + i) * 32 + (lane & 3) * 8;
      const u32x2 lo = ldtr(a), hi = ldtr(a + 4 * 5152);
      st128<16>(rw, WS_WOP + wo_frag(5 * w + i, mi - 64) + lane * 16, u32x4{lo[0], lo[1], hi[0], hi[1]});
    }
  } else {
    const int i0 = (mi - 84) * 512 + tid;
    unsigned pf0, pf1;
    {
      const unsigned* cm = CODE_ADDR(SYM_MAIN);
      const unsigned* p = (const unsigned*)wqkv + min(14336 + i0 - 4096, 19199) * 32;
      if (mi < 92) p = tid < 80 ? (const unsigned*)&g_ct + tid * 32 : cm + min(tid - 80, CODE_LINES - 1) * 32;
      asm volatile("global_load_dword %0, %1, off" : "=v"(pf0) : "v"(p) : "memory");
      asm volatile("global_load_dword %0, %1, off" : "=v"(pf1) : "v"((const unsigned*)wqkv + i0 * 32) : "memory");
    }
    nap<K1DELAY>();
    if (i0 < 6272) st128<16>(rw, WS_FL + i0 * 16, u32x4{0u, 0u, 0u, 0u});
#pragma unroll
    for (int it = 0; it < 6; it++) { const int i = 98304 + i0 + it * 14336; if (i < (int)RBSZ / 16) st128<16>(rw, WS_RB1 + i * 16, u32x4{EMPTY, EMPTY, EMPTY, EMPTY}); }
    asm volatile("s_waitcnt vmcnt(0)" : "+v"(pf0), "+v"(pf1));
  }
}

#define PINF(x) asm volatile("" : "+v"(x))
DI void put_scale(lptr lds, int q, int tid, const u32x2* sp) {
  f32x4 r = {640.f * 1e-5f, 640.f * 1e-5f, 640.f * 1e-5f, 640.f * 1e-5f};
#pragma unroll
  for (int i = 0; i < 8; i++) r += f32x4{bflo(sp[i][0]), bfhi(sp[i][0]), bflo(sp[i][1]), bfhi(sp[i][1])};
#pragma unroll
  for (int e = 0; e < 4; e++) r[e] = __builtin_amdgcn_rsqf(r[e] * (2048.f * 2048.f / 640.f));
  ((LDS_AS f32x4*)(lds + 2 * SLOT32))[q * 256 + tid] = r;
}
template <bool FIRST>
DI void col_phase(rsrc_t rw, lptr lds, int c SARG) {
  int lane = threadIdx.x & 63; PINF(lane);
  const int wv = wave_id(), w = wv & 3, q = wv >> 2, tid = w * 64 + lane, g = lane >> 4, r16 = lane & 15;
  const lptr img = lds + q * SLOT32;
  f32x4 xr[2], xi[2], rs[2]; u32x4 wf[2], xb[2];
  u32x4 c64[2], s64[2], n64[2], c32[2], s32[2], n32[2];
  const u32x4* ct = (const u32x4*)&g_ct + lane;
  auto ld_const = [&]() { c64[0] = ct[(2 * w) * 64]; s64[0] = ct[(2 * w + 1) * 64]; c32[0] = ct[8 * 64]; s32[0] = ct[9 * 64]; };
  auto ld_filter = [&]() {
#pragma unroll
  for (int t = 0; t < 2; t++) wf[t] = gl128<16>(rw, WS_WF + c * 8192 + ((4 * t + g) * 64 + 16 * w + r16) * 16);
  };
  if (FIRST) ld_const();
  SB();
  f32x4 t1c[2], t1s[2], t2c[2], t2s[2];
  {
    const float f1 = (float)(16 * w + r16) * (1.f / 2048.f), b1 = (float)(4 * g), f2 = (float)(16 * w + 4 * g) * (1.f / 2048.f), b2 = (float)r16;
#pragma unroll
    for (int t = 0; t < 2; t++) {
#pragma unroll
      for (int e = 0; e < 4; e++) {
        const float a1 = (b1 + (float)(16 * t + e)) * f1, a2 = (b2 + (float)(16 * t)) * (f2 + (float)e * (1.f / 2048.f));
        t1c[t][e] = __builtin_amdgcn_cosf(a1); t1s[t][e] = __builtin_amdgcn_sinf(-a1);
        t2c[t][e] = __builtin_amdgcn_cosf(a2); t2s[t][e] = __builtin_amdgcn_sinf(a2);
      }
      PINF(t1c[t]); PINF(t1s[t]); PINF(t2c[t]); PINF(t2s[t]);
    }
  }
  int wo_ = r16 * 128 + (((2 * w + (g >> 1)) ^ (r16 >> 1)) * 16) + (g & 1) * 8, ro_ = r16 * 128 + ((g ^ (r16 >> 1)) * 16), ro2 = ro_ ^ 64;
  PINF(wo_); PINF(ro_); PINF(ro2);
  SB();
  if (FIRST) {
    raw_bar();
#pragma unroll
    for (int t = 0; t < 2; t++) { xb[t] = gl128<16>(rw, WS_RB0 + ((tid >> 2) * 641 + ((tid >> 2) & 1) + 2 * c + t) * 64 + (tid & 3) * 16); rs[t] = asf(gl128<16>(rw, WS_RS1 + (1024 * t + 4 * tid) * 4)); }
    ld_filter();
    SB();
#pragma unroll
    for (int t = 0; t < 2; t++) {
      xr[t] = f32x4{bflo(xb[0][2 * t]), bfhi(xb[0][2 * t]), bflo(xb[0][2 * t + 1]), bfhi(xb[0][2 * t + 1])};
      xi[t] = f32x4{bflo(xb[1][2 * t]), bfhi(xb[1][2 * t]), bflo(xb[1][2 * t + 1]), bfhi(xb[1][2 * t + 1])};
    }
  } else {
    MARK(23);
    drain();
    MARK(11);
    nap<YDELAY>();
    u32x2 sp[8];
    const unsigned so = WS_SSQ + (blockIdx.x & 7) * 32768 + (1024 * q + 4 * tid) * 2, xo = WS_R3 + ((tid >> 2) * 642 + 2 * c) * 64 + (tid & 3) * 16;
    bool bs = true, bx = true;
#pragma unroll 1
    for (int spins = 0; spins < (1 << 18); spins++) {
      if (bx) {
#pragma unroll
        for (int t = 0; t < 2; t++) { xr[t] = asf(gl128<16>(rw, xo + 64 * 642 * 64 * t)); xi[t] = asf(gl128<16>(rw, xo + 64 * 642 * 64 * t + 64)); }
      }
      if (bs) {
#pragma unroll
        for (int i = 0; i < 8; i++) sp[i] = gl64<16>(rw, so + i * 4096);
      }
      unsigned m = 0;
#pragma unroll
      for (int t = 0; t < 2; t++) m = umax4(umax4(m, asu(xr[t])), asu(xi[t]));
      bx = __builtin_amdgcn_ballot_w64(m == EMPTY) != 0;
      m = 0;
#pragma unroll
      for (int i = 0; i < 8; i++) m = max(max(m, sp[i][0]), sp[i][1]);
      bs = __builtin_amdgcn_ballot_w64(m == EMPTY) != 0;
      if (!bs && !bx) break;
      nap<YSLEEP>();
    }
    ld_const(); ld_filter();
    SB();
    put_scale(lds, q, tid, sp);
    LDS_AS f32x4* ex = (LDS_AS f32x4*)(lds + 2 * SLOT32) + tid;
    lds_bar();
    rs[0] = ex[0]; rs[1] = ex[256];
  }
  MARK(FIRST ? 9 : 21);
#pragma unroll
  for (int t = 0; t < 2; t++) {
    u32x2 pb = pk4(xi[t] * rs[t]);
    if (c == 0) pb = u32x2{0u, 0u};
    *(LDS_AS u32x2*)(img + wo_ + t * 2048) = pk4(xr[t] * rs[t]);
    *(LDS_AS u32x2*)(img + 4096 + wo_ + t * 2048) = pb;
  }
  lds_bar();
  u32x4 ur[2][2], ui[2][2], br, bi;
  f32x4 ar[2], ai[2];
  const f32x4 z4 = {0.f, 0.f, 0.f, 0.f};
  auto rd_in = [&](int off) {
#pragma unroll
    for (int ks = 0; ks < 2; ks++)
#pragma unroll
      for (int t = 0; t < 2; t++) { const lptr p = img + off + (ks ? ro2 : ro_) + t * 2048; ur[t][ks] = ld128(p); ui[t][ks] = ld128(p + 4096); }
    SB();
  };
  auto cmul = [&](const f32x4* cc, const f32x4* sn) {
    br = pk8(ar[0] * cc[0] - ai[0] * sn[0], ar[1] * cc[1] - ai[1] * sn[1]);
    bi = pk8(ai[0] * cc[0] + ar[0] * sn[0], ai[1] * cc[1] + ar[1] * sn[1]);
  };
  rd_in(0);
  {
    const unsigned par = (lane & 1) ? 0x80008000u : 0u;
    c64[1] = c64[0] ^ par; s64[1] = s64[0] ^ par; n64[0] = s64[0] ^ 0x80008000u; n64[1] = s64[1] ^ 0x80008000u;
    c32[1] = c32[0] ^ 0x80000000u; s32[1] = s32[0] ^ 0x80000000u; n32[0] = s32[0] ^ 0x80008000u; n32[1] = s32[0] ^ 0x00008000u;
    PINF(c64[1]); PINF(s64[1]); PINF(n64[0]); PINF(n64[1]); PINF(c32[1]); PINF(s32[1]); PINF(n32[0]); PINF(n32[1]);
  }
  SB();
#pragma unroll
  for (int ks = 0; ks < 2; ks++) {
#pragma unroll
    for (int t = 0; t < 2; t++) { ar[t] = mfma16(ur[t][ks], c64[ks], ks ? ar[t] : z4); ai[t] = mfma16(ui[t][ks], c64[ks], ks ? ai[t] : z4); }
#pragma unroll
    for (int t = 0; t < 2; t++) { ar[t] = mfma16(ui[t][ks], s64[ks], ar[t]); ai[t] = mfma16(ur[t][ks], n64[ks], ai[t]); }
  }
  const f32x4 fa[2] = {asf(wf[0] << 16), asf(wf[1] << 16)}, fb[2] = {asf(wf[0] & 0xFFFF0000u), asf(wf[1] & 0xFFFF0000u)};
  cmul(t1c, t1s);
#pragma unroll
  for (int t = 0; t < 2; t++) { ar[t] = mfma16(c32[t], br, z4); ai[t] = mfma16(c32[t], bi, z4); }
#pragma unroll
  for (int t = 0; t < 2; t++) { ar[t] = mfma16(s32[t], bi, ar[t]); ai[t] = mfma16(n32[t], br, ai[t]); }
  cmul(fa, fb);
#pragma unroll
  for (int t = 0; t < 2; t++) { ar[t] = mfma16(br, c32[t], z4); ai[t] = mfma16(bi, c32[t], z4); }
#pragma unroll
  for (int t = 0; t < 2; t++) { ar[t] = mfma16(bi, n32[t], ar[t]); ai[t] = mfma16(br, s32[t], ai[t]); }
  cmul(t2c, t2s);
#pragma unroll
  for (int t = 0; t < 2; t++) {
    *(LDS_AS u32x2*)(img + 8192 + wo_ + t * 2048) = u32x2{br[2 * t], br[2 * t + 1]};
    *(LDS_AS u32x2*)(img + 12288 + wo_ + t * 2048) = u32x2{bi[2 * t], bi[2 * t + 1]};
  }
  lds_bar();
  rd_in(8192);
#pragma unroll
  for (int ks = 0; ks < 2; ks++) {
#pragma unroll
    for (int t = 0; t < 2; t++) { ar[t] = mfma16(c64[ks], ur[t][ks], ks ? ar[t] : xr[t]); ai[t] = mfma16(c64[ks], ui[t][ks], ks ? ai[t] : xi[t]); }
#pragma unroll
    for (int t = 0; t < 2; t++) { ar[t] = mfma16(n64[ks], ui[t][ks], ar[t]); ai[t] = mfma16(s64[ks], ur[t][ks], ai[t]); }
  }
  const unsigned ob = (FIRST ? WS_RB1 : WS_RB2) + (2 * c * RBP + 8 * tid) * 2;
  st128<16>(rw, ob, clean(pk8(ar[0], ar[1])));
  st128<16>(rw, ob + RBP * 2, c == 0 ? u32x4{0u, 0u, 0u, 0u} : clean(pk8(ai[0], ai[1])));
}

DI f32x16 mfma32(u32x4 a, u32x4 b, f32x16 c) {
  return __builtin_amdgcn_mfma_f32_32x32x16_bf16(__builtin_bit_cast(bf16x8, a), __builtin_bit_cast(bf16x8, b), c, 0, 0, 0);
}
DI f32x16 mfma32k8(u32x2 a, u32x2 b, f32x16 c) {
  return __builtin_amdgcn_mfma_f32_32x32x8bf16_1k(__builtin_bit_cast(s16x4, a), __builtin_bit_cast(s16x4, b), c, 0, 0, 0);
}
DI f32x16 scores(u32x4 k1, u32x2 k2, u32x4 q1, u32x2 q2) {
  f32x16 z;
#pragma unroll
  for (int i = 0; i < 16; i++) z[i] = 0.f;
  f32x16 s = mfma32(k1, q1, z);
  asm volatile("s_nop 11" : "+v"(s));
  return mfma32k8(k2, q2, s);
}
DI float max16(const f32x16& s) {
  float m = fmaxf(s[0], s[1]);
#pragma unroll
  for (int i = 2; i < 16; i++) m = fmaxf(m, s[i]);
  return m;
}
#define STR_(x) #x
#define STR(x) STR_(x)
#define KOF(t) ((((t)&7)>>1)*11264+((t)&1)*1280)
#define VOF(t) ((((t)&7)>>1)*11264+5120+((t)&1)*1536)
#define EXP(d, s) "v_exp_f32 v" #d ", v" #s "\n\t"
#define CVT(d, s) "v_cvt_pk_bf16_f32 v" #d ", v" #s ", v[" #s "+1]\n\t"
#define STEPA(t) \
  "v_mfma_f32_32x32x16_bf16 v[48:63], v[80:83], v[136:139], v[120:135]\n\t" \
  "ds_read_b128 v[80:83], v100 offset:" STR(KOF(t+1)) "\n\t" \
  EXP(64,32) EXP(65,33) CVT(78,68) CVT(79,70) EXP(66,34) EXP(67,35) \
  "v_mfma_f32_32x32x16_bf16 v[104:119], v[90:93], v[76:79], v[104:119]\n\t" \
  "ds_read_b64_tr_b16 v[90:91], v102 offset:" STR(VOF(t)+768) "\n\t" \
  "ds_read_b64_tr_b16 v[92:93], v102 offset:" STR(VOF(t)+1152) "\n\t" \
  EXP(68,36) EXP(69,37) CVT(72,64) CVT(73,66) EXP(70,38) EXP(71,39) \
  "v_mfma_f32_32x32x8_bf16 v[48:63], v[84:85], v[140:141], v[48:63]\n\t" \
  "ds_read_b64 v[84:85], v101 offset:" STR(KOF(t+1)) "\n\t" \
  EXP(64,40) EXP(65,41) CVT(74,68) CVT(75,70) EXP(66,42) EXP(67,43) \
  "s_waitcnt lgkmcnt(4)\n\t" \
  "v_mfma_f32_32x32x16_bf16 v[0:15], v[86:89], v[72:75], v[0:15]\n\t" \
  EXP(68,44) EXP(69,45) CVT(76,64) CVT(77,66) EXP(70,46) EXP(71,47)
#define STEPB(t) \
  "s_waitcnt lgkmcnt(0)\n\t" \
  "v_mfma_f32_32x32x16_bf16 v[32:47], v[80:83], v[94:97], v[16:31]\n\t" \
  EXP(64,48) EXP(65,49) CVT(78,68) CVT(79,70) EXP(66,50) EXP(67,51) \
  "v_mfma_f32_32x32x16_bf16 v[0:15], v[90:93], v[76:79], v[0:15]\n\t" \
  EXP(68,52) EXP(69,53) CVT(72,64) CVT(73,66) EXP(70,54) EXP(71,55) \
  "v_mfma_f32_32x32x8_bf16 v[32:47], v[84:85], v[98:99], v[32:47]\n\t" \
  EXP(64,56) EXP(65,57) CVT(74,68) CVT(75,70) EXP(66,58) EXP(67,59) \
  "v_mfma_f32_32x32x16_bf16 v[104:119], v[86:89], v[72:75], v[104:119]\n\t" \
  "ds_read_b64_tr_b16 v[86:87], v102 offset:" STR(VOF(t+1)) "\n\t" \
  "ds_read_b64_tr_b16 v[88:89], v102 offset:" STR(VOF(t+1)+384) "\n\t" \
  EXP(68,60) EXP(69,61) CVT(76,64) CVT(77,66) EXP(70,62) EXP(71,63)
#define PR(n) "s_setprio " #n "\n\t"
#define CHUNK(i) PR(1) STEPA(2*(i)) PR(0) STEPB(2*(i)) "s_barrier\n\t" PR(2) STEPA(2*(i)+1) STEPB(2*(i)+1)
#define Z4(a) "v_mov_b64 v[" #a ":" #a "+1], 0\n\tv_mov_b64 v[" #a "+2:" #a "+3], 0\n\t"

#define R4(b) "v[" STR(b) ":" STR(b) "+3]"
#define MM(d, a, b) "v_mfma_f32_16x16x32_bf16 " R4(d) ", " R4(a) ", " R4(b) ", " R4(d) "\n\t"
#define GM(B, m, i) MM(20*m+4*i, B+8+4*i, B+4*m)
#define RT(B, m, hf, k) "ds_read_b64_tr_b16 v[" STR(B+4*m+2*hf) ":" STR(B+4*m+2*hf+1) "], v10" #m " offset:" STR(k*8192+hf*1024) "\n\t"
#define RS(B, i, k) "ds_read_b128 " R4(B+8+4*i) ", v102 offset:" STR(i*2048+k*1024) "\n\t"
#define IF5_4(x) ""
#define IF5_5(x) x
#define GSTEP(N, B, C, k) GM(B,0,0) RT(C,0,0,k) RT(C,0,1,k) GM(B,1,0) RS(C,0,k) RT(C,1,0,k) GM(B,0,1) RT(C,1,1,k) RS(C,1,k) GM(B,1,1) RS(C,2,k) \
  GM(B,0,2) RS(C,3,k) GM(B,1,2) IF5_##N(RS(C,4,k)) GM(B,0,3) GM(B,1,3) IF5_##N(GM(B,0,4) GM(B,1,4)) MM(40,B,B)
#define GEMM_ASM(N) \
  Z4(0) Z4(4) Z4(8) Z4(12) Z4(16) Z4(20) Z4(24) Z4(28) Z4(32) Z4(36) Z4(40) "s_mov_b32 %[c], 0\n\ts_barrier\n\t" \
  RT(44,0,0,0) RT(44,0,1,0) RS(44,0,0) RT(44,1,0,0) RT(44,1,1,0) RS(44,1,0) RS(44,2,0) RS(44,3,0) IF5_##N(RS(44,4,0)) \
  "L_gemm_%=:\n\ts_add_u32 %[c], %[c], 1\n\ts_and_b32 %[t], %[c], 3\n\ts_cselect_b32 %[t], 1, -3\n\ts_mul_i32 %[t], %[t], 36864\n\ts_waitcnt lgkmcnt(0)\n\t" \
  GSTEP(N,44,72,1) \
  "v_add_u32 v100, %[t], v100\n\tv_add_u32 v101, %[t], v101\n\tv_add_u32 v102, %[t], v102\n\ts_waitcnt lgkmcnt(0)\n\ts_barrier\n\t" \
  GSTEP(N,72,44,0) \
  "s_cmp_lg_u32 %[c], 9\n\ts_cbranch_scc1 L_gemm_%=\n\ts_waitcnt lgkmcnt(0)\n\t" \
  GSTEP(N,44,72,1) "s_waitcnt lgkmcnt(0)\n\t" \
  MM(40,72,72) GM(72,0,0) GM(72,1,0) GM(72,0,1) GM(72,1,1) "s_nop 4\n\t" \
  "v_cndmask_b32 v44, v40, v41, %[m1]\n\tv_cndmask_b32 v45, v42, v43, %[m1]\n\tv_mov_b32 v46, 0x3727c5ac\n\tv_cndmask_b32 v44, v44, v45, %[m2]\n\t" \
  "v_fmac_f32 v46, 0x3acccccd, v44\n\tv_cmp_u_f32 vcc, v44, v44\n\tv_rsq_f32 v46, v46\n\ts_cbranch_vccz L_gok_%=\n\tds_write_b32 v106, v106\n\t" \
  "L_gok_%=:\n\tds_write_b32 v103, v46\n\ts_waitcnt lgkmcnt(0)\n\ts_barrier\n\t" \
  "ds_read_b32 v44, v104\n\tds_read_b32 v45, v105\n\tds_read_b32 v46, v106\n\t" \
  GM(72,0,2) GM(72,1,2) GM(72,0,3) GM(72,1,3) IF5_##N(GM(72,0,4) GM(72,1,4)) "s_waitcnt lgkmcnt(0)"
#define GEMM_OPS \
  : "={v[0:3]}"(acc[0][0]), "={v[4:7]}"(acc[0][1]), "={v[8:11]}"(acc[0][2]), "={v[12:15]}"(acc[0][3]), "={v[16:19]}"(acc[0][4]), \
    "={v[20:23]}"(acc[1][0]), "={v[24:27]}"(acc[1][1]), "={v[28:31]}"(acc[1][2]), "={v[32:35]}"(acc[1][3]), "={v[36:39]}"(acc[1][4]), \
    "={v44}"(rs[0]), "={v45}"(rs[1]), "={v46}"(vote), "+{v100}"(am0), "+{v101}"(am1), "+{v102}"(as), [c] "=&s"(c_), [t] "=&s"(t_) \
  : "{v103}"(a_w), "{v104}"(a_r[0]), "{v105}"(a_r[1]), "{v106}"(a_v), [m1] "s"(0xAAAAAAAAAAAAAAAAull), [m2] "s"(0xCCCCCCCCCCCCCCCCull) \
  : "memory", "scc", "vcc", "v40", "v41", "v42", "v43", "v47", "v48", "v49", "v50", "v51", "v52", "v53", "v54", "v55", "v56", "v57", "v58", "v59", "v60", \
    "v61", "v62", "v63", "v64", "v65", "v66", "v67", "v68", "v69", "v70", "v71", "v72", "v73", "v74", "v75", "v76", "v77", "v78", "v79", "v80", "v81", \
    "v82", "v83", "v84", "v85", "v86", "v87", "v88", "v89", "v90", "v91", "v92", "v93", "v94", "v95", "v96", "v97", "v98", "v99"

constexpr int HR = 4 * KVC;
constexpr int L_A = 2 * HR;
constexpr int L_EX = 15360;
constexpr int L_MISC = L_A + 70 * 1024;
constexpr int LDS_FUSED = L_MISC + 2048;

DI bool poll16(rsrc_t rw, unsigned off, unsigned need) {
  const int lane = threadIdx.x & 63;
#pragma unroll 1
  for (int spins = 0; spins < (1 << 20); spins++) {
    const unsigned v = gl32<16>(rw, off + (lane & 15) * 4);
    const unsigned got = (unsigned)__builtin_amdgcn_ballot_w64(v != 0) & 0xFFFFu;
    if ((got & need) == need) return true;
    nap<1>();
  }
  return false;
}

DI void poll_aux(rsrc_t rw, unsigned off) {
#pragma unroll 1
  for (int spins = 0; spins < (1 << 20); spins++) {
    const u32x4 v = gl128<16>(rw, off + (threadIdx.x & 63) * 16);
    if (!__builtin_amdgcn_ballot_w64(min(min(v[0], v[1]), min(v[2], v[3])) == 0u)) break;
    nap<4>();
  }
}
DI void poll_tag(rsrc_t rw, u32x2 tag, unsigned long long need) {
  const unsigned o = WS_F0 + (blockIdx.x & 7) * 2048 + (threadIdx.x & 63) * 32;
#pragma unroll 1
  for (int spins = 0; spins < (1 << 20); spins++) {
    const u32x4 a = gl128<16>(rw, o), b = gl128<16>(rw, o + 16);
    const unsigned d = (a[0] ^ tag[0]) | (a[1] ^ tag[1]) | (a[2] ^ tag[0]) | (a[3] ^ tag[1]) | (b[0] ^ tag[0]) | (b[1] ^ tag[1]) | (b[2] ^ tag[0]) | (b[3] ^ tag[1]);
    if (!(__builtin_amdgcn_ballot_w64(d != 0u) & need)) break;
    nap<TSLEEP>();
  }
}
#define LAYER_VARS \
  extern __shared__ __attribute__((aligned(16))) char smem[]; \
  const lptr lds = (lptr)smem; \
  const rsrc_t rw = mk_rsrc(ws, WS_END); \
  int lane = threadIdx.x & 63; asm volatile("" : "+v"(lane)); \
  const int w = wave_id(), g = lane >> 4, r16 = lane & 15; \
  const int xcd = blockIdx.x & 7, bi = blockIdx.x >> 3, u = 8 * (xcd & 1) + (bi & 7), qt = 4 * (xcd >> 1) + (bi >> 3); \
  const int hf = u & 1, ct = u >> 1;     \
  constexpr unsigned FKV = WS_FL + (FINAL ? 32768 : 0), KV = WS_KV + (FINAL ? LAYER2 : 0), OB = WS_O + (FINAL ? LAYER2 : 0); \
  constexpr unsigned AUX = WS_AUX + (FINAL ? 1024 : 0), RB = FINAL ? WS_RB2 : WS_RB1; \
  constexpr int MK = FINAL ? 12 : 0; (void)MK; (void)hf; (void)AUX; (void)RB; (void)FKV; \
  LDS_AS unsigned* misc = (LDS_AS unsigned*)(lds + L_MISC + 1024) + (FINAL ? 8 : 0);

template <bool FINAL>
DI void loader(char* ws, u32x2 tag SARG) {
  LAYER_VARS
  {
    const int lw = w - 8;
    {
      unsigned vm[4];
#pragma unroll
      for (int r = 0; r < 4; r++) vm[r] = (4 * (4 * lw + r) + g) * (RBP * 2) + 256 * qt + (((r16 >> 1) ^ (g | ((r & 2) << 1))) * 32) + (r16 & 1) * 16 + 3072 - 1024 * r;
      const unsigned l0 = lds_addr(lds) + lw * 4096, v0 = 2 * lw * 20480 + lane * 16, v2 = ((8 + (lw >> 1)) * 20 + (lw & 1)) * 1024 + lane * 16;
      constexpr int CNT = FINAL ? 9 : 8;
      auto lds_w = [&](int s) {
        const char* sb = ws + WS_WQ + u * 204800 + s * 2048;
        dma_s(v0, v0 + 20480 - 2048, sb, l0 + (s & 3) * SLOT32 + 16384 + (lw >> 1) * 2048);
        if (FINAL) dma1_sc1(v2, sb, lds_addr(lds) + (s & 3) * SLOT32 + 24576 + (lw >> 1) * 10240 + (lw & 1) * 1024);
      };
      auto lds_a = [&](int s) {
        dma_a(vm, ws + RB - 3072 + s * (64 * RBP * 2), l0 + (s & 3) * SLOT32); };
#pragma unroll 1
      for (int att = 0;; att++) {
        if (att == 0) { raw_bar(); if (FINAL) raw_bar(); else poll_tag(rw, tag, 0x7FFF0000ull); }
        lds_w(0); lds_w(1);
        if (att == 0) raw_bar();
        lds_w(2); lds_w(3);
        auto probe = [&](int s0, int n) {
          const int pl = min(lane, n - 1);
          const unsigned po = RB + ((64 * (s0 + (pl >> 3)) + 16 * lw + 2 * (pl & 7) + 1) * RBP + 128 * qt + 126) * 2;
#pragma unroll 1
          for (int spins = 0; spins < (1 << 20); spins++) {
            if (!__builtin_amdgcn_ballot_w64(gl32<16>(rw, po) == EMPTY)) break;
            nap<XSLEEP>();
          }
        };
        if (att == 0) {
          raw_bar();
          if (blockIdx.x >= 64) nap<XDELAY>();
          MARK(MK + 7);
          probe(0, 48);
        }
        drain();
        MARK(MK + 8);
        unsigned nd = 0;
        if (att == 0 && lane == 0) nd = __hip_atomic_fetch_add(misc + 4, 1u, __ATOMIC_RELAXED, __HIP_MEMORY_SCOPE_WORKGROUP);
        if (nd == 11) st32<16>(rw, AUX + blockIdx.x * 4, 1u);
#pragma unroll 1
        for (int s = 0; s < 4; s++) lds_a(s);
        vm_bar<12>(); vm_bar<8>();
        if (att == 0) probe(6, 32);
        lds_a(4); lds_w(4);
        vm_bar<4 + CNT>();
#pragma unroll 1
        for (int s = 5; s < 10; s++) { lds_a(s); lds_w(s); vm_bar<2 * CNT>(); }
        vm_bar<CNT>();
        poll_aux(rw, AUX);
        if (!FINAL) { poll_tag(rw, tag, ~0ull); if (blockIdx.x == 0 && threadIdx.x == 512) st64<16>(rw, WS_EP, tag); }
        vm_bar<0>();
        raw_bar();
        if (XNL || att || !*(volatile LDS_AS unsigned*)(misc + 2)) break;
      }
    }
    MARK(MK + 0);
    const int hh = lw >> 1, hp = lw & 1;
    const unsigned kvv = KV + (2 * u + hh) * 16 * KVC + hp * 5120 + lane * 16;
    const unsigned lkv = lds_addr(lds) + hh * HR + hp * 5120;
    const unsigned wv = (FINAL ? WS_WOP : WS_WOF) + (ct * 2 + hh) * 51200 + lane * 16;
    const unsigned myfl = FKV + (u * 16 + qt) * 128;
    raw_bar();
    MARK(MK + 1);
    {
      u32x4 v[6];
#pragma unroll
      for (int i = 0; i < 6; i++) v[i] = ld128(lds + hh * HR + hp * 5120 + lane * 16 + i * 1024);
#pragma unroll
      for (int i = 0; i < 6; i++) st128<16>(rw, kvv + qt * KVC + i * 1024, clean(v[i]));
    }
    drain();
    unsigned cnt = 0;
    if (lane == 0) cnt = __hip_atomic_fetch_add(misc, 1u, __ATOMIC_RELAXED, __HIP_MEMORY_SCOPE_WORKGROUP);
    if (__builtin_amdgcn_readfirstlane(cnt) == 3 && lane < 16) st32<16>(rw, FKV + (u * 16 + lane) * 128 + qt * 4, 1u);
    MARK(MK + 2);
    auto ldkv = [&](int seq) {
      const int ch = seq == 0 ? qt : seq < 13 ? ((qt & 12) + 3 + seq) & 15 : (qt & 12) | ((qt + seq) & 3);
      dma6_sc1(kvv + ch * KVC, ws, lkv + (seq & 3) * KVC);
    };
    auto fetch = [&](int seq) {
      const unsigned src = kvv + ((((qt & 12) + 3 + seq) & 15)) * KVC;
      u32x4 v[6];
#pragma unroll 1
      for (int spins = 0; spins < (1 << 18); spins++) {
#pragma unroll
        for (int i = 0; i < 6; i++) v[i] = gl128<16>(rw, src + i * 1024);
        unsigned m = 0;
#pragma unroll
        for (int i = 0; i < 6; i++) m = umax4(m, v[i]);
        if (!__builtin_amdgcn_ballot_w64(m == EMPTY)) break;
        nap<2>();
      }
#pragma unroll
      for (int i = 0; i < 6; i++) *(LDS_AS u32x4*)(lds + hh * HR + hp * 5120 + seq * KVC + lane * 16 + i * 1024) = v[i];
    };
#pragma unroll 1
    for (int attempt = 0;; attempt++) {
      int c = -2;
      if (attempt) { ldkv(0); vm_bar<0>(); }
      else {
        fetch(1); MARK(MK + 3); lds_bar();
        MARK(MK + 4);
        fetch(2); poll16(rw, myfl, 12u << (((qt & 12) + 4) & 15)); ldkv(3); MARK(MK + 5); lds_bar();
        c = 1;
      }
#pragma unroll 1
      for (; c < 13; c++) {
        if (c == 2 && !attempt) poll16(rw, myfl, 0xFFFFu);
        if (!FINAL && !attempt) {
          if (c == 10) {
            unsigned n = 0;
            if (lane == 0) n = __hip_atomic_fetch_add(misc + 3, 1u, __ATOMIC_RELAXED, __HIP_MEMORY_SCOPE_WORKGROUP);
            if (__builtin_amdgcn_readfirstlane(n) == 3 && lane < 32) st32<16>(rw, WS_FL + 65536 + (lane < 16 ? (u * 16 + lane) * 128 + qt * 4 : (r16 * 16 + qt) * 128 + 64 + u * 4), 1u);
          }
        }
        if (c >= 2) { const int e = min(4 * (c - 2) + 2 * hp, 33); dma2(wv + e * 1024, ws, lds_addr(lds) + L_A + (hh * 35 + e) * 1024); }
        ldkv(c + 3);
        if (c == 0) vm_bar<12>();
        if (!FINAL && !attempt && c >= 2 && c < 9) {
          const u32x4 e = {EMPTY, EMPTY, EMPTY, EMPTY};
          const unsigned o = OB + LAYER2 + (((c - 2) * 256 + blockIdx.x) * 4 + lw) * 2048 + lane * 16;
          st128<16>(rw, o, e); st128<16>(rw, o + 1024, e);
          st128<16>(rw, ((qt & 3) < 2 && c < 8) ? kvv + LAYER2 + qt * KVC + (c - 2) * 1024 : WS_END, e);
          st128<16>(rw, c < 5 ? OB + ((4 * qt + lw) * 32 + 2 * u) * 1280 + min((c - 2) * 1024, 1536) + lane * 16 : WS_END, e);
          if (c == 2) vm_bar<12>(); else vm_bar<16>();
        } else if (c >= 2) vm_bar<8>(); else if (c >= 0) vm_bar<6>();
      }
      if (!FINAL && !attempt) poll16(rw, myfl + 65536 + hp * 64, 0xFFFFu);
      vm_bar<0>();
      dma_s(wv + (35 + 7 * hp) * 1024, wv + (35 + 7 * hp) * 1024, ws, lds_addr(lds) + hh * HR + 7 * hp * 1024);
      dma_s(wv + (39 + 7 * hp) * 1024, wv + (39 + 7 * hp) * 1024, ws, lds_addr(lds) + hh * HR + (4 + 7 * hp) * 1024);
      raw_bar();
      vm_bar<0>();
      if (attempt || !*(volatile LDS_AS unsigned*)(misc + 1)) break;
      raw_bar();
    }
    if (!FINAL) { raw_bar(); raw_bar(); }
  }
}

template <bool FINAL>
DI void compute(char* ws, u16* out SARG) {
  LAYER_VARS
  const int wm = w & 3, wn = w >> 2, h = lane >> 5;
  if (blockIdx.x < 64 || wn == 0) col_phase<!FINAL>(rw, lds, blockIdx.x < 64 ? 192 + 2 * blockIdx.x + wn : blockIdx.x - 64 SPASS);
  else if (FINAL) {
    drain();
    nap<YIDLE>();
    u32x2 sp[8];
#pragma unroll 1
    for (int spins = 0; spins < (1 << 18); spins++) {
      unsigned m = 0;
#pragma unroll
      for (int i = 0; i < 8; i++) sp[i] = gl64<16>(rw, WS_SSQ + (blockIdx.x & 7) * 32768 + (1024 + 4 * (wm * 64 + lane)) * 2 + i * 4096);
#pragma unroll
      for (int i = 0; i < 8; i++) m = max(max(m, sp[i][0]), sp[i][1]);
      if (!__builtin_amdgcn_ballot_w64(m == EMPTY)) break;
      nap<YSLEEP>();
    }
    put_scale(lds, 1, wm * 64 + lane, sp);
    lds_bar(); raw_bar(); raw_bar();
  } else {
    raw_bar(); raw_bar(); raw_bar();
  }
  MARK(MK + 10);
  lds_bar();
  {
    drain();
    unsigned n = 0;
    if (lane == 0) n = __hip_atomic_fetch_add(misc + 4, 1u, __ATOMIC_RELAXED, __HIP_MEMORY_SCOPE_WORKGROUP);
    if (n == 11) st32<16>(rw, AUX + blockIdx.x * 4, 1u);
  }

  {
    constexpr int NT = FINAL ? 5 : 4;
    f32x4 acc[2][5]; float rs[2];
    unsigned mo[2], a0[2], a1[2], a2[2], a_r[2];
#pragma unroll
    for (int m = 0; m < 2; m++) {
      const int q = r16 >> 2, pr = 16 * (m ^ wn) + r16;
      mo[m] = lds_addr(lds) + (8 * g + q) * 256 + (((2 * wm + (m ^ wn)) ^ (q | ((g & 1) << 2))) * 32) + (lane & 3) * 8;
      const unsigned qb = L_A + (wn * 4 + wm) * 1280, kb = wn * HR + wm * 1280, vr = wn * HR + 5120 + (32 * wm + pr) * 48 + (g == 3 ? 32 : g * 16);
      a0[m] = ((g >> 1) ? kb : qb) + (g & 1) * 512 + pr * 16;
      a1[m] = g == 2 ? qb + 1024 + pr * 8 : vr; a2[m] = g == 2 ? kb + 1024 + pr * 8 : vr + 8;
      a_r[m] = lds_addr(lds) + L_MISC + (32 * wm + pr) * 4;
      asm volatile("" : "+v"(a0[m]), "+v"(a1[m]), "+v"(a2[m]));
    }
    const unsigned a_w = a_r[0] + (g == (r16 >> 2) ? 0 : 512), a_v = lds_addr((lptr)(misc + 2));
#pragma unroll 1
    for (int att = 0;; att++) {
      unsigned am0 = mo[0], am1 = mo[1], as = lds_addr(lds) + 16384 + wn * 10240 + lane * 16, vote; int c_, t_;
      if (FINAL) asm volatile(GEMM_ASM(5) GEMM_OPS); else asm volatile(GEMM_ASM(4) GEMM_OPS);
      if (XNL || att || !__builtin_amdgcn_readfirstlane(vote)) break;
    }
    SB();
    MARK(MK + 0);
#pragma unroll
    for (int m = 0; m < 2; m++) *(LDS_AS u32x4*)(lds + a0[m]) = pk8(acc[m][0] * rs[m], acc[m][1] * rs[m]);
    asm volatile("s_nop 1" : "+v"(acc[0][2]), "+v"(acc[1][2]), "+v"(acc[0][3]), "+v"(acc[1][3]), "+v"(acc[0][NT - 1]), "+v"(acc[1][NT - 1]) :: "memory");
#pragma unroll
    for (int m = 0; m < 2; m++) {
      u32x2 hi = pk4(acc[m][3] * rs[m]);
      if (g == 3) hi = u32x2{0x3F80u, 0u};
      *(LDS_AS u32x2*)(lds + a1[m]) = pk4(acc[m][2] * rs[m]);
      *(LDS_AS u32x2*)(lds + a2[m]) = hi;
    }
    lds_bar();
    if (FINAL) {
      const unsigned x4 = WS_X4 + (((u * 16 + qt) * 4 + wm) * 2 + wn) * 1280;
      const u32x2 p0 = pk4(acc[0][NT - 1]), p1 = pk4(acc[1][NT - 1]), q0 = pk4(acc[0][3]), q1 = pk4(acc[1][3]);
      st128<16>(rw, x4 + lane * 16, clean(u32x4{p0[0], p0[1], p1[0], p1[1]}));
      if (g == 3) st128<16>(rw, x4 + 1024 + r16 * 16, clean(u32x4{q0[0], q0[1], q1[0], q1[1]}));
    }
  }
  MARK(MK + 1);
  const int head = 2 * u + wn, kh = w & 1;
  const lptr ring = lds + wn * HR;
  const int pblk = 128 * qt + 64 * hf + 32 * (wm >> 1);
  const unsigned r3t = WS_R3 + (64 * (wm & 1) + (pblk >> 5)) * (642 * 64);
  u32x2 x4v[3]; float dc = 0.f;
  unsigned os = OB + ((4 * qt + wm) * 32 + head) * 1280 + (((lane << 2) & 16) + ((lane >> 1) & 12) + (lane & 3)) * 8;
  unsigned o1 = OB + ((pblk >> 5) * 32 + 16 * wn) * 1280 + (g >> 1) * 1280 + (g & 1) * 512 + (wm & 1) * 256 + r16 * 16;
  unsigned o2 = OB + ((pblk >> 5) * 32 + 16 * wn) * 1280 + 2 * g * 1280 + 1024 + (wm & 1) * 128 + r16 * 8;
  u32x4 o3 = {0u, 0u, 0u, 0u};
  if (!FINAL) {
#pragma unroll
    for (int i = 0; i < 3; i++) {
      const int j = 80 * ct + 48 * wn + 16 * (wn ? (i & 1) : i) + r16;
      o3[i] = (j == 1 ? WS_RB0 + ((pblk >> 5) * 641 + ((pblk >> 5) & 1) + 1) * 64 : WS_RB1 + (j * RBP + pblk) * 2) + 16 * g + 8 * (wm & 1);
    }
  } else {
    o3[3] = r3t + 64 + r16 * 4;
#pragma unroll
    for (int i = 0; i < 3; i++) {
      const int cg = 12 * wn + 4 * ((i == 2 && wn) ? 0 : i) + g, pw = cg / 5, gq = cg - 5 * pw;
      o3[i] = WS_X4 + ((((2 * ct + (pw >> 1)) * 16 + qt) * 4 + 2 * hf + (wm >> 1)) * 2 + (pw & 1)) * 1280 + (gq * 16 + (trow(wm & 1, r16) & 15)) * 16 + ((((r16 >> 3) ^ pw) & 1) * 8);
    }
  }
  asm volatile("" : "+v"(os), "+v"(o1), "+v"(o2), "+v"(o3));
  f32x4 r[3];
  {
    const lptr qpa = lds + L_A + (wn * 4 + wm) * 1280, qpb = lds + L_A + (wn * 4 + (wm ^ 1)) * 1280;
    const u32x4 qa1 = ld128(qpa + lane * 16), qb1 = ld128(qpb + lane * 16);
    u32x2 qa2 = ld64(qpa + 1024 + (lane & 31) * 8), qb2 = ld64(qpb + 1024 + (lane & 31) * 8);
    if (h) { qa2 = u32x2{0u, 0u}; qb2 = qa2; }
    const unsigned ka = lds_addr(ring) + kh * 2560 + lane * 16, kb = lds_addr(ring) + kh * 2560 + 1024 + (lane & 31) * 8;
    const unsigned va = lds_addr(ring) + kh * 3072 + (4 * h + (r16 >> 2)) * 48 + ((g & 1) ? ((lane & 3) ? 40 : 32) : (lane & 3) * 8);
    float ma, mb;
    {
      const u32x4 k1 = ld128(ring + lane * 16); const u32x2 k2 = ld64(ring + 1024 + (lane & 31) * 8);
      ma = max16(scores(k1, k2, qa1, qa2)); mb = max16(scores(k1, k2, qb1, qb2));
    }
    for (int attempt = 0;; attempt++) {
      ma = fmaxf(ma, __shfl_xor(ma, 32)); mb = fmaxf(mb, __shfl_xor(mb, 32));
      f32x16 cia, cib, oa, ob;
#pragma unroll
      for (int i = 0; i < 16; i++) { cia[i] = -ma; cib[i] = -mb; }
      int cnt = 4;
      MARK(MK + 2);
      asm volatile(
          "ds_read_b128 v[80:83], v100\n\t"
          "ds_read_b64 v[84:85], v101\n\t"
          "ds_read_b64_tr_b16 v[86:87], v102 offset:5120\n\t"
          "ds_read_b64_tr_b16 v[88:89], v102 offset:5504\n\t"
          Z4(0) Z4(68) Z4(76) Z4(90)
          "s_waitcnt lgkmcnt(0)\n\t"
          "v_mfma_f32_32x32x16_bf16 v[32:47], v[80:83], v[94:97], v[16:31]\n\t"
          Z4(4) Z4(8) Z4(12) Z4(104) Z4(108) Z4(112) Z4(116)
          "v_mfma_f32_32x32x8_bf16 v[32:47], v[84:85], v[98:99], v[32:47]\n\ts_nop 9\n\t"
          "L_attn_%=:\n\t"
          CHUNK(0) CHUNK(1) CHUNK(2) CHUNK(3)
          "s_sub_u32 %[cnt], %[cnt], 1\n\ts_cmp_lg_u32 %[cnt], 0\n\ts_cbranch_scc1 L_attn_%=\n\t"
          "s_setprio 0\n\t" CVT(78,68) CVT(79,70) "s_waitcnt lgkmcnt(0)\n\ts_nop 1\n\t"
          "v_mfma_f32_32x32x16_bf16 v[104:119], v[90:93], v[76:79], v[104:119]\n\ts_nop 15\n\t"
          : "={v[0:15]}"(oa), "={v[104:119]}"(ob), [cnt] "+s"(cnt)
          : "{v[16:31]}"(cia), "{v[120:135]}"(cib), "{v[94:97]}"(qa1), "{v[98:99]}"(qa2), "{v[136:139]}"(qb1), "{v[140:141]}"(qb2),
            "{v100}"(ka), "{v101}"(kb), "{v102}"(va)
          : "memory", "scc", "v32", "v33", "v34", "v35", "v36", "v37", "v38", "v39", "v40", "v41", "v42", "v43", "v44", "v45", "v46", "v47",
            "v48", "v49", "v50", "v51", "v52", "v53", "v54", "v55", "v56", "v57", "v58", "v59", "v60", "v61", "v62", "v63", "v64", "v65",
            "v66", "v67", "v68", "v69", "v70", "v71", "v72", "v73", "v74", "v75", "v76", "v77", "v78", "v79", "v80", "v81", "v82", "v83",
            "v84", "v85", "v86", "v87", "v88", "v89", "v90", "v91", "v92", "v93");
      MARK(MK + 3);
      LDS_AS f32x4* ex = (LDS_AS f32x4*)(ring + L_EX + wm * 3072) + lane;
#pragma unroll
      for (int i = 0; i < 3; i++) ex[i * 64] = f32x4{ob[4 * i], ob[4 * i + 1], ob[4 * i + 2], ob[4 * i + 3]};
      f32x4 cs = {0.f, 0.f, 0.f, 0.f};
#pragma unroll
      for (int i = 0; i < 3; i++) cs += f32x4{oa[4 * i], oa[4 * i + 1], oa[4 * i + 2], oa[4 * i + 3]} + f32x4{ob[4 * i], ob[4 * i + 1], ob[4 * i + 2], ob[4 * i + 3]};
      if (!(fabsf(cs[0] + cs[1] + cs[2] + cs[3]) < INFINITY) || (h && !(oa[8] + ob[8] < 0x1p60f))) misc[1] = 1u;
      lds_bar();
      const unsigned vote = *(volatile LDS_AS unsigned*)(misc + 1);
      LDS_AS f32x4* ey = (LDS_AS f32x4*)(ring + L_EX + (wm ^ 1) * 3072) + lane;
#pragma unroll
      for (int i = 0; i < 3; i++) r[i] = ey[i * 64] + f32x4{oa[4 * i], oa[4 * i + 1], oa[4 * i + 2], oa[4 * i + 3]};
      if (attempt || !vote) break;
      lds_bar();
      ma = -INFINITY; mb = -INFINITY;
      for (int t = 0; t < 64; t++) {
        const unsigned kb_ = KV + (head * 16 + (t >> 2)) * KVC + (t & 3) * 1280;
        const u32x4 k1 = gl128<16>(rw, kb_ + lane * 16); const u32x2 k2 = gl64<16>(rw, kb_ + 1024 + (lane & 31) * 8);
        ma = fmaxf(ma, max16(scores(k1, k2, qa1, qa2))); mb = fmaxf(mb, max16(scores(k1, k2, qb1, qb2)));
      }
      raw_bar();
    }
  }
  {
    const unsigned lb = __float_as_uint(r[2][0]);
    const float il = __builtin_amdgcn_rcpf(__uint_as_float(__builtin_amdgcn_permlane32_swap(lb, lb, false, false)[1]));
    const unsigned sl8 = os & 255;
    st128<16>(rw, os + sl8 + h * 512, clean(pk8(r[0] * il, r[1] * il)));
    const u32x4 t = clean(pk8(r[2] * il, r[2] * il));
    st64<16>(rw, h ? WS_END : os + 1024, u32x2{t[0], t[1]});
  }
  MARK(MK + 4);
  const lptr wa = lds + L_A + wn * 35 * 1024 + lane * 16, wb = ring + lane * 16;
  constexpr int NPRE = 15;
  u32x4 bf[50];
  f32x4 acc[5] = {};
#pragma unroll
  for (int j = 0; j < NPRE; j++) bf[j] = ld128(wa + j * 1024);
#pragma unroll
  for (int j = 0; j < 5; j++) asm volatile("" : "+v"(acc[j]));
  if (FINAL) dc = __uint_as_float(gl32<16>(rw, o3[3]));
#pragma unroll 1
  for (int spins = 0; spins < (1 << 18); spins++) {
#pragma unroll
    for (int i = 0; i < 3; i++) x4v[i] = gl64<16>(rw, o3[i]);
    if (!FINAL || !__builtin_amdgcn_ballot_w64(umax4(max(x4v[0][0], x4v[0][1]), u32x4{x4v[1][0], x4v[1][1], x4v[2][0], x4v[2][1]}) == EMPTY)) break;
    nap<8>();
  }
  drain();
  nap<ODELAY>();
  MARK(MK + 5);
  {
    u32x4 af[10];
    auto ldf = [&](int i) {
      if (i < 8) af[i] = __builtin_amdgcn_raw_buffer_load_b128(rw, o1, i * 2 * 1280, 16);
      else { const u32x2 a = __builtin_amdgcn_raw_buffer_load_b64(rw, o2, (i - 8) * 8 * 1280, 16), b = __builtin_amdgcn_raw_buffer_load_b64(rw, o2, ((i - 8) * 8 + 1) * 1280, 16);
             af[i] = u32x4{a[0], a[1], b[0], b[1]}; }
    };
#pragma unroll
    for (int i = 0; i < 10; i++) ldf(i);
#pragma unroll 1
    for (int spins = 0; spins < (1 << 18); spins++) {
      unsigned m = 0;
#pragma unroll
      for (int i = 0; i < 10; i++) m = umax4(m, af[i]);
      if (!__builtin_amdgcn_ballot_w64(m == EMPTY)) break;
      nap<RSLEEP>();
#pragma unroll
      for (int i = 0; i < 10; i++) if (__builtin_amdgcn_ballot_w64(umax4(0, af[i]) == EMPTY)) ldf(i);
    }
    MARK(MK + 8);
#pragma unroll
    for (int i = 0; i < 10; i++) asm volatile("" : "+v"(af[i]));
    asm volatile("s_nop 1" : "+v"(acc[0]), "+v"(acc[1]), "+v"(acc[2]), "+v"(acc[3]), "+v"(acc[4]));
    SB();
#pragma unroll
    for (int i = 0; i < 10; i++) {
#pragma unroll
      for (int a = 0; a < 5; a++) {
        if (FINAL) mfma16v(acc[a], bf[5 * i + a], af[i]); else mfma16v(acc[a], af[i], bf[5 * i + a]);
        const int j = 5 * i + a + NPRE; if (j < 50) bf[j] = ld128(j < 35 ? wa + j * 1024 : wb + (j - 35) * 1024);
        SB();
      }
    }
  }
  asm volatile("s_nop 7\n\ts_nop 1" : "+v"(acc[0]), "+v"(acc[1]), "+v"(acc[2]), "+v"(acc[3]), "+v"(acc[4]) :: "memory");
  MARK(MK + 6);
  {
    LDS_AS f32x4* ex = (LDS_AS f32x4*)(ring + L_EX + 12288 + wm * 3072) + lane;
    LDS_AS f32x4* ey = (LDS_AS f32x4*)(lds + (wn ^ 1) * HR + L_EX + 12288 + wm * 3072) + lane + 64 * wn;
    ex[0] = acc[2]; ex[64] = acc[3]; ex[128] = acc[4];
    dc *= 0.039528470752104741f;
#pragma unroll
    for (int i = 0; i < 3; i++) { acc[i] += f32x4{bflo(x4v[i][0]), bfhi(x4v[i][0]), bflo(x4v[i][1]), bfhi(x4v[i][1])}; if (FINAL) acc[i] += dc; }
    lds_bar();
#pragma unroll
    for (int i = 0; i < 3; i++) acc[i] += ey[64 * i];
  }
  if (FINAL) {
    const rsrc_t ro = mk_rsrc(out, 2048 * 640 * 2);
    const unsigned op = (rowof(pblk + trow(wm & 1, r16)) * 640 + 80 * ct + 48 * wn + 4 * g) * 2;
#pragma unroll
    for (int i = 0; i < 3; i++)
      if (i < 2 || wn == 0) st64<OUTAUX>(ro, op + 32 * i, pk4(acc[i]));
  } else {
    f32x4 sq = {0.f, 0.f, 0.f, 0.f};
#pragma unroll
    for (int i = 0; i < 3; i++)
      if (i < 2 || wn == 0) {
        const int j = 80 * ct + 48 * wn + 16 * i + r16;
        st128<16>(rw, r3t + j * 64 + g * 16, clean(asu(acc[i])));
        if (j != 1) sq += acc[i] * acc[i];
      }
#pragma unroll
    for (int e = 0; e < 4; e++) sq[e] = row16_sum(sq[e]);
    LDS_AS float* sl = (LDS_AS float*)(lds + L_MISC);
    if (r16 == 0) *(LDS_AS f32x4*)(sl + wn * 64 + 32 * (wm & 1) + 16 * (wm >> 1) + 4 * g) = sq;
    lds_bar();
    if (w == 0) {
      LDS_AS f32x4* s4 = (LDS_AS f32x4*)sl + 2 * (lane & 7);
      st128<16>(rw, WS_SSQ + (lane >> 3) * 32768 + (ct * 2048 + 1024 * ((lane >> 2) & 1) + 64 * qt + 32 * hf + 8 * (lane & 3)) * 2, clean(pk8(s4[0] + s4[16], s4[1] + s4[17])));
    }
  }
  MARK(MK + 7);
}

__global__ void __launch_bounds__(768) k_main(const u16* x, const u16* wr, const u16* wi, const u16* wqkv, const u16* wo, char* ws, u16* out) {
  STAMP(1);
  const unsigned long long tm0 = __builtin_amdgcn_s_memrealtime();
  if (wave_id() >= 8) {
    extern __shared__ __attribute__((aligned(16))) char smem[];
    const unsigned long long t0 = __builtin_amdgcn_s_memrealtime();
    const rsrc_t rw = mk_rsrc(ws, WS_END);
    __builtin_amdgcn_s_setprio(3);
    if (threadIdx.x < 528) ((LDS_AS unsigned*)((lptr)smem + L_MISC + 1024))[threadIdx.x - 512] = 0u;
    u32x2 tag = gl64<16>(rw, WS_EP);
    tag[0] += 1u;
    const int job = blockIdx.x;
    if (job < 228) raw_bar();
    if (job < 124) raw_bar();
    asm volatile("s_waitcnt vmcnt(0) lgkmcnt(0)\n\ts_barrier" : "+v"(tag) :: "memory");
    if (wave_id() == 8) {
      if (threadIdx.x < 520) st64<16>(rw, WS_F0 + (threadIdx.x - 512) * 2048 + job * 8, tag);
      MARK(9);
      if (job >= 248) {
        while (__builtin_amdgcn_s_memrealtime() - t0 < TPOLL) nap<2>();
        poll_tag(rw, tag, 0xFE0FFFF00000FFFFull);
        if (threadIdx.x < 544) st64<16>(rw, WS_GO + ((threadIdx.x - 512) * 8 + (job & 7)) * 64, tag);
      }
#pragma unroll 1
      for (int spins = 0; spins < (1 << 20); spins++) {
        const u32x2 v = gl64<16>(rw, WS_GO + job * 64);
        if (__builtin_amdgcn_readfirstlane((v[0] ^ tag[0]) | (v[1] ^ tag[1])) == 0) break;
        nap<GSLEEP>();
      }
      MARK(10);
    }
    if (wave_id() > 8) {
    while (__builtin_amdgcn_s_memrealtime() - t0 < TFILL) nap<2>();
    const int i0 = blockIdx.x * 192 + threadIdx.x - 576;
    for (int pc = i0; pc < 640 * 80; pc += 192 * 256) {
      const int n = pc % 640, kq = pc / 640;
      const int u_ = n / 40, rn = n - 40 * u_, hn = rn / 20, dn = rn - 20 * hn, gq = dn >> 2;
      const int T = gq < 4 ? 8 + hn : 4 * hn + 3, gs = gq < 4 ? gq : 3;
      float v[8];
#pragma unroll
      for (int e = 0; e < 4; e++) {
        const float a = (float)((freq_of(kq >> 2, 4 * (kq & 3) + e) * n) % 640) * (1.f / 640.f);
        v[2 * e] = 0.055901699437494742f * __builtin_amdgcn_cosf(a);
        v[2 * e + 1] = -0.055901699437494742f * __builtin_amdgcn_sinf(a);
      }
      if (kq == 0) { v[0] = (n & 1) ? -0.039528470752104741f : 0.039528470752104741f; v[1] = 0.039528470752104741f; }
      st128<16>(rw, WS_WQ + ((u_ * 10 + T) * 20 + (kq >> 2)) * 1024 + ((kq & 3) * 16 + 4 * gs + (dn & 3)) * 16,
                  u32x4{pk2(v[0], v[1]), pk2(v[2], v[3]), pk2(v[4], v[5]), pk2(v[6], v[7])});
    }
#pragma unroll
    for (int it = 0; it < 4; it++) {
      const int k = it * 49152 + i0, b = k / 1408;
      st128<16>(rw, k < 128 * 1408 ? WS_KV + b * 4 * KVC + (k - b * 1408) * 16 : WS_END, u32x4{EMPTY, EMPTY, EMPTY, EMPTY});
    }
    }
    raw_bar();
    if (threadIdx.x == 512) { __hip_atomic_store((unsigned long long*)(ws + (60u << 20)) + blockIdx.x, tm0, __ATOMIC_RELAXED, __HIP_MEMORY_SCOPE_AGENT); __hip_atomic_store((unsigned long long*)(ws + (60u << 20)) + 256 + blockIdx.x, __builtin_amdgcn_s_memrealtime(), __ATOMIC_RELAXED, __HIP_MEMORY_SCOPE_AGENT); }
    loader<false>(ws, tag SPASS);
    loader<true>(ws, tag SPASS);
  } else {
    prep(x, wr, wi, wqkv, wo, ws);
    asm volatile("s_waitcnt vmcnt(0) lgkmcnt(0)\n\ts_barrier" ::: "memory");
    compute<false>(ws, out SPASS);
    compute<true>(ws, out SPASS);
    if (blockIdx.x == 0 && threadIdx.x < 64) {
      unsigned long long mn = ~0ull, mx = 0, g1 = 0;
      for (int i = 0; i < 256; i++) { unsigned long long v = __hip_atomic_load((unsigned long long*)(ws + (60u << 20)) + i, __ATOMIC_RELAXED, __HIP_MEMORY_SCOPE_AGENT); mn = v < mn ? v : mn; mx = v > mx ? v : mx;
        v = __hip_atomic_load((unsigned long long*)(ws + (60u << 20)) + 256 + i, __ATOMIC_RELAXED, __HIP_MEMORY_SCOPE_AGENT); g1 = v > g1 ? v : g1; }
      const unsigned long long until = mn + 40000;
      while (__builtin_amdgcn_s_memrealtime() < until) __builtin_amdgcn_s_sleep(1);
    }
  }
}

#define STOPAT(n)
extern "C" void kernel_launch(void* const* d_in, const int* in_sizes, int n_in, void* d_out, int out_size, void* d_ws, size_t ws_size,
                              hipStream_t stream) {
  const u16* x = (const u16*)d_in[0];
  const u16* wr = (const u16*)d_in[1];
  const u16* wi = (const u16*)d_in[2];
  const u16* wqkv = (const u16*)d_in[4];
  const u16* wo = (const u16*)d_in[5];
  char* ws = (char*)d_ws;
  (void)hipFuncSetAttribute((const void*)k_main, hipFuncAttributeMaxDynamicSharedMemorySize, LDS_FUSED);
  k_main<<<256, 768, LDS_FUSED, stream>>>(x, wr, wi, wqkv, wo, ws, (u16*)d_out);
}
```

```cpp
#include <hip/hip_runtime.h>
#include <hip/hip_bf16.h>
#include <utility>
#include <type_traits>

typedef unsigned short u16;
typedef __attribute__((ext_vector_type(16))) float f32x16;
typedef __attribute__((ext_vector_type(4))) float f32x4;
typedef __attribute__((ext_vector_type(2))) float f32x2;
typedef __attribute__((ext_vector_type(8))) __bf16 bf16x8;
typedef __attribute__((ext_vector_type(2))) __bf16 bf16x2;
typedef __attribute__((ext_vector_type(4))) short s16x4;
typedef __attribute__((ext_vector_type(4))) unsigned u32x4;
typedef __attribute__((ext_vector_type(2))) unsigned u32x2;
#define DI __device__ __forceinline__
#define LDS_AS __attribute__((address_space(3)))
typedef LDS_AS char* lptr;

constexpr double cx_cos_small(double x) { double x2 = x * x, t = 1, s = 1; for (int i = 1; i < 12; i++) { t *= -x2 / ((2 * i - 1) * (2 * i)); s += t; } return s; }
constexpr double cx_sin_small(double x) { double x2 = x * x, t = x, s = x; for (int i = 1; i < 12; i++) { t *= -x2 / ((2 * i) * (2 * i + 1)); s += t; } return s; }
constexpr double cx_cos640(int m) {
  m %= 640; if (m > 320) m = 640 - m; double sg = 1; if (m > 160) { m = 320 - m; sg = -1; }
  const double w = 6.283185307179586476925 / 640; return sg * (m <= 80 ? cx_cos_small(w * m) : cx_sin_small(w * (160 - m)));
}
constexpr u16 cx_bf16(double d) { float f = (float)d; unsigned u = __builtin_bit_cast(unsigned, f); u += 0x7FFFu + ((u >> 16) & 1u); return (u16)(u >> 16); }
struct Cos640 { double v[640]; };
constexpr Cos640 mk_cos() { Cos640 c{}; for (int m = 0; m < 640; m++) c.v[m] = cx_cos640(m); return c; }
constexpr Cos640 COS = mk_cos();
__host__ __device__ constexpr int kf_of(int G, int i) { return G ? G + 40 * i : (i == 0 ? 320 : i < 8 ? 40 * i : 40 * i - 300); }
__host__ __device__ constexpr int freq_of(int G, int i) { return kf_of(G, i) > 320 ? 640 - kf_of(G, i) : kf_of(G, i); }
constexpr double a1val(int out, int a) {
  if (out >= 40 || a >= 40) return 0;
  const int G = out >> 1, part = out & 1;
  if (G == 0) return (part && (a & 1)) ? -1 : 1;
  return COS.v[(16 * a * G + 160 * part) % 640];
}
constexpr double m2val(int G, int o, int k) {
  const int i = o >> 1, po = o & 1, part = k >> 4, b = k & 15, kf = kf_of(G, i);
  const double c = COS.v[(b * kf) % 640], sn = COS.v[(b * kf + 480) % 640];
  if (G == 0) {
    if (part != (i >= 8)) return 0;
    if (i == 0) return 0.039528470752104741 * (po ? 1.0 : c);
    return 0.055901699437494742 * (po ? -sn : c);
  }
  const double v = po ? (part ? c : -sn) : (part ? sn : c);
  return 0.055901699437494742 * ((po && kf > 320) ? -v : v);
}
struct DTab { u16 a1[3][64][8]; u16 a1b[3][64][4]; u16 pad[256]; u16 m2[20][2][64][8]; };
constexpr DTab mk_dtab() {
  DTab t{};
  for (int l = 0; l < 64; l++) {
    const int r = l & 15, g = l >> 4;
    for (int T = 0; T < 3; T++) {
      for (int e = 0; e < 8; e++) t.a1[T][l][e] = cx_bf16(a1val(16 * T + r, 16 * (e >> 2) + 4 * g + (e & 3)));
      for (int e = 0; e < 4; e++) t.a1b[T][l][e] = cx_bf16(a1val(16 * T + r, 32 + 4 * g + e));
    }
    for (int G = 0; G < 20; G++) for (int T = 0; T < 2; T++) for (int e = 0; e < 8; e++)
      t.m2[G][T][l][e] = cx_bf16(m2val(G, 8 * (r >> 2) + 4 * T + (r & 3), 8 * g + e));
  }
  return t;
}
static __device__ const __attribute__((aligned(1024))) DTab g_dt = mk_dtab();

struct CTab { u16 v[10 * 512]; };
constexpr CTab mk_ctab() {
  CTab c{}; u16 cs[64] = {};
  for (int m = 0; m < 64; m++) cs[m] = cx_bf16(cx_cos640(10 * m));
  for (int f = 0; f < 10; f++) for (int l = 0; l < 64; l++) for (int e = 0; e < 8; e++) {
    const int r = l & 15, g = l >> 4;
    const int m = f < 8 ? (8 * g + e) * (16 * (f >> 1) + r) : 2 * r * (16 * (e >> 2) + 4 * g + (e & 3));
    c.v[(f * 64 + l) * 8 + e] = cs[(m + (f & 1) * 48) & 63];
  }
  return c;
}
static __device__ const __attribute__((aligned(1024))) CTab g_ct = mk_ctab();

constexpr int RP = 2048 + 64, RBP = 2048 + 128;
constexpr unsigned RBSZ = 640 * RBP * 2;
constexpr unsigned WS_FL = 0;
constexpr unsigned WS_AUX = WS_FL + 3 * 32768;
constexpr unsigned WS_F0 = WS_AUX + 2048;
constexpr unsigned WS_GO = WS_F0 + 8 * 2048;
constexpr unsigned WS_EP = WS_GO + 256 * 64;
constexpr unsigned WS_RS1 = WS_EP + 256;
constexpr unsigned WS_RB0 = WS_RS1 + 8192;
constexpr unsigned WS_RB1 = WS_RB0 + RBSZ;
constexpr unsigned WS_WF = WS_RB1 + RBSZ;
constexpr unsigned WS_WQ = WS_WF + 320 * 8192;
constexpr unsigned WS_WOF = WS_WQ + 16 * 10 * 20 * 1024;
constexpr unsigned WS_WOP = WS_WOF + 40 * 20 * 1024;
constexpr int KVC = 11264;
constexpr unsigned WS_KV = WS_WOP + 40 * 20 * 1024;
constexpr unsigned WS_O = WS_KV + 32 * 16 * KVC;
constexpr unsigned LAYER2 = 32 * 16 * KVC + 32 * 64 * 1280;
constexpr unsigned WS_X4 = WS_KV + 2 * LAYER2;
constexpr unsigned WS_R3 = WS_X4 + 2048 * 640 * 2;
constexpr unsigned WS_RB2 = WS_R3 + 640 * RP * 4;
constexpr unsigned WS_SSQ = WS_RB2 + RBSZ;
constexpr unsigned EMPTY = 0xFFFFFFFFu;
constexpr unsigned WS_END = WS_SSQ + 8 * 2048 * 16;
#define FILLDELAY 10
#define OUTAUX 0
#define XDELAY 20
#define SW1 50
#define SW2 46
#define XNL 0
#define K1DELAY 40
#define YDELAY 0
#define ODELAY 0
#define WMODE 0
#define YIDLE 8
#define PROBE 1
#define TPOLL 250
#define TSLEEP 1
#define GSLEEP 2
#define LDELAY 30
#define TFILL 400
#define PFDELAY 40
#define YSLEEP 4
#define XSLEEP 1
#define RSLEEP 2
#define AUXB 16

#define STW 0
#define STAMP(k)
#define MARK(i)
#define SARG
#define SPASS
#define SB() __builtin_amdgcn_sched_barrier(0)
typedef __amdgpu_buffer_rsrc_t rsrc_t;
DI rsrc_t mk_rsrc(const void* p, unsigned bytes) { return __builtin_amdgcn_make_buffer_rsrc((void*)p, 0, bytes, 0x00020000); }
template <int AUX> DI void st128(rsrc_t r, unsigned off, u32x4 v) { __builtin_amdgcn_raw_buffer_store_b128(v, r, off, 0, AUX); }
template <int AUX> DI void st64(rsrc_t r, unsigned off, u32x2 v) { __builtin_amdgcn_raw_buffer_store_b64(v, r, off, 0, AUX); }
template <int AUX> DI void st32(rsrc_t r, unsigned off, unsigned v) { __builtin_amdgcn_raw_buffer_store_b32(v, r, off, 0, AUX); }
template <int AUX> DI void st16(rsrc_t r, unsigned off, u16 v) { __builtin_amdgcn_raw_buffer_store_b16(v, r, off, 0, AUX); }
template <int AUX> DI u32x4 gl128(rsrc_t r, unsigned off) { return __builtin_amdgcn_raw_buffer_load_b128(r, off, 0, AUX); }
template <int AUX> DI u32x2 gl64(rsrc_t r, unsigned off) { return __builtin_amdgcn_raw_buffer_load_b64(r, off, 0, AUX); }
template <int AUX> DI unsigned gl32(rsrc_t r, unsigned off) { return __builtin_amdgcn_raw_buffer_load_b32(r, off, 0, AUX); }
DI u32x4 asu(f32x4 v) { return __builtin_bit_cast(u32x4, v); }
DI f32x4 asf(u32x4 v) { return __builtin_bit_cast(f32x4, v); }
DI unsigned pk2(float a, float b) { f32x2 v = {a, b}; return __builtin_bit_cast(unsigned, __builtin_convertvector(v, bf16x2)); }
DI u32x2 pk4(f32x4 a) { return u32x2{pk2(a[0], a[1]), pk2(a[2], a[3])}; }
DI u32x4 pk8(f32x4 a, f32x4 b) { return u32x4{pk2(a[0], a[1]), pk2(a[2], a[3]), pk2(b[0], b[1]), pk2(b[2], b[3])}; }
DI u32x4 clean(u32x4 v) { return __builtin_elementwise_min(v, u32x4{EMPTY - 1, EMPTY - 1, EMPTY - 1, EMPTY - 1}); }
DI unsigned umax4(unsigned m, u32x4 v) { return max(max(max(m, v[0]), v[1]), max(v[2], v[3])); }
DI float bflo(unsigned u) { return __uint_as_float(u << 16); }
DI float bfhi(unsigned u) { return __uint_as_float(u & 0xFFFF0000u); }
DI float dot2(unsigned a, unsigned b, float c) { return __builtin_amdgcn_fdot2_f32_bf16(__builtin_bit_cast(bf16x2, a), __builtin_bit_cast(bf16x2, b), c, false); }
DI int rowof(int p) { return 32 * (((p >> 5) & 0x3C) + (p & 3)) + ((p << 2) & 16) + ((p >> 3) & 15); }
DI int rpos(int p) { return ((p << 8) & 1024) + ((p >> 3) << 2) + (p & 3); }
DI int trow(int T, int r) { return 8 * (r >> 2) + 4 * T + (r & 3); }
template <int CTRL> DI float dpp_add(float v) { return v + __int_as_float(__builtin_amdgcn_update_dpp(0, __float_as_int(v), CTRL, 0xF, 0xF, true)); }
DI float row16_sum(float v) { v = dpp_add<0x128>(v); v = dpp_add<0x124>(v); v = dpp_add<0x122>(v); return dpp_add<0x121>(v); }
DI f32x4 mfma16(u32x4 a, u32x4 b, f32x4 c) {
  return __builtin_amdgcn_mfma_f32_16x16x32_bf16(__builtin_bit_cast(bf16x8, a), __builtin_bit_cast(bf16x8, b), c, 0, 0, 0);
}
DI void mfma16v(f32x4& c, u32x4 a, u32x4 b) { asm volatile("v_mfma_f32_16x16x32_bf16 %0, %1, %2, %0" : "+v"(c) : "v"(a), "v"(b)); }
DI u32x2 ldtr(lptr p) { return __builtin_bit_cast(u32x2, __builtin_amdgcn_ds_read_tr16_b64_v4i16((LDS_AS s16x4*)p)); }
DI u32x4 ld128(lptr p) { return *(LDS_AS u32x4*)p; }
DI u32x2 ld64(lptr p) { return *(LDS_AS u32x2*)p; }
#define GL(v, s, o, x) "global_load_lds_dwordx4 " v ", " s " offset:" #o x "\n\t"
#define M0SET "s_mov_b32 %[sv], m0\n\ts_mov_b32 m0, %[l]\n\ts_nop 2\n\t"
#define M0ADD(x) "s_add_u32 m0, m0, " x "\n\ts_nop 0\n\t"
#define M0RST "s_mov_b32 m0, %[sv]"
DI void dma2(unsigned v, const char* sb, unsigned l) { unsigned sv;
  asm volatile(M0SET GL("%[v]", "%[s]", 0, " sc1") GL("%[v]", "%[s]", 1024, " sc1") M0RST : [sv] "=&s"(sv) : [v] "v"(v), [s] "s"(sb), [l] "s"(l) : "memory"); }
DI void dma4(unsigned v, const char* sb, unsigned l) { unsigned sv;
  asm volatile(M0SET GL("%[v]", "%[s]", 0, "") GL("%[v]", "%[s]", 1024, "") GL("%[v]", "%[s]", 2048, "") GL("%[v]", "%[s]", 3072, "") M0RST
               : [sv] "=&s"(sv) : [v] "v"(v), [s] "s"(sb), [l] "s"(l) : "memory"); }
DI void dma6_sc1(unsigned v, const char* sb, unsigned l) { unsigned sv;
  asm volatile(M0SET GL("%[v]", "%[s]", 0, " sc1") GL("%[v]", "%[s]", 1024, " sc1") GL("%[v]", "%[s]", 2048, " sc1") M0ADD("3072")
               GL("%[w]", "%[s]", 0, " sc1") GL("%[w]", "%[s]", 1024, " sc1") GL("%[w]", "%[s]", 2048, " sc1") M0RST
               : [sv] "=&s"(sv) : [v] "v"(v), [w] "v"(v + 3072), [s] "s"(sb), [l] "s"(l) : "memory", "scc"); }
template <int N> DI void vm_bar() { asm volatile("s_waitcnt vmcnt(%0)\n\ts_barrier" ::"i"(N) : "memory"); }
DI void lds_bar() { asm volatile("s_waitcnt lgkmcnt(0)\n\ts_barrier" ::: "memory"); }
DI void raw_bar() { asm volatile("s_barrier" ::: "memory"); }
template <int N> DI void nap() { __builtin_amdgcn_s_sleep(N); asm volatile("" ::: "memory"); }
DI void drain() { asm volatile("s_waitcnt vmcnt(0)" ::: "memory"); }
#define CODE_ADDR(sym) ({ unsigned lo_, hi_; \
    asm volatile("s_getpc_b64 vcc\n\ts_add_u32 %0, vcc_lo, " sym "@rel32@lo+4\n\ts_addc_u32 %1, vcc_hi, " sym "@rel32@hi+12" : "=s"(lo_), "=s"(hi_) :: "vcc", "scc"); \
    (const unsigned*)(((unsigned long long)hi_ << 32) | lo_); })
#define SYM_MAIN "_Z6k_mainPKtS0_S0_S0_S0_PcPt"
constexpr int CODE_LINES = 368;
DI int wave_id() { return __builtin_amdgcn_readfirstlane(threadIdx.x >> 6); }
DI unsigned lds_addr(lptr p) { return (unsigned)(size_t)p; }

DI int kappa_to_i(int kap) {
  int ks = kap >> 5, g = (kap >> 3) & 3, e = kap & 7;
  if (ks < 16) return (2 * ks + (g >> 1)) * 20 + 8 * (e >> 2) + 4 * (g & 1) + (e & 3);
  return (8 * (ks - 16) + 2 * g + (e >> 2)) * 20 + 16 + (e & 3);
}

DI unsigned wo_frag(int nt, int ks) {
  const int ct = nt / 5, c5 = nt - 5 * ct, kh = ks < 16 ? ks >> 3 : (ks - 16) >> 1, i = ks < 16 ? ks & 7 : 8 + (ks & 1);
  const int a = kh ? (c5 >= 3 ? c5 - 3 : c5 + 2) : c5;
  return ((ct * 2 + kh) * 50 + 5 * i + a) * 1024;
}

constexpr int SLOT32 = 36864;
DI void dma_a(const unsigned* vm, const char* mb, unsigned l) { unsigned sv;
  asm volatile(M0SET GL("%[a0]", "%[m]", 0, " sc1") GL("%[a1]", "%[m]", 1024, " sc1") GL("%[a2]", "%[m]", 2048, " sc1") GL("%[a3]", "%[m]", 3072, " sc1") M0RST
               : [sv] "=&s"(sv) : [a0] "v"(vm[0]), [a1] "v"(vm[1]), [a2] "v"(vm[2]), [a3] "v"(vm[3]), [m] "s"(mb), [l] "s"(l) : "memory"); }
DI void dma_s(unsigned v0, unsigned v1, const char* sb, unsigned l) { unsigned sv;
  asm volatile(M0SET GL("%[v0]", "%[s]", 0, " sc1") GL("%[v0]", "%[s]", 1024, " sc1") GL("%[v1]", "%[s]", 2048, " sc1") GL("%[v1]", "%[s]", 3072, " sc1") M0RST
               : [sv] "=&s"(sv) : [v0] "v"(v0), [v1] "v"(v1), [s] "s"(sb), [l] "s"(l) : "memory"); }
DI void dma1_sc1(unsigned v, const char* sb, unsigned l) { unsigned sv;
  asm volatile(M0SET GL("%[v]", "%[s]", 0, " sc1") M0RST : [sv] "=&s"(sv) : [v] "v"(v), [s] "s"(sb), [l] "s"(l) : "memory"); }
DI void dma1(unsigned v, const char* sb, unsigned l) { unsigned sv;
  asm volatile(M0SET GL("%[v]", "%[s]", 0, "") M0RST : [sv] "=&s"(sv) : [v] "v"(v), [s] "s"(sb), [l] "s"(l) : "memory"); }

constexpr int L_I2 = 40 * 1056, LDS_PREP = L_I2 + 20 * 2064 + 1024;
DI void prep(const u16* x, const u16* wr, const u16* wi, const u16* wqkv, const u16* wo, char* ws) {
  extern __shared__ __attribute__((aligned(16))) char smem[];
  const lptr lds = (lptr)smem;
  const int tid = threadIdx.x, lane = tid & 63, w = wave_id(), g = lane >> 4, r16 = lane & 15;
  const int job = blockIdx.x;
  if (__builtin_expect(job < 144, 1)) {
    const bool cols = job >= 64 && job < 124;
    const int cj = job - 64, cb = cj < 48 ? (cj & 48) + 2 * (cj & 7) + ((cj >> 3) & 1) : cj;
    u32x4 a1[3], m2[3][2]; u32x2 a1b[3];
    if (w < 5) dma1(tid * 16, (const char*)&g_dt, lds_addr(lds) + LDS_PREP + w * 1024);
    if (cols) {
#pragma unroll
      for (int i = 0; i < 5; i++) dma1((16 * (5 * w + i) + (lane >> 2)) * 3840 + cb * 64 + (lane & 3) * 16, (const char*)wqkv, lds_addr(lds) + (5 * w + i) * 1056);
    } else {
      const char* src = (const char*)(job < 64 ? x : wo);
      const int sl = 8 * (w & 3) + 4 * (w >> 2);
      src += (job < 64 ? rowof(32 * job + sl) : kappa_to_i(32 * (job - 124) + sl)) * 1280;
      const int st = (job < 64 ? 31 : 0) * 1280;
#pragma unroll
      for (int i = 0; i < 5; i++) dma1(1024 * i + lane * 16 + ((i == 4 || lane < 16 * i) ? i - 1 : i) * st, src, lds_addr(lds) + (5 * w + i) * 1024);
    }
    unsigned so[3][2]; float sc[2] = {1.f, 1.f};
    if (cols) {
#pragma unroll
      for (int m = 0; m < 2; m++) {
        const int c = 32 * cb + 16 * m + r16;
        const int which = c / 640, rem = c - which * 640, head = rem / 20, d = rem - head * 20, grp = d >> 2;
        const int T = grp < 4 ? (which == 2 ? 2 : 0) + (grp & 1) : (which == 1 ? 3 : 2), gs = grp < 4 ? (which == 1 ? 2 : 0) + (grp >> 1) : (which == 2 ? 3 : 2);
        if (which == 0) sc[m] = 0.22360679774997896f * 1.4426950408889634f;
#pragma unroll
        for (int i = 0; i < 3; i++) so[i][m] = WS_WQ + (((head >> 1) * 10 + 4 * (head & 1) + T) * 20 + w + 8 * i) * 1024 + (g * 16 + 4 * gs + (d & 3)) * 16;
      }
    } else if (job < 64) {
#pragma unroll
      for (int i = 0; i < 3; i++)
#pragma unroll
        for (int T = 0; T < 2; T++) so[i][T] = WS_RB0 + (job * 641 + (job & 1) + 32 * (w + 8 * i) + trow(T, r16)) * 64 + g * 16;
    } else {
#pragma unroll
      for (int i = 0; i < 3; i++) {
        const unsigned f = wo_frag(2 * (w + 8 * i) + (r16 >> 3), job - 124);
#pragma unroll
        for (int T = 0; T < 2; T++) so[i][T] = WS_WOF + f + (g * 16 + (trow(T, r16) & 15)) * 16;
      }
    }
    if (w >= 4) { so[2][0] = WS_END; so[2][1] = WS_END; }
    asm volatile("" : "+v"(so[0][0]), "+v"(so[0][1]), "+v"(so[1][0]), "+v"(so[1][1]), "+v"(so[2][0]), "+v"(so[2][1]), "+v"(sc[0]), "+v"(sc[1]));
    const f32x4 z4 = {0.f, 0.f, 0.f, 0.f};
    const lptr i2 = lds + L_I2;
    drain();
#pragma unroll
    for (int i = 0; i < 3; i++)
#pragma unroll
      for (int T = 0; T < 2; T++)
        asm volatile("global_load_dwordx4 %0, %1, %2" : "=v"(m2[i][T]) : "v"(((((i < 2 || w < 4) ? w + 8 * i : w) * 2 + T) * 64 + lane) * 16), "s"(&g_dt.m2[0][0][0][0]) : "memory");
    raw_bar();
#pragma unroll
    for (int T = 0; T < 3; T++) { a1[T] = ld128(lds + LDS_PREP + T * 1024 + lane * 16); a1b[T] = ld64(lds + LDS_PREP + 3072 + T * 512 + lane * 8); }
    f32x4 t1[4][3];
    if (cols) {
      const lptr src = lds + (4 * g + (r16 >> 2)) * 1056 + (w >> 1) * 256 + (w & 1) * 32 + (lane & 3) * 8, srb = src + (32 - 8 * (g >> 1)) * 1056;
#pragma unroll
      for (int i = 0; i < 4; i++) {
        const u32x2 f0 = ldtr(src + i * 64), f1 = ldtr(src + 16 * 1056 + i * 64), f2 = ldtr(srb + i * 64);
#pragma unroll
        for (int T = 0; T < 3; T++) t1[i][T] = mfma16(u32x4{a1b[T][0], a1b[T][1], 0u, 0u}, u32x4{f2[0], f2[1], 0u, 0u}, mfma16(a1[T], u32x4{f0[0], f0[1], f1[0], f1[1]}, z4));
      }
      const int xo = ((w >> 1) * 8) ^ (((r16 >> 1) & 3) * 16);
      const lptr dst = i2 + 2 * g * 2064 + (16 * (w & 1) + r16) * 64;
#pragma unroll
      for (int T = 0; T < 3; T++)
#pragma unroll
        for (int e = 0; e < 4; e++)
          if (T < 2 || g < 2) *(LDS_AS u32x2*)(dst + (8 * T + (e >> 1)) * 2064 + (xo ^ ((e & 1) * 32))) = u32x2{pk2(t1[0][T][e], t1[1][T][e]), pk2(t1[2][T][e], t1[3][T][e])};
    } else {
      const lptr src = lds + w * 5120 + lane * 8, srb = lds + w * 5120 + 1024 + (lane & 31) * 8;
#pragma unroll
      for (int i = 0; i < 4; i++) {
        const u32x2 f0 = ldtr(src + i * 1280), f1 = ldtr(src + i * 1280 + 512), f2 = ldtr(srb + i * 1280);
#pragma unroll
        for (int T = 0; T < 3; T++) t1[i][T] = mfma16(u32x4{f2[0], f2[1], 0u, 0u}, u32x4{a1b[T][0], a1b[T][1], 0u, 0u}, mfma16(u32x4{f0[0], f0[1], f1[0], f1[1]}, a1[T], z4));
      }
      const int xo = ((r16 & 1) * 32 + g * 8) ^ ((w & 1) * 32);
      const lptr dst = i2 + (r16 >> 1) * 2064 + w * 256;
#pragma unroll
      for (int i = 0; i < 4; i++)
#pragma unroll
        for (int T = 0; T < 3; T++)
          if (T < 2 || r16 < 8) *(LDS_AS u32x2*)(dst + T * 8 * 2064 + i * 64 + (xo ^ ((i >> 1) * 16))) = pk4(t1[i][T]);
    }
    lds_bar();
    const rsrc_t rw = mk_rsrc(ws, WS_END);
    asm volatile("s_waitcnt vmcnt(0)" : "+v"(m2[0][0]), "+v"(m2[0][1]), "+v"(m2[1][0]), "+v"(m2[1][1]), "+v"(m2[2][0]), "+v"(m2[2][1]));
    f32x4 sq[2] = {z4, z4};
    u32x4 f[3][2];
#pragma unroll
    for (int i = 0; i < 3; i++) {
      const lptr fp = i2 + ((i < 2 || w < 4) ? w + 8 * i : 0) * 2064 + r16 * 64 + ((g ^ ((r16 >> 1) & 3)) * 16);
      f[i][0] = ld128(fp); f[i][1] = ld128(fp + 1024);
    }
    if (cols) {
#pragma unroll
      for (int i = 0; i < 3; i++)
#pragma unroll
        for (int m = 0; m < 2; m++) st128<16>(rw, so[i][m], pk8(mfma16(m2[i][0], f[i][m], z4) * sc[m], mfma16(m2[i][1], f[i][m], z4) * sc[m]));
    } else {
#pragma unroll
      for (int i = 0; i < 3; i++)
#pragma unroll
        for (int T = 0; T < 2; T++) {
          const f32x4 d0 = mfma16(f[i][0], m2[i][T], z4), d1 = mfma16(f[i][1], m2[i][T], z4);
          st128<16>(rw, so[i][T], pk8(d0, d1));
          const float k = ((i == 2 && w >= 4) || (i == 0 && T == 0 && tid < 64 && r16 == 1)) ? 0.f : 1.f;
          sq[0] += d0 * d0 * k; sq[1] += d1 * d1 * k;
        }
    }
    LDS_AS float* sl = (LDS_AS float*)(lds + LDS_PREP - 1024);
    if (job < 64) {
#pragma unroll
      for (int m = 0; m < 2; m++) {
#pragma unroll
        for (int e = 0; e < 4; e++) sq[m][e] = row16_sum(sq[m][e]);
        if (r16 == 0) *(LDS_AS f32x4*)(sl + w * 32 + 8 * g + 4 * m) = sq[m];
      }
    }
    if (job < 64) {
      lds_bar();
      if (tid < 32) {
        float v = 0.f;
#pragma unroll
        for (int i = 0; i < 8; i++) v += sl[i * 32 + tid];
        st32<16>(rw, WS_RS1 + rpos(32 * job + tid) * 4, __float_as_uint(__builtin_amdgcn_rsqf((v + 640.f * 1e-5f) * (2048.f * 2048.f / 640.f))));
      }
    }
    return;
  }
  const int mi = job - 144;
  const rsrc_t rw = mk_rsrc(ws, WS_END);
  if (mi < 64) {
    const int m = mi >> 3, oc = mi & 7;
    const char* src = (const char*)((w >> 2) ? wi : wr) + (64 * (4 * m + (w & 3)) + 8 * oc) * 642;
    dma4(lane * 16, src, lds_addr(lds) + w * 5152);
    dma1(4096 + lane * 16, src, lds_addr(lds) + w * 5152 + 4096);
    if (lane == 0) dma1(5120, src, lds_addr(lds) + w * 5152 + 5120);
    vm_bar<0>();
    LDS_AS u16* L = (LDS_AS u16*)lds + (tid & 7) * 321;
#pragma unroll
    for (int it = 0; it < 5; it++) {
      const int c = (tid >> 3) + 64 * it, col = freq_of(c >> 4, c & 15);
      u32x4 o;
#pragma unroll
      for (int e = 0; e < 4; e++) o[e] = (unsigned)L[e * 2576 + col] | ((unsigned)L[(4 + e) * 2576 + col] << 16);
      st128<16>(rw, WS_WF + c * 8192 + (m * 64 + 8 * oc + (tid & 7)) * 16, o);
    }
#pragma unroll
    for (int it = 0; it < 3; it++) st128<16>(rw, WS_RB1 + ((it * 64 + mi) * 512 + tid) * 16, u32x4{EMPTY, EMPTY, EMPTY, EMPTY});
  } else if (mi < 84) {
    unsigned pf0;
    {
      const unsigned* cm = CODE_ADDR(SYM_MAIN);
      const unsigned* p = (mi & 8) ? (const unsigned*)&g_dt.m2[0][0][0][0] + min(tid, 319) * 32 : tid < 80 ? (const unsigned*)&g_ct + tid * 32 : cm + min(tid - 80, CODE_LINES - 1) * 32;
      asm volatile("global_load_dword %0, %1, off" : "=v"(pf0) : "v"(p) : "memory");
    }
    const char* src = (const char*)wo + kappa_to_i(32 * (mi - 64) + 8 * (w & 3) + 4 * (w >> 2)) * 1280;
    dma4(lane * 16, src, lds_addr(lds) + w * 5152);
    dma1(4096 + lane * 16, src, lds_addr(lds) + w * 5152 + 4096);
    asm volatile("s_waitcnt vmcnt(0)\n\ts_barrier" : "+v"(pf0) :: "memory");
#pragma unroll
    for (int i = 0; i < 5; i++) {
      const lptr a = lds + g * 5152 + (r16 >> 2) * 1280 + (5 * w + i) * 32 + (lane & 3) * 8;
      const u32x2 lo = ldtr(a), hi = ldtr(a + 4 * 5152);
      st128<16>(rw, WS_WOP + wo_frag(5 * w + i, mi - 64) + lane * 16, u32x4{lo[0], lo[1], hi[0], hi[1]});
    }
  } else {
    const int i0 = (mi - 84) * 512 + tid;
    nap<K1DELAY>();
    if (i0 < 6272) st128<16>(rw, WS_FL + i0 * 16, u32x4{0u, 0u, 0u, 0u});
#pragma unroll
    for (int it = 0; it < 6; it++) { const int i = 98304 + i0 + it * 14336; if (i < (int)RBSZ / 16) st128<16>(rw, WS_RB1 + i * 16, u32x4{EMPTY, EMPTY, EMPTY, EMPTY}); }
  }
}

#define PINF(x) asm volatile("" : "+v"(x))
DI void put_scale(lptr lds, int q, int tid, const u32x2* sp) {
  f32x4 r = {640.f * 1e-5f, 640.f * 1e-5f, 640.f * 1e-5f, 640.f * 1e-5f};
#pragma unroll
  for (int i = 0; i < 8; i++) r += f32x4{bflo(sp[i][0]), bfhi(sp[i][0]), bflo(sp[i][1]), bfhi(sp[i][1])};
#pragma unroll
  for (int e = 0; e < 4; e++) r[e] = __builtin_amdgcn_rsqf(r[e] * (2048.f * 2048.f / 640.f));
  ((LDS_AS f32x4*)(lds + 2 * SLOT32))[q * 256 + tid] = r;
}
template <bool FIRST>
DI void col_phase(rsrc_t rw, lptr lds, int c SARG) {
  int lane = threadIdx.x & 63; PINF(lane);
  const int wv = wave_id(), w = wv & 3, q = wv >> 2, tid = w * 64 + lane, g = lane >> 4, r16 = lane & 15;
  const lptr img = lds + q * SLOT32;
  f32x4 xr[2], xi[2], rs[2]; u32x4 wf[2], xb[2];
  u32x4 c64[2], s64[2], n64[2], c32[2], s32[2], n32[2];
  const u32x4* ct = (const u32x4*)&g_ct + lane;
  auto ld_const = [&]() { c64[0] = ct[(2 * w) * 64]; s64[0] = ct[(2 * w + 1) * 64]; c32[0] = ct[8 * 64]; s32[0] = ct[9 * 64]; };
  auto ld_filter = [&]() {
#pragma unroll
  for (int t = 0; t < 2; t++) wf[t] = gl128<16>(rw, WS_WF + c * 8192 + ((4 * t + g) * 64 + 16 * w + r16) * 16);
  };
  if (FIRST) ld_const();
  SB();
  f32x4 t1c[2], t1s[2], t2c[2], t2s[2];
  {
    const float f1 = (float)(16 * w + r16) * (1.f / 2048.f), b1 = (float)(4 * g), f2 = (float)(16 * w + 4 * g) * (1.f / 2048.f), b2 = (float)r16;
#pragma unroll
    for (int t = 0; t < 2; t++) {
#pragma unroll
      for (int e = 0; e < 4; e++) {
        const float a1 = (b1 + (float)(16 * t + e)) * f1, a2 = (b2 + (float)(16 * t)) * (f2 + (float)e * (1.f / 2048.f));
        t1c[t][e] = __builtin_amdgcn_cosf(a1); t1s[t][e] = __builtin_amdgcn_sinf(-a1);
        t2c[t][e] = __builtin_amdgcn_cosf(a2); t2s[t][e] = __builtin_amdgcn_sinf(a2);
      }
      PINF(t1c[t]); PINF(t1s[t]); PINF(t2c[t]); PINF(t2s[t]);
    }
  }
  int wo_ = r16 * 128 + (((2 * w + (g >> 1)) ^ (r16 >> 1)) * 16) + (g & 1) * 8, ro_ = r16 * 128 + ((g ^ (r16 >> 1)) * 16), ro2 = ro_ ^ 64;
  PINF(wo_); PINF(ro_); PINF(ro2);
  SB();
  if (FIRST) {
    raw_bar();
#pragma unroll
    for (int t = 0; t < 2; t++) { xb[t] = gl128<16>(rw, WS_RB0 + ((tid >> 2) * 641 + ((tid >> 2) & 1) + 2 * c + t) * 64 + (tid & 3) * 16); rs[t] = asf(gl128<16>(rw, WS_RS1 + (1024 * t + 4 * tid) * 4)); }
    ld_filter();
    SB();
#pragma unroll
    for (int t = 0; t < 2; t++) {
      xr[t] = f32x4{bflo(xb[0][2 * t]), bfhi(xb[0][2 * t]), bflo(xb[0][2 * t + 1]), bfhi(xb[0][2 * t + 1])};
      xi[t] = f32x4{bflo(xb[1][2 * t]), bfhi(xb[1][2 * t]), bflo(xb[1][2 * t + 1]), bfhi(xb[1][2 * t + 1])};
    }
  } else {
    MARK(23);
    drain();
    MARK(11);
    nap<YDELAY>();
    u32x2 sp[8];
    const unsigned so = WS_SSQ + (blockIdx.x & 7) * 32768 + (1024 * q + 4 * tid) * 2, xo = WS_R3 + ((tid >> 2) * 642 + 2 * c) * 64 + (tid & 3) * 16;
    bool bs = true, bx = true;
#pragma unroll 1
    for (int spins = 0; spins < (1 << 18); spins++) {
      if (bx) {
#pragma unroll
        for (int t = 0; t < 2; t++) { xr[t] = asf(gl128<16>(rw, xo + 64 * 642 * 64 * t)); xi[t] = asf(gl128<16>(rw, xo + 64 * 642 * 64 * t + 64)); }
      }
      if (bs) {
#pragma unroll
        for (int i = 0; i < 8; i++) sp[i] = gl64<16>(rw, so + i * 4096);
      }
      unsigned m = 0;
#pragma unroll
      for (int t = 0; t < 2; t++) m = umax4(umax4(m, asu(xr[t])), asu(xi[t]));
      bx = __builtin_amdgcn_ballot_w64(m == EMPTY) != 0;
      m = 0;
#pragma unroll
      for (int i = 0; i < 8; i++) m = max(max(m, sp[i][0]), sp[i][1]);
      bs = __builtin_amdgcn_ballot_w64(m == EMPTY) != 0;
      if (!bs && !bx) break;
      nap<YSLEEP>();
    }
    ld_const(); ld_filter();
    SB();
    put_scale(lds, q, tid, sp);
    LDS_AS f32x4* ex = (LDS_AS f32x4*)(lds + 2 * SLOT32) + tid;
    lds_bar();
    rs[0] = ex[0]; rs[1] = ex[256];
  }
  MARK(FIRST ? 9 : 21);
#pragma unroll
  for (int t = 0; t < 2; t++) {
    u32x2 pb = pk4(xi[t] * rs[t]);
    if (c == 0) pb = u32x2{0u, 0u};
    *(LDS_AS u32x2*)(img + wo_ + t * 2048) = pk4(xr[t] * rs[t]);
    *(LDS_AS u32x2*)(img + 4096 + wo_ + t * 2048) = pb;
  }
  lds_bar();
  u32x4 ur[2][2], ui[2][2], br, bi;
  f32x4 ar[2], ai[2];
  const f32x4 z4 = {0.f, 0.f, 0.f, 0.f};
  auto rd_in = [&](int off) {
#pragma unroll
    for (int ks = 0; ks < 2; ks++)
#pragma unroll
      for (int t = 0; t < 2; t++) { const lptr p = img + off + (ks ? ro2 : ro_) + t * 2048; ur[t][ks] = ld128(p); ui[t][ks] = ld128(p + 4096); }
    SB();
  };
  auto cmul = [&](const f32x4* cc, const f32x4* sn) {
    br = pk8(ar[0] * cc[0] - ai[0] * sn[0], ar[1] * cc[1] - ai[1] * sn[1]);
    bi = pk8(ai[0] * cc[0] + ar[0] * sn[0], ai[1] * cc[1] + ar[1] * sn[1]);
  };
  rd_in(0);
  {
    const unsigned par = (lane & 1) ? 0x80008000u : 0u;
    c64[1] = c64[0] ^ par; s64[1] = s64[0] ^ par; n64[0] = s64[0] ^ 0x80008000u; n64[1] = s64[1] ^ 0x80008000u;
    c32[1] = c32[0] ^ 0x80000000u; s32[1] = s32[0] ^ 0x80000000u; n32[0] = s32[0] ^ 0x80008000u; n32[1] = s32[0] ^ 0x00008000u;
    PINF(c64[1]); PINF(s64[1]); PINF(n64[0]); PINF(n64[1]); PINF(c32[1]); PINF(s32[1]); PINF(n32[0]); PINF(n32[1]);
  }
  SB();
#pragma unroll
  for (int ks = 0; ks < 2; ks++) {
#pragma unroll
    for (int t = 0; t < 2; t++) { ar[t] = mfma16(ur[t][ks], c64[ks], ks ? ar[t] : z4); ai[t] = mfma16(ui[t][ks], c64[ks], ks ? ai[t] : z4); }
#pragma unroll
    for (int t = 0; t < 2; t++) { ar[t] = mfma16(ui[t][ks], s64[ks], ar[t]); ai[t] = mfma16(ur[t][ks], n64[ks], ai[t]); }
  }
  const f32x4 fa[2] = {asf(wf[0] << 16), asf(wf[1] << 16)}, fb[2] = {asf(wf[0] & 0xFFFF0000u), asf(wf[1] & 0xFFFF0000u)};
  cmul(t1c, t1s);
#pragma unroll
  for (int t = 0; t < 2; t++) { ar[t] = mfma16(c32[t], br, z4); ai[t] = mfma16(c32[t], bi, z4); }
#pragma unroll
  for (int t = 0; t < 2; t++) { ar[t] = mfma16(s32[t], bi, ar[t]); ai[t] = mfma16(n32[t], br, ai[t]); }
  cmul(fa, fb);
#pragma unroll
  for (int t = 0; t < 2; t++) { ar[t] = mfma16(br, c32[t], z4); ai[t] = mfma16(bi, c32[t], z4); }
#pragma unroll
  for (int t = 0; t < 2; t++) { ar[t] = mfma16(bi, n32[t], ar[t]); ai[t] = mfma16(br, s32[t], ai[t]); }
  cmul(t2c, t2s);
#pragma unroll
  for (int t = 0; t < 2; t++) {
    *(LDS_AS u32x2*)(img + 8192 + wo_ + t * 2048) = u32x2{br[2 * t], br[2 * t + 1]};
    *(LDS_AS u32x2*)(img + 12288 + wo_ + t * 2048) = u32x2{bi[2 * t], bi[2 * t + 1]};
  }
  lds_bar();
  rd_in(8192);
#pragma unroll
  for (int ks = 0; ks < 2; ks++) {
#pragma unroll
    for (int t = 0; t < 2; t++) { ar[t] = mfma16(c64[ks], ur[t][ks], ks ? ar[t] : xr[t]); ai[t] = mfma16(c64[ks], ui[t][ks], ks ? ai[t] : xi[t]); }
#pragma unroll
    for (int t = 0; t < 2; t++) { ar[t] = mfma16(n64[ks], ui[t][ks], ar[t]); ai[t] = mfma16(s64[ks], ur[t][ks], ai[t]); }
  }
  const unsigned ob = (FIRST ? WS_RB1 : WS_RB2) + (2 * c * RBP + 8 * tid) * 2;
  st128<16>(rw, ob, clean(pk8(ar[0], ar[1])));
  st128<16>(rw, ob + RBP * 2, c == 0 ? u32x4{0u, 0u, 0u, 0u} : clean(pk8(ai[0], ai[1])));
}

DI f32x16 mfma32(u32x4 a, u32x4 b, f32x16 c) {
  return __builtin_amdgcn_mfma_f32_32x32x16_bf16(__builtin_bit_cast(bf16x8, a), __builtin_bit_cast(bf16x8, b), c, 0, 0, 0);
}
DI f32x16 mfma32k8(u32x2 a, u32x2 b, f32x16 c) {
  return __builtin_amdgcn_mfma_f32_32x32x8bf16_1k(__builtin_bit_cast(s16x4, a), __builtin_bit_cast(s16x4, b), c, 0, 0, 0);
}
DI f32x16 scores(u32x4 k1, u32x2 k2, u32x4 q1, u32x2 q2) {
  f32x16 z;
#pragma unroll
  for (int i = 0; i < 16; i++) z[i] = 0.f;
  f32x16 s = mfma32(k1, q1, z);
  asm volatile("s_nop 11" : "+v"(s));
  return mfma32k8(k2, q2, s);
}
DI float max16(const f32x16& s) {
  float m = fmaxf(s[0], s[1]);
#pragma unroll
  for (int i = 2; i < 16; i++) m = fmaxf(m, s[i]);
  return m;
}
#define STR_(x) #x
#define STR(x) STR_(x)
#define KOF(t) ((((t)&7)>>1)*11264+((t)&1)*1280)
#define VOF(t) ((((t)&7)>>1)*11264+5120+((t)&1)*1536)
#define EXP(d, s) "v_exp_f32 v" #d ", v" #s "\n\t"
#define CVT(d, s) "v_cvt_pk_bf16_f32 v" #d ", v" #s ", v[" #s "+1]\n\t"
#define STEPA(t) \
  "v_mfma_f32_32x32x16_bf16 v[48:63], v[80:83], v[136:139], v[120:135]\n\t" \
  "ds_read_b128 v[80:83], v100 offset:" STR(KOF(t+1)) "\n\t" \
  EXP(64,32) EXP(65,33) CVT(78,68) CVT(79,70) EXP(66,34) EXP(67,35) \
  "v_mfma_f32_32x32x16_bf16 v[104:119], v[90:93], v[76:79], v[104:119]\n\t" \
  "ds_read_b64_tr_b16 v[90:91], v102 offset:" STR(VOF(t)+768) "\n\t" \
  "ds_read_b64_tr_b16 v[92:93], v102 offset:" STR(VOF(t)+1152) "\n\t" \
  EXP(68,36) EXP(69,37) CVT(72,64) CVT(73,66) EXP(70,38) EXP(71,39) \
  "v_mfma_f32_32x32x8_bf16 v[48:63], v[84:85], v[140:141], v[48:63]\n\t" \
  "ds_read_b64 v[84:85], v101 offset:" STR(KOF(t+1)) "\n\t" \
  EXP(64,40) EXP(65,41) CVT(74,68) CVT(75,70) EXP(66,42) EXP(67,43) \
  "s_waitcnt lgkmcnt(4)\n\t" \
  "v_mfma_f32_32x32x16_bf16 v[0:15], v[86:89], v[72:75], v[0:15]\n\t" \
  EXP(68,44) EXP(69,45) CVT(76,64) CVT(77,66) EXP(70,46) EXP(71,47)
#define STEPB(t) \
  "s_waitcnt lgkmcnt(0)\n\t" \
  "v_mfma_f32_32x32x16_bf16 v[32:47], v[80:83], v[94:97], v[16:31]\n\t" \
  EXP(64,48) EXP(65,49) CVT(78,68) CVT(79,70) EXP(66,50) EXP(67,51) \
  "v_mfma_f32_32x32x16_bf16 v[0:15], v[90:93], v[76:79], v[0:15]\n\t" \
  EXP(68,52) EXP(69,53) CVT(72,64) CVT(73,66) EXP(70,54) EXP(71,55) \
  "v_mfma_f32_32x32x8_bf16 v[32:47], v[84:85], v[98:99], v[32:47]\n\t" \
  EXP(64,56) EXP(65,57) CVT(74,68) CVT(75,70) EXP(66,58) EXP(67,59) \
  "v_mfma_f32_32x32x16_bf16 v[104:119], v[86:89], v[72:75], v[104:119]\n\t" \
  "ds_read_b64_tr_b16 v[86:87], v102 offset:" STR(VOF(t+1)) "\n\t" \
  "ds_read_b64_tr_b16 v[88:89], v102 offset:" STR(VOF(t+1)+384) "\n\t" \
  EXP(68,60) EXP(69,61) CVT(76,64) CVT(77,66) EXP(70,62) EXP(71,63)
#define PR(n) "s_setprio " #n "\n\t"
#define CHUNK(i) PR(1) STEPA(2*(i)) PR(0) STEPB(2*(i)) "s_barrier\n\t" PR(2) STEPA(2*(i)+1) STEPB(2*(i)+1)
#define Z4(a) "v_mov_b64 v[" #a ":" #a "+1], 0\n\tv_mov_b64 v[" #a "+2:" #a "+3], 0\n\t"

#define R4(b) "v[" STR(b) ":" STR(b) "+3]"
#define MM(d, a, b) "v_mfma_f32_16x16x32_bf16 " R4(d) ", " R4(a) ", " R4(b) ", " R4(d) "\n\t"
#define GM(B, m, i) MM(20*m+4*i, B+8+4*i, B+4*m)
#define RT(B, m, hf, k) "ds_read_b64_tr_b16 v[" STR(B+4*m+2*hf) ":" STR(B+4*m+2*hf+1) "], v10" #m " offset:" STR(k*8192+hf*1024) "\n\t"
#define RS(B, i, k) "ds_read_b128 " R4(B+8+4*i) ", v102 offset:" STR(i*2048+k*1024) "\n\t"
#define IF5_4(x) ""
#define IF5_5(x) x
#define GSTEP(N, B, C, k) GM(B,0,0) RT(C,0,0,k) RT(C,0,1,k) GM(B,1,0) RS(C,0,k) RT(C,1,0,k) GM(B,0,1) RT(C,1,1,k) RS(C,1,k) GM(B,1,1) RS(C,2,k) \
  GM(B,0,2) RS(C,3,k) GM(B,1,2) IF5_##N(RS(C,4,k)) GM(B,0,3) GM(B,1,3) IF5_##N(GM(B,0,4) GM(B,1,4)) MM(40,B,B)
#define GEMM_ASM(N) \
  Z4(0) Z4(4) Z4(8) Z4(12) Z4(16) Z4(20) Z4(24) Z4(28) Z4(32) Z4(36) Z4(40) "s_mov_b32 %[c], 0\n\ts_barrier\n\t" \
  RT(44,0,0,0) RT(44,0,1,0) RS(44,0,0) RT(44,1,0,0) RT(44,1,1,0) RS(44,1,0) RS(44,2,0) RS(44,3,0) IF5_##N(RS(44,4,0)) \
  "L_gemm_%=:\n\ts_add_u32 %[c], %[c], 1\n\ts_and_b32 %[t], %[c], 3\n\ts_cselect_b32 %[t], 1, -3\n\ts_mul_i32 %[t], %[t], 36864\n\ts_waitcnt lgkmcnt(0)\n\t" \
  GSTEP(N,44,72,1) \
  "v_add_u32 v100, %[t], v100\n\tv_add_u32 v101, %[t], v101\n\tv_add_u32 v102, %[t], v102\n\ts_waitcnt lgkmcnt(0)\n\ts_barrier\n\t" \
  GSTEP(N,72,44,0) \
  "s_cmp_lg_u32 %[c], 9\n\ts_cbranch_scc1 L_gemm_%=\n\ts_waitcnt lgkmcnt(0)\n\t" \
  GSTEP(N,44,72,1) "s_waitcnt lgkmcnt(0)\n\t" \
  MM(40,72,72) GM(72,0,0) GM(72,1,0) GM(72,0,1) GM(72,1,1) "s_nop 4\n\t" \
  "v_cndmask_b32 v44, v40, v41, %[m1]\n\tv_cndmask_b32 v45, v42, v43, %[m1]\n\tv_mov_b32 v46, 0x3727c5ac\n\tv_cndmask_b32 v44, v44, v45, %[m2]\n\t" \
  "v_fmac_f32 v46, 0x3acccccd, v44\n\tv_cmp_u_f32 vcc, v44, v44\n\tv_rsq_f32 v46, v46\n\ts_cbranch_vccz L_gok_%=\n\tds_write_b32 v106, v106\n\t" \
  "L_gok_%=:\n\tds_write_b32 v103, v46\n\ts_waitcnt lgkmcnt(0)\n\ts_barrier\n\t" \
  "ds_read_b32 v44, v104\n\tds_read_b32 v45, v105\n\tds_read_b32 v46, v106\n\t" \
  GM(72,0,2) GM(72,1,2) GM(72,0,3) GM(72,1,3) IF5_##N(GM(72,0,4) GM(72,1,4)) "s_waitcnt lgkmcnt(0)"
#define GEMM_OPS \
  : "={v[0:3]}"(acc[0][0]), "={v[4:7]}"(acc[0][1]), "={v[8:11]}"(acc[0][2]), "={v[12:15]}"(acc[0][3]), "={v[16:19]}"(acc[0][4]), \
    "={v[20:23]}"(acc[1][0]), "={v[24:27]}"(acc[1][1]), "={v[28:31]}"(acc[1][2]), "={v[32:35]}"(acc[1][3]), "={v[36:39]}"(acc[1][4]), \
    "={v44}"(rs[0]), "={v45}"(rs[1]), "={v46}"(vote), "+{v100}"(am0), "+{v101}"(am1), "+{v102}"(as), [c] "=&s"(c_), [t] "=&s"(t_) \
  : "{v103}"(a_w), "{v104}"(a_r[0]), "{v105}"(a_r[1]), "{v106}"(a_v), [m1] "s"(0xAAAAAAAAAAAAAAAAull), [m2] "s"(0xCCCCCCCCCCCCCCCCull) \
  : "memory", "scc", "vcc", "v40", "v41", "v42", "v43", "v47", "v48", "v49", "v50", "v51", "v52", "v53", "v54", "v55", "v56", "v57", "v58", "v59", "v60", \
    "v61", "v62", "v63", "v64", "v65", "v66", "v67", "v68", "v69", "v70", "v71", "v72", "v73", "v74", "v75", "v76", "v77", "v78", "v79", "v80", "v81", \
    "v82", "v83", "v84", "v85", "v86", "v87", "v88", "v89", "v90", "v91", "v92", "v93", "v94", "v95", "v96", "v97", "v98", "v99"

constexpr int HR = 4 * KVC;
constexpr int L_A = 2 * HR;
constexpr int L_EX = 15360;
constexpr int L_MISC = L_A + 70 * 1024;
constexpr int LDS_FUSED = L_MISC + 2048;

DI bool poll16(rsrc_t rw, unsigned off, unsigned need) {
  const int lane = threadIdx.x & 63;
#pragma unroll 1
  for (int spins = 0; spins < (1 << 20); spins++) {
    const unsigned v = gl32<16>(rw, off + (lane & 15) * 4);
    const unsigned got = (unsigned)__builtin_amdgcn_ballot_w64(v != 0) & 0xFFFFu;
    if ((got & need) == need) return true;
    nap<1>();
  }
  return false;
}

DI void poll_aux(rsrc_t rw, unsigned off) {
#pragma unroll 1
  for (int spins = 0; spins < (1 << 20); spins++) {
    const u32x4 v = gl128<16>(rw, off + (threadIdx.x & 63) * 16);
    if (!__builtin_amdgcn_ballot_w64(min(min(v[0], v[1]), min(v[2], v[3])) == 0u)) break;
    nap<4>();
  }
}
DI void poll_tag(rsrc_t rw, u32x2 tag, unsigned long long need) {
  const unsigned o = WS_F0 + (blockIdx.x & 7) * 2048 + (threadIdx.x & 63) * 32;
#pragma unroll 1
  for (int spins = 0; spins < (1 << 20); spins++) {
    const u32x4 a = gl128<16>(rw, o), b = gl128<16>(rw, o + 16);
    const unsigned d = (a[0] ^ tag[0]) | (a[1] ^ tag[1]) | (a[2] ^ tag[0]) | (a[3] ^ tag[1]) | (b[0] ^ tag[0]) | (b[1] ^ tag[1]) | (b[2] ^ tag[0]) | (b[3] ^ tag[1]);
    if (!(__builtin_amdgcn_ballot_w64(d != 0u) & need)) break;
    nap<TSLEEP>();
  }
}
#define LAYER_VARS \
  extern __shared__ __attribute__((aligned(16))) char smem[]; \
  const lptr lds = (lptr)smem; \
  const rsrc_t rw = mk_rsrc(ws, WS_END); \
  int lane = threadIdx.x & 63; asm volatile("" : "+v"(lane)); \
  const int w = wave_id(), g = lane >> 4, r16 = lane & 15; \
  const int xcd = blockIdx.x & 7, bi = blockIdx.x >> 3, u = 8 * (xcd & 1) + (bi & 7), qt = 4 * (xcd >> 1) + (bi >> 3); \
  const int hf = u & 1, ct = u >> 1;     \
  constexpr unsigned FKV = WS_FL + (FINAL ? 32768 : 0), KV = WS_KV + (FINAL ? LAYER2 : 0), OB = WS_O + (FINAL ? LAYER2 : 0); \
  constexpr unsigned AUX = WS_AUX + (FINAL ? 1024 : 0), RB = FINAL ? WS_RB2 : WS_RB1; \
  constexpr int MK = FINAL ? 12 : 0; (void)MK; (void)hf; (void)AUX; (void)RB; (void)FKV; \
  LDS_AS unsigned* misc = (LDS_AS unsigned*)(lds + L_MISC + 1024) + (FINAL ? 8 : 0);

template <bool FINAL>
DI void loader(char* ws, u32x2 tag SARG) {
  LAYER_VARS
  {
    const int lw = w - 8;
    {
      unsigned vm[4];
#pragma unroll
      for (int r = 0; r < 4; r++) vm[r] = (4 * (4 * lw + r) + g) * (RBP * 2) + 256 * qt + (((r16 >> 1) ^ (g | ((r & 2) << 1))) * 32) + (r16 & 1) * 16 + 3072 - 1024 * r;
      const unsigned l0 = lds_addr(lds) + lw * 4096, v0 = 2 * lw * 20480 + lane * 16, v2 = ((8 + (lw >> 1)) * 20 + (lw & 1)) * 1024 + lane * 16;
      constexpr int CNT = FINAL ? 9 : 8;
      auto lds_w = [&](int s) {
        const char* sb = ws + WS_WQ + u * 204800 + s * 2048;
        dma_s(v0, v0 + 20480 - 2048, sb, l0 + (s & 3) * SLOT32 + 16384 + (lw >> 1) * 2048);
        if (FINAL) dma1_sc1(v2, sb, lds_addr(lds) + (s & 3) * SLOT32 + 24576 + (lw >> 1) * 10240 + (lw & 1) * 1024);
      };
      auto lds_a = [&](int s) {
        dma_a(vm, ws + RB - 3072 + s * (64 * RBP * 2), l0 + (s & 3) * SLOT32); };
#pragma unroll 1
      for (int att = 0;; att++) {
        if (att == 0) { raw_bar(); if (FINAL) raw_bar(); else poll_tag(rw, tag, 0x7FFF0000ull); }
        lds_w(0); lds_w(1);
        if (att == 0) raw_bar();
        lds_w(2); lds_w(3);
        auto probe = [&](int s0, int n) {
          const int pl = min(lane, n - 1);
          const unsigned po = RB + ((64 * (s0 + (pl >> 3)) + 16 * lw + 2 * (pl & 7) + 1) * RBP + 128 * qt + 126) * 2;
#pragma unroll 1
          for (int spins = 0; spins < (1 << 20); spins++) {
            if (!__builtin_amdgcn_ballot_w64(gl32<16>(rw, po) == EMPTY)) break;
            nap<XSLEEP>();
          }
        };
        if (att == 0) {
          raw_bar();
          if (blockIdx.x >= 64) nap<XDELAY>();
          MARK(MK + 7);
          probe(0, 48);
        }
        drain();
        MARK(MK + 8);
        unsigned nd = 0;
        if (att == 0 && lane == 0) nd = __hip_atomic_fetch_add(misc + 4, 1u, __ATOMIC_RELAXED, __HIP_MEMORY_SCOPE_WORKGROUP);
        if (nd == 11) st32<16>(rw, AUX + blockIdx.x * 4, 1u);
#pragma unroll 1
        for (int s = 0; s < 4; s++) lds_a(s);
        vm_bar<12>(); vm_bar<8>();
        if (att == 0) probe(6, 32);
        lds_a(4); lds_w(4);
        vm_bar<4 + CNT>();
#pragma unroll 1
        for (int s = 5; s < 10; s++) { lds_a(s); lds_w(s); vm_bar<2 * CNT>(); }
        vm_bar<CNT>();
        poll_aux(rw, AUX);
        if (!FINAL) { poll_tag(rw, tag, ~0ull); if (blockIdx.x == 0 && threadIdx.x == 512) st64<16>(rw, WS_EP, tag); }
        vm_bar<0>();
        raw_bar();
        if (XNL || att || !*(volatile LDS_AS unsigned*)(misc + 2)) break;
      }
    }
    MARK(MK + 0);
    const int hh = lw >> 1, hp = lw & 1;
    const unsigned kvv = KV + (2 * u + hh) * 16 * KVC + hp * 5120 + lane * 16;
    const unsigned lkv = lds_addr(lds) + hh * HR + hp * 5120;
    const unsigned wv = (FINAL ? WS_WOP : WS_WOF) + (ct * 2 + hh) * 51200 + lane * 16;
    const unsigned myfl = FKV + (u * 16 + qt) * 128;
    raw_bar();
    MARK(MK + 1);
    {
      u32x4 v[6];
#pragma unroll
      for (int i = 0; i < 6; i++) v[i] = ld128(lds + hh * HR + hp * 5120 + lane * 16 + i * 1024);
#pragma unroll
      for (int i = 0; i < 6; i++) st128<16>(rw, kvv + qt * KVC + i * 1024, clean(v[i]));
    }
    drain();
    unsigned cnt = 0;
    if (lane == 0) cnt = __hip_atomic_fetch_add(misc, 1u, __ATOMIC_RELAXED, __HIP_MEMORY_SCOPE_WORKGROUP);
    if (__builtin_amdgcn_readfirstlane(cnt) == 3 && lane < 16) st32<16>(rw, FKV + (u * 16 + lane) * 128 + qt * 4, 1u);
    MARK(MK + 2);
    auto ldkv = [&](int seq) {
      const int ch = seq == 0 ? qt : seq < 13 ? ((qt & 12) + 3 + seq) & 15 : (qt & 12) | ((qt + seq) & 3);
      dma6_sc1(kvv + ch * KVC, ws, lkv + (seq & 3) * KVC);
    };
    auto fetch = [&](int seq) {
      const unsigned src = kvv + ((((qt & 12) + 3 + seq) & 15)) * KVC;
      u32x4 v[6];
#pragma unroll 1
      for (int spins = 0; spins < (1 << 18); spins++) {
#pragma unroll
        for (int i = 0; i < 6; i++) v[i] = gl128<16>(rw, src + i * 1024);
        unsigned m = 0;
#pragma unroll
        for (int i = 0; i < 6; i++) m = umax4(m, v[i]);
        if (!__builtin_amdgcn_ballot_w64(m == EMPTY)) break;
        nap<2>();
      }
#pragma unroll
      for (int i = 0; i < 6; i++) *(LDS_AS u32x4*)(lds + hh * HR + hp * 5120 + seq * KVC + lane * 16 + i * 1024) = v[i];
    };
#pragma unroll 1
    for (int attempt = 0;; attempt++) {
      int c = -2;
      if (attempt) { ldkv(0); vm_bar<0>(); }
      else {
        fetch(1); MARK(MK + 3); lds_bar();
        MARK(MK + 4);
        fetch(2); poll16(rw, myfl, 12u << (((qt & 12) + 4) & 15)); ldkv(3); MARK(MK + 5); lds_bar();
        c = 1;
      }
#pragma unroll 1
      for (; c < 13; c++) {
        if (c == 2 && !attempt) poll16(rw, myfl, 0xFFFFu);
        if (!FINAL && !attempt) {
          if (c == 10) {
            unsigned n = 0;
            if (lane == 0) n = __hip_atomic_fetch_add(misc + 3, 1u, __ATOMIC_RELAXED, __HIP_MEMORY_SCOPE_WORKGROUP);
            if (__builtin_amdgcn_readfirstlane(n) == 3 && lane < 32) st32<16>(rw, WS_FL + 65536 + (lane < 16 ? (u * 16 + lane) * 128 + qt * 4 : (r16 * 16 + qt) * 128 + 64 + u * 4), 1u);
          }
        }
        if (c >= 2) { const int e = min(4 * (c - 2) + 2 * hp, 33); dma2(wv + e * 1024, ws, lds_addr(lds) + L_A + (hh * 35 + e) * 1024); }
        ldkv(c + 3);
        if (c == 0) vm_bar<12>();
        if (!FINAL && !attempt && c >= 2 && c < 9) {
          const u32x4 e = {EMPTY, EMPTY, EMPTY, EMPTY};
          const unsigned o = OB + LAYER2 + (((c - 2) * 256 + blockIdx.x) * 4 + lw) * 2048 + lane * 16;
          st128<16>(rw, o, e); st128<16>(rw, o + 1024, e);
          st128<16>(rw, ((qt & 3) < 2 && c < 8) ? kvv + LAYER2 + qt * KVC + (c - 2) * 1024 : WS_END, e);
          st128<16>(rw, c < 5 ? OB + ((4 * qt + lw) * 32 + 2 * u) * 1280 + min((c - 2) * 1024, 1536) + lane * 16 : WS_END, e);
          if (c == 2) vm_bar<12>(); else vm_bar<16>();
        } else if (c >= 2) vm_bar<8>(); else if (c >= 0) vm_bar<6>();
      }
      if (!FINAL && !attempt) poll16(rw, myfl + 65536 + hp * 64, 0xFFFFu);
      vm_bar<0>();
      dma_s(wv + (35 + 7 * hp) * 1024, wv + (35 + 7 * hp) * 1024, ws, lds_addr(lds) + hh * HR + 7 * hp * 1024);
      dma_s(wv + (39 + 7 * hp) * 1024, wv + (39 + 7 * hp) * 1024, ws, lds_addr(lds) + hh * HR + (4 + 7 * hp) * 1024);
      raw_bar();
      vm_bar<0>();
      if (attempt || !*(volatile LDS_AS unsigned*)(misc + 1)) break;
      raw_bar();
    }
    if (!FINAL) { raw_bar(); raw_bar(); }
  }
}

template <bool FINAL>
DI void compute(char* ws, u16* out SARG) {
  LAYER_VARS
  const int wm = w & 3, wn = w >> 2, h = lane >> 5;
  if (blockIdx.x < 64 || wn == 0) col_phase<!FINAL>(rw, lds, blockIdx.x < 64 ? 192 + 2 * blockIdx.x + wn : blockIdx.x - 64 SPASS);
  else if (FINAL) {
    drain();
    nap<YIDLE>();
    u32x2 sp[8];
#pragma unroll 1
    for (int spins = 0; spins < (1 << 18); spins++) {
      unsigned m = 0;
#pragma unroll
      for (int i = 0; i < 8; i++) sp[i] = gl64<16>(rw, WS_SSQ + (blockIdx.x & 7) * 32768 + (1024 + 4 * (wm * 64 + lane)) * 2 + i * 4096);
#pragma unroll
      for (int i = 0; i < 8; i++) m = max(max(m, sp[i][0]), sp[i][1]);
      if (!__builtin_amdgcn_ballot_w64(m == EMPTY)) break;
      nap<YSLEEP>();
    }
    put_scale(lds, 1, wm * 64 + lane, sp);
    lds_bar(); raw_bar(); raw_bar();
  } else {
    raw_bar(); raw_bar(); raw_bar();
  }
  MARK(MK + 10);
  lds_bar();
  {
    drain();
    unsigned n = 0;
    if (lane == 0) n = __hip_atomic_fetch_add(misc + 4, 1u, __ATOMIC_RELAXED, __HIP_MEMORY_SCOPE_WORKGROUP);
    if (n == 11) st32<16>(rw, AUX + blockIdx.x * 4, 1u);
  }

  {
    constexpr int NT = FINAL ? 5 : 4;
    f32x4 acc[2][5]; float rs[2];
    unsigned mo[2], a0[2], a1[2], a2[2], a_r[2];
#pragma unroll
    for (int m = 0; m < 2; m++) {
      const int q = r16 >> 2, pr = 16 * (m ^ wn) + r16;
      mo[m] = lds_addr(lds) + (8 * g + q) * 256 + (((2 * wm + (m ^ wn)) ^ (q | ((g & 1) << 2))) * 32) + (lane & 3) * 8;
      const unsigned qb = L_A + (wn * 4 + wm) * 1280, kb = wn * HR + wm * 1280, vr = wn * HR + 5120 + (32 * wm + pr) * 48 + (g == 3 ? 32 : g * 16);
      a0[m] = ((g >> 1) ? kb : qb) + (g & 1) * 512 + pr * 16;
      a1[m] = g == 2 ? qb + 1024 + pr * 8 : vr; a2[m] = g == 2 ? kb + 1024 + pr * 8 : vr + 8;
      a_r[m] = lds_addr(lds) + L_MISC + (32 * wm + pr) * 4;
      asm volatile("" : "+v"(a0[m]), "+v"(a1[m]), "+v"(a2[m]));
    }
    const unsigned a_w = a_r[0] + (g == (r16 >> 2) ? 0 : 512), a_v = lds_addr((lptr)(misc + 2));
#pragma unroll 1
    for (int att = 0;; att++) {
      unsigned am0 = mo[0], am1 = mo[1], as = lds_addr(lds) + 16384 + wn * 10240 + lane * 16, vote; int c_, t_;
      if (FINAL) asm volatile(GEMM_ASM(5) GEMM_OPS); else asm volatile(GEMM_ASM(4) GEMM_OPS);
      if (XNL || att || !__builtin_amdgcn_readfirstlane(vote)) break;
    }
    SB();
    MARK(MK + 0);
#pragma unroll
    for (int m = 0; m < 2; m++) *(LDS_AS u32x4*)(lds + a0[m]) = pk8(acc[m][0] * rs[m], acc[m][1] * rs[m]);
    asm volatile("s_nop 1" : "+v"(acc[0][2]), "+v"(acc[1][2]), "+v"(acc[0][3]), "+v"(acc[1][3]), "+v"(acc[0][NT - 1]), "+v"(acc[1][NT - 1]) :: "memory");
#pragma unroll
    for (int m = 0; m < 2; m++) {
      u32x2 hi = pk4(acc[m][3] * rs[m]);
      if (g == 3) hi = u32x2{0x3F80u, 0u};
      *(LDS_AS u32x2*)(lds + a1[m]) = pk4(acc[m][2] * rs[m]);
      *(LDS_AS u32x2*)(lds + a2[m]) = hi;
    }
    lds_bar();
    if (FINAL) {
      const unsigned x4 = WS_X4 + (((u * 16 + qt) * 4 + wm) * 2 + wn) * 1280;
      const u32x2 p0 = pk4(acc[0][NT - 1]), p1 = pk4(acc[1][NT - 1]), q0 = pk4(acc[0][3]), q1 = pk4(acc[1][3]);
      st128<16>(rw, x4 + lane * 16, clean(u32x4{p0[0], p0[1], p1[0], p1[1]}));
      if (g == 3) st128<16>(rw, x4 + 1024 + r16 * 16, clean(u32x4{q0[0], q0[1], q1[0], q1[1]}));
    }
  }
  MARK(MK + 1);
  const int head = 2 * u + wn, kh = w & 1;
  const lptr ring = lds + wn * HR;
  const int pblk = 128 * qt + 64 * hf + 32 * (wm >> 1);
  const unsigned r3t = WS_R3 + (64 * (wm & 1) + (pblk >> 5)) * (642 * 64);
  u32x2 x4v[3]; float dc = 0.f;
  unsigned os = OB + ((4 * qt + wm) * 32 + head) * 1280 + (((lane << 2) & 16) + ((lane >> 1) & 12) + (lane & 3)) * 8;
  unsigned o1 = OB + ((pblk >> 5) * 32 + 16 * wn) * 1280 + (g >> 1) * 1280 + (g & 1) * 512 + (wm & 1) * 256 + r16 * 16;
  unsigned o2 = OB + ((pblk >> 5) * 32 + 16 * wn) * 1280 + 2 * g * 1280 + 1024 + (wm & 1) * 128 + r16 * 8;
  u32x4 o3 = {0u, 0u, 0u, 0u};
  if (!FINAL) {
#pragma unroll
    for (int i = 0; i < 3; i++) {
      const int j = 80 * ct + 48 * wn + 16 * (wn ? (i & 1) : i) + r16;
      o3[i] = (j == 1 ? WS_RB0 + ((pblk >> 5) * 641 + ((pblk >> 5) & 1) + 1) * 64 : WS_RB1 + (j * RBP + pblk) * 2) + 16 * g + 8 * (wm & 1);
    }
  } else {
    o3[3] = r3t + 64 + r16 * 4;
#pragma unroll
    for (int i = 0; i < 3; i++) {
      const int cg = 12 * wn + 4 * ((i == 2 && wn) ? 0 : i) + g, pw = cg / 5, gq = cg - 5 * pw;
      o3[i] = WS_X4 + ((((2 * ct + (pw >> 1)) * 16 + qt) * 4 + 2 * hf + (wm >> 1)) * 2 + (pw & 1)) * 1280 + (gq * 16 + (trow(wm & 1, r16) & 15)) * 16 + ((((r16 >> 3) ^ pw) & 1) * 8);
    }
  }
  asm volatile("" : "+v"(os), "+v"(o1), "+v"(o2), "+v"(o3));
  f32x4 r[3];
  {
    const lptr qpa = lds + L_A + (wn * 4 + wm) * 1280, qpb = lds + L_A + (wn * 4 + (wm ^ 1)) * 1280;
    const u32x4 qa1 = ld128(qpa + lane * 16), qb1 = ld128(qpb + lane * 16);
    u32x2 qa2 = ld64(qpa + 1024 + (lane & 31) * 8), qb2 = ld64(qpb + 1024 + (lane & 31) * 8);
    if (h) { qa2 = u32x2{0u, 0u}; qb2 = qa2; }
    const unsigned ka = lds_addr(ring) + kh * 2560 + lane * 16, kb = lds_addr(ring) + kh * 2560 + 1024 + (lane & 31) * 8;
    const unsigned va = lds_addr(ring) + kh * 3072 + (4 * h + (r16 >> 2)) * 48 + ((g & 1) ? ((lane & 3) ? 40 : 32) : (lane & 3) * 8);
    float ma, mb;
    {
      const u32x4 k1 = ld128(ring + lane * 16); const u32x2 k2 = ld64(ring + 1024 + (lane & 31) * 8);
      ma = max16(scores(k1, k2, qa1, qa2)); mb = max16(scores(k1, k2, qb1, qb2));
    }
    for (int attempt = 0;; attempt++) {
      ma = fmaxf(ma, __shfl_xor(ma, 32)); mb = fmaxf(mb, __shfl_xor(mb, 32));
      f32x16 cia, cib, oa, ob;
#pragma unroll
      for (int i = 0; i < 16; i++) { cia[i] = -ma; cib[i] = -mb; }
      int cnt = 4;
      MARK(MK + 2);
      asm volatile(
          "ds_read_b128 v[80:83], v100\n\t"
          "ds_read_b64 v[84:85], v101\n\t"
          "ds_read_b64_tr_b16 v[86:87], v102 offset:5120\n\t"
          "ds_read_b64_tr_b16 v[88:89], v102 offset:5504\n\t"
          Z4(0) Z4(68) Z4(76) Z4(90)
          "s_waitcnt lgkmcnt(0)\n\t"
          "v_mfma_f32_32x32x16_bf16 v[32:47], v[80:83], v[94:97], v[16:31]\n\t"
          Z4(4) Z4(8) Z4(12) Z4(104) Z4(108) Z4(112) Z4(116)
          "v_mfma_f32_32x32x8_bf16 v[32:47], v[84:85], v[98:99], v[32:47]\n\ts_nop 9\n\t"
          "L_attn_%=:\n\t"
          CHUNK(0) CHUNK(1) CHUNK(2) CHUNK(3)
          "s_sub_u32 %[cnt], %[cnt], 1\n\ts_cmp_lg_u32 %[cnt], 0\n\ts_cbranch_scc1 L_attn_%=\n\t"
          "s_setprio 0\n\t" CVT(78,68) CVT(79,70) "s_waitcnt lgkmcnt(0)\n\ts_nop 1\n\t"
          "v_mfma_f32_32x32x16_bf16 v[104:119], v[90:93], v[76:79], v[104:119]\n\ts_nop 15\n\t"
          : "={v[0:15]}"(oa), "={v[104:119]}"(ob), [cnt] "+s"(cnt)
          : "{v[16:31]}"(cia), "{v[120:135]}"(cib), "{v[94:97]}"(qa1), "{v[98:99]}"(qa2), "{v[136:139]}"(qb1), "{v[140:141]}"(qb2),
            "{v100}"(ka), "{v101}"(kb), "{v102}"(va)
          : "memory", "scc", "v32", "v33", "v34", "v35", "v36", "v37", "v38", "v39", "v40", "v41", "v42", "v43", "v44", "v45", "v46", "v47",
            "v48", "v49", "v50", "v51", "v52", "v53", "v54", "v55", "v56", "v57", "v58", "v59", "v60", "v61", "v62", "v63", "v64", "v65",
            "v66", "v67", "v68", "v69", "v70", "v71", "v72", "v73", "v74", "v75", "v76", "v77", "v78", "v79", "v80", "v81", "v82", "v83",
            "v84", "v85", "v86", "v87", "v88", "v89", "v90", "v91", "v92", "v93");
      MARK(MK + 3);
      LDS_AS f32x4* ex = (LDS_AS f32x4*)(ring + L_EX + wm * 3072) + lane;
#pragma unroll
      for (int i = 0; i < 3; i++) ex[i * 64] = f32x4{ob[4 * i], ob[4 * i + 1], ob[4 * i + 2], ob[4 * i + 3]};
      f32x4 cs = {0.f, 0.f, 0.f, 0.f};
#pragma unroll
      for (int i = 0; i < 3; i++) cs += f32x4{oa[4 * i], oa[4 * i + 1], oa[4 * i + 2], oa[4 * i + 3]} + f32x4{ob[4 * i], ob[4 * i + 1], ob[4 * i + 2], ob[4 * i + 3]};
      if (!(fabsf(cs[0] + cs[1] + cs[2] + cs[3]) < INFINITY) || (h && !(oa[8] + ob[8] < 0x1p60f))) misc[1] = 1u;
      lds_bar();
      const unsigned vote = *(volatile LDS_AS unsigned*)(misc + 1);
      LDS_AS f32x4* ey = (LDS_AS f32x4*)(ring + L_EX + (wm ^ 1) * 3072) + lane;
#pragma unroll
      for (int i = 0; i < 3; i++) r[i] = ey[i * 64] + f32x4{oa[4 * i], oa[4 * i + 1], oa[4 * i + 2], oa[4 * i + 3]};
      if (attempt || !vote) break;
      lds_bar();
      ma = -INFINITY; mb = -INFINITY;
      for (int t = 0; t < 64; t++) {
        const unsigned kb_ = KV + (head * 16 + (t >> 2)) * KVC + (t & 3) * 1280;
        const u32x4 k1 = gl128<16>(rw, kb_ + lane * 16); const u32x2 k2 = gl64<16>(rw, kb_ + 1024 + (lane & 31) * 8);
        ma = fmaxf(ma, max16(scores(k1, k2, qa1, qa2))); mb = fmaxf(mb, max16(scores(k1, k2, qb1, qb2)));
      }
      raw_bar();
    }
  }
  {
    const unsigned lb = __float_as_uint(r[2][0]);
    const float il = __builtin_amdgcn_rcpf(__uint_as_float(__builtin_amdgcn_permlane32_swap(lb, lb, false, false)[1]));
    const unsigned sl8 = os & 255;
    st128<16>(rw, os + sl8 + h * 512, clean(pk8(r[0] * il, r[1] * il)));
    const u32x4 t = clean(pk8(r[2] * il, r[2] * il));
    st64<16>(rw, h ? WS_END : os + 1024, u32x2{t[0], t[1]});
  }
  MARK(MK + 4);
  const lptr wa = lds + L_A + wn * 35 * 1024 + lane * 16, wb = ring + lane * 16;
  constexpr int NPRE = 15;
  u32x4 bf[50];
  f32x4 acc[5] = {};
#pragma unroll
  for (int j = 0; j < NPRE; j++) bf[j] = ld128(wa + j * 1024);
#pragma unroll
  for (int j = 0; j < 5; j++) asm volatile("" : "+v"(acc[j]));
  if (FINAL) dc = __uint_as_float(gl32<16>(rw, o3[3]));
#pragma unroll 1
  for (int spins = 0; spins < (1 << 18); spins++) {
#pragma unroll
    for (int i = 0; i < 3; i++) x4v[i] = gl64<16>(rw, o3[i]);
    if (!FINAL || !__builtin_amdgcn_ballot_w64(umax4(max(x4v[0][0], x4v[0][1]), u32x4{x4v[1][0], x4v[1][1], x4v[2][0], x4v[2][1]}) == EMPTY)) break;
    nap<8>();
  }
  drain();
  nap<ODELAY>();
  MARK(MK + 5);
  {
    u32x4 af[10];
    auto ldf = [&](int i) {
      if (i < 8) af[i] = __builtin_amdgcn_raw_buffer_load_b128(rw, o1, i * 2 * 1280, 16);
      else { const u32x2 a = __builtin_amdgcn_raw_buffer_load_b64(rw, o2, (i - 8) * 8 * 1280, 16), b = __builtin_amdgcn_raw_buffer_load_b64(rw, o2, ((i - 8) * 8 + 1) * 1280, 16);
             af[i] = u32x4{a[0], a[1], b[0], b[1]}; }
    };
#pragma unroll
    for (int i = 0; i < 10; i++) ldf(i);
#pragma unroll 1
    for (int spins = 0; spins < (1 << 18); spins++) {
      unsigned m = 0;
#pragma unroll
      for (int i = 0; i < 10; i++) m = umax4(m, af[i]);
      if (!__builtin_amdgcn_ballot_w64(m == EMPTY)) break;
      nap<RSLEEP>();
#pragma unroll
      for (int i = 0; i < 10; i++) if (__builtin_amdgcn_ballot_w64(umax4(0, af[i]) == EMPTY)) ldf(i);
    }
    MARK(MK + 8);
#pragma unroll
    for (int i = 0; i < 10; i++) asm volatile("" : "+v"(af[i]));
    asm volatile("s_nop 1" : "+v"(acc[0]), "+v"(acc[1]), "+v"(acc[2]), "+v"(acc[3]), "+v"(acc[4]));
    SB();
#pragma unroll
    for (int i = 0; i < 10; i++) {
#pragma unroll
      for (int a = 0; a < 5; a++) {
        if (FINAL) mfma16v(acc[a], bf[5 * i + a], af[i]); else mfma16v(acc[a], af[i], bf[5 * i + a]);
        const int j = 5 * i + a + NPRE; if (j < 50) bf[j] = ld128(j < 35 ? wa + j * 1024 : wb + (j - 35) * 1024);
        SB();
      }
    }
  }
  asm volatile("s_nop 7\n\ts_nop 1" : "+v"(acc[0]), "+v"(acc[1]), "+v"(acc[2]), "+v"(acc[3]), "+v"(acc[4]) :: "memory");
  MARK(MK + 6);
  {
    LDS_AS f32x4* ex = (LDS_AS f32x4*)(ring + L_EX + 12288 + wm * 3072) + lane;
    LDS_AS f32x4* ey = (LDS_AS f32x4*)(lds + (wn ^ 1) * HR + L_EX + 12288 + wm * 3072) + lane + 64 * wn;
    ex[0] = acc[2]; ex[64] = acc[3]; ex[128] = acc[4];
    dc *= 0.039528470752104741f;
#pragma unroll
    for (int i = 0; i < 3; i++) { acc[i] += f32x4{bflo(x4v[i][0]), bfhi(x4v[i][0]), bflo(x4v[i][1]), bfhi(x4v[i][1])}; if (FINAL) acc[i] += dc; }
    lds_bar();
#pragma unroll
    for (int i = 0; i < 3; i++) acc[i] += ey[64 * i];
  }
  if (FINAL) {
    const rsrc_t ro = mk_rsrc(out, 2048 * 640 * 2);
    const unsigned op = (rowof(pblk + trow(wm & 1, r16)) * 640 + 80 * ct + 48 * wn + 4 * g) * 2;
#pragma unroll
    for (int i = 0; i < 3; i++)
      if (i < 2 || wn == 0) st64<OUTAUX>(ro, op + 32 * i, pk4(acc[i]));
  } else {
    f32x4 sq = {0.f, 0.f, 0.f, 0.f};
#pragma unroll
    for (int i = 0; i < 3; i++)
      if (i < 2 || wn == 0) {
        const int j = 80 * ct + 48 * wn + 16 * i + r16;
        st128<16>(rw, r3t + j * 64 + g * 16, clean(asu(acc[i])));
        if (j != 1) sq += acc[i] * acc[i];
      }
#pragma unroll
    for (int e = 0; e < 4; e++) sq[e] = row16_sum(sq[e]);
    LDS_AS float* sl = (LDS_AS float*)(lds + L_MISC);
    if (r16 == 0) *(LDS_AS f32x4*)(sl + wn * 64 + 32 * (wm & 1) + 16 * (wm >> 1) + 4 * g) = sq;
    lds_bar();
    if (w == 0) {
      LDS_AS f32x4* s4 = (LDS_AS f32x4*)sl + 2 * (lane & 7);
      st128<16>(rw, WS_SSQ + (lane >> 3) * 32768 + (ct * 2048 + 1024 * ((lane >> 2) & 1) + 64 * qt + 32 * hf + 8 * (lane & 3)) * 2, clean(pk8(s4[0] + s4[16], s4[1] + s4[17])));
    }
  }
  MARK(MK + 7);
}

__global__ void __launch_bounds__(768) k_main(const u16* x, const u16* wr, const u16* wi, const u16* wqkv, const u16* wo, char* ws, u16* out) {
  STAMP(1);
  if (wave_id() >= 8) {
    extern __shared__ __attribute__((aligned(16))) char smem[];
    const unsigned long long t0 = __builtin_amdgcn_s_memrealtime();
    const int job = blockIdx.x;
    if (job < 144) raw_bar();
    const rsrc_t rw = mk_rsrc(ws, WS_END);
    __builtin_amdgcn_s_setprio(3);
    if (threadIdx.x < 528) ((LDS_AS unsigned*)((lptr)smem + L_MISC + 1024))[threadIdx.x - 512] = 0u;
    u32x2 tag = gl64<16>(rw, WS_EP);
    tag[0] += 1u;
    if (job < 228) raw_bar();
    if (job < 64) raw_bar();
    asm volatile("s_waitcnt vmcnt(0) lgkmcnt(0)\n\ts_barrier" : "+v"(tag) :: "memory");
    if (wave_id() == 8) {
      if (threadIdx.x < 520) st64<16>(rw, WS_F0 + (threadIdx.x - 512) * 2048 + job * 8, tag);
      MARK(9);
      if (job >= 248) {
        while (__builtin_amdgcn_s_memrealtime() - t0 < TPOLL) nap<2>();
        poll_tag(rw, tag, 0xFE0FFFF00000FFFFull);
        if (threadIdx.x < 544) st64<16>(rw, WS_GO + ((threadIdx.x - 512) * 8 + (job & 7)) * 64, tag);
      }
#pragma unroll 1
      for (int spins = 0; spins < (1 << 20); spins++) {
        const u32x2 v = gl64<16>(rw, WS_GO + job * 64);
        if (__builtin_amdgcn_readfirstlane((v[0] ^ tag[0]) | (v[1] ^ tag[1])) == 0) break;
        nap<GSLEEP>();
      }
      MARK(10);
    }
    if (wave_id() > 8) {
    while (__builtin_amdgcn_s_memrealtime() - t0 < TFILL) nap<2>();
    const int i0 = blockIdx.x * 192 + threadIdx.x - 576;
    for (int pc = i0; pc < 640 * 80; pc += 192 * 256) {
      const int n = pc % 640, kq = pc / 640;
      const int u_ = n / 40, rn = n - 40 * u_, hn = rn / 20, dn = rn - 20 * hn, gq = dn >> 2;
      const int T = gq < 4 ? 8 + hn : 4 * hn + 3, gs = gq < 4 ? gq : 3;
      float v[8];
#pragma unroll
      for (int e = 0; e < 4; e++) {
        const float a = (float)((freq_of(kq >> 2, 4 * (kq & 3) + e) * n) % 640) * (1.f / 640.f);
        v[2 * e] = 0.055901699437494742f * __builtin_amdgcn_cosf(a);
        v[2 * e + 1] = -0.055901699437494742f * __builtin_amdgcn_sinf(a);
      }
      if (kq == 0) { v[0] = (n & 1) ? -0.039528470752104741f : 0.039528470752104741f; v[1] = 0.039528470752104741f; }
      st128<16>(rw, WS_WQ + ((u_ * 10 + T) * 20 + (kq >> 2)) * 1024 + ((kq & 3) * 16 + 4 * gs + (dn & 3)) * 16,
                  u32x4{pk2(v[0], v[1]), pk2(v[2], v[3]), pk2(v[4], v[5]), pk2(v[6], v[7])});
    }
#pragma unroll
    for (int it = 0; it < 4; it++) {
      const int k = it * 49152 + i0, b = k / 1408;
      st128<16>(rw, k < 128 * 1408 ? WS_KV + b * 4 * KVC + (k - b * 1408) * 16 : WS_END, u32x4{EMPTY, EMPTY, EMPTY, EMPTY});
    }
    }
    raw_bar();
    loader<false>(ws, tag SPASS);
    loader<true>(ws, tag SPASS);
  } else {
    prep(x, wr, wi, wqkv, wo, ws);
    asm volatile("s_waitcnt vmcnt(0) lgkmcnt(0)\n\ts_barrier" ::: "memory");
    compute<false>(ws, out SPASS);
    compute<true>(ws, out SPASS);
  }
}

#define STOPAT(n)
extern "C" void kernel_launch(void* const* d_in, const int* in_sizes, int n_in, void* d_out, int out_size, void* d_ws, size_t ws_size,
                              hipStream_t stream) {
  const u16* x = (const u16*)d_in[0];
  const u16* wr = (const u16*)d_in[1];
  const u16* wi = (const u16*)d_in[2];
  const u16* wqkv = (const u16*)d_in[4];
  const u16* wo = (const u16*)d_in[5];
  char* ws = (char*)d_ws;
  (void)hipFuncSetAttribute((const void*)k_main, hipFuncAttributeMaxDynamicSharedMemorySize, LDS_FUSED);
  k_main<<<256, 768, LDS_FUSED, stream>>>(x, wr, wi, wqkv, wo, ws, (u16*)d_out);
}
```

```cpp
#include <hip/hip_runtime.h>
#include <hip/hip_bf16.h>

typedef unsigned short u16;
typedef __attribute__((ext_vector_type(16))) float f32x16;
typedef __attribute__((ext_vector_type(4))) float f32x4;
typedef __attribute__((ext_vector_type(2))) float f32x2;
typedef __attribute__((ext_vector_type(8))) __bf16 bf16x8;
typedef __attribute__((ext_vector_type(2))) __bf16 bf16x2;
typedef __attribute__((ext_vector_type(4))) short s16x4;
typedef __attribute__((ext_vector_type(4))) unsigned u32x4;
typedef __attribute__((ext_vector_type(2))) unsigned u32x2;
#define DI __device__ __forceinline__
#define LDS_AS __attribute__((address_space(3)))
typedef LDS_AS char* lptr;

constexpr double cx_cos_small(double x) { double x2 = x * x, t = 1, s = 1; for (int i = 1; i < 12; i++) { t *= -x2 / ((2 * i - 1) * (2 * i)); s += t; } return s; }
constexpr double cx_sin_small(double x) { double x2 = x * x, t = x, s = x; for (int i = 1; i < 12; i++) { t *= -x2 / ((2 * i) * (2 * i + 1)); s += t; } return s; }
constexpr double cx_cos640(int m) {
  m %= 640; if (m > 320) m = 640 - m; double sg = 1; if (m > 160) { m = 320 - m; sg = -1; }
  const double w = 6.283185307179586476925 / 640; return sg * (m <= 80 ? cx_cos_small(w * m) : cx_sin_small(w * (160 - m)));
}
constexpr u16 cx_bf16(double d) { float f = (float)d; unsigned u = __builtin_bit_cast(unsigned, f); u += 0x7FFFu + ((u >> 16) & 1u); return (u16)(u >> 16); }
struct Cos640 { double v[640]; };
constexpr Cos640 mk_cos() { Cos640 c{}; for (int m = 0; m < 640; m++) c.v[m] = cx_cos640(m); return c; }
constexpr Cos640 COS = mk_cos();
__host__ __device__ constexpr int kf_of(int G, int i) { return G ? G + 40 * i : (i == 0 ? 320 : i < 8 ? 40 * i : 40 * i - 300); }
__host__ __device__ constexpr int freq_of(int G, int i) { return kf_of(G, i) > 320 ? 640 - kf_of(G, i) : kf_of(G, i); }
constexpr double SA = 1.91318, SA0 = 1.9140625, S64 = 1.86954, S32 = 1.6245;
constexpr double a1val(int out, int a) {
  if (out >= 40 || a >= 40) return 0;
  const int G = out >> 1, part = out & 1;
  if (G == 0) return (part && (a & 1)) ? -SA0 : SA0;
  return SA * COS.v[(16 * a * G + 160 * part) % 640];
}
constexpr double m2val(int G, int o, int k) {
  const int i = o >> 1, po = o & 1, part = k >> 4, b = k & 15, kf = kf_of(G, i);
  const double c = COS.v[(b * kf) % 640], sn = COS.v[(b * kf + 480) % 640];
  if (G == 0) {
    if (part != (i >= 8)) return 0;
    if (i == 0) return 0.039528470752104741 / SA0 * (po ? 1.0 : c);
    return 0.055901699437494742 / SA0 * (po ? -sn : c);
  }
  const double v = po ? (part ? c : -sn) : (part ? sn : c);
  return 0.055901699437494742 / SA * ((po && kf > 320) ? -v : v);
}
struct DTab { u16 a1[3][64][8]; u16 a1b[3][64][4]; u16 pad[256]; u16 m2[20][2][64][8]; };
constexpr DTab mk_dtab() {
  DTab t{};
  for (int l = 0; l < 64; l++) {
    const int r = l & 15, g = l >> 4;
    for (int T = 0; T < 3; T++) {
      for (int e = 0; e < 8; e++) t.a1[T][l][e] = cx_bf16(a1val(16 * T + r, 16 * (e >> 2) + 4 * g + (e & 3)));
      for (int e = 0; e < 4; e++) t.a1b[T][l][e] = cx_bf16(a1val(16 * T + r, 32 + 4 * g + e));
    }
    for (int G = 0; G < 20; G++) for (int T = 0; T < 2; T++) for (int e = 0; e < 8; e++)
      t.m2[G][T][l][e] = cx_bf16(m2val(G, 8 * (r >> 2) + 4 * T + (r & 3), 8 * g + e));
  }
  return t;
}
static __device__ const __attribute__((aligned(1024))) DTab g_dt = mk_dtab();

struct CTab { u16 v[10 * 512]; };
constexpr CTab mk_ctab() {
  CTab c{};
  for (int f = 0; f < 10; f++) for (int l = 0; l < 64; l++) for (int e = 0; e < 8; e++) {
    const int r = l & 15, g = l >> 4;
    const int m = f < 8 ? (8 * g + e) * (16 * (f >> 1) + r) : 2 * r * (16 * (e >> 2) + 4 * g + (e & 3));
    c.v[(f * 64 + l) * 8 + e] = cx_bf16((f < 8 ? S64 : S32) * COS.v[10 * ((m + (f & 1) * 48) & 63)]);
  }
  return c;
}
static __device__ const __attribute__((aligned(1024))) CTab g_ct = mk_ctab();
constexpr float RSK = (float)(2048.0 * S64 * S64 * S32 * S32 * 2048.0 * S64 * S64 * S32 * S32 / 640.0);

constexpr int RP = 2048 + 64, RBP = 2048 + 128;
constexpr unsigned RBSZ = 640 * RBP * 2;
constexpr unsigned WS_FL = 0;
constexpr unsigned WS_AUX = WS_FL + 3 * 32768;
constexpr unsigned WS_F0 = WS_AUX + 2048;
constexpr unsigned WS_GO = WS_F0 + 8 * 2048;
constexpr unsigned WS_EP = WS_GO + 256 * 64;
constexpr unsigned WS_RS1 = WS_EP + 256;
constexpr unsigned WS_RB0 = WS_RS1 + 8192;
constexpr unsigned WS_RB1 = WS_RB0 + RBSZ;
constexpr unsigned WS_WF = WS_RB1 + RBSZ;
constexpr unsigned WS_WQ = WS_WF + 320 * 8192;
constexpr unsigned WS_WOF = WS_WQ + 16 * 10 * 20 * 1024;
constexpr unsigned WS_WOP = WS_WOF + 40 * 20 * 1024;
constexpr int KVC = 11264;
constexpr unsigned WS_KV = WS_WOP + 40 * 20 * 1024;
constexpr unsigned WS_O = WS_KV + 32 * 16 * KVC;
constexpr unsigned LAYER2 = 32 * 16 * KVC + 32 * 64 * 1280;
constexpr unsigned WS_X4 = WS_KV + 2 * LAYER2;
constexpr unsigned WS_R3 = WS_X4 + 2048 * 640 * 2;
constexpr unsigned WS_RB2 = WS_R3 + 640 * RP * 4;
constexpr unsigned WS_SSQ = WS_RB2 + RBSZ;
constexpr unsigned EMPTY = 0xFFFFFFFFu;
constexpr unsigned WS_END = WS_SSQ + 8 * 2048 * 16;

constexpr int XDELAY = 20, K1DELAY = 40, YIDLE = 8, TPOLL = 250, TSLEEP = 1, GSLEEP = 2, TFILL = 400, YSLEEP = 4, XSLEEP = 1, RSLEEP = 2;

#define SB() __builtin_amdgcn_sched_barrier(0)
typedef __amdgpu_buffer_rsrc_t rsrc_t;
DI rsrc_t mk_rsrc(const void* p, unsigned bytes) { return __builtin_amdgcn_make_buffer_rsrc((void*)p, 0, bytes, 0x00020000); }
template <int AUX> DI void st128(rsrc_t r, unsigned off, u32x4 v) { __builtin_amdgcn_raw_buffer_store_b128(v, r, off, 0, AUX); }
template <int AUX> DI void st64(rsrc_t r, unsigned off, u32x2 v) { __builtin_amdgcn_raw_buffer_store_b64(v, r, off, 0, AUX); }
template <int AUX> DI void st32(rsrc_t r, unsigned off, unsigned v) { __builtin_amdgcn_raw_buffer_store_b32(v, r, off, 0, AUX); }
template <int AUX> DI u32x4 gl128(rsrc_t r, unsigned off) { return __builtin_amdgcn_raw_buffer_load_b128(r, off, 0, AUX); }
template <int AUX> DI u32x2 gl64(rsrc_t r, unsigned off) { return __builtin_amdgcn_raw_buffer_load_b64(r, off, 0, AUX); }
template <int AUX> DI unsigned gl32(rsrc_t r, unsigned off) { return __builtin_amdgcn_raw_buffer_load_b32(r, off, 0, AUX); }
DI u32x4 asu(f32x4 v) { return __builtin_bit_cast(u32x4, v); }
DI f32x4 asf(u32x4 v) { return __builtin_bit_cast(f32x4, v); }
DI unsigned pk2(float a, float b) { f32x2 v = {a, b}; return __builtin_bit_cast(unsigned, __builtin_convertvector(v, bf16x2)); }
DI u32x2 pk4(f32x4 a) { return u32x2{pk2(a[0], a[1]), pk2(a[2], a[3])}; }
DI u32x4 pk8(f32x4 a, f32x4 b) { return u32x4{pk2(a[0], a[1]), pk2(a[2], a[3]), pk2(b[0], b[1]), pk2(b[2], b[3])}; }
DI u32x4 clean(u32x4 v) { return __builtin_elementwise_min(v, u32x4{EMPTY - 1, EMPTY - 1, EMPTY - 1, EMPTY - 1}); }
DI unsigned umax4(unsigned m, u32x4 v) { return max(max(max(m, v[0]), v[1]), max(v[2], v[3])); }
DI float bflo(unsigned u) { return __uint_as_float(u << 16); }
DI float bfhi(unsigned u) { return __uint_as_float(u & 0xFFFF0000u); }
DI int rowof(int p) { return 32 * (((p >> 5) & 0x3C) + (p & 3)) + ((p << 2) & 16) + ((p >> 3) & 15); }
DI int rpos(int p) { return ((p << 8) & 1024) + ((p >> 3) << 2) + (p & 3); }
DI int trow(int T, int r) { return 8 * (r >> 2) + 4 * T + (r & 3); }
template <int CTRL> DI float dpp_add(float v) { return v + __int_as_float(__builtin_amdgcn_update_dpp(0, __float_as_int(v), CTRL, 0xF, 0xF, true)); }
DI float row16_sum(float v) { v = dpp_add<0x128>(v); v = dpp_add<0x124>(v); v = dpp_add<0x122>(v); return dpp_add<0x121>(v); }
DI f32x4 mfma16(u32x4 a, u32x4 b, f32x4 c) {
  return __builtin_amdgcn_mfma_f32_16x16x32_bf16(__builtin_bit_cast(bf16x8, a), __builtin_bit_cast(bf16x8, b), c, 0, 0, 0);
}
DI void mfma16v(f32x4& c, u32x4 a, u32x4 b) { asm volatile("v_mfma_f32_16x16x32_bf16 %0, %1, %2, %0" : "+v"(c) : "v"(a), "v"(b)); }
DI u32x2 ldtr(lptr p) { return __builtin_bit_cast(u32x2, __builtin_amdgcn_ds_read_tr16_b64_v4i16((LDS_AS s16x4*)p)); }
DI u32x4 ld128(lptr p) { return *(LDS_AS u32x4*)p; }
DI u32x2 ld64(lptr p) { return *(LDS_AS u32x2*)p; }
#define GL(v, s, o, x) "global_load_lds_dwordx4 " v ", " s " offset:" #o x "\n\t"
#define M0SET "s_mov_b32 %[sv], m0\n\ts_mov_b32 m0, %[l]\n\ts_nop 2\n\t"
#define M0ADD(x) "s_add_u32 m0, m0, " x "\n\ts_nop 0\n\t"
#define M0RST "s_mov_b32 m0, %[sv]"
DI void dma2(unsigned v, const char* sb, unsigned l) { unsigned sv;
  asm volatile(M0SET GL("%[v]", "%[s]", 0, " sc1") GL("%[v]", "%[s]", 1024, " sc1") M0RST : [sv] "=&s"(sv) : [v] "v"(v), [s] "s"(sb), [l] "s"(l) : "memory"); }
DI void dma4(unsigned v, const char* sb, unsigned l) { unsigned sv;
  asm volatile(M0SET GL("%[v]", "%[s]", 0, "") GL("%[v]", "%[s]", 1024, "") GL("%[v]", "%[s]", 2048, "") GL("%[v]", "%[s]", 3072, "") M0RST
               : [sv] "=&s"(sv) : [v] "v"(v), [s] "s"(sb), [l] "s"(l) : "memory"); }
DI void dma6_sc1(unsigned v, const char* sb, unsigned l) { unsigned sv;
  asm volatile(M0SET GL("%[v]", "%[s]", 0, " sc1") GL("%[v]", "%[s]", 1024, " sc1") GL("%[v]", "%[s]", 2048, " sc1") M0ADD("3072")
               GL("%[w]", "%[s]", 0, " sc1") GL("%[w]", "%[s]", 1024, " sc1") GL("%[w]", "%[s]", 2048, " sc1") M0RST
               : [sv] "=&s"(sv) : [v] "v"(v), [w] "v"(v + 3072), [s] "s"(sb), [l] "s"(l) : "memory", "scc"); }
template <int N> DI void vm_bar() { asm volatile("s_waitcnt vmcnt(%0)\n\ts_barrier" ::"i"(N) : "memory"); }
DI void lds_bar() { asm volatile("s_waitcnt lgkmcnt(0)\n\ts_barrier" ::: "memory"); }
DI void raw_bar() { asm volatile("s_barrier" ::: "memory"); }
template <int N> DI void nap() { __builtin_amdgcn_s_sleep(N); asm volatile("" ::: "memory"); }
DI void drain() { asm volatile("s_waitcnt vmcnt(0)" ::: "memory"); }
#define CODE_ADDR(sym) ({ unsigned lo_, hi_; \
    asm volatile("s_getpc_b64 vcc\n\ts_add_u32 %0, vcc_lo, " sym "@rel32@lo+4\n\ts_addc_u32 %1, vcc_hi, " sym "@rel32@hi+12" : "=s"(lo_), "=s"(hi_) :: "vcc", "scc"); \
    (const unsigned*)(((unsigned long long)hi_ << 32) | lo_); })
#define SYM_MAIN "_Z6k_mainPKtS0_S0_S0_S0_PcPt"
constexpr int CODE_LINES = 368;
DI int wave_id() { return __builtin_amdgcn_readfirstlane(threadIdx.x >> 6); }
DI unsigned lds_addr(lptr p) { return (unsigned)(size_t)p; }

DI int kappa_to_i(int kap) {
  int ks = kap >> 5, g = (kap >> 3) & 3, e = kap & 7;
  if (ks < 16) return (2 * ks + (g >> 1)) * 20 + 8 * (e >> 2) + 4 * (g & 1) + (e & 3);
  return (8 * (ks - 16) + 2 * g + (e >> 2)) * 20 + 16 + (e & 3);
}

DI unsigned wo_frag(int nt, int ks) {
  const int ct = nt / 5, c5 = nt - 5 * ct, kh = ks < 16 ? ks >> 3 : (ks - 16) >> 1, i = ks < 16 ? ks & 7 : 8 + (ks & 1);
  const int a = kh ? (c5 >= 3 ? c5 - 3 : c5 + 2) : c5;
  return ((ct * 2 + kh) * 50 + 5 * i + a) * 1024;
}

constexpr int SLOT32 = 36864;
DI void dma_a(const unsigned* vm, const char* mb, unsigned l) { unsigned sv;
  asm volatile(M0SET GL("%[a0]", "%[m]", 0, " sc1") GL("%[a1]", "%[m]", 1024, " sc1") GL("%[a2]", "%[m]", 2048, " sc1") GL("%[a3]", "%[m]", 3072, " sc1") M0RST
               : [sv] "=&s"(sv) : [a0] "v"(vm[0]), [a1] "v"(vm[1]), [a2] "v"(vm[2]), [a3] "v"(vm[3]), [m] "s"(mb), [l] "s"(l) : "memory"); }
DI void dma_s(unsigned v0, unsigned v1, const char* sb, unsigned l) { unsigned sv;
  asm volatile(M0SET GL("%[v0]", "%[s]", 0, " sc1") GL("%[v0]", "%[s]", 1024, " sc1") GL("%[v1]", "%[s]", 2048, " sc1") GL("%[v1]", "%[s]", 3072, " sc1") M0RST
               : [sv] "=&s"(sv) : [v0] "v"(v0), [v1] "v"(v1), [s] "s"(sb), [l] "s"(l) : "memory"); }
DI void dma1_sc1(unsigned v, const char* sb, unsigned l) { unsigned sv;
  asm volatile(M0SET GL("%[v]", "%[s]", 0, " sc1") M0RST : [sv] "=&s"(sv) : [v] "v"(v), [s] "s"(sb), [l] "s"(l) : "memory"); }
DI void dma1(unsigned v, const char* sb, unsigned l) { unsigned sv;
  asm volatile(M0SET GL("%[v]", "%[s]", 0, "") M0RST : [sv] "=&s"(sv) : [v] "v"(v), [s] "s"(sb), [l] "s"(l) : "memory"); }

constexpr int L_I2 = 40 * 1056, LDS_PREP = L_I2 + 20 * 2064 + 1024;
DI void prep(const u16* x, const u16* wr, const u16* wi, const u16* wqkv, const u16* wo, char* ws) {
  extern __shared__ __attribute__((aligned(16))) char smem[];
  const lptr lds = (lptr)smem;
  const int tid = threadIdx.x, lane = tid & 63, w = wave_id(), g = lane >> 4, r16 = lane & 15;
  const int job = blockIdx.x;
  if (__builtin_expect(job < 144, 1)) {
    const bool cols = job >= 64 && job < 124;
    const int cj = job - 64, cb = cj < 48 ? (cj & 48) + 2 * (cj & 7) + ((cj >> 3) & 1) : cj;
    u32x4 a1[3], m2[3][2]; u32x2 a1b[3];
    if (w < 5) dma1(tid * 16, (const char*)&g_dt, lds_addr(lds) + LDS_PREP + w * 1024);
    if (cols) {
#pragma unroll
      for (int i = 0; i < 5; i++) dma1((16 * (5 * w + i) + (lane >> 2)) * 3840 + cb * 64 + (lane & 3) * 16, (const char*)wqkv, lds_addr(lds) + (5 * w + i) * 1056);
    } else {
      const char* src = (const char*)(job < 64 ? x : wo);
      const int sl = 8 * (w & 3) + 4 * (w >> 2);
      src += (job < 64 ? rowof(32 * job + sl) : kappa_to_i(32 * (job - 124) + sl)) * 1280;
      const int st = (job < 64 ? 31 : 0) * 1280;
#pragma unroll
      for (int i = 0; i < 5; i++) dma1(1024 * i + lane * 16 + ((i == 4 || lane < 16 * i) ? i - 1 : i) * st, src, lds_addr(lds) + (5 * w + i) * 1024);
    }
    unsigned so[3][2]; float sc[2] = {1.f, 1.f};
    if (cols) {
#pragma unroll
      for (int m = 0; m < 2; m++) {
        const int c = 32 * cb + 16 * m + r16;
        const int which = c / 640, rem = c - which * 640, head = rem / 20, d = rem - head * 20, grp = d >> 2;
        const int T = grp < 4 ? (which == 2 ? 2 : 0) + (grp & 1) : (which == 1 ? 3 : 2), gs = grp < 4 ? (which == 1 ? 2 : 0) + (grp >> 1) : (which == 2 ? 3 : 2);
        if (which == 0) sc[m] = 0.22360679774997896f * 1.4426950408889634f;
#pragma unroll
        for (int i = 0; i < 3; i++) so[i][m] = WS_WQ + (((head >> 1) * 10 + 4 * (head & 1) + T) * 20 + w + 8 * i) * 1024 + (g * 16 + 4 * gs + (d & 3)) * 16;
      }
    } else if (job < 64) {
#pragma unroll
      for (int i = 0; i < 3; i++)
#pragma unroll
        for (int T = 0; T < 2; T++) so[i][T] = WS_RB0 + (job * 641 + (job & 1) + 32 * (w + 8 * i) + trow(T, r16)) * 64 + g * 16;
    } else {
#pragma unroll
      for (int i = 0; i < 3; i++) {
        const unsigned f = wo_frag(2 * (w + 8 * i) + (r16 >> 3), job - 124);
#pragma unroll
        for (int T = 0; T < 2; T++) so[i][T] = WS_WOF + f + (g * 16 + (trow(T, r16) & 15)) * 16;
      }
    }
    if (w >= 4) { so[2][0] = WS_END; so[2][1] = WS_END; }
    asm volatile("" : "+v"(so[0][0]), "+v"(so[0][1]), "+v"(so[1][0]), "+v"(so[1][1]), "+v"(so[2][0]), "+v"(so[2][1]), "+v"(sc[0]), "+v"(sc[1]));
    const f32x4 z4 = {0.f, 0.f, 0.f, 0.f};
    const lptr i2 = lds + L_I2;
    drain();
#pragma unroll
    for (int i = 0; i < 3; i++)
#pragma unroll
      for (int T = 0; T < 2; T++)
        asm volatile("global_load_dwordx4 %0, %1, %2" : "=v"(m2[i][T]) : "v"(((((i < 2 || w < 4) ? w + 8 * i : w) * 2 + T) * 64 + lane) * 16), "s"(&g_dt.m2[0][0][0][0]) : "memory");
    raw_bar();
#pragma unroll
    for (int T = 0; T < 3; T++) { a1[T] = ld128(lds + LDS_PREP + T * 1024 + lane * 16); a1b[T] = ld64(lds + LDS_PREP + 3072 + T * 512 + lane * 8); }
    f32x4 t1[4][3];
    if (cols) {
      const lptr src = lds + (4 * g + (r16 >> 2)) * 1056 + (w >> 1) * 256 + (w & 1) * 32 + (lane & 3) * 8, srb = src + (32 - 8 * (g >> 1)) * 1056;
#pragma unroll
      for (int i = 0; i < 4; i++) {
        const u32x2 f0 = ldtr(src + i * 64), f1 = ldtr(src + 16 * 1056 + i * 64), f2 = ldtr(srb + i * 64);
#pragma unroll
        for (int T = 0; T < 3; T++) t1[i][T] = mfma16(u32x4{a1b[T][0], a1b[T][1], 0u, 0u}, u32x4{f2[0], f2[1], 0u, 0u}, mfma16(a1[T], u32x4{f0[0], f0[1], f1[0], f1[1]}, z4));
      }
      const int xo = ((w >> 1) * 8) ^ (((r16 >> 1) & 3) * 16);
      const lptr dst = i2 + 2 * g * 2064 + (16 * (w & 1) + r16) * 64;
#pragma unroll
      for (int T = 0; T < 3; T++)
#pragma unroll
        for (int e = 0; e < 4; e++)
          if (T < 2 || g < 2) *(LDS_AS u32x2*)(dst + (8 * T + (e >> 1)) * 2064 + (xo ^ ((e & 1) * 32))) = u32x2{pk2(t1[0][T][e], t1[1][T][e]), pk2(t1[2][T][e], t1[3][T][e])};
    } else {
      const lptr src = lds + w * 5120 + lane * 8, srb = lds + w * 5120 + 1024 + (lane & 31) * 8;
#pragma unroll
      for (int i = 0; i < 4; i++) {
        const u32x2 f0 = ldtr(src + i * 1280), f1 = ldtr(src + i * 1280 + 512), f2 = ldtr(srb + i * 1280);
#pragma unroll
        for (int T = 0; T < 3; T++) t1[i][T] = mfma16(u32x4{f2[0], f2[1], 0u, 0u}, u32x4{a1b[T][0], a1b[T][1], 0u, 0u}, mfma16(u32x4{f0[0], f0[1], f1[0], f1[1]}, a1[T], z4));
      }
      const int xo = ((r16 & 1) * 32 + g * 8) ^ ((w & 1) * 32);
      const lptr dst = i2 + (r16 >> 1) * 2064 + w * 256;
#pragma unroll
      for (int i = 0; i < 4; i++)
#pragma unroll
        for (int T = 0; T < 3; T++)
          if (T < 2 || r16 < 8) *(LDS_AS u32x2*)(dst + T * 8 * 2064 + i * 64 + (xo ^ ((i >> 1) * 16))) = pk4(t1[i][T]);
    }
    lds_bar();
    const rsrc_t rw = mk_rsrc(ws, WS_END);
    asm volatile("s_waitcnt vmcnt(0)" : "+v"(m2[0][0]), "+v"(m2[0][1]), "+v"(m2[1][0]), "+v"(m2[1][1]), "+v"(m2[2][0]), "+v"(m2[2][1]));
    f32x4 sq[2] = {z4, z4};
    u32x4 f[3][2];
#pragma unroll
    for (int i = 0; i < 3; i++) {
      const lptr fp = i2 + ((i < 2 || w < 4) ? w + 8 * i : 0) * 2064 + r16 * 64 + ((g ^ ((r16 >> 1) & 3)) * 16);
      f[i][0] = ld128(fp); f[i][1] = ld128(fp + 1024);
    }
    if (cols) {
#pragma unroll
      for (int i = 0; i < 3; i++)
#pragma unroll
        for (int m = 0; m < 2; m++) st128<16>(rw, so[i][m], pk8(mfma16(m2[i][0], f[i][m], z4) * sc[m], mfma16(m2[i][1], f[i][m], z4) * sc[m]));
    } else {
#pragma unroll
      for (int i = 0; i < 3; i++)
#pragma unroll
        for (int T = 0; T < 2; T++) {
          const f32x4 d0 = mfma16(f[i][0], m2[i][T], z4), d1 = mfma16(f[i][1], m2[i][T], z4);
          st128<16>(rw, so[i][T], pk8(d0, d1));
          const float k = ((i == 2 && w >= 4) || (i == 0 && T == 0 && tid < 64 && r16 == 1)) ? 0.f : 1.f;
          sq[0] += d0 * d0 * k; sq[1] += d1 * d1 * k;
        }
    }
    LDS_AS float* sl = (LDS_AS float*)(lds + LDS_PREP - 1024);
    if (job < 64) {
#pragma unroll
      for (int m = 0; m < 2; m++) {
#pragma unroll
        for (int e = 0; e < 4; e++) sq[m][e] = row16_sum(sq[m][e]);
        if (r16 == 0) *(LDS_AS f32x4*)(sl + w * 32 + 8 * g + 4 * m) = sq[m];
      }
    }
    if (job < 64) {
      lds_bar();
      if (tid < 32) {
        float v = 0.f;
#pragma unroll
        for (int i = 0; i < 8; i++) v += sl[i * 32 + tid];
        st32<16>(rw, WS_RS1 + rpos(32 * job + tid) * 4, __float_as_uint(__builtin_amdgcn_rsqf((v + 640.f * 1e-5f) * RSK)));
      }
    }
    return;
  }
  const int mi = job - 144;
  const rsrc_t rw = mk_rsrc(ws, WS_END);
  if (mi < 64) {
    const int m = mi >> 3, oc = mi & 7;
    const char* src = (const char*)((w >> 2) ? wi : wr) + (64 * (4 * m + (w & 3)) + 8 * oc) * 642;
    dma4(lane * 16, src, lds_addr(lds) + w * 5152);
    dma1(4096 + lane * 16, src, lds_addr(lds) + w * 5152 + 4096);
    if (lane == 0) dma1(5120, src, lds_addr(lds) + w * 5152 + 5120);
    vm_bar<0>();
    LDS_AS u16* L = (LDS_AS u16*)lds + (tid & 7) * 321;
#pragma unroll
    for (int it = 0; it < 5; it++) {
      const int c = (tid >> 3) + 64 * it, col = freq_of(c >> 4, c & 15);
      u32x4 o;
#pragma unroll
      for (int e = 0; e < 4; e++) o[e] = (unsigned)L[e * 2576 + col] | ((unsigned)L[(4 + e) * 2576 + col] << 16);
      st128<16>(rw, WS_WF + c * 8192 + (m * 64 + 8 * oc + (tid & 7)) * 16, o);
    }
#pragma unroll
    for (int it = 0; it < 3; it++) st128<16>(rw, WS_RB1 + ((it * 64 + mi) * 512 + tid) * 16, u32x4{EMPTY, EMPTY, EMPTY, EMPTY});
  } else if (mi < 84) {
    unsigned pf0;
    {
      const unsigned* cm = CODE_ADDR(SYM_MAIN);
      const unsigned* p = (mi & 8) ? (const unsigned*)&g_dt.m2[0][0][0][0] + min(tid, 319) * 32 : tid < 80 ? (const unsigned*)&g_ct + tid * 32 : cm + min(tid - 80, CODE_LINES - 1) * 32;
      asm volatile("global_load_dword %0, %1, off" : "=v"(pf0) : "v"(p) : "memory");
    }
    const char* src = (const char*)wo + kappa_to_i(32 * (mi - 64) + 8 * (w & 3) + 4 * (w >> 2)) * 1280;
    dma4(lane * 16, src, lds_addr(lds) + w * 5152);
    dma1(4096 + lane * 16, src, lds_addr(lds) + w * 5152 + 4096);
    asm volatile("s_waitcnt vmcnt(0)\n\ts_barrier" : "+v"(pf0) :: "memory");
#pragma unroll
    for (int i = 0; i < 5; i++) {
      const lptr a = lds + g * 5152 + (r16 >> 2) * 1280 + (5 * w + i) * 32 + (lane & 3) * 8;
      const u32x2 lo = ldtr(a), hi = ldtr(a + 4 * 5152);
      st128<16>(rw, WS_WOP + wo_frag(5 * w + i, mi - 64) + lane * 16, u32x4{lo[0], lo[1], hi[0], hi[1]});
    }
  } else {
    const int i0 = (mi - 84) * 512 + tid;
    nap<K1DELAY>();
    if (i0 < 6272) st128<16>(rw, WS_FL + i0 * 16, u32x4{0u, 0u, 0u, 0u});
#pragma unroll
    for (int it = 0; it < 6; it++) { const int i = 98304 + i0 + it * 14336; if (i < (int)RBSZ / 16) st128<16>(rw, WS_RB1 + i * 16, u32x4{EMPTY, EMPTY, EMPTY, EMPTY}); }
  }
}

#define PINF(x) asm volatile("" : "+v"(x))
DI void put_scale(lptr lds, int q, int tid, const u32x2* sp) {
  f32x4 r = {640.f * 1e-5f, 640.f * 1e-5f, 640.f * 1e-5f, 640.f * 1e-5f};
#pragma unroll
  for (int i = 0; i < 8; i++) r += f32x4{bflo(sp[i][0]), bfhi(sp[i][0]), bflo(sp[i][1]), bfhi(sp[i][1])};
#pragma unroll
  for (int e = 0; e < 4; e++) r[e] = __builtin_amdgcn_rsqf(r[e] * RSK);
  ((LDS_AS f32x4*)(lds + 2 * SLOT32))[q * 256 + tid] = r;
}
struct ColIn { u32x4 c64, s64, c32, s32, wf[2]; };
DI int col_of() { return blockIdx.x < 64 ? 192 + 2 * blockIdx.x + (wave_id() >> 2) : blockIdx.x - 64; }
DI void col_load(ColIn& k, rsrc_t rw, bool tab, bool filt) {
  int lane = threadIdx.x & 63; PINF(lane);
  const int w = wave_id() & 3;
  const u32x4* ct = (const u32x4*)&g_ct + lane;
  if (tab) { k.c64 = ct[(2 * w) * 64]; k.s64 = ct[(2 * w + 1) * 64]; k.c32 = ct[8 * 64]; k.s32 = ct[9 * 64]; }
  if (filt) {
#pragma unroll
    for (int t = 0; t < 2; t++) k.wf[t] = gl128<16>(rw, WS_WF + col_of() * 8192 + ((4 * t + (lane >> 4)) * 64 + 16 * w + (lane & 15)) * 16);
  }
}
template <bool FIRST>
DI void col_phase(rsrc_t rw, lptr lds, ColIn kin) {
  int lane = threadIdx.x & 63; PINF(lane);
  const int wv = wave_id(), w = wv & 3, q = wv >> 2, tid = w * 64 + lane, g = lane >> 4, r16 = lane & 15;
  const int c = col_of();
  const lptr img = lds + q * SLOT32;
  f32x4 xr[2], xi[2], rs[2]; u32x4 xb[2];
  u32x4 c64[2], s64[2], n64[2], c32[2], s32[2], n32[2];
  if (FIRST) col_load(kin, rw, true, false);
  SB();
  f32x4 t1c[2], t1s[2], t2c[2], t2s[2];
  {
    const float f1 = (float)(16 * w + r16) * (1.f / 2048.f), b1 = (float)(4 * g), f2 = (float)(16 * w + 4 * g) * (1.f / 2048.f), b2 = (float)r16;
#pragma unroll
    for (int t = 0; t < 2; t++) {
#pragma unroll
      for (int e = 0; e < 4; e++) {
        const float a1 = (b1 + (float)(16 * t + e)) * f1, a2 = (b2 + (float)(16 * t)) * (f2 + (float)e * (1.f / 2048.f));
        t1c[t][e] = __builtin_amdgcn_cosf(a1); t1s[t][e] = __builtin_amdgcn_sinf(-a1);
        t2c[t][e] = __builtin_amdgcn_cosf(a2); t2s[t][e] = __builtin_amdgcn_sinf(a2);
      }
      PINF(t1c[t]); PINF(t1s[t]); PINF(t2c[t]); PINF(t2s[t]);
    }
  }
  int wo_ = r16 * 128 + (((2 * w + (g >> 1)) ^ (r16 >> 1)) * 16) + (g & 1) * 8, ro_ = r16 * 128 + ((g ^ (r16 >> 1)) * 16), ro2 = ro_ ^ 64;
  PINF(wo_); PINF(ro_); PINF(ro2);
  {
    const unsigned par = (lane & 1) ? 0x80008000u : 0u;
    c64[0] = kin.c64; s64[0] = kin.s64; c32[0] = kin.c32; s32[0] = kin.s32;
    c64[1] = c64[0] ^ par; s64[1] = s64[0] ^ par; n64[0] = s64[0] ^ 0x80008000u; n64[1] = s64[1] ^ 0x80008000u;
    c32[1] = c32[0] ^ 0x80000000u; s32[1] = s32[0] ^ 0x80000000u; n32[0] = s32[0] ^ 0x80008000u; n32[1] = s32[0] ^ 0x00008000u;
    PINF(c64[1]); PINF(s64[1]); PINF(n64[0]); PINF(n64[1]); PINF(c32[1]); PINF(s32[1]); PINF(n32[0]); PINF(n32[1]);
  }
  SB();
  if (FIRST) {
    raw_bar();
#pragma unroll
    for (int t = 0; t < 2; t++) { xb[t] = gl128<16>(rw, WS_RB0 + ((tid >> 2) * 641 + ((tid >> 2) & 1) + 2 * c + t) * 64 + (tid & 3) * 16); rs[t] = asf(gl128<16>(rw, WS_RS1 + (1024 * t + 4 * tid) * 4)); }
    col_load(kin, rw, false, true);
    SB();
#pragma unroll
    for (int t = 0; t < 2; t++) {
      xr[t] = f32x4{bflo(xb[0][2 * t]), bfhi(xb[0][2 * t]), bflo(xb[0][2 * t + 1]), bfhi(xb[0][2 * t + 1])};
      xi[t] = f32x4{bflo(xb[1][2 * t]), bfhi(xb[1][2 * t]), bflo(xb[1][2 * t + 1]), bfhi(xb[1][2 * t + 1])};
    }
  } else {
    drain();
    u32x2 sp[8];
    const unsigned so = WS_SSQ + (blockIdx.x & 7) * 32768 + (1024 * q + 4 * tid) * 2, xo = WS_R3 + ((tid >> 2) * 642 + 2 * c) * 64 + (tid & 3) * 16;
    bool bs = true, bx = true;
#pragma unroll 1
    for (int spins = 0; spins < (1 << 18); spins++) {
      if (bx) {
#pragma unroll
        for (int t = 0; t < 2; t++) { xr[t] = asf(gl128<16>(rw, xo + 64 * 642 * 64 * t)); xi[t] = asf(gl128<16>(rw, xo + 64 * 642 * 64 * t + 64)); }
      }
      if (bs) {
#pragma unroll
        for (int i = 0; i < 8; i++) sp[i] = gl64<16>(rw, so + i * 4096);
      }
      unsigned m = 0;
#pragma unroll
      for (int t = 0; t < 2; t++) m = umax4(umax4(m, asu(xr[t])), asu(xi[t]));
      bx = __builtin_amdgcn_ballot_w64(m == EMPTY) != 0;
      m = 0;
#pragma unroll
      for (int i = 0; i < 8; i++) m = max(max(m, sp[i][0]), sp[i][1]);
      bs = __builtin_amdgcn_ballot_w64(m == EMPTY) != 0;
      if (!bs && !bx) break;
      nap<YSLEEP>();
    }
    put_scale(lds, q, tid, sp);
    LDS_AS f32x4* ex = (LDS_AS f32x4*)(lds + 2 * SLOT32) + tid;
    lds_bar();
    rs[0] = ex[0]; rs[1] = ex[256];
  }
#pragma unroll
  for (int t = 0; t < 2; t++) {
    u32x2 pb = pk4(xi[t] * rs[t]);
    if (c == 0) pb = u32x2{0u, 0u};
    *(LDS_AS u32x2*)(img + wo_ + t * 2048) = pk4(xr[t] * rs[t]);
    *(LDS_AS u32x2*)(img + 4096 + wo_ + t * 2048) = pb;
  }
  lds_bar();
  u32x4 ur[2][2], ui[2][2], br, bi;
  f32x4 ar[2], ai[2];
  const f32x4 z4 = {0.f, 0.f, 0.f, 0.f};
  auto rd_in = [&](int off) {
#pragma unroll
    for (int ks = 0; ks < 2; ks++)
#pragma unroll
      for (int t = 0; t < 2; t++) { const lptr p = img + off + (ks ? ro2 : ro_) + t * 2048; ur[t][ks] = ld128(p); ui[t][ks] = ld128(p + 4096); }
    SB();
  };
  auto cmul = [&](const f32x4* cc, const f32x4* sn) {
    br = pk8(ar[0] * cc[0] - ai[0] * sn[0], ar[1] * cc[1] - ai[1] * sn[1]);
    bi = pk8(ai[0] * cc[0] + ar[0] * sn[0], ai[1] * cc[1] + ar[1] * sn[1]);
  };
  rd_in(0);
#pragma unroll
  for (int ks = 0; ks < 2; ks++) {
#pragma unroll
    for (int t = 0; t < 2; t++) { ar[t] = mfma16(ur[t][ks], c64[ks], ks ? ar[t] : z4); ai[t] = mfma16(ui[t][ks], c64[ks], ks ? ai[t] : z4); }
#pragma unroll
    for (int t = 0; t < 2; t++) { ar[t] = mfma16(ui[t][ks], s64[ks], ar[t]); ai[t] = mfma16(ur[t][ks], n64[ks], ai[t]); }
  }
  const f32x4 fa[2] = {asf(kin.wf[0] << 16), asf(kin.wf[1] << 16)}, fb[2] = {asf(kin.wf[0] & 0xFFFF0000u), asf(kin.wf[1] & 0xFFFF0000u)};
  cmul(t1c, t1s);
#pragma unroll
  for (int t = 0; t < 2; t++) { ar[t] = mfma16(c32[t], br, z4); ai[t] = mfma16(c32[t], bi, z4); }
#pragma unroll
  for (int t = 0; t < 2; t++) { ar[t] = mfma16(s32[t], bi, ar[t]); ai[t] = mfma16(n32[t], br, ai[t]); }
  cmul(fa, fb);
#pragma unroll
  for (int t = 0; t < 2; t++) { ar[t] = mfma16(br, c32[t], z4); ai[t] = mfma16(bi, c32[t], z4); }
#pragma unroll
  for (int t = 0; t < 2; t++) { ar[t] = mfma16(bi, n32[t], ar[t]); ai[t] = mfma16(br, s32[t], ai[t]); }
  cmul(t2c, t2s);
#pragma unroll
  for (int t = 0; t < 2; t++) {
    *(LDS_AS u32x2*)(img + 8192 + wo_ + t * 2048) = u32x2{br[2 * t], br[2 * t + 1]};
    *(LDS_AS u32x2*)(img + 12288 + wo_ + t * 2048) = u32x2{bi[2 * t], bi[2 * t + 1]};
  }
  lds_bar();
  rd_in(8192);
#pragma unroll
  for (int ks = 0; ks < 2; ks++) {
#pragma unroll
    for (int t = 0; t < 2; t++) { ar[t] = mfma16(c64[ks], ur[t][ks], ks ? ar[t] : xr[t]); ai[t] = mfma16(c64[ks], ui[t][ks], ks ? ai[t] : xi[t]); }
#pragma unroll
    for (int t = 0; t < 2; t++) { ar[t] = mfma16(n64[ks], ui[t][ks], ar[t]); ai[t] = mfma16(s64[ks], ur[t][ks], ai[t]); }
  }
  const unsigned ob = (FIRST ? WS_RB1 : WS_RB2) + (2 * c * RBP + 8 * tid) * 2;
  st128<16>(rw, ob, clean(pk8(ar[0], ar[1])));
  st128<16>(rw, ob + RBP * 2, c == 0 ? u32x4{0u, 0u, 0u, 0u} : clean(pk8(ai[0], ai[1])));
}

DI f32x16 mfma32(u32x4 a, u32x4 b, f32x16 c) {
  return __builtin_amdgcn_mfma_f32_32x32x16_bf16(__builtin_bit_cast(bf16x8, a), __builtin_bit_cast(bf16x8, b), c, 0, 0, 0);
}
DI f32x16 mfma32k8(u32x2 a, u32x2 b, f32x16 c) {
  return __builtin_amdgcn_mfma_f32_32x32x8bf16_1k(__builtin_bit_cast(s16x4, a), __builtin_bit_cast(s16x4, b), c, 0, 0, 0);
}
DI f32x16 scores(u32x4 k1, u32x2 k2, u32x4 q1, u32x2 q2) {
  f32x16 z;
#pragma unroll
  for (int i = 0; i < 16; i++) z[i] = 0.f;
  f32x16 s = mfma32(k1, q1, z);
  asm volatile("s_nop 11" : "+v"(s));
  return mfma32k8(k2, q2, s);
}
DI float max16(const f32x16& s) {
  float m = fmaxf(s[0], s[1]);
#pragma unroll
  for (int i = 2; i < 16; i++) m = fmaxf(m, s[i]);
  return m;
}
#define STR_(x) #x
#define STR(x) STR_(x)
#define KOF(t) ((((t)&7)>>1)*11264+((t)&1)*1280)
#define VOF(t) ((((t)&7)>>1)*11264+5120+((t)&1)*1536)
#define EXP(d, s) "v_exp_f32 v" #d ", v" #s "\n\t"
#define CVT(d, s) "v_cvt_pk_bf16_f32 v" #d ", v" #s ", v[" #s "+1]\n\t"
#define STEPA(t) \
  "v_mfma_f32_32x32x16_bf16 v[48:63], v[80:83], v[136:139], v[120:135]\n\t" \
  "ds_read_b128 v[80:83], v100 offset:" STR(KOF(t+1)) "\n\t" \
  EXP(64,32) EXP(65,33) CVT(78,68) CVT(79,70) EXP(66,34) EXP(67,35) \
  "v_mfma_f32_32x32x16_bf16 v[104:119], v[90:93], v[76:79], v[104:119]\n\t" \
  "ds_read_b64_tr_b16 v[90:91], v102 offset:" STR(VOF(t)+768) "\n\t" \
  "ds_read_b64_tr_b16 v[92:93], v102 offset:" STR(VOF(t)+1152) "\n\t" \
  EXP(68,36) EXP(69,37) CVT(72,64) CVT(73,66) EXP(70,38) EXP(71,39) \
  "v_mfma_f32_32x32x8_bf16 v[48:63], v[84:85], v[140:141], v[48:63]\n\t" \
  "ds_read_b64 v[84:85], v101 offset:" STR(KOF(t+1)) "\n\t" \
  EXP(64,40) EXP(65,41) CVT(74,68) CVT(75,70) EXP(66,42) EXP(67,43) \
  "s_waitcnt lgkmcnt(4)\n\t" \
  "v_mfma_f32_32x32x16_bf16 v[0:15], v[86:89], v[72:75], v[0:15]\n\t" \
  EXP(68,44) EXP(69,45) CVT(76,64) CVT(77,66) EXP(70,46) EXP(71,47)
#define STEPB(t) \
  "s_waitcnt lgkmcnt(0)\n\t" \
  "v_mfma_f32_32x32x16_bf16 v[32:47], v[80:83], v[94:97], v[16:31]\n\t" \
  EXP(64,48) EXP(65,49) CVT(78,68) CVT(79,70) EXP(66,50) EXP(67,51) \
  "v_mfma_f32_32x32x16_bf16 v[0:15], v[90:93], v[76:79], v[0:15]\n\t" \
  EXP(68,52) EXP(69,53) CVT(72,64) CVT(73,66) EXP(70,54) EXP(71,55) \
  "v_mfma_f32_32x32x8_bf16 v[32:47], v[84:85], v[98:99], v[32:47]\n\t" \
  EXP(64,56) EXP(65,57) CVT(74,68) CVT(75,70) EXP(66,58) EXP(67,59) \
  "v_mfma_f32_32x32x16_bf16 v[104:119], v[86:89], v[72:75], v[104:119]\n\t" \
  "ds_read_b64_tr_b16 v[86:87], v102 offset:" STR(VOF(t+1)) "\n\t" \
  "ds_read_b64_tr_b16 v[88:89], v102 offset:" STR(VOF(t+1)+384) "\n\t" \
  EXP(68,60) EXP(69,61) CVT(76,64) CVT(77,66) EXP(70,62) EXP(71,63)
#define PR(n) "s_setprio " #n "\n\t"
#define CHUNK(i) PR(1) STEPA(2*(i)) PR(0) STEPB(2*(i)) "s_barrier\n\t" PR(2) STEPA(2*(i)+1) STEPB(2*(i)+1)
#define Z4(a) "v_mov_b64 v[" #a ":" #a "+1], 0\n\tv_mov_b64 v[" #a "+2:" #a "+3], 0\n\t"

#define R4(b) "v[" STR(b) ":" STR(b) "+3]"
#define MM(d, a, b) "v_mfma_f32_16x16x32_bf16 " R4(d) ", " R4(a) ", " R4(b) ", " R4(d) "\n\t"
#define GM(B, m, i) MM(20*m+4*i, B+8+4*i, B+4*m)
#define RT(B, m, hf, k) "ds_read_b64_tr_b16 v[" STR(B+4*m+2*hf) ":" STR(B+4*m+2*hf+1) "], v10" #m " offset:" STR(k*8192+hf*1024) "\n\t"
#define RS(B, i, k) "ds_read_b128 " R4(B+8+4*i) ", v102 offset:" STR(i*2048+k*1024) "\n\t"
#define IF5_4(x) ""
#define IF5_5(x) x
#define GSTEP(N, B, C, k) GM(B,0,0) RT(C,0,0,k) RT(C,0,1,k) GM(B,1,0) RS(C,0,k) RT(C,1,0,k) GM(B,0,1) RT(C,1,1,k) RS(C,1,k) GM(B,1,1) RS(C,2,k) \
  GM(B,0,2) RS(C,3,k) GM(B,1,2) IF5_##N(RS(C,4,k)) GM(B,0,3) GM(B,1,3) IF5_##N(GM(B,0,4) GM(B,1,4)) MM(40,B,B)
#define GEMM_ASM(N) \
  Z4(0) Z4(4) Z4(8) Z4(12) Z4(16) Z4(20) Z4(24) Z4(28) Z4(32) Z4(36) Z4(40) "s_mov_b32 %[c], 0\n\ts_barrier\n\t" \
  RT(44,0,0,0) RT(44,0,1,0) RS(44,0,0) RT(44,1,0,0) RT(44,1,1,0) RS(44,1,0) RS(44,2,0) RS(44,3,0) IF5_##N(RS(44,4,0)) \
  "L_gemm_%=:\n\ts_add_u32 %[c], %[c], 1\n\ts_and_b32 %[t], %[c], 3\n\ts_cselect_b32 %[t], 1, -3\n\ts_mul_i32 %[t], %[t], 36864\n\ts_waitcnt lgkmcnt(0)\n\t" \
  GSTEP(N,44,72,1) \
  "v_add_u32 v100, %[t], v100\n\tv_add_u32 v101, %[t], v101\n\tv_add_u32 v102, %[t], v102\n\ts_waitcnt lgkmcnt(0)\n\ts_barrier\n\ts_setprio 1\n\t" \
  GSTEP(N,72,44,0) "s_setprio 0\n\t" \
  "s_cmp_lg_u32 %[c], 9\n\ts_cbranch_scc1 L_gemm_%=\n\ts_waitcnt lgkmcnt(0)\n\t" \
  GSTEP(N,44,72,1) "s_waitcnt lgkmcnt(0)\n\t" \
  MM(40,72,72) GM(72,0,0) GM(72,1,0) GM(72,0,1) GM(72,1,1) "s_nop 4\n\t" \
  "v_cndmask_b32 v44, v40, v41, %[m1]\n\tv_cndmask_b32 v45, v42, v43, %[m1]\n\tv_mov_b32 v46, 0x3727c5ac\n\tv_cndmask_b32 v44, v44, v45, %[m2]\n\t" \
  "v_fmac_f32 v46, 0x3acccccd, v44\n\tv_cmp_u_f32 vcc, v44, v44\n\tv_rsq_f32 v46, v46\n\ts_cbranch_vccz L_gok_%=\n\tds_write_b32 v106, v106\n\t" \
  "L_gok_%=:\n\tds_write_b32 v103, v46\n\ts_waitcnt lgkmcnt(0)\n\ts_barrier\n\t" \
  "ds_read_b32 v44, v104\n\tds_read_b32 v45, v105\n\tds_read_b32 v46, v106\n\t" \
  GM(72,0,2) GM(72,1,2) GM(72,0,3) GM(72,1,3) IF5_##N(GM(72,0,4) GM(72,1,4)) "s_waitcnt lgkmcnt(0)"
#define GEMM_OPS \
  : "={v[0:3]}"(acc[0][0]), "={v[4:7]}"(acc[0][1]), "={v[8:11]}"(acc[0][2]), "={v[12:15]}"(acc[0][3]), "={v[16:19]}"(acc[0][4]), \
    "={v[20:23]}"(acc[1][0]), "={v[24:27]}"(acc[1][1]), "={v[28:31]}"(acc[1][2]), "={v[32:35]}"(acc[1][3]), "={v[36:39]}"(acc[1][4]), \
    "={v44}"(rs[0]), "={v45}"(rs[1]), "={v46}"(vote), "+{v100}"(am0), "+{v101}"(am1), "+{v102}"(as), [c] "=&s"(c_), [t] "=&s"(t_) \
  : "{v103}"(a_w), "{v104}"(a_r[0]), "{v105}"(a_r[1]), "{v106}"(a_v), [m1] "s"(0xAAAAAAAAAAAAAAAAull), [m2] "s"(0xCCCCCCCCCCCCCCCCull) \
  : "memory", "scc", "vcc", "v40", "v41", "v42", "v43", "v47", "v48", "v49", "v50", "v51", "v52", "v53", "v54", "v55", "v56", "v57", "v58", "v59", "v60", \
    "v61", "v62", "v63", "v64", "v65", "v66", "v67", "v68", "v69", "v70", "v71", "v72", "v73", "v74", "v75", "v76", "v77", "v78", "v79", "v80", "v81", \
    "v82", "v83", "v84", "v85", "v86", "v87", "v88", "v89", "v90", "v91", "v92", "v93", "v94", "v95", "v96", "v97", "v98", "v99"

constexpr int HR = 4 * KVC;
constexpr int L_A = 2 * HR;
constexpr int L_EX = 15360;
constexpr int L_MISC = L_A + 70 * 1024;
constexpr int LDS_FUSED = L_MISC + 2048;

DI bool poll16(rsrc_t rw, unsigned off, unsigned need) {
  const int lane = threadIdx.x & 63;
#pragma unroll 1
  for (int spins = 0; spins < (1 << 20); spins++) {
    const unsigned v = gl32<16>(rw, off + (lane & 15) * 4);
    const unsigned got = (unsigned)__builtin_amdgcn_ballot_w64(v != 0) & 0xFFFFu;
    if ((got & need) == need) return true;
    nap<1>();
  }
  return false;
}

DI void poll_aux(rsrc_t rw, unsigned off) {
#pragma unroll 1
  for (int spins = 0; spins < (1 << 20); spins++) {
    const u32x4 v = gl128<16>(rw, off + (threadIdx.x & 63) * 16);
    if (!__builtin_amdgcn_ballot_w64(min(min(v[0], v[1]), min(v[2], v[3])) == 0u)) break;
    nap<4>();
  }
}
DI void poll_tag(rsrc_t rw, u32x2 tag, unsigned long long need) {
  const unsigned o = WS_F0 + (blockIdx.x & 7) * 2048 + (threadIdx.x & 63) * 32;
#pragma unroll 1
  for (int spins = 0; spins < (1 << 20); spins++) {
    const u32x4 a = gl128<16>(rw, o), b = gl128<16>(rw, o + 16);
    const unsigned d = (a[0] ^ tag[0]) | (a[1] ^ tag[1]) | (a[2] ^ tag[0]) | (a[3] ^ tag[1]) | (b[0] ^ tag[0]) | (b[1] ^ tag[1]) | (b[2] ^ tag[0]) | (b[3] ^ tag[1]);
    if (!(__builtin_amdgcn_ballot_w64(d != 0u) & need)) break;
    nap<TSLEEP>();
  }
}
#define LAYER_VARS \
  extern __shared__ __attribute__((aligned(16))) char smem[]; \
  const lptr lds = (lptr)smem; \
  const rsrc_t rw = mk_rsrc(ws, WS_END); \
  int lane = threadIdx.x & 63; asm volatile("" : "+v"(lane)); \
  const int w = wave_id(), g = lane >> 4, r16 = lane & 15; \
  const int xcd = blockIdx.x & 7, bi = blockIdx.x >> 3, u = 8 * (xcd & 1) + (bi & 7), qt = 4 * (xcd >> 1) + (bi >> 3); \
  const int hf = u & 1, ct = u >> 1;     \
  constexpr unsigned FKV = WS_FL + (FINAL ? 32768 : 0), KV = WS_KV + (FINAL ? LAYER2 : 0), OB = WS_O + (FINAL ? LAYER2 : 0); \
  constexpr unsigned AUX = WS_AUX + (FINAL ? 1024 : 0), RB = FINAL ? WS_RB2 : WS_RB1; \
  (void)hf; (void)AUX; (void)RB; (void)FKV; \
  LDS_AS unsigned* misc = (LDS_AS unsigned*)(lds + L_MISC + 1024) + (FINAL ? 8 : 0);

template <bool FINAL>
DI void loader(char* ws, u32x2 tag) {
  LAYER_VARS
  {
    const int lw = w - 8;
    {
      unsigned vm[4];
#pragma unroll
      for (int r = 0; r < 4; r++) vm[r] = (4 * (4 * lw + r) + g) * (RBP * 2) + 256 * qt + (((r16 >> 1) ^ (g | ((r & 2) << 1))) * 32) + (r16 & 1) * 16 + 3072 - 1024 * r;
      const unsigned l0 = lds_addr(lds) + lw * 4096, v0 = 2 * lw * 20480 + lane * 16, v2 = ((8 + (lw >> 1)) * 20 + (lw & 1)) * 1024 + lane * 16;
      constexpr int CNT = FINAL ? 9 : 8;
      auto lds_w = [&](int s) {
        const char* sb = ws + WS_WQ + u * 204800 + s * 2048;
        dma_s(v0, v0 + 20480 - 2048, sb, l0 + (s & 3) * SLOT32 + 16384 + (lw >> 1) * 2048);
        if (FINAL) dma1_sc1(v2, sb, lds_addr(lds) + (s & 3) * SLOT32 + 24576 + (lw >> 1) * 10240 + (lw & 1) * 1024);
      };
      auto lds_a = [&](int s) {
        dma_a(vm, ws + RB - 3072 + s * (64 * RBP * 2), l0 + (s & 3) * SLOT32); };
#pragma unroll 1
      for (int att = 0;; att++) {
        if (att == 0) { raw_bar(); if (FINAL) raw_bar(); else poll_tag(rw, tag, 0x7FFF0000ull); }
        lds_w(0); lds_w(1);
        if (att == 0) raw_bar();
        lds_w(2); lds_w(3);
        auto probe = [&](int s0, int n) {
          const int pl = min(lane, n - 1);
          const unsigned po = RB + ((64 * (s0 + (pl >> 3)) + 16 * lw + 2 * (pl & 7) + 1) * RBP + 128 * qt + 126) * 2;
#pragma unroll 1
          for (int spins = 0; spins < (1 << 20); spins++) {
            if (!__builtin_amdgcn_ballot_w64(gl32<16>(rw, po) == EMPTY)) break;
            nap<XSLEEP>();
          }
        };
        if (att == 0) {
          raw_bar();
          if (blockIdx.x >= 64) nap<XDELAY>();
          probe(0, 48);
        }
        drain();
        unsigned nd = 0;
        if (att == 0 && lane == 0) nd = __hip_atomic_fetch_add(misc + 4, 1u, __ATOMIC_RELAXED, __HIP_MEMORY_SCOPE_WORKGROUP);
        if (nd == 11) st32<16>(rw, AUX + blockIdx.x * 4, 1u);
#pragma unroll 1
        for (int s = 0; s < 4; s++) lds_a(s);
        vm_bar<12>(); vm_bar<8>();
        if (att == 0) probe(6, 32);
        lds_a(4); lds_w(4);
        vm_bar<4 + CNT>();
#pragma unroll 1
        for (int s = 5; s < 10; s++) { lds_a(s); lds_w(s); vm_bar<2 * CNT>(); }
        vm_bar<CNT>();
        poll_aux(rw, AUX);
        if (!FINAL) { poll_tag(rw, tag, ~0ull); if (blockIdx.x == 0 && threadIdx.x == 512) st64<16>(rw, WS_EP, tag); }
        vm_bar<0>();
        raw_bar();
        if (att || !*(volatile LDS_AS unsigned*)(misc + 2)) break;
      }
    }
    const int hh = lw >> 1, hp = lw & 1;
    const unsigned kvv = KV + (2 * u + hh) * 16 * KVC + hp * 5120 + lane * 16;
    const unsigned lkv = lds_addr(lds) + hh * HR + hp * 5120;
    const unsigned wv = (FINAL ? WS_WOP : WS_WOF) + (ct * 2 + hh) * 51200 + lane * 16;
    const unsigned myfl = FKV + (u * 16 + qt) * 128;
    raw_bar();
    {
      u32x4 v[6];
#pragma unroll
      for (int i = 0; i < 6; i++) v[i] = ld128(lds + hh * HR + hp * 5120 + lane * 16 + i * 1024);
#pragma unroll
      for (int i = 0; i < 6; i++) st128<16>(rw, kvv + qt * KVC + i * 1024, clean(v[i]));
    }
    drain();
    unsigned cnt = 0;
    if (lane == 0) cnt = __hip_atomic_fetch_add(misc, 1u, __ATOMIC_RELAXED, __HIP_MEMORY_SCOPE_WORKGROUP);
    if (__builtin_amdgcn_readfirstlane(cnt) == 3 && lane < 16) st32<16>(rw, FKV + (u * 16 + lane) * 128 + qt * 4, 1u);
    auto ldkv = [&](int seq) {
      const int ch = seq == 0 ? qt : seq < 13 ? ((qt & 12) + 3 + seq) & 15 : (qt & 12) | ((qt + seq) & 3);
      dma6_sc1(kvv + ch * KVC, ws, lkv + (seq & 3) * KVC);
    };
    auto fetch = [&](int seq) {
      const unsigned src = kvv + ((((qt & 12) + 3 + seq) & 15)) * KVC;
      u32x4 v[6];
#pragma unroll 1
      for (int spins = 0; spins < (1 << 18); spins++) {
#pragma unroll
        for (int i = 0; i < 6; i++) v[i] = gl128<16>(rw, src + i * 1024);
        unsigned m = 0;
#pragma unroll
        for (int i = 0; i < 6; i++) m = umax4(m, v[i]);
        if (!__builtin_amdgcn_ballot_w64(m == EMPTY)) break;
        nap<2>();
      }
#pragma unroll
      for (int i = 0; i < 6; i++) *(LDS_AS u32x4*)(lds + hh * HR + hp * 5120 + seq * KVC + lane * 16 + i * 1024) = v[i];
    };
#pragma unroll 1
    for (int attempt = 0;; attempt++) {
      int c = -2;
      if (attempt) { ldkv(0); vm_bar<0>(); }
      else {
        fetch(1); lds_bar();
        fetch(2); poll16(rw, myfl, 12u << (((qt & 12) + 4) & 15)); ldkv(3); lds_bar();
        c = 1;
      }
#pragma unroll 1
      for (; c < 13; c++) {
        if (c == 2 && !attempt) poll16(rw, myfl, 0xFFFFu);
        if (!FINAL && !attempt) {
          if (c >= 2 && c < 9) {
            const u32x4 e = {EMPTY, EMPTY, EMPTY, EMPTY};
            const unsigned o = OB + LAYER2 + (((c - 2) * 256 + blockIdx.x) * 4 + lw) * 2048 + lane * 16;
            st128<16>(rw, o, e); st128<16>(rw, o + 1024, e);
            st128<16>(rw, ((qt & 3) < 2 && c < 8) ? kvv + LAYER2 + qt * KVC + (c - 2) * 1024 : WS_END, e);
            st128<16>(rw, c < 5 ? OB + ((4 * qt + lw) * 32 + 2 * u) * 1280 + min((c - 2) * 1024, 1536) + lane * 16 : WS_END, e);
          }
          if (c == 10) {
            drain();
            unsigned n = 0;
            if (lane == 0) n = __hip_atomic_fetch_add(misc + 3, 1u, __ATOMIC_RELAXED, __HIP_MEMORY_SCOPE_WORKGROUP);
            if (__builtin_amdgcn_readfirstlane(n) == 3 && lane < 32) st32<16>(rw, WS_FL + 65536 + (lane < 16 ? (u * 16 + lane) * 128 + qt * 4 : (r16 * 16 + qt) * 128 + 64 + u * 4), 1u);
          }
        }
        if (c >= 2) { const int e = min(4 * (c - 2) + 2 * hp, 33); dma2(wv + e * 1024, ws, lds_addr(lds) + L_A + (hh * 35 + e) * 1024); }
        ldkv(c + 3);
        if (c == 0) vm_bar<12>();
        if (c >= 2) vm_bar<8>(); else if (c >= 0) vm_bar<6>();
      }
      if (!FINAL && !attempt) poll16(rw, myfl + 65536 + hp * 64, 0xFFFFu);
      vm_bar<0>();
      dma_s(wv + (35 + 7 * hp) * 1024, wv + (35 + 7 * hp) * 1024, ws, lds_addr(lds) + hh * HR + 7 * hp * 1024);
      dma_s(wv + (39 + 7 * hp) * 1024, wv + (39 + 7 * hp) * 1024, ws, lds_addr(lds) + hh * HR + (4 + 7 * hp) * 1024);
      raw_bar();
      vm_bar<0>();
      if (attempt || !*(volatile LDS_AS unsigned*)(misc + 1)) break;
      raw_bar();
    }
    if (!FINAL) { raw_bar(); raw_bar(); }
  }
}

template <bool FINAL>
DI void compute(char* ws, u16* out, ColIn& kin) {
  LAYER_VARS
  const int wm = w & 3, wn = w >> 2, h = lane >> 5;
  if (blockIdx.x < 64 || wn == 0) col_phase<!FINAL>(rw, lds, kin);
  else if (FINAL) {
    drain();
    nap<YIDLE>();
    u32x2 sp[8];
#pragma unroll 1
    for (int spins = 0; spins < (1 << 18); spins++) {
      unsigned m = 0;
#pragma unroll
      for (int i = 0; i < 8; i++) sp[i] = gl64<16>(rw, WS_SSQ + (blockIdx.x & 7) * 32768 + (1024 + 4 * (wm * 64 + lane)) * 2 + i * 4096);
#pragma unroll
      for (int i = 0; i < 8; i++) m = max(max(m, sp[i][0]), sp[i][1]);
      if (!__builtin_amdgcn_ballot_w64(m == EMPTY)) break;
      nap<YSLEEP>();
    }
    put_scale(lds, 1, wm * 64 + lane, sp);
    lds_bar(); raw_bar(); raw_bar();
  } else {
    raw_bar(); raw_bar(); raw_bar();
  }
  lds_bar();
  {
    drain();
    unsigned n = 0;
    if (lane == 0) n = __hip_atomic_fetch_add(misc + 4, 1u, __ATOMIC_RELAXED, __HIP_MEMORY_SCOPE_WORKGROUP);
    if (n == 11) st32<16>(rw, AUX + blockIdx.x * 4, 1u);
  }

  {
    constexpr int NT = FINAL ? 5 : 4;
    f32x4 acc[2][5]; float rs[2];
    unsigned mo[2], a0[2], a1[2], a2[2], a_r[2];
#pragma unroll
    for (int m = 0; m < 2; m++) {
      const int q = r16 >> 2, pr = 16 * (m ^ wn) + r16;
      mo[m] = lds_addr(lds) + (8 * g + q) * 256 + (((2 * wm + (m ^ wn)) ^ (q | ((g & 1) << 2))) * 32) + (lane & 3) * 8;
      const unsigned qb = L_A + (wn * 4 + wm) * 1280, kb = wn * HR + wm * 1280, vr = wn * HR + 5120 + (32 * wm + pr) * 48 + (g == 3 ? 32 : g * 16);
      a0[m] = ((g >> 1) ? kb : qb) + (g & 1) * 512 + pr * 16;
      a1[m] = g == 2 ? qb + 1024 + pr * 8 : vr; a2[m] = g == 2 ? kb + 1024 + pr * 8 : vr + 8;
      a_r[m] = lds_addr(lds) + L_MISC + (32 * wm + pr) * 4;
      asm volatile("" : "+v"(a0[m]), "+v"(a1[m]), "+v"(a2[m]));
    }
    const unsigned a_w = a_r[0] + (g == (r16 >> 2) ? 0 : 512), a_v = lds_addr((lptr)(misc + 2));
#pragma unroll 1
    for (int att = 0;; att++) {
      unsigned am0 = mo[0], am1 = mo[1], as = lds_addr(lds) + 16384 + wn * 10240 + lane * 16, vote; int c_, t_;
      if (FINAL) asm volatile(GEMM_ASM(5) GEMM_OPS); else asm volatile(GEMM_ASM(4) GEMM_OPS);
      if (att || !__builtin_amdgcn_readfirstlane(vote)) break;
    }
    SB();
#pragma unroll
    for (int m = 0; m < 2; m++) *(LDS_AS u32x4*)(lds + a0[m]) = pk8(acc[m][0] * rs[m], acc[m][1] * rs[m]);
    asm volatile("s_nop 1" : "+v"(acc[0][2]), "+v"(acc[1][2]), "+v"(acc[0][3]), "+v"(acc[1][3]), "+v"(acc[0][NT - 1]), "+v"(acc[1][NT - 1]) :: "memory");
#pragma unroll
    for (int m = 0; m < 2; m++) {
      u32x2 hi = pk4(acc[m][3] * rs[m]);
      if (g == 3) hi = u32x2{0x3F80u, 0u};
      *(LDS_AS u32x2*)(lds + a1[m]) = pk4(acc[m][2] * rs[m]);
      *(LDS_AS u32x2*)(lds + a2[m]) = hi;
    }
    lds_bar();
    if (FINAL) {
      const unsigned x4 = WS_X4 + (((u * 16 + qt) * 4 + wm) * 2 + wn) * 1280;
      const u32x2 p0 = pk4(acc[0][NT - 1]), p1 = pk4(acc[1][NT - 1]), q0 = pk4(acc[0][3]), q1 = pk4(acc[1][3]);
      st128<16>(rw, x4 + lane * 16, clean(u32x4{p0[0], p0[1], p1[0], p1[1]}));
      if (g == 3) st128<16>(rw, x4 + 1024 + r16 * 16, clean(u32x4{q0[0], q0[1], q1[0], q1[1]}));
    }
  }
  const int head = 2 * u + wn, kh = w & 1;
  const lptr ring = lds + wn * HR;
  const int pblk = 128 * qt + 64 * hf + 32 * (wm >> 1);
  const unsigned r3t = WS_R3 + (64 * (wm & 1) + (pblk >> 5)) * (642 * 64);
  u32x2 x4v[3]; float dc = 0.f;
  unsigned os = OB + ((4 * qt + wm) * 32 + head) * 1280 + (((lane << 2) & 16) + ((lane >> 1) & 12) + (lane & 3)) * 8;
  unsigned o1 = OB + ((pblk >> 5) * 32 + 16 * wn) * 1280 + (g >> 1) * 1280 + (g & 1) * 512 + (wm & 1) * 256 + r16 * 16;
  unsigned o2 = OB + ((pblk >> 5) * 32 + 16 * wn) * 1280 + 2 * g * 1280 + 1024 + (wm & 1) * 128 + r16 * 8;
  u32x4 o3 = {0u, 0u, 0u, 0u};
  if (!FINAL) {
#pragma unroll
    for (int i = 0; i < 3; i++) {
      const int j = 80 * ct + 48 * wn + 16 * (wn ? (i & 1) : i) + r16;
      o3[i] = (j == 1 ? WS_RB0 + ((pblk >> 5) * 641 + ((pblk >> 5) & 1) + 1) * 64 : WS_RB1 + (j * RBP + pblk) * 2) + 16 * g + 8 * (wm & 1);
    }
  } else {
    o3[3] = r3t + 64 + r16 * 4;
#pragma unroll
    for (int i = 0; i < 3; i++) {
      const int cg = 12 * wn + 4 * ((i == 2 && wn) ? 0 : i) + g, pw = cg / 5, gq = cg - 5 * pw;
      o3[i] = WS_X4 + ((((2 * ct + (pw >> 1)) * 16 + qt) * 4 + 2 * hf + (wm >> 1)) * 2 + (pw & 1)) * 1280 + (gq * 16 + (trow(wm & 1, r16) & 15)) * 16 + ((((r16 >> 3) ^ pw) & 1) * 8);
    }
  }
  asm volatile("" : "+v"(os), "+v"(o1), "+v"(o2), "+v"(o3));
  f32x4 r[3];
  {
    const lptr qpa = lds + L_A + (wn * 4 + wm) * 1280, qpb = lds + L_A + (wn * 4 + (wm ^ 1)) * 1280;
    const u32x4 qa1 = ld128(qpa + lane * 16), qb1 = ld128(qpb + lane * 16);
    u32x2 qa2 = ld64(qpa + 1024 + (lane & 31) * 8), qb2 = ld64(qpb + 1024 + (lane & 31) * 8);
    if (h) { qa2 = u32x2{0u, 0u}; qb2 = qa2; }
    const unsigned ka = lds_addr(ring) + kh * 2560 + lane * 16, kb = lds_addr(ring) + kh * 2560 + 1024 + (lane & 31) * 8;
    const unsigned va = lds_addr(ring) + kh * 3072 + (4 * h + (r16 >> 2)) * 48 + ((g & 1) ? ((lane & 3) ? 40 : 32) : (lane & 3) * 8);
    float ma, mb;
    {
      const u32x4 k1 = ld128(ring + lane * 16); const u32x2 k2 = ld64(ring + 1024 + (lane & 31) * 8);
      ma = max16(scores(k1, k2, qa1, qa2)); mb = max16(scores(k1, k2, qb1, qb2));
    }
    for (int attempt = 0;; attempt++) {
      ma = fmaxf(ma, __shfl_xor(ma, 32)); mb = fmaxf(mb, __shfl_xor(mb, 32));
      f32x16 cia, cib, oa, ob;
#pragma unroll
      for (int i = 0; i < 16; i++) { cia[i] = -ma; cib[i] = -mb; }
      int cnt = 4;
      asm volatile(
          "ds_read_b128 v[80:83], v100\n\t"
          "ds_read_b64 v[84:85], v101\n\t"
          "ds_read_b64_tr_b16 v[86:87], v102 offset:5120\n\t"
          "ds_read_b64_tr_b16 v[88:89], v102 offset:5504\n\t"
          Z4(0) Z4(68) Z4(76) Z4(90)
          "s_waitcnt lgkmcnt(0)\n\t"
          "v_mfma_f32_32x32x16_bf16 v[32:47], v[80:83], v[94:97], v[16:31]\n\t"
          Z4(4) Z4(8) Z4(12) Z4(104) Z4(108) Z4(112) Z4(116)
          "v_mfma_f32_32x32x8_bf16 v[32:47], v[84:85], v[98:99], v[32:47]\n\ts_nop 9\n\t"
          "L_attn_%=:\n\t"
          CHUNK(0) CHUNK(1) CHUNK(2) CHUNK(3)
          "s_sub_u32 %[cnt], %[cnt], 1\n\ts_cmp_lg_u32 %[cnt], 0\n\ts_cbranch_scc1 L_attn_%=\n\t"
          "s_setprio 0\n\t" CVT(78,68) CVT(79,70) "s_waitcnt lgkmcnt(0)\n\ts_nop 1\n\t"
          "v_mfma_f32_32x32x16_bf16 v[104:119], v[90:93], v[76:79], v[104:119]\n\ts_nop 15\n\t"
          : "={v[0:15]}"(oa), "={v[104:119]}"(ob), [cnt] "+s"(cnt)
          : "{v[16:31]}"(cia), "{v[120:135]}"(cib), "{v[94:97]}"(qa1), "{v[98:99]}"(qa2), "{v[136:139]}"(qb1), "{v[140:141]}"(qb2),
            "{v100}"(ka), "{v101}"(kb), "{v102}"(va)
          : "memory", "scc", "v32", "v33", "v34", "v35", "v36", "v37", "v38", "v39", "v40", "v41", "v42", "v43", "v44", "v45", "v46", "v47",
            "v48", "v49", "v50", "v51", "v52", "v53", "v54", "v55", "v56", "v57", "v58", "v59", "v60", "v61", "v62", "v63", "v64", "v65",
            "v66", "v67", "v68", "v69", "v70", "v71", "v72", "v73", "v74", "v75", "v76", "v77", "v78", "v79", "v80", "v81", "v82", "v83",
            "v84", "v85", "v86", "v87", "v88", "v89", "v90", "v91", "v92", "v93");
      LDS_AS f32x4* ex = (LDS_AS f32x4*)(ring + L_EX + wm * 3072) + lane;
#pragma unroll
      for (int i = 0; i < 3; i++) ex[i * 64] = f32x4{ob[4 * i], ob[4 * i + 1], ob[4 * i + 2], ob[4 * i + 3]};
      f32x4 cs = {0.f, 0.f, 0.f, 0.f};
#pragma unroll
      for (int i = 0; i < 3; i++) cs += f32x4{oa[4 * i], oa[4 * i + 1], oa[4 * i + 2], oa[4 * i + 3]} + f32x4{ob[4 * i], ob[4 * i + 1], ob[4 * i + 2], ob[4 * i + 3]};
      if (!(fabsf(cs[0] + cs[1] + cs[2] + cs[3]) < INFINITY) || (h && !(oa[8] + ob[8] < 0x1p60f))) misc[1] = 1u;
      lds_bar();
      const unsigned vote = *(volatile LDS_AS unsigned*)(misc + 1);
      LDS_AS f32x4* ey = (LDS_AS f32x4*)(ring + L_EX + (wm ^ 1) * 3072) + lane;
#pragma unroll
      for (int i = 0; i < 3; i++) r[i] = ey[i * 64] + f32x4{oa[4 * i], oa[4 * i + 1], oa[4 * i + 2], oa[4 * i + 3]};
      if (attempt || !vote) break;
      lds_bar();
      ma = -INFINITY; mb = -INFINITY;
      for (int t = 0; t < 64; t++) {
        const unsigned kb_ = KV + (head * 16 + (t >> 2)) * KVC + (t & 3) * 1280;
        const u32x4 k1 = gl128<16>(rw, kb_ + lane * 16); const u32x2 k2 = gl64<16>(rw, kb_ + 1024 + (lane & 31) * 8);
        ma = fmaxf(ma, max16(scores(k1, k2, qa1, qa2))); mb = fmaxf(mb, max16(scores(k1, k2, qb1, qb2)));
      }
      raw_bar();
    }
  }
  {
    const unsigned lb = __float_as_uint(r[2][0]);
    const float il = __builtin_amdgcn_rcpf(__uint_as_float(__builtin_amdgcn_permlane32_swap(lb, lb, false, false)[1]));
    const unsigned sl8 = os & 255;
    st128<16>(rw, os + sl8 + h * 512, clean(pk8(r[0] * il, r[1] * il)));
    const u32x4 t = clean(pk8(r[2] * il, r[2] * il));
    st64<16>(rw, h ? WS_END : os + 1024, u32x2{t[0], t[1]});
  }
  const lptr wa = lds + L_A + wn * 35 * 1024 + lane * 16, wb = ring + lane * 16;
  constexpr int NPRE = 15;
  u32x4 bf[50];
  f32x4 acc[5] = {};
#pragma unroll
  for (int j = 0; j < NPRE; j++) bf[j] = ld128(wa + j * 1024);
#pragma unroll
  for (int j = 0; j < 5; j++) asm volatile("" : "+v"(acc[j]));
  if (FINAL) dc = __uint_as_float(gl32<16>(rw, o3[3]));
#pragma unroll 1
  for (int spins = 0; spins < (1 << 18); spins++) {
#pragma unroll
    for (int i = 0; i < 3; i++) x4v[i] = gl64<16>(rw, o3[i]);
    if (!FINAL || !__builtin_amdgcn_ballot_w64(umax4(max(x4v[0][0], x4v[0][1]), u32x4{x4v[1][0], x4v[1][1], x4v[2][0], x4v[2][1]}) == EMPTY)) break;
    nap<8>();
  }
  drain();
  {
    u32x4 af[10];
    auto ldf = [&](int i) {
      if (i < 8) af[i] = __builtin_amdgcn_raw_buffer_load_b128(rw, o1, i * 2 * 1280, 16);
      else { const u32x2 a = __builtin_amdgcn_raw_buffer_load_b64(rw, o2, (i - 8) * 8 * 1280, 16), b = __builtin_amdgcn_raw_buffer_load_b64(rw, o2, ((i - 8) * 8 + 1) * 1280, 16);
             af[i] = u32x4{a[0], a[1], b[0], b[1]}; }
    };
#pragma unroll
    for (int i = 0; i < 10; i++) ldf(i);
#pragma unroll 1
    for (int spins = 0; spins < (1 << 18); spins++) {
      unsigned m = 0;
#pragma unroll
      for (int i = 0; i < 10; i++) m = umax4(m, af[i]);
      if (!__builtin_amdgcn_ballot_w64(m == EMPTY)) break;
      nap<RSLEEP>();
#pragma unroll
      for (int i = 0; i < 10; i++) if (__builtin_amdgcn_ballot_w64(umax4(0, af[i]) == EMPTY)) ldf(i);
    }
#pragma unroll
    for (int i = 0; i < 10; i++) asm volatile("" : "+v"(af[i]));
    asm volatile("s_nop 1" : "+v"(acc[0]), "+v"(acc[1]), "+v"(acc[2]), "+v"(acc[3]), "+v"(acc[4]));
    SB();
#pragma unroll
    for (int i = 0; i < 10; i++) {
#pragma unroll
      for (int a = 0; a < 5; a++) {
        if (FINAL) mfma16v(acc[a], bf[5 * i + a], af[i]); else mfma16v(acc[a], af[i], bf[5 * i + a]);
        const int j = 5 * i + a + NPRE; if (j < 50) bf[j] = ld128(j < 35 ? wa + j * 1024 : wb + (j - 35) * 1024);
        SB();
      }
    }
  }
  asm volatile("s_nop 7\n\ts_nop 1" : "+v"(acc[0]), "+v"(acc[1]), "+v"(acc[2]), "+v"(acc[3]), "+v"(acc[4]) :: "memory");
  if (!FINAL) { col_load(kin, rw, true, true); SB(); }
  {
    LDS_AS f32x4* ex = (LDS_AS f32x4*)(ring + L_EX + 12288 + wm * 3072) + lane;
    LDS_AS f32x4* ey = (LDS_AS f32x4*)(lds + (wn ^ 1) * HR + L_EX + 12288 + wm * 3072) + lane + 64 * wn;
    ex[0] = acc[2]; ex[64] = acc[3]; ex[128] = acc[4];
    dc *= 0.039528470752104741f;
#pragma unroll
    for (int i = 0; i < 3; i++) { acc[i] += f32x4{bflo(x4v[i][0]), bfhi(x4v[i][0]), bflo(x4v[i][1]), bfhi(x4v[i][1])}; if (FINAL) acc[i] += dc; }
    lds_bar();
#pragma unroll
    for (int i = 0; i < 3; i++) acc[i] += ey[64 * i];
  }
  if (FINAL) {
    const rsrc_t ro = mk_rsrc(out, 2048 * 640 * 2);
    const unsigned op = (rowof(pblk + trow(wm & 1, r16)) * 640 + 80 * ct + 48 * wn + 4 * g) * 2;
#pragma unroll
    for (int i = 0; i < 3; i++)
      if (i < 2 || wn == 0) st64<0>(ro, op + 32 * i, pk4(acc[i]));
  } else {
    f32x4 sq = {0.f, 0.f, 0.f, 0.f};
#pragma unroll
    for (int i = 0; i < 3; i++)
      if (i < 2 || wn == 0) {
        const int j = 80 * ct + 48 * wn + 16 * i + r16;
        st128<16>(rw, r3t + j * 64 + g * 16, clean(asu(acc[i])));
        if (j != 1) sq += acc[i] * acc[i];
      }
#pragma unroll
    for (int e = 0; e < 4; e++) sq[e] = row16_sum(sq[e]);
    LDS_AS float* sl = (LDS_AS float*)(lds + L_MISC);
    if (r16 == 0) *(LDS_AS f32x4*)(sl + wn * 64 + 32 * (wm & 1) + 16 * (wm >> 1) + 4 * g) = sq;
    lds_bar();
    if (w == 0) {
      LDS_AS f32x4* s4 = (LDS_AS f32x4*)sl + 2 * (lane & 7);
      st128<16>(rw, WS_SSQ + (lane >> 3) * 32768 + (ct * 2048 + 1024 * ((lane >> 2) & 1) + 64 * qt + 32 * hf + 8 * (lane & 3)) * 2, clean(pk8(s4[0] + s4[16], s4[1] + s4[17])));
    }
  }
}

__global__ void __launch_bounds__(768, 3) k_main(const u16* x, const u16* wr, const u16* wi, const u16* wqkv, const u16* wo, char* ws, u16* out) {
  if (wave_id() >= 8) {
    extern __shared__ __attribute__((aligned(16))) char smem[];
    const unsigned long long t0 = __builtin_amdgcn_s_memrealtime();
    const int job = blockIdx.x;
    if (job < 144) raw_bar();
    const rsrc_t rw = mk_rsrc(ws, WS_END);
    __builtin_amdgcn_s_setprio(3);
    if (threadIdx.x < 528) ((LDS_AS unsigned*)((lptr)smem + L_MISC + 1024))[threadIdx.x - 512] = 0u;
    u32x2 tag = gl64<16>(rw, WS_EP);
    tag[0] += 1u;
    if (job < 228) raw_bar();
    if (job < 64) raw_bar();
    asm volatile("s_waitcnt vmcnt(0) lgkmcnt(0)\n\ts_barrier" : "+v"(tag) :: "memory");
    if (wave_id() == 8) {
      if (threadIdx.x < 520) st64<16>(rw, WS_F0 + (threadIdx.x - 512) * 2048 + job * 8, tag);
      if (job >= 248) {
        while (__builtin_amdgcn_s_memrealtime() - t0 < TPOLL) nap<2>();
        poll_tag(rw, tag, 0xFE0FFFF00000FFFFull);
        if (threadIdx.x < 544) st64<16>(rw, WS_GO + ((threadIdx.x - 512) * 8 + (job & 7)) * 64, tag);
      }
#pragma unroll 1
      for (int spins = 0; spins < (1 << 20); spins++) {
        const u32x2 v = gl64<16>(rw, WS_GO + job * 64);
        if (__builtin_amdgcn_readfirstlane((v[0] ^ tag[0]) | (v[1] ^ tag[1])) == 0) break;
        nap<GSLEEP>();
      }
    }
    if (wave_id() > 8) {
    while (__builtin_amdgcn_s_memrealtime() - t0 < TFILL) nap<2>();
    const int i0 = blockIdx.x * 192 + threadIdx.x - 576;
    for (int pc = i0; pc < 640 * 80; pc += 192 * 256) {
      const int n = pc % 640, kq = pc / 640;
      const int u_ = n / 40, rn = n - 40 * u_, hn = rn / 20, dn = rn - 20 * hn, gq = dn >> 2;
      const int T = gq < 4 ? 8 + hn : 4 * hn + 3, gs = gq < 4 ? gq : 3;
      float v[8];
#pragma unroll
      for (int e = 0; e < 4; e++) {
        const float a = (float)((freq_of(kq >> 2, 4 * (kq & 3) + e) * n) % 640) * (1.f / 640.f);
        v[2 * e] = 0.055901699437494742f * __builtin_amdgcn_cosf(a);
        v[2 * e + 1] = -0.055901699437494742f * __builtin_amdgcn_sinf(a);
      }
      if (kq == 0) { v[0] = (n & 1) ? -0.039528470752104741f : 0.039528470752104741f; v[1] = 0.039528470752104741f; }
      st128<16>(rw, WS_WQ + ((u_ * 10 + T) * 20 + (kq >> 2)) * 1024 + ((kq & 3) * 16 + 4 * gs + (dn & 3)) * 16,
                  u32x4{pk2(v[0], v[1]), pk2(v[2], v[3]), pk2(v[4], v[5]), pk2(v[6], v[7])});
    }
#pragma unroll
    for (int it = 0; it < 4; it++) {
      const int k = it * 49152 + i0, b = k / 1408;
      st128<16>(rw, k < 128 * 1408 ? WS_KV + b * 4 * KVC + (k - b * 1408) * 16 : WS_END, u32x4{EMPTY, EMPTY, EMPTY, EMPTY});
    }
    }
    raw_bar();
    loader<false>(ws, tag);
    loader<true>(ws, tag);
  } else {
    prep(x, wr, wi, wqkv, wo, ws);
    asm volatile("s_waitcnt vmcnt(0) lgkmcnt(0)\n\ts_barrier" ::: "memory");
    ColIn kin = {};
    compute<false>(ws, out, kin);
    compute<true>(ws, out, kin);
  }
}

extern "C" void kernel_launch(void* const* d_in, const int* in_sizes, int n_in, void* d_out, int out_size, void* d_ws, size_t ws_size,
                              hipStream_t stream) {
  const u16* x = (const u16*)d_in[0];
  const u16* wr = (const u16*)d_in[1];
  const u16* wi = (const u16*)d_in[2];
  const u16* wqkv = (const u16*)d_in[4];
  const u16* wo = (const u16*)d_in[5];
  char* ws = (char*)d_ws;
  (void)hipFuncSetAttribute((const void*)k_main, hipFuncAttributeMaxDynamicSharedMemorySize, LDS_FUSED);
  k_main<<<256, 768, LDS_FUSED, stream>>>(x, wr, wi, wqkv, wo, ws, (u16*)d_out);
}
```

```cpp
#include <hip/hip_runtime.h>
#include <hip/hip_bf16.h>

typedef unsigned short u16;
typedef __attribute__((ext_vector_type(16))) float f32x16;
typedef __attribute__((ext_vector_type(4))) float f32x4;
typedef __attribute__((ext_vector_type(2))) float f32x2;
typedef __attribute__((ext_vector_type(8))) __bf16 bf16x8;
typedef __attribute__((ext_vector_type(2))) __bf16 bf16x2;
typedef __attribute__((ext_vector_type(4))) short s16x4;
typedef __attribute__((ext_vector_type(4))) unsigned u32x4;
typedef __attribute__((ext_vector_type(2))) unsigned u32x2;
typedef __attribute__((ext_vector_type(8))) unsigned u32x8;
#define DI __device__ __forceinline__
#define LDS_AS __attribute__((address_space(3)))
typedef LDS_AS char* lptr;

constexpr double cx_cos_small(double x) { double x2 = x * x, t = 1, s = 1; for (int i = 1; i < 12; i++) { t *= -x2 / ((2 * i - 1) * (2 * i)); s += t; } return s; }
constexpr double cx_sin_small(double x) { double x2 = x * x, t = x, s = x; for (int i = 1; i < 12; i++) { t *= -x2 / ((2 * i) * (2 * i + 1)); s += t; } return s; }
constexpr double cx_cos640(int m) {
  m %= 640; if (m > 320) m = 640 - m; double sg = 1; if (m > 160) { m = 320 - m; sg = -1; }
  const double w = 6.283185307179586476925 / 640; return sg * (m <= 80 ? cx_cos_small(w * m) : cx_sin_small(w * (160 - m)));
}
constexpr u16 cx_bf16(double d) { float f = (float)d; unsigned u = __builtin_bit_cast(unsigned, f); u += 0x7FFFu + ((u >> 16) & 1u); return (u16)(u >> 16); }
struct Cos640 { double v[640]; };
constexpr Cos640 mk_cos() { Cos640 c{}; for (int m = 0; m < 640; m++) c.v[m] = cx_cos640(m); return c; }
constexpr Cos640 COS = mk_cos();
__host__ __device__ constexpr int kf_of(int G, int i) { return G ? G + 40 * i : (i == 0 ? 320 : i < 8 ? 40 * i : 40 * i - 300); }
__host__ __device__ constexpr int freq_of(int G, int i) { return kf_of(G, i) > 320 ? 640 - kf_of(G, i) : kf_of(G, i); }
constexpr double SA = 1.91318, SA0 = 1.9140625, S64 = 1.86954, S32 = 1.6245;
constexpr double a1val(int out, int a) {
  if (out >= 40 || a >= 40) return 0;
  const int G = out >> 1, part = out & 1;
  if (G == 0) return (part && (a & 1)) ? -SA0 : SA0;
  return SA * COS.v[(16 * a * G + 160 * part) % 640];
}
constexpr double m2val(int G, int o, int k) {
  const int i = o >> 1, po = o & 1, part = k >> 4, b = k & 15, kf = kf_of(G, i);
  const double c = COS.v[(b * kf) % 640], sn = COS.v[(b * kf + 480) % 640];
  if (G == 0) {
    if (part != (i >= 8)) return 0;
    if (i == 0) return 0.039528470752104741 / SA0 * (po ? 1.0 : c);
    return 0.055901699437494742 / SA0 * (po ? -sn : c);
  }
  const double v = po ? (part ? c : -sn) : (part ? sn : c);
  return 0.055901699437494742 / SA * ((po && kf > 320) ? -v : v);
}
struct DTab { u16 a1[3][64][8]; u16 a1b[3][64][4]; u16 pad[256]; u16 m2[20][2][64][8]; };
constexpr DTab mk_dtab() {
  DTab t{};
  for (int l = 0; l < 64; l++) {
    const int r = l & 15, g = l >> 4;
    for (int T = 0; T < 3; T++) {
      for (int e = 0; e < 8; e++) t.a1[T][l][e] = cx_bf16(a1val(16 * T + r, 16 * (e >> 2) + 4 * g + (e & 3)));
      for (int e = 0; e < 4; e++) t.a1b[T][l][e] = cx_bf16(a1val(16 * T + r, 32 + 4 * g + e));
    }
    for (int G = 0; G < 20; G++) for (int T = 0; T < 2; T++) for (int e = 0; e < 8; e++)
      t.m2[G][T][l][e] = cx_bf16(m2val(G, 8 * (r >> 2) + 4 * T + (r & 3), 8 * g + e));
  }
  return t;
}
static __device__ const __attribute__((aligned(1024))) DTab g_dt = mk_dtab();

struct ITab { u16 m2[20][2][64][8]; u16 a1[3][64][8]; u16 a1b[3][64][4]; u16 pad[1792]; };
constexpr ITab mk_itab() {
  ITab t{};
  for (int l = 0; l < 64; l++) {
    const int r = l & 15, g = l >> 4;
    for (int T = 0; T < 3; T++) {
      for (int e = 0; e < 8; e++) t.a1[T][l][e] = cx_bf16(a1val(16 * (e >> 2) + 4 * g + (e & 3), 16 * T + r));
      for (int e = 0; e < 4; e++) t.a1b[T][l][e] = cx_bf16(a1val(32 + 4 * g + e, 16 * T + r));
    }
    for (int G = 0; G < 20; G++) for (int P = 0; P < 2; P++) for (int e = 0; e < 8; e++) t.m2[G][P][l][e] = cx_bf16(m2val(G, 8 * g + e, 16 * P + r));
  }
  return t;
}
static __device__ const __attribute__((aligned(1024))) ITab g_it = mk_itab();

struct CTab { u16 v[10 * 512]; };
constexpr CTab mk_ctab() {
  CTab c{};
  for (int f = 0; f < 10; f++) for (int l = 0; l < 64; l++) for (int e = 0; e < 8; e++) {
    const int r = l & 15, g = l >> 4;
    const int m = f < 8 ? (8 * g + e) * (16 * (f >> 1) + r) : 2 * r * (16 * (e >> 2) + 4 * g + (e & 3));
    c.v[(f * 64 + l) * 8 + e] = cx_bf16((f < 8 ? S64 : S32) * COS.v[10 * ((m + (f & 1) * 48) & 63)]);
  }
  return c;
}
static __device__ const __attribute__((aligned(1024))) CTab g_ct = mk_ctab();
constexpr float RSK = (float)(2048.0 * S64 * S64 * S32 * S32 * 2048.0 * S64 * S64 * S32 * S32 / 640.0);

constexpr int RP = 2048 + 64, RBP = 2048 + 128;
constexpr unsigned RBSZ = 640 * RBP * 2;
constexpr unsigned WS_FL = 0;
constexpr unsigned WS_AUX = WS_FL + 3 * 32768;
constexpr unsigned WS_F0 = WS_AUX + 2048;
constexpr unsigned WS_GO = WS_F0 + 8 * 2048;
constexpr unsigned WS_EP = WS_GO + 256 * 64;
constexpr unsigned WS_RS1 = WS_EP + 256;
constexpr unsigned WS_RB0 = WS_RS1 + 8192;
constexpr unsigned WS_RB1 = WS_RB0 + RBSZ;
constexpr unsigned WS_WF = WS_RB1 + RBSZ;
constexpr unsigned WS_WQ = WS_WF + 320 * 8192;
constexpr unsigned WS_WOF = WS_WQ + 16 * 8 * 20 * 1024;
constexpr unsigned WS_WOP = WS_WOF + 40 * 20 * 1024;
constexpr int KVC = 11264;
constexpr unsigned WS_KV = WS_WOP + 40 * 20 * 1024;
constexpr unsigned WS_O = WS_KV + 32 * 16 * KVC;
constexpr unsigned LAYER2 = 32 * 16 * KVC + 32 * 64 * 1280;
constexpr unsigned WS_X4 = WS_KV + 2 * LAYER2;
constexpr unsigned WS_R3 = WS_X4 + 2048 * 640 * 2;
constexpr unsigned WS_RB2 = WS_R3 + 640 * RP * 4;
constexpr unsigned WS_SSQ = WS_RB2 + RBSZ;
constexpr unsigned EMPTY = 0xFFFFFFFFu;
constexpr unsigned WS_END = WS_SSQ + 8 * 2048 * 16;

constexpr int XDELAY = 20, K1DELAY = 40, YIDLE = 12, TPOLL = 250, TSLEEP = 1, GSLEEP = 2, TFILL = 400, YSLEEP = 4, XSLEEP = 1, RSLEEP = 2;

#define SB() __builtin_amdgcn_sched_barrier(0)
typedef __amdgpu_buffer_rsrc_t rsrc_t;
DI rsrc_t mk_rsrc(const void* p, unsigned bytes) { return __builtin_amdgcn_make_buffer_rsrc((void*)p, 0, bytes, 0x00020000); }
template <int AUX> DI void st128(rsrc_t r, unsigned off, u32x4 v) { __builtin_amdgcn_raw_buffer_store_b128(v, r, off, 0, AUX); }
template <int AUX> DI void st64(rsrc_t r, unsigned off, u32x2 v) { __builtin_amdgcn_raw_buffer_store_b64(v, r, off, 0, AUX); }
template <int AUX> DI void st32(rsrc_t r, unsigned off, unsigned v) { __builtin_amdgcn_raw_buffer_store_b32(v, r, off, 0, AUX); }
template <int AUX> DI u32x4 gl128(rsrc_t r, unsigned off) { return __builtin_amdgcn_raw_buffer_load_b128(r, off, 0, AUX); }
template <int AUX> DI u32x2 gl64(rsrc_t r, unsigned off) { return __builtin_amdgcn_raw_buffer_load_b64(r, off, 0, AUX); }
template <int AUX> DI unsigned gl32(rsrc_t r, unsigned off) { return __builtin_amdgcn_raw_buffer_load_b32(r, off, 0, AUX); }
DI u32x4 asu(f32x4 v) { return __builtin_bit_cast(u32x4, v); }
DI f32x4 asf(u32x4 v) { return __builtin_bit_cast(f32x4, v); }
DI unsigned pk2(float a, float b) { f32x2 v = {a, b}; return __builtin_bit_cast(unsigned, __builtin_convertvector(v, bf16x2)); }
DI u32x2 pk4(f32x4 a) { return u32x2{pk2(a[0], a[1]), pk2(a[2], a[3])}; }
DI u32x4 pk8(f32x4 a, f32x4 b) { return u32x4{pk2(a[0], a[1]), pk2(a[2], a[3]), pk2(b[0], b[1]), pk2(b[2], b[3])}; }
DI u32x4 clean(u32x4 v) { return __builtin_elementwise_min(v, u32x4{EMPTY - 1, EMPTY - 1, EMPTY - 1, EMPTY - 1}); }
DI unsigned umax4(unsigned m, u32x4 v) { return max(max(max(m, v[0]), v[1]), max(v[2], v[3])); }
DI float bflo(unsigned u) { return __uint_as_float(u << 16); }
DI float bfhi(unsigned u) { return __uint_as_float(u & 0xFFFF0000u); }
DI int rowof(int p) { return 32 * (((p >> 5) & 0x3C) + (p & 3)) + ((p << 2) & 16) + ((p >> 3) & 15); }
DI int rpos(int p) { return ((p << 8) & 1024) + ((p >> 3) << 2) + (p & 3); }
DI int trow(int T, int r) { return 8 * (r >> 2) + 4 * T + (r & 3); }
template <int CTRL> DI float dpp_add(float v) { return v + __int_as_float(__builtin_amdgcn_update_dpp(0, __float_as_int(v), CTRL, 0xF, 0xF, true)); }
DI float row16_sum(float v) { v = dpp_add<0x128>(v); v = dpp_add<0x124>(v); v = dpp_add<0x122>(v); return dpp_add<0x121>(v); }
DI f32x4 mfma16(u32x4 a, u32x4 b, f32x4 c) {
  return __builtin_amdgcn_mfma_f32_16x16x32_bf16(__builtin_bit_cast(bf16x8, a), __builtin_bit_cast(bf16x8, b), c, 0, 0, 0);
}
DI void mfma16v(f32x4& c, u32x4 a, u32x4 b) { asm volatile("v_mfma_f32_16x16x32_bf16 %0, %1, %2, %0" : "+v"(c) : "v"(a), "v"(b)); }
DI u32x2 ldtr(lptr p) { return __builtin_bit_cast(u32x2, __builtin_amdgcn_ds_read_tr16_b64_v4i16((LDS_AS s16x4*)p)); }
DI u32x4 ld128(lptr p) { return *(LDS_AS u32x4*)p; }
DI u32x2 ld64(lptr p) { return *(LDS_AS u32x2*)p; }
#define GL(v, s, o, x) "global_load_lds_dwordx4 " v ", " s " offset:" #o x "\n\t"
#define M0SET "s_mov_b32 %[sv], m0\n\ts_mov_b32 m0, %[l]\n\ts_nop 2\n\t"
#define M0ADD(x) "s_add_u32 m0, m0, " x "\n\ts_nop 0\n\t"
#define M0RST "s_mov_b32 m0, %[sv]"
DI void dma2(unsigned v, const char* sb, unsigned l) { unsigned sv;
  asm volatile(M0SET GL("%[v]", "%[s]", 0, " sc1") GL("%[v]", "%[s]", 1024, " sc1") M0RST : [sv] "=&s"(sv) : [v] "v"(v), [s] "s"(sb), [l] "s"(l) : "memory"); }
DI void dma4(unsigned v, const char* sb, unsigned l) { unsigned sv;
  asm volatile(M0SET GL("%[v]", "%[s]", 0, "") GL("%[v]", "%[s]", 1024, "") GL("%[v]", "%[s]", 2048, "") GL("%[v]", "%[s]", 3072, "") M0RST
               : [sv] "=&s"(sv) : [v] "v"(v), [s] "s"(sb), [l] "s"(l) : "memory"); }
DI void dma6_sc1(unsigned v, const char* sb, unsigned l) { unsigned sv;
  asm volatile(M0SET GL("%[v]", "%[s]", 0, " sc1") GL("%[v]", "%[s]", 1024, " sc1") GL("%[v]", "%[s]", 2048, " sc1") M0ADD("3072")
               GL("%[w]", "%[s]", 0, " sc1") GL("%[w]", "%[s]", 1024, " sc1") GL("%[w]", "%[s]", 2048, " sc1") M0RST
               : [sv] "=&s"(sv) : [v] "v"(v), [w] "v"(v + 3072), [s] "s"(sb), [l] "s"(l) : "memory", "scc"); }
template <int N> DI void vm_bar() { asm volatile("s_waitcnt vmcnt(%0)\n\ts_barrier" ::"i"(N) : "memory"); }
DI void lds_bar() { asm volatile("s_waitcnt lgkmcnt(0)\n\ts_barrier" ::: "memory"); }
DI void raw_bar() { asm volatile("s_barrier" ::: "memory"); }
template <int N> DI void nap() { __builtin_amdgcn_s_sleep(N); asm volatile("" ::: "memory"); }
DI void drain() { asm volatile("s_waitcnt vmcnt(0)" ::: "memory"); }
#define CODE_ADDR(sym) ({ unsigned lo_, hi_; \
    asm volatile("s_getpc_b64 vcc\n\ts_add_u32 %0, vcc_lo, " sym "@rel32@lo+4\n\ts_addc_u32 %1, vcc_hi, " sym "@rel32@hi+12" : "=s"(lo_), "=s"(hi_) :: "vcc", "scc"); \
    (const unsigned*)(((unsigned long long)hi_ << 32) | lo_); })
#define SYM_MAIN "_Z6k_mainPKtS0_S0_S0_S0_PcPt"
constexpr int CODE_LINES = 360;
DI int wave_id() { return __builtin_amdgcn_readfirstlane(threadIdx.x >> 6); }
DI unsigned lds_addr(lptr p) { return (unsigned)(size_t)p; }

DI int kappa_to_i(int kap) {
  int ks = kap >> 5, g = (kap >> 3) & 3, e = kap & 7;
  if (ks < 16) return (2 * ks + (g >> 1)) * 20 + 8 * (e >> 2) + 4 * (g & 1) + (e & 3);
  return (8 * (ks - 16) + 2 * g + (e >> 2)) * 20 + 16 + (e & 3);
}

DI unsigned wo_frag(int nt, int ks) {
  const int ct = nt / 5, c5 = nt - 5 * ct, kh = ks < 16 ? ks >> 3 : (ks - 16) >> 1, i = ks < 16 ? ks & 7 : 8 + (ks & 1);
  const int a = kh ? (c5 >= 3 ? c5 - 3 : c5 + 2) : c5;
  return ((ct * 2 + kh) * 50 + 5 * i + a) * 1024;
}

constexpr int SLOT32 = 32768;
DI void dma_a(const unsigned* vm, const char* mb, unsigned l) { unsigned sv;
  asm volatile(M0SET GL("%[a0]", "%[m]", 0, " sc1") GL("%[a1]", "%[m]", 1024, " sc1") GL("%[a2]", "%[m]", 2048, " sc1") GL("%[a3]", "%[m]", 3072, " sc1") M0RST
               : [sv] "=&s"(sv) : [a0] "v"(vm[0]), [a1] "v"(vm[1]), [a2] "v"(vm[2]), [a3] "v"(vm[3]), [m] "s"(mb), [l] "s"(l) : "memory"); }
DI void dma_s(unsigned v0, unsigned v1, const char* sb, unsigned l) { unsigned sv;
  asm volatile(M0SET GL("%[v0]", "%[s]", 0, " sc1") GL("%[v0]", "%[s]", 1024, " sc1") GL("%[v1]", "%[s]", 2048, " sc1") GL("%[v1]", "%[s]", 3072, " sc1") M0RST
               : [sv] "=&s"(sv) : [v0] "v"(v0), [v1] "v"(v1), [s] "s"(sb), [l] "s"(l) : "memory"); }
DI void dma1_sc1(unsigned v, const char* sb, unsigned l) { unsigned sv;
  asm volatile(M0SET GL("%[v]", "%[s]", 0, " sc1") M0RST : [sv] "=&s"(sv) : [v] "v"(v), [s] "s"(sb), [l] "s"(l) : "memory"); }
DI void dma1(unsigned v, const char* sb, unsigned l) { unsigned sv;
  asm volatile(M0SET GL("%[v]", "%[s]", 0, "") M0RST : [sv] "=&s"(sv) : [v] "v"(v), [s] "s"(sb), [l] "s"(l) : "memory"); }

constexpr int L_I2 = 40 * 1056, LDS_PREP = L_I2 + 20 * 2064 + 1024;
DI void prep(const u16* x, const u16* wr, const u16* wi, const u16* wqkv, const u16* wo, char* ws) {
  extern __shared__ __attribute__((aligned(16))) char smem[];
  const lptr lds = (lptr)smem;
  const int tid = threadIdx.x, lane = tid & 63, w = wave_id(), g = lane >> 4, r16 = lane & 15;
  const int job = blockIdx.x;
  if (__builtin_expect(job < 144, 1)) {
    const bool cols = job >= 64 && job < 124;
    const int cj = job - 64, cb = cj < 48 ? (cj & 48) + 2 * (cj & 7) + ((cj >> 3) & 1) : cj;
    u32x4 a1[3], m2[3][2]; u32x2 a1b[3];
    if (w < 5) dma1(tid * 16, (const char*)&g_dt, lds_addr(lds) + LDS_PREP + w * 1024);
    if (cols) {
#pragma unroll
      for (int i = 0; i < 5; i++) dma1((16 * (5 * w + i) + (lane >> 2)) * 3840 + cb * 64 + (lane & 3) * 16, (const char*)wqkv, lds_addr(lds) + (5 * w + i) * 1056);
    } else {
      const char* src = (const char*)(job < 64 ? x : wo);
      const int sl = 8 * (w & 3) + 4 * (w >> 2);
      src += (job < 64 ? rowof(32 * job + sl) : kappa_to_i(32 * (job - 124) + sl)) * 1280;
      const int st = (job < 64 ? 31 : 0) * 1280;
#pragma unroll
      for (int i = 0; i < 5; i++) dma1(1024 * i + lane * 16 + ((i == 4 || lane < 16 * i) ? i - 1 : i) * st, src, lds_addr(lds) + (5 * w + i) * 1024);
    }
    unsigned so[3][2]; float sc[2] = {1.f, 1.f};
    if (cols) {
#pragma unroll
      for (int m = 0; m < 2; m++) {
        const int c = 32 * cb + 16 * m + r16;
        const int which = c / 640, rem = c - which * 640, head = rem / 20, d = rem - head * 20, grp = d >> 2;
        const int T = grp < 4 ? (which == 2 ? 2 : 0) + (grp & 1) : (which == 1 ? 3 : 2), gs = grp < 4 ? (which == 1 ? 2 : 0) + (grp >> 1) : (which == 2 ? 3 : 2);
        if (which == 0) sc[m] = 0.22360679774997896f * 1.4426950408889634f;
#pragma unroll
        for (int i = 0; i < 3; i++) so[i][m] = WS_WQ + ((head * 4 + T) * 20 + w + 8 * i) * 1024 + (g * 16 + 4 * gs + (d & 3)) * 16;
      }
    } else if (job < 64) {
#pragma unroll
      for (int i = 0; i < 3; i++)
#pragma unroll
        for (int T = 0; T < 2; T++) so[i][T] = WS_RB0 + (job * 641 + (job & 1) + 32 * (w + 8 * i) + trow(T, r16)) * 64 + g * 16;
    } else {
#pragma unroll
      for (int i = 0; i < 3; i++) {
        const unsigned f = wo_frag(2 * (w + 8 * i) + (r16 >> 3), job - 124);
#pragma unroll
        for (int T = 0; T < 2; T++) so[i][T] = WS_WOF + f + (g * 16 + (trow(T, r16) & 15)) * 16;
      }
    }
    if (w >= 4) { so[2][0] = WS_END; so[2][1] = WS_END; }
    asm volatile("" : "+v"(so[0][0]), "+v"(so[0][1]), "+v"(so[1][0]), "+v"(so[1][1]), "+v"(so[2][0]), "+v"(so[2][1]), "+v"(sc[0]), "+v"(sc[1]));
    const f32x4 z4 = {0.f, 0.f, 0.f, 0.f};
    const lptr i2 = lds + L_I2;
    drain();
#pragma unroll
    for (int i = 0; i < 3; i++)
#pragma unroll
      for (int T = 0; T < 2; T++)
        asm volatile("global_load_dwordx4 %0, %1, %2" : "=v"(m2[i][T]) : "v"(((((i < 2 || w < 4) ? w + 8 * i : w) * 2 + T) * 64 + lane) * 16), "s"(&g_dt.m2[0][0][0][0]) : "memory");
    raw_bar();
#pragma unroll
    for (int T = 0; T < 3; T++) { a1[T] = ld128(lds + LDS_PREP + T * 1024 + lane * 16); a1b[T] = ld64(lds + LDS_PREP + 3072 + T * 512 + lane * 8); }
    f32x4 t1[4][3];
    if (cols) {
      const lptr src = lds + (4 * g + (r16 >> 2)) * 1056 + (w >> 1) * 256 + (w & 1) * 32 + (lane & 3) * 8, srb = src + (32 - 8 * (g >> 1)) * 1056;
#pragma unroll
      for (int i = 0; i < 4; i++) {
        const u32x2 f0 = ldtr(src + i * 64), f1 = ldtr(src + 16 * 1056 + i * 64), f2 = ldtr(srb + i * 64);
#pragma unroll
        for (int T = 0; T < 3; T++) t1[i][T] = mfma16(u32x4{a1b[T][0], a1b[T][1], 0u, 0u}, u32x4{f2[0], f2[1], 0u, 0u}, mfma16(a1[T], u32x4{f0[0], f0[1], f1[0], f1[1]}, z4));
      }
      const int xo = ((w >> 1) * 8) ^ (((r16 >> 1) & 3) * 16);
      const lptr dst = i2 + 2 * g * 2064 + (16 * (w & 1) + r16) * 64;
#pragma unroll
      for (int T = 0; T < 3; T++)
#pragma unroll
        for (int e = 0; e < 4; e++)
          if (T < 2 || g < 2) *(LDS_AS u32x2*)(dst + (8 * T + (e >> 1)) * 2064 + (xo ^ ((e & 1) * 32))) = u32x2{pk2(t1[0][T][e], t1[1][T][e]), pk2(t1[2][T][e], t1[3][T][e])};
    } else {
      const lptr src = lds + w * 5120 + lane * 8, srb = lds + w * 5120 + 1024 + (lane & 31) * 8;
#pragma unroll
      for (int i = 0; i < 4; i++) {
        const u32x2 f0 = ldtr(src + i * 1280), f1 = ldtr(src + i * 1280 + 512), f2 = ldtr(srb + i * 1280);
#pragma unroll
        for (int T = 0; T < 3; T++) t1[i][T] = mfma16(u32x4{f2[0], f2[1], 0u, 0u}, u32x4{a1b[T][0], a1b[T][1], 0u, 0u}, mfma16(u32x4{f0[0], f0[1], f1[0], f1[1]}, a1[T], z4));
      }
      const int xo = ((r16 & 1) * 32 + g * 8) ^ ((w & 1) * 32);
      const lptr dst = i2 + (r16 >> 1) * 2064 + w * 256;
#pragma unroll
      for (int i = 0; i < 4; i++)
#pragma unroll
        for (int T = 0; T < 3; T++)
          if (T < 2 || r16 < 8) *(LDS_AS u32x2*)(dst + T * 8 * 2064 + i * 64 + (xo ^ ((i >> 1) * 16))) = pk4(t1[i][T]);
    }
    lds_bar();
    const rsrc_t rw = mk_rsrc(ws, WS_END);
    asm volatile("s_waitcnt vmcnt(0)" : "+v"(m2[0][0]), "+v"(m2[0][1]), "+v"(m2[1][0]), "+v"(m2[1][1]), "+v"(m2[2][0]), "+v"(m2[2][1]));
    f32x4 sq[2] = {z4, z4};
    u32x4 f[3][2];
#pragma unroll
    for (int i = 0; i < 3; i++) {
      const lptr fp = i2 + ((i < 2 || w < 4) ? w + 8 * i : 0) * 2064 + r16 * 64 + ((g ^ ((r16 >> 1) & 3)) * 16);
      f[i][0] = ld128(fp); f[i][1] = ld128(fp + 1024);
    }
    if (cols) {
#pragma unroll
      for (int i = 0; i < 3; i++)
#pragma unroll
        for (int m = 0; m < 2; m++) st128<16>(rw, so[i][m], pk8(mfma16(m2[i][0], f[i][m], z4) * sc[m], mfma16(m2[i][1], f[i][m], z4) * sc[m]));
    } else {
#pragma unroll
      for (int i = 0; i < 3; i++)
#pragma unroll
        for (int T = 0; T < 2; T++) {
          const f32x4 d0 = mfma16(f[i][0], m2[i][T], z4), d1 = mfma16(f[i][1], m2[i][T], z4);
          st128<16>(rw, so[i][T], pk8(d0, d1));
          const float k = ((i == 2 && w >= 4) || (i == 0 && T == 0 && tid < 64 && r16 == 1)) ? 0.f : 1.f;
          sq[0] += d0 * d0 * k; sq[1] += d1 * d1 * k;
        }
    }
    LDS_AS float* sl = (LDS_AS float*)(lds + LDS_PREP - 1024);
    if (job < 64) {
#pragma unroll
      for (int m = 0; m < 2; m++) {
#pragma unroll
        for (int e = 0; e < 4; e++) sq[m][e] = row16_sum(sq[m][e]);
        if (r16 == 0) *(LDS_AS f32x4*)(sl + w * 32 + 8 * g + 4 * m) = sq[m];
      }
    }
    if (job < 64) {
      lds_bar();
      if (tid < 32) {
        float v = 0.f;
#pragma unroll
        for (int i = 0; i < 8; i++) v += sl[i * 32 + tid];
        st32<16>(rw, WS_RS1 + rpos(32 * job + tid) * 4, __float_as_uint(__builtin_amdgcn_rsqf((v + 640.f * 1e-5f) * RSK)));
      }
    }
    return;
  }
  const int mi = job - 144;
  const rsrc_t rw = mk_rsrc(ws, WS_END);
  if (mi < 64) {
    const int m = mi >> 3, oc = mi & 7;
    const char* src = (const char*)((w >> 2) ? wi : wr) + (64 * (4 * m + (w & 3)) + 8 * oc) * 642;
    dma4(lane * 16, src, lds_addr(lds) + w * 5152);
    dma1(4096 + lane * 16, src, lds_addr(lds) + w * 5152 + 4096);
    if (lane == 0) dma1(5120, src, lds_addr(lds) + w * 5152 + 5120);
    vm_bar<0>();
    LDS_AS u16* L = (LDS_AS u16*)lds + (tid & 7) * 321;
#pragma unroll
    for (int it = 0; it < 5; it++) {
      const int c = (tid >> 3) + 64 * it, col = freq_of(c >> 4, c & 15);
      u32x4 o;
#pragma unroll
      for (int e = 0; e < 4; e++) o[e] = (unsigned)L[e * 2576 + col] | ((unsigned)L[(4 + e) * 2576 + col] << 16);
      st128<16>(rw, WS_WF + c * 8192 + (m * 64 + 8 * oc + (tid & 7)) * 16, o);
    }
#pragma unroll
    for (int it = 0; it < 3; it++) st128<16>(rw, WS_RB1 + ((it * 64 + mi) * 512 + tid) * 16, u32x4{EMPTY, EMPTY, EMPTY, EMPTY});
  } else if (mi < 84) {
    unsigned pf0;
    {
      const unsigned* cm = CODE_ADDR(SYM_MAIN);
      const unsigned* p = mi >= 80 ? (const unsigned*)&g_it + min(tid, 359) * 32 : (mi & 8) ? (const unsigned*)&g_dt.m2[0][0][0][0] + min(tid, 319) * 32 : tid < 80 ? (const unsigned*)&g_ct + tid * 32 : cm + min(tid - 80, CODE_LINES - 1) * 32;
      asm volatile("global_load_dword %0, %1, off" : "=v"(pf0) : "v"(p) : "memory");
    }
    const char* src = (const char*)wo + kappa_to_i(32 * (mi - 64) + 8 * (w & 3) + 4 * (w >> 2)) * 1280;
    dma4(lane * 16, src, lds_addr(lds) + w * 5152);
    dma1(4096 + lane * 16, src, lds_addr(lds) + w * 5152 + 4096);
    asm volatile("s_waitcnt vmcnt(0)\n\ts_barrier" : "+v"(pf0) :: "memory");
#pragma unroll
    for (int i = 0; i < 5; i++) {
      const lptr a = lds + g * 5152 + (r16 >> 2) * 1280 + (5 * w + i) * 32 + (lane & 3) * 8;
      const u32x2 lo = ldtr(a), hi = ldtr(a + 4 * 5152);
      st128<16>(rw, WS_WOP + wo_frag(5 * w + i, mi - 64) + lane * 16, u32x4{lo[0], lo[1], hi[0], hi[1]});
    }
  } else {
    const int i0 = (mi - 84) * 512 + tid;
    nap<K1DELAY>();
    if (i0 < 6272) st128<16>(rw, WS_FL + i0 * 16, u32x4{0u, 0u, 0u, 0u});
#pragma unroll
    for (int it = 0; it < 6; it++) { const int i = 98304 + i0 + it * 14336; if (i < (int)RBSZ / 16) st128<16>(rw, WS_RB1 + i * 16, u32x4{EMPTY, EMPTY, EMPTY, EMPTY}); }
  }
}

#define PINF(x) asm volatile("" : "+v"(x))
DI void wave_sync(LDS_AS unsigned* c, unsigned n) {
  asm volatile("s_waitcnt lgkmcnt(0)" ::: "memory");
  if ((threadIdx.x & 63) == 0) __hip_atomic_fetch_add(c, 1u, __ATOMIC_RELAXED, __HIP_MEMORY_SCOPE_WORKGROUP);
  while (__builtin_amdgcn_readfirstlane(*(volatile LDS_AS unsigned*)c) < n) {}
  asm volatile("" ::: "memory");
}
DI void put_scale(lptr lds, int q, int tid, const u32x2* sp) {
  f32x4 r = {640.f * 1e-5f, 640.f * 1e-5f, 640.f * 1e-5f, 640.f * 1e-5f};
#pragma unroll
  for (int i = 0; i < 8; i++) r += f32x4{bflo(sp[i][0]), bfhi(sp[i][0]), bflo(sp[i][1]), bfhi(sp[i][1])};
#pragma unroll
  for (int e = 0; e < 4; e++) r[e] = __builtin_amdgcn_rsqf(r[e] * RSK);
  ((LDS_AS f32x4*)(lds + 2 * SLOT32))[q * 256 + tid] = r;
}
struct ColIn { u32x4 c64, s64, c32, s32, wf[2]; };
DI int col_of() { return blockIdx.x < 64 ? 192 + 2 * blockIdx.x + (wave_id() >> 2) : blockIdx.x - 64; }
DI void col_load(ColIn& k, rsrc_t rw, bool tab, bool filt) {
  int lane = threadIdx.x & 63; PINF(lane);
  const int w = wave_id() & 3;
  const u32x4* ct = (const u32x4*)&g_ct + lane;
  if (tab) { k.c64 = ct[(2 * w) * 64]; k.s64 = ct[(2 * w + 1) * 64]; k.c32 = ct[8 * 64]; k.s32 = ct[9 * 64]; }
  if (filt) {
#pragma unroll
    for (int t = 0; t < 2; t++) k.wf[t] = gl128<16>(rw, WS_WF + col_of() * 8192 + ((4 * t + (lane >> 4)) * 64 + 16 * w + (lane & 15)) * 16);
  }
}
template <bool FIRST>
DI void col_phase(rsrc_t rw, lptr lds, ColIn kin) {
  int lane = threadIdx.x & 63; PINF(lane);
  const int wv = wave_id(), w = wv & 3, q = wv >> 2, tid = w * 64 + lane, g = lane >> 4, r16 = lane & 15;
  const int c = col_of();
  const lptr img = lds + q * SLOT32;
  f32x4 xr[2], xi[2], rs[2]; u32x4 xb[2];
  u32x4 c64[2], s64[2], n64[2], c32[2], s32[2], n32[2];
  if (FIRST) col_load(kin, rw, true, false);
  SB();
  f32x4 t1c[2], t1s[2], t2c[2], t2s[2];
  {
    const float f1 = (float)(16 * w + r16) * (1.f / 2048.f), b1 = (float)(4 * g), f2 = (float)(16 * w + 4 * g) * (1.f / 2048.f), b2 = (float)r16;
#pragma unroll
    for (int t = 0; t < 2; t++) {
#pragma unroll
      for (int e = 0; e < 4; e++) {
        const float a1 = (b1 + (float)(16 * t + e)) * f1, a2 = (b2 + (float)(16 * t)) * (f2 + (float)e * (1.f / 2048.f));
        t1c[t][e] = __builtin_amdgcn_cosf(a1); t1s[t][e] = __builtin_amdgcn_sinf(-a1);
        t2c[t][e] = __builtin_amdgcn_cosf(a2); t2s[t][e] = __builtin_amdgcn_sinf(a2);
      }
      PINF(t1c[t]); PINF(t1s[t]); PINF(t2c[t]); PINF(t2s[t]);
    }
  }
  int wo_ = r16 * 128 + (((2 * w + (g >> 1)) ^ (r16 >> 1)) * 16) + (g & 1) * 8, ro_ = r16 * 128 + ((g ^ (r16 >> 1)) * 16), ro2 = ro_ ^ 64;
  PINF(wo_); PINF(ro_); PINF(ro2);
  {
    const unsigned par = (lane & 1) ? 0x80008000u : 0u;
    c64[0] = kin.c64; s64[0] = kin.s64; c32[0] = kin.c32; s32[0] = kin.s32;
    c64[1] = c64[0] ^ par; s64[1] = s64[0] ^ par; n64[0] = s64[0] ^ 0x80008000u; n64[1] = s64[1] ^ 0x80008000u;
    c32[1] = c32[0] ^ 0x80000000u; s32[1] = s32[0] ^ 0x80000000u; n32[0] = s32[0] ^ 0x80008000u; n32[1] = s32[0] ^ 0x00008000u;
    PINF(c64[1]); PINF(s64[1]); PINF(n64[0]); PINF(n64[1]); PINF(c32[1]); PINF(s32[1]); PINF(n32[0]); PINF(n32[1]);
  }
  SB();
  if (FIRST) {
    raw_bar();
#pragma unroll
    for (int t = 0; t < 2; t++) { xb[t] = gl128<16>(rw, WS_RB0 + ((tid >> 2) * 641 + ((tid >> 2) & 1) + 2 * c + t) * 64 + (tid & 3) * 16); rs[t] = asf(gl128<16>(rw, WS_RS1 + (1024 * t + 4 * tid) * 4)); }
    col_load(kin, rw, false, true);
    SB();
#pragma unroll
    for (int t = 0; t < 2; t++) {
      xr[t] = f32x4{bflo(xb[0][2 * t]), bfhi(xb[0][2 * t]), bflo(xb[0][2 * t + 1]), bfhi(xb[0][2 * t + 1])};
      xi[t] = f32x4{bflo(xb[1][2 * t]), bfhi(xb[1][2 * t]), bflo(xb[1][2 * t + 1]), bfhi(xb[1][2 * t + 1])};
    }
  } else {
    drain();
    u32x2 sp[8];
    const unsigned so = WS_SSQ + (blockIdx.x & 7) * 32768 + (1024 * q + 4 * tid) * 2, xo = WS_R3 + ((tid >> 2) * 642 + 2 * c) * 64 + (tid & 3) * 16;
    bool bs = true, bx = true;
#pragma unroll 1
    for (int spins = 0; spins < (1 << 18); spins++) {
      if (bx) {
#pragma unroll
        for (int t = 0; t < 2; t++) { xr[t] = asf(gl128<16>(rw, xo + 64 * 642 * 64 * t)); xi[t] = asf(gl128<16>(rw, xo + 64 * 642 * 64 * t + 64)); }
      }
      if (bs) {
#pragma unroll
        for (int i = 0; i < 8; i++) sp[i] = gl64<16>(rw, so + i * 4096);
      }
      unsigned m = 0;
#pragma unroll
      for (int t = 0; t < 2; t++) m = umax4(umax4(m, asu(xr[t])), asu(xi[t]));
      bx = __builtin_amdgcn_ballot_w64(m == EMPTY) != 0;
      m = 0;
#pragma unroll
      for (int i = 0; i < 8; i++) m = max(max(m, sp[i][0]), sp[i][1]);
      bs = __builtin_amdgcn_ballot_w64(m == EMPTY) != 0;
      if (!bs && !bx) break;
      nap<YSLEEP>();
    }
    put_scale(lds, q, tid, sp);
    LDS_AS f32x4* ex = (LDS_AS f32x4*)(lds + 2 * SLOT32) + tid;
    lds_bar();
    rs[0] = ex[0]; rs[1] = ex[256];
  }
#pragma unroll
  for (int t = 0; t < 2; t++) {
    u32x2 pb = pk4(xi[t] * rs[t]);
    if (c == 0) pb = u32x2{0u, 0u};
    *(LDS_AS u32x2*)(img + wo_ + t * 2048) = pk4(xr[t] * rs[t]);
    *(LDS_AS u32x2*)(img + 4096 + wo_ + t * 2048) = pb;
  }
  lds_bar();
  u32x4 ur[2][2], ui[2][2], br, bi;
  f32x4 ar[2], ai[2];
  const f32x4 z4 = {0.f, 0.f, 0.f, 0.f};
  auto rd_in = [&](int off) {
#pragma unroll
    for (int ks = 0; ks < 2; ks++)
#pragma unroll
      for (int t = 0; t < 2; t++) { const lptr p = img + off + (ks ? ro2 : ro_) + t * 2048; ur[t][ks] = ld128(p); ui[t][ks] = ld128(p + 4096); }
    SB();
  };
  auto cmul = [&](const f32x4* cc, const f32x4* sn) {
    br = pk8(ar[0] * cc[0] - ai[0] * sn[0], ar[1] * cc[1] - ai[1] * sn[1]);
    bi = pk8(ai[0] * cc[0] + ar[0] * sn[0], ai[1] * cc[1] + ar[1] * sn[1]);
  };
  rd_in(0);
#pragma unroll
  for (int ks = 0; ks < 2; ks++) {
#pragma unroll
    for (int t = 0; t < 2; t++) { ar[t] = mfma16(ur[t][ks], c64[ks], ks ? ar[t] : z4); ai[t] = mfma16(ui[t][ks], c64[ks], ks ? ai[t] : z4); }
#pragma unroll
    for (int t = 0; t < 2; t++) { ar[t] = mfma16(ui[t][ks], s64[ks], ar[t]); ai[t] = mfma16(ur[t][ks], n64[ks], ai[t]); }
  }
  const f32x4 fa[2] = {asf(kin.wf[0] << 16), asf(kin.wf[1] << 16)}, fb[2] = {asf(kin.wf[0] & 0xFFFF0000u), asf(kin.wf[1] & 0xFFFF0000u)};
  cmul(t1c, t1s);
#pragma unroll
  for (int t = 0; t < 2; t++) { ar[t] = mfma16(c32[t], br, z4); ai[t] = mfma16(c32[t], bi, z4); }
#pragma unroll
  for (int t = 0; t < 2; t++) { ar[t] = mfma16(s32[t], bi, ar[t]); ai[t] = mfma16(n32[t], br, ai[t]); }
  cmul(fa, fb);
#pragma unroll
  for (int t = 0; t < 2; t++) { ar[t] = mfma16(br, c32[t], z4); ai[t] = mfma16(bi, c32[t], z4); }
#pragma unroll
  for (int t = 0; t < 2; t++) { ar[t] = mfma16(bi, n32[t], ar[t]); ai[t] = mfma16(br, s32[t], ai[t]); }
  cmul(t2c, t2s);
#pragma unroll
  for (int t = 0; t < 2; t++) {
    *(LDS_AS u32x2*)(img + 8192 + wo_ + t * 2048) = u32x2{br[2 * t], br[2 * t + 1]};
    *(LDS_AS u32x2*)(img + 12288 + wo_ + t * 2048) = u32x2{bi[2 * t], bi[2 * t + 1]};
  }
  lds_bar();
  rd_in(8192);
#pragma unroll
  for (int ks = 0; ks < 2; ks++) {
#pragma unroll
    for (int t = 0; t < 2; t++) { ar[t] = mfma16(c64[ks], ur[t][ks], ks ? ar[t] : xr[t]); ai[t] = mfma16(c64[ks], ui[t][ks], ks ? ai[t] : xi[t]); }
#pragma unroll
    for (int t = 0; t < 2; t++) { ar[t] = mfma16(n64[ks], ui[t][ks], ar[t]); ai[t] = mfma16(s64[ks], ur[t][ks], ai[t]); }
  }
  const unsigned ob = (FIRST ? WS_RB1 : WS_RB2) + (2 * c * RBP + 8 * tid) * 2;
  st128<16>(rw, ob, clean(pk8(ar[0], ar[1])));
  st128<16>(rw, ob + RBP * 2, c == 0 ? u32x4{0u, 0u, 0u, 0u} : clean(pk8(ai[0], ai[1])));
}

DI f32x16 mfma32(u32x4 a, u32x4 b, f32x16 c) {
  return __builtin_amdgcn_mfma_f32_32x32x16_bf16(__builtin_bit_cast(bf16x8, a), __builtin_bit_cast(bf16x8, b), c, 0, 0, 0);
}
DI f32x16 mfma32k8(u32x2 a, u32x2 b, f32x16 c) {
  return __builtin_amdgcn_mfma_f32_32x32x8bf16_1k(__builtin_bit_cast(s16x4, a), __builtin_bit_cast(s16x4, b), c, 0, 0, 0);
}
DI f32x16 scores(u32x4 k1, u32x2 k2, u32x4 q1, u32x2 q2) {
  f32x16 z;
#pragma unroll
  for (int i = 0; i < 16; i++) z[i] = 0.f;
  f32x16 s = mfma32(k1, q1, z);
  asm volatile("s_nop 11" : "+v"(s));
  return mfma32k8(k2, q2, s);
}
DI float max16(const f32x16& s) {
  float m = fmaxf(s[0], s[1]);
#pragma unroll
  for (int i = 2; i < 16; i++) m = fmaxf(m, s[i]);
  return m;
}
#define STR_(x) #x
#define STR(x) STR_(x)
#define KOF(t) ((((t)&7)>>1)*11264+((t)&1)*1280)
#define VOF(t) ((((t)&7)>>1)*11264+5120+((t)&1)*1536)
#define EXP(d, s) "v_exp_f32 v" #d ", v" #s "\n\t"
#define CVT(d, s) "v_cvt_pk_bf16_f32 v" #d ", v" #s ", v[" #s "+1]\n\t"
#define STEPA(t) \
  "v_mfma_f32_32x32x16_bf16 v[48:63], v[80:83], v[136:139], v[120:135]\n\t" \
  "ds_read_b128 v[80:83], v100 offset:" STR(KOF(t+1)) "\n\t" \
  EXP(64,32) EXP(65,33) CVT(78,68) CVT(79,70) EXP(66,34) EXP(67,35) \
  "v_mfma_f32_32x32x16_bf16 v[104:119], v[90:93], v[76:79], v[104:119]\n\t" \
  "ds_read_b64_tr_b16 v[90:91], v102 offset:" STR(VOF(t)+768) "\n\t" \
  "ds_read_b64_tr_b16 v[92:93], v102 offset:" STR(VOF(t)+1152) "\n\t" \
  EXP(68,36) EXP(69,37) CVT(72,64) CVT(73,66) EXP(70,38) EXP(71,39) \
  "v_mfma_f32_32x32x8_bf16 v[48:63], v[84:85], v[140:141], v[48:63]\n\t" \
  "ds_read_b64 v[84:85], v101 offset:" STR(KOF(t+1)) "\n\t" \
  EXP(64,40) EXP(65,41) CVT(74,68) CVT(75,70) EXP(66,42) EXP(67,43) \
  "s_waitcnt lgkmcnt(4)\n\t" \
  "v_mfma_f32_32x32x16_bf16 v[0:15], v[86:89], v[72:75], v[0:15]\n\t" \
  EXP(68,44) EXP(69,45) CVT(76,64) CVT(77,66) EXP(70,46) EXP(71,47)
#define STEPB(t) \
  "s_waitcnt lgkmcnt(0)\n\t" \
  "v_mfma_f32_32x32x16_bf16 v[32:47], v[80:83], v[94:97], v[16:31]\n\t" \
  EXP(64,48) EXP(65,49) CVT(78,68) CVT(79,70) EXP(66,50) EXP(67,51) \
  "v_mfma_f32_32x32x16_bf16 v[0:15], v[90:93], v[76:79], v[0:15]\n\t" \
  EXP(68,52) EXP(69,53) CVT(72,64) CVT(73,66) EXP(70,54) EXP(71,55) \
  "v_mfma_f32_32x32x8_bf16 v[32:47], v[84:85], v[98:99], v[32:47]\n\t" \
  EXP(64,56) EXP(65,57) CVT(74,68) CVT(75,70) EXP(66,58) EXP(67,59) \
  "v_mfma_f32_32x32x16_bf16 v[104:119], v[86:89], v[72:75], v[104:119]\n\t" \
  "ds_read_b64_tr_b16 v[86:87], v102 offset:" STR(VOF(t+1)) "\n\t" \
  "ds_read_b64_tr_b16 v[88:89], v102 offset:" STR(VOF(t+1)+384) "\n\t" \
  EXP(68,60) EXP(69,61) CVT(76,64) CVT(77,66) EXP(70,62) EXP(71,63)
#define PR(n) "s_setprio " #n "\n\t"
#define CHUNK(i) PR(1) STEPA(2*(i)) PR(0) STEPB(2*(i)) "s_barrier\n\t" PR(2) STEPA(2*(i)+1) STEPB(2*(i)+1)
#define Z4(a) "v_mov_b64 v[" #a ":" #a "+1], 0\n\tv_mov_b64 v[" #a "+2:" #a "+3], 0\n\t"

#define R4(b) "v[" STR(b) ":" STR(b) "+3]"
#define MM(d, a, b) "v_mfma_f32_16x16x32_bf16 " R4(d) ", " R4(a) ", " R4(b) ", " R4(d) "\n\t"
#define GM(B, m, i) MM(16*m+4*i, B+8+4*i, B+4*m)
#define RT(B, m, hf, k) "ds_read_b64_tr_b16 v[" STR(B+4*m+2*hf) ":" STR(B+4*m+2*hf+1) "], v[84+" #m "] offset:" STR(k*8192+hf*1024) "\n\t"
#define RS(B, i, k) "ds_read_b128 " R4(B+8+4*i) ", v86 offset:" STR(i*2048+k*1024) "\n\t"
#define GSTEP(B, C, k) GM(B,0,0) RT(C,0,0,k) RT(C,0,1,k) GM(B,1,0) RS(C,0,k) RT(C,1,0,k) GM(B,0,1) RT(C,1,1,k) RS(C,1,k) GM(B,1,1) RS(C,2,k) \
  GM(B,0,2) RS(C,3,k) GM(B,1,2) GM(B,0,3) GM(B,1,3) MM(32,B,B)
#define GEMM_ASM(EXTRA) \
  Z4(0) Z4(4) Z4(8) Z4(12) Z4(16) Z4(20) Z4(24) Z4(28) Z4(32) "s_mov_b32 %[c], 0\n\ts_barrier\n\t" \
  RT(36,0,0,0) RT(36,0,1,0) RS(36,0,0) RT(36,1,0,0) RT(36,1,1,0) RS(36,1,0) RS(36,2,0) RS(36,3,0) EXTRA \
  "L_gemm_%=:\n\ts_add_u32 %[c], %[c], 1\n\ts_and_b32 %[t], %[c], 3\n\ts_cselect_b32 %[t], 1, -3\n\ts_lshl_b32 %[t], %[t], 15\n\ts_waitcnt lgkmcnt(0)\n\t" \
  GSTEP(36,60,1) \
  "v_add_u32 v84, %[t], v84\n\tv_add_u32 v85, %[t], v85\n\tv_add_u32 v86, %[t], v86\n\ts_waitcnt lgkmcnt(0)\n\ts_barrier\n\ts_setprio 1\n\t" \
  GSTEP(60,36,0) "s_setprio 0\n\t" \
  "s_cmp_lg_u32 %[c], 9\n\ts_cbranch_scc1 L_gemm_%=\n\ts_waitcnt lgkmcnt(0)\n\t" \
  GSTEP(36,60,1) "s_waitcnt lgkmcnt(0)\n\t" \
  MM(32,60,60) GM(60,0,0) GM(60,1,0) GM(60,0,1) GM(60,1,1) "s_nop 4\n\t" \
  "v_cndmask_b32 v36, v32, v33, %[m1]\n\tv_cndmask_b32 v37, v34, v35, %[m1]\n\tv_mov_b32 v38, 0x3727c5ac\n\tv_cndmask_b32 v36, v36, v37, %[m2]\n\t" \
  "v_fmac_f32 v38, 0x3acccccd, v36\n\tv_cmp_u_f32 vcc, v36, v36\n\tv_rsq_f32 v38, v38\n\ts_cbranch_vccz L_gok_%=\n\tds_write_b32 v90, v90\n\t" \
  "L_gok_%=:\n\tds_write_b32 v87, v38\n\ts_waitcnt lgkmcnt(0)\n\ts_barrier\n\t" \
  "ds_read_b32 v36, v88\n\tds_read_b32 v37, v89\n\tds_read_b32 v38, v90\n\t" \
  GM(60,0,2) GM(60,1,2) GM(60,0,3) GM(60,1,3) "s_waitcnt vmcnt(0) lgkmcnt(0)"
#define GEMM_OUT "={v[0:3]}"(acc[0][0]), "={v[4:7]}"(acc[0][1]), "={v[8:11]}"(acc[0][2]), "={v[12:15]}"(acc[0][3]), \
    "={v[16:19]}"(acc[1][0]), "={v[20:23]}"(acc[1][1]), "={v[24:27]}"(acc[1][2]), "={v[28:31]}"(acc[1][3]), \
    "={v36}"(rs[0]), "={v37}"(rs[1]), "={v38}"(vote), "+{v84}"(am0), "+{v85}"(am1), "+{v86}"(as), [c] "=&s"(c_), [t] "=&s"(t_)
#define GEMM_IN "{v87}"(a_w), "{v88}"(a_r[0]), "{v89}"(a_r[1]), "{v90}"(a_v), [m1] "s"(0xAAAAAAAAAAAAAAAAull), [m2] "s"(0xCCCCCCCCCCCCCCCCull)
#define GEMM_CLOB "memory", "scc", "vcc", "v32", "v33", "v34", "v35", "v39", "v40", "v41", "v42", "v43", "v44", "v45", "v46", "v47", "v48", "v49", "v50", \
    "v51", "v52", "v53", "v54", "v55", "v56", "v57", "v58", "v59", "v60", "v61", "v62", "v63", "v64", "v65", "v66", "v67", "v68", "v69", "v70", "v71", \
    "v72", "v73", "v74", "v75", "v76", "v77", "v78", "v79", "v80", "v81", "v82", "v83"
#define ITL(d, a) "global_load_dwordx4 " R4(d) ", v" #a ", %[it]\n\tglobal_load_dwordx4 " R4(d+4) ", v" #a ", %[it] offset:1024\n\t"

constexpr int HR = 4 * KVC;
constexpr int L_A = 2 * HR;
constexpr int L_EX = 15360;
constexpr int L_T = 4 * SLOT32, L_IA = L_T + 10240, L_U = L_A + 10240;
constexpr int L_MISC = L_A + 70 * 1024;
constexpr int LDS_FUSED = L_MISC + 2048;

DI bool poll16(rsrc_t rw, unsigned off, unsigned need) {
  const int lane = threadIdx.x & 63;
#pragma unroll 1
  for (int spins = 0; spins < (1 << 20); spins++) {
    const unsigned v = gl32<16>(rw, off + (lane & 15) * 4);
    const unsigned got = (unsigned)__builtin_amdgcn_ballot_w64(v != 0) & 0xFFFFu;
    if ((got & need) == need) return true;
    nap<1>();
  }
  return false;
}

DI void poll_aux(rsrc_t rw, unsigned off) {
#pragma unroll 1
  for (int spins = 0; spins < (1 << 20); spins++) {
    const u32x4 v = gl128<16>(rw, off + (threadIdx.x & 63) * 16);
    if (!__builtin_amdgcn_ballot_w64(min(min(v[0], v[1]), min(v[2], v[3])) == 0u)) break;
    nap<4>();
  }
}
DI void poll_tag(rsrc_t rw, u32x2 tag, unsigned long long need) {
  const unsigned o = WS_F0 + (blockIdx.x & 7) * 2048 + (threadIdx.x & 63) * 32;
#pragma unroll 1
  for (int spins = 0; spins < (1 << 20); spins++) {
    const u32x4 a = gl128<16>(rw, o), b = gl128<16>(rw, o + 16);
    const unsigned d = (a[0] ^ tag[0]) | (a[1] ^ tag[1]) | (a[2] ^ tag[0]) | (a[3] ^ tag[1]) | (b[0] ^ tag[0]) | (b[1] ^ tag[1]) | (b[2] ^ tag[0]) | (b[3] ^ tag[1]);
    if (!(__builtin_amdgcn_ballot_w64(d != 0u) & need)) break;
    nap<TSLEEP>();
  }
}
#define LAYER_VARS \
  extern __shared__ __attribute__((aligned(16))) char smem[]; \
  const lptr lds = (lptr)smem; \
  const rsrc_t rw = mk_rsrc(ws, WS_END); \
  int lane = threadIdx.x & 63; asm volatile("" : "+v"(lane)); \
  const int w = wave_id(), g = lane >> 4, r16 = lane & 15; \
  const int xcd = blockIdx.x & 7, bi = blockIdx.x >> 3, u = 8 * (xcd & 1) + (bi & 7), qt = 4 * (xcd >> 1) + (bi >> 3); \
  const int hf = u & 1, ct = u >> 1;     \
  constexpr unsigned FKV = WS_FL + (FINAL ? 32768 : 0), KV = WS_KV + (FINAL ? LAYER2 : 0), OB = WS_O + (FINAL ? LAYER2 : 0); \
  constexpr unsigned AUX = WS_AUX + (FINAL ? 1024 : 0), RB = FINAL ? WS_RB2 : WS_RB1; \
  (void)hf; (void)AUX; (void)RB; (void)FKV; \
  LDS_AS unsigned* misc = (LDS_AS unsigned*)(lds + L_MISC + 1024) + (FINAL ? 8 : 0);

template <bool FINAL>
DI void loader(char* ws, u32x2 tag) {
  LAYER_VARS
  {
    const int lw = w - 8;
    {
      unsigned vm[4];
#pragma unroll
      for (int r = 0; r < 4; r++) vm[r] = (4 * (4 * lw + r) + g) * (RBP * 2) + 256 * qt + (((r16 >> 1) ^ (g | ((r & 2) << 1))) * 32) + (r16 & 1) * 16 + 3072 - 1024 * r;
      const unsigned l0 = lds_addr(lds) + lw * 4096, v0 = 2 * lw * 20480 + lane * 16;
      constexpr int CNT = 8;
      auto lds_w = [&](int s) {
        const char* sb = ws + WS_WQ + u * 163840 + s * 2048;
        dma_s(v0, v0 + 20480 - 2048, sb, l0 + (s & 3) * SLOT32 + 16384);
      };
      auto lds_a = [&](int s) {
        dma_a(vm, ws + RB - 3072 + s * (64 * RBP * 2), l0 + (s & 3) * SLOT32); };
#pragma unroll 1
      for (int att = 0;; att++) {
        if (att == 0) { raw_bar(); if (FINAL) raw_bar(); else poll_tag(rw, tag, 0x7FFF0000ull); }
        lds_w(0); lds_w(1);
        if (att == 0) raw_bar();
        lds_w(2); lds_w(3);
        if (FINAL) dma2(40960 + lw * 2048 + lane * 16, (const char*)&g_it, lds_addr(lds) + L_IA + lw * 2048);
        auto probe = [&](int s0, int n) {
          const int pl = min(lane, n - 1);
          const unsigned po = RB + ((64 * (s0 + (pl >> 3)) + 16 * lw + 2 * (pl & 7) + 1) * RBP + 128 * qt + 126) * 2;
#pragma unroll 1
          for (int spins = 0; spins < (1 << 20); spins++) {
            if (!__builtin_amdgcn_ballot_w64(gl32<16>(rw, po) == EMPTY)) break;
            nap<XSLEEP>();
          }
        };
        if (att == 0) {
          raw_bar();
          if (blockIdx.x >= 64) nap<XDELAY>();
          probe(0, 48);
        }
        drain();
        unsigned nd = 0;
        if (att == 0 && lane == 0) nd = __hip_atomic_fetch_add(misc + 4, 1u, __ATOMIC_RELAXED, __HIP_MEMORY_SCOPE_WORKGROUP);
        if (nd == 11) st32<16>(rw, AUX + blockIdx.x * 4, 1u);
#pragma unroll 1
        for (int s = 0; s < 4; s++) lds_a(s);
        vm_bar<12>(); vm_bar<8>();
        if (att == 0) probe(6, 32);
        lds_a(4); lds_w(4);
        vm_bar<4 + CNT>();
#pragma unroll 1
        for (int s = 5; s < 10; s++) { lds_a(s); lds_w(s); vm_bar<2 * CNT>(); }
        vm_bar<CNT>();
        poll_aux(rw, AUX);
        if (!FINAL) { poll_tag(rw, tag, ~0ull); if (blockIdx.x == 0 && threadIdx.x == 512) st64<16>(rw, WS_EP, tag); }
        vm_bar<0>();
        if (FINAL) {
#pragma unroll
          for (int i = 0; i < 3; i++) { const int pc = min(3 * lw + i, 9); dma1_sc1(RB + ((64 * pc + lane) * RBP + 128 * qt + 8 * u) * 2, ws, lds_addr(lds) + L_T + pc * 1024); }
        }
        raw_bar();
        if (att || !*(volatile LDS_AS unsigned*)(misc + 2)) break;
      }
    }
    const int hh = lw >> 1, hp = lw & 1;
    const unsigned kvv = KV + (2 * u + hh) * 16 * KVC + hp * 5120 + lane * 16;
    const unsigned lkv = lds_addr(lds) + hh * HR + hp * 5120;
    const unsigned wv = (FINAL ? WS_WOP : WS_WOF) + (ct * 2 + hh) * 51200 + lane * 16;
    const unsigned myfl = FKV + (u * 16 + qt) * 128;
    vm_bar<0>();
    {
      u32x4 v[6];
#pragma unroll
      for (int i = 0; i < 6; i++) v[i] = ld128(lds + hh * HR + hp * 5120 + lane * 16 + i * 1024);
#pragma unroll
      for (int i = 0; i < 6; i++) st128<16>(rw, kvv + qt * KVC + i * 1024, clean(v[i]));
    }
    drain();
    unsigned cnt = 0;
    if (lane == 0) cnt = __hip_atomic_fetch_add(misc, 1u, __ATOMIC_RELAXED, __HIP_MEMORY_SCOPE_WORKGROUP);
    if (__builtin_amdgcn_readfirstlane(cnt) == 3 && lane < 16) st32<16>(rw, FKV + (u * 16 + lane) * 128 + qt * 4, 1u);
    auto ldkv = [&](int seq) {
      const int ch = seq == 0 ? qt : seq < 13 ? ((qt & 12) + 3 + seq) & 15 : (qt & 12) | ((qt + seq) & 3);
      dma6_sc1(kvv + ch * KVC, ws, lkv + (seq & 3) * KVC);
    };
    auto fetch = [&](int seq) {
      const unsigned src = kvv + ((((qt & 12) + 3 + seq) & 15)) * KVC;
      u32x4 v[6];
#pragma unroll 1
      for (int spins = 0; spins < (1 << 18); spins++) {
#pragma unroll
        for (int i = 0; i < 6; i++) v[i] = gl128<16>(rw, src + i * 1024);
        unsigned m = 0;
#pragma unroll
        for (int i = 0; i < 6; i++) m = umax4(m, v[i]);
        if (!__builtin_amdgcn_ballot_w64(m == EMPTY)) break;
        nap<2>();
      }
#pragma unroll
      for (int i = 0; i < 6; i++) *(LDS_AS u32x4*)(lds + hh * HR + hp * 5120 + seq * KVC + lane * 16 + i * 1024) = v[i];
    };
#pragma unroll 1
    for (int attempt = 0;; attempt++) {
      int c = -2;
      if (attempt) { ldkv(0); vm_bar<0>(); }
      else {
        fetch(1); lds_bar();
        fetch(2); poll16(rw, myfl, 12u << (((qt & 12) + 4) & 15)); ldkv(3); lds_bar();
        c = 1;
      }
#pragma unroll 1
      for (; c < 13; c++) {
        if (c == 2 && !attempt) poll16(rw, myfl, 0xFFFFu);
        if (!FINAL && !attempt) {
          if (c >= 2 && c < 9) {
            const u32x4 e = {EMPTY, EMPTY, EMPTY, EMPTY};
            const unsigned o = OB + LAYER2 + (((c - 2) * 256 + blockIdx.x) * 4 + lw) * 2048 + lane * 16;
            st128<16>(rw, o, e); st128<16>(rw, o + 1024, e);
            st128<16>(rw, ((qt & 3) < 2 && c < 8) ? kvv + LAYER2 + qt * KVC + (c - 2) * 1024 : WS_END, e);
            st128<16>(rw, c < 5 ? OB + ((4 * qt + lw) * 32 + 2 * u) * 1280 + min((c - 2) * 1024, 1536) + lane * 16 : WS_END, e);
          }
          if (c == 10) {
            drain();
            unsigned n = 0;
            if (lane == 0) n = __hip_atomic_fetch_add(misc + 3, 1u, __ATOMIC_RELAXED, __HIP_MEMORY_SCOPE_WORKGROUP);
            if (__builtin_amdgcn_readfirstlane(n) == 3 && lane < 32) st32<16>(rw, WS_FL + 65536 + (lane < 16 ? (u * 16 + lane) * 128 + qt * 4 : (r16 * 16 + qt) * 128 + 64 + u * 4), 1u);
          }
        }
        if (c >= 2) { const int e = min(4 * (c - 2) + 2 * hp, 33); dma2(wv + e * 1024, ws, lds_addr(lds) + L_A + (hh * 35 + e) * 1024); }
        ldkv(c + 3);
        if (c == 0) vm_bar<12>();
        if (c >= 2) vm_bar<8>(); else if (c >= 0) vm_bar<6>();
      }
      if (!FINAL && !attempt) poll16(rw, myfl + 65536 + hp * 64, 0xFFFFu);
      vm_bar<0>();
      dma_s(wv + (35 + 7 * hp) * 1024, wv + (35 + 7 * hp) * 1024, ws, lds_addr(lds) + hh * HR + 7 * hp * 1024);
      dma_s(wv + (39 + 7 * hp) * 1024, wv + (39 + 7 * hp) * 1024, ws, lds_addr(lds) + hh * HR + (4 + 7 * hp) * 1024);
      raw_bar();
      vm_bar<0>();
      if (attempt || !*(volatile LDS_AS unsigned*)(misc + 1)) break;
      raw_bar();
    }
    if (!FINAL) { raw_bar(); raw_bar(); }
  }
}

template <bool FINAL>
DI void compute(char* ws, u16* out, ColIn& kin) {
  LAYER_VARS
  const int wm = w & 3, wn = w >> 2, h = lane >> 5;
  if (blockIdx.x < 64 || wn == 0) col_phase<!FINAL>(rw, lds, kin);
  else if (FINAL) {
    drain();
    nap<YIDLE>();
    u32x2 sp[8];
#pragma unroll 1
    for (int spins = 0; spins < (1 << 18); spins++) {
      unsigned m = 0;
#pragma unroll
      for (int i = 0; i < 8; i++) sp[i] = gl64<16>(rw, WS_SSQ + (blockIdx.x & 7) * 32768 + (1024 + 4 * (wm * 64 + lane)) * 2 + i * 4096);
#pragma unroll
      for (int i = 0; i < 8; i++) m = max(max(m, sp[i][0]), sp[i][1]);
      if (!__builtin_amdgcn_ballot_w64(m == EMPTY)) break;
      nap<YSLEEP>();
    }
    put_scale(lds, 1, wm * 64 + lane, sp);
    lds_bar(); raw_bar(); raw_bar();
  } else {
    raw_bar(); raw_bar(); raw_bar();
  }
  lds_bar();
  {
    drain();
    unsigned n = 0;
    if (lane == 0) n = __hip_atomic_fetch_add(misc + 4, 1u, __ATOMIC_RELAXED, __HIP_MEMORY_SCOPE_WORKGROUP);
    if (n == 11) st32<16>(rw, AUX + blockIdx.x * 4, 1u);
  }

  const int head = 2 * u + wn, kh = w & 1;
  const lptr ring = lds + wn * HR;
  const int pblk = 128 * qt + 64 * hf + 32 * (wm >> 1);
  const unsigned r3t = WS_R3 + (64 * (wm & 1) + (pblk >> 5)) * (642 * 64);
  u32x2 x4v[3]; float dc = 0.f;
  unsigned os = OB + ((4 * qt + wm) * 32 + head) * 1280 + (((lane << 2) & 16) + ((lane >> 1) & 12) + (lane & 3)) * 8;
  unsigned o1 = OB + ((pblk >> 5) * 32 + 16 * wn) * 1280 + (g >> 1) * 1280 + (g & 1) * 512 + (wm & 1) * 256 + r16 * 16;
  unsigned o2 = OB + ((pblk >> 5) * 32 + 16 * wn) * 1280 + 2 * g * 1280 + 1024 + (wm & 1) * 128 + r16 * 8;
  u32x4 o3 = {0u, 0u, 0u, 0u};
  if (!FINAL) {
#pragma unroll
    for (int i = 0; i < 3; i++) {
      const int j = 80 * ct + 48 * wn + 16 * (wn ? (i & 1) : i) + r16;
      o3[i] = (j == 1 ? WS_RB0 + ((pblk >> 5) * 641 + ((pblk >> 5) & 1) + 1) * 64 : WS_RB1 + (j * RBP + pblk) * 2) + 16 * g + 8 * (wm & 1);
    }
  } else {
    o3[3] = r3t + 64 + r16 * 4;
#pragma unroll
    for (int i = 0; i < 3; i++) o3[i] = WS_X4 + ((pblk + trow(wm & 1, r16)) * 640 + 80 * ct + 48 * wn + 16 * (wn ? (i & 1) : i) + 4 * g) * 2;
  }
  asm volatile("" : "+v"(os), "+v"(o1), "+v"(o2), "+v"(o3));
  unsigned ka = lds_addr(ring) + kh * 2560 + lane * 16, kb = lds_addr(ring) + kh * 2560 + 1024 + (lane & 31) * 8;
  unsigned va = lds_addr(ring) + kh * 3072 + (4 * h + (r16 >> 2)) * 48 + ((g & 1) ? ((lane & 3) ? 40 : 32) : (lane & 3) * 8);
  asm volatile("" : "+v"(ka), "+v"(kb), "+v"(va));
  {
    f32x4 acc[2][4]; float rs[2]; u32x8 im[3];
    unsigned mo[2], a0[2], a1[2], a2[2], a_r[2];
#pragma unroll
    for (int m = 0; m < 2; m++) {
      const int q = r16 >> 2, pr = 16 * (m ^ wn) + r16;
      mo[m] = lds_addr(lds) + (8 * g + q) * 256 + (((2 * wm + (m ^ wn)) ^ (q | ((g & 1) << 2))) * 32) + (lane & 3) * 8;
      const unsigned qb = L_A + (wn * 4 + wm) * 1280, kb = wn * HR + wm * 1280, vr = wn * HR + 5120 + (32 * wm + pr) * 48 + (g == 3 ? 32 : g * 16);
      a0[m] = ((g >> 1) ? kb : qb) + (g & 1) * 512 + pr * 16;
      a1[m] = g == 2 ? qb + 1024 + pr * 8 : vr; a2[m] = g == 2 ? kb + 1024 + pr * 8 : vr + 8;
      a_r[m] = lds_addr(lds) + L_MISC + (32 * wm + pr) * 4;
      asm volatile("" : "+v"(a0[m]), "+v"(a1[m]), "+v"(a2[m]));
    }
    const unsigned a_w = a_r[0] + (g == (r16 >> 2) ? 0 : 512), a_v = lds_addr((lptr)(misc + 2));
#pragma unroll 1
    for (int att = 0;; att++) {
      unsigned am0 = mo[0], am1 = mo[1], as = lds_addr(lds) + 16384 + wn * 8192 + lane * 16, vote; int c_, t_;
      if (!FINAL) asm volatile(GEMM_ASM("") : GEMM_OUT : GEMM_IN : GEMM_CLOB);
      else asm volatile(GEMM_ASM(ITL(96, 91) ITL(104, 92) ITL(112, 93)) : GEMM_OUT, "={v[96:103]}"(im[0]), "={v[104:111]}"(im[1]), "={v[112:119]}"(im[2])
                        : GEMM_IN, "{v91}"(w * 2048 + lane * 16), "{v92}"((w + 8) * 2048 + lane * 16), "{v93}"((w < 4 ? w + 16 : w) * 2048 + lane * 16), [it] "s"(&g_it) : GEMM_CLOB);
      if (att || !__builtin_amdgcn_readfirstlane(vote)) break;
    }
    SB();
#pragma unroll
    for (int m = 0; m < 2; m++) *(LDS_AS u32x4*)(lds + a0[m]) = pk8(acc[m][0] * rs[m], acc[m][1] * rs[m]);
    asm volatile("s_nop 1" : "+v"(acc[0][2]), "+v"(acc[1][2]), "+v"(acc[0][3]), "+v"(acc[1][3]) :: "memory");
#pragma unroll
    for (int m = 0; m < 2; m++) {
      u32x2 hi = pk4(acc[m][3] * rs[m]);
      if (g == 3) hi = u32x2{0x3F80u, 0u};
      *(LDS_AS u32x2*)(lds + a1[m]) = pk4(acc[m][2] * rs[m]);
      *(LDS_AS u32x2*)(lds + a2[m]) = hi;
    }
    lds_bar();
    if (FINAL) {
      u32x4 ia[3]; u32x2 ib[3];
#pragma unroll
      for (int T = 0; T < 3; T++) { ia[T] = ld128(lds + L_IA + T * 1024 + lane * 16); ib[T] = ld64(lds + L_IA + 3072 + T * 512 + lane * 8); }
      const f32x4 z4 = {0.f, 0.f, 0.f, 0.f};
#pragma unroll
      for (int i = 0; i < 3; i++) {
        const int G = (i < 2 || w < 4) ? w + 8 * i : w;
        const lptr tp = lds + L_T + (32 * G + 8 * g + (r16 >> 2)) * 16 + (lane & 1) * 8;
        const u32x2 f0 = ldtr(tp), f1 = ldtr(tp + 64);
#pragma unroll
        for (int P = 0; P < 2; P++) *(LDS_AS u32x2*)(lds + L_U + (r16 & 7) * 1296 + (2 * G + P) * 32 + g * 8) = pk4(mfma16(u32x4{im[i][4 * P], im[i][4 * P + 1], im[i][4 * P + 2], im[i][4 * P + 3]}, u32x4{f0[0], f0[1], f1[0], f1[1]}, z4));
      }
      wave_sync(misc + 5, 8);
      const lptr up = lds + L_U + w * 1296;
      const u32x2 f0 = ldtr(up + lane * 8), f1 = ldtr(up + 512 + lane * 8), f2 = ldtr(up + 1024 + (lane & 31) * 8);
#pragma unroll
      for (int T = 0; T < 3; T++) {
        const u32x2 v = pk4(mfma16(u32x4{f2[0], f2[1], 0u, 0u}, u32x4{ib[T][0], ib[T][1], 0u, 0u}, mfma16(u32x4{f0[0], f0[1], f1[0], f1[1]}, ia[T], z4)));
        st64<16>(rw, (T < 2 || r16 < 8) ? WS_X4 + ((128 * qt + 8 * u + w) * 640 + 16 * (16 * T + r16) + 4 * g) * 2 : WS_END, u32x2{min(v[0], EMPTY - 1), min(v[1], EMPTY - 1)});
      }
    }
  }
  f32x4 r[3];
  {
    const lptr qpa = lds + L_A + (wn * 4 + wm) * 1280, qpb = lds + L_A + (wn * 4 + (wm ^ 1)) * 1280;
    const u32x4 qa1 = ld128(qpa + lane * 16), qb1 = ld128(qpb + lane * 16);
    u32x2 qa2 = ld64(qpa + 1024 + (lane & 31) * 8), qb2 = ld64(qpb + 1024 + (lane & 31) * 8);
    if (h) { qa2 = u32x2{0u, 0u}; qb2 = qa2; }
    float ma, mb;
    {
      const u32x4 k1 = ld128(ring + lane * 16); const u32x2 k2 = ld64(ring + 1024 + (lane & 31) * 8);
      ma = max16(scores(k1, k2, qa1, qa2)); mb = max16(scores(k1, k2, qb1, qb2));
    }
    for (int attempt = 0;; attempt++) {
      ma = fmaxf(ma, __shfl_xor(ma, 32)); mb = fmaxf(mb, __shfl_xor(mb, 32));
      f32x16 cia, cib, oa, ob;
#pragma unroll
      for (int i = 0; i < 16; i++) { cia[i] = -ma; cib[i] = -mb; }
      int cnt = 4;
      asm volatile(
          "ds_read_b128 v[80:83], v100\n\t"
          "ds_read_b64 v[84:85], v101\n\t"
          "ds_read_b64_tr_b16 v[86:87], v102 offset:5120\n\t"
          "ds_read_b64_tr_b16 v[88:89], v102 offset:5504\n\t"
          Z4(0) Z4(68) Z4(76) Z4(90)
          "s_waitcnt lgkmcnt(0)\n\t"
          "v_mfma_f32_32x32x16_bf16 v[32:47], v[80:83], v[94:97], v[16:31]\n\t"
          Z4(4) Z4(8) Z4(12) Z4(104) Z4(108) Z4(112) Z4(116)
          "v_mfma_f32_32x32x8_bf16 v[32:47], v[84:85], v[98:99], v[32:47]\n\ts_nop 9\n\t"
          "L_attn_%=:\n\t"
          CHUNK(0) CHUNK(1) CHUNK(2) CHUNK(3)
          "s_sub_u32 %[cnt], %[cnt], 1\n\ts_cmp_lg_u32 %[cnt], 0\n\ts_cbranch_scc1 L_attn_%=\n\t"
          "s_setprio 0\n\t" CVT(78,68) CVT(79,70) "s_waitcnt lgkmcnt(0)\n\ts_nop 1\n\t"
          "v_mfma_f32_32x32x16_bf16 v[104:119], v[90:93], v[76:79], v[104:119]\n\ts_nop 15\n\t"
          : "={v[0:15]}"(oa), "={v[104:119]}"(ob), [cnt] "+s"(cnt)
          : "{v[16:31]}"(cia), "{v[120:135]}"(cib), "{v[94:97]}"(qa1), "{v[98:99]}"(qa2), "{v[136:139]}"(qb1), "{v[140:141]}"(qb2),
            "{v100}"(ka), "{v101}"(kb), "{v102}"(va)
          : "memory", "scc", "v32", "v33", "v34", "v35", "v36", "v37", "v38", "v39", "v40", "v41", "v42", "v43", "v44", "v45", "v46", "v47",
            "v48", "v49", "v50", "v51", "v52", "v53", "v54", "v55", "v56", "v57", "v58", "v59", "v60", "v61", "v62", "v63", "v64", "v65",
            "v66", "v67", "v68", "v69", "v70", "v71", "v72", "v73", "v74", "v75", "v76", "v77", "v78", "v79", "v80", "v81", "v82", "v83",
            "v84", "v85", "v86", "v87", "v88", "v89", "v90", "v91", "v92", "v93");
      LDS_AS f32x4* ex = (LDS_AS f32x4*)(ring + L_EX + wm * 3072) + lane;
#pragma unroll
      for (int i = 0; i < 3; i++) ex[i * 64] = f32x4{ob[4 * i], ob[4 * i + 1], ob[4 * i + 2], ob[4 * i + 3]};
      f32x4 cs = {0.f, 0.f, 0.f, 0.f};
#pragma unroll
      for (int i = 0; i < 3; i++) cs += f32x4{oa[4 * i], oa[4 * i + 1], oa[4 * i + 2], oa[4 * i + 3]} + f32x4{ob[4 * i], ob[4 * i + 1], ob[4 * i + 2], ob[4 * i + 3]};
      if (!(fabsf(cs[0] + cs[1] + cs[2] + cs[3]) < INFINITY) || (h && !(oa[8] + ob[8] < 0x1p60f))) misc[1] = 1u;
      lds_bar();
      const unsigned vote = *(volatile LDS_AS unsigned*)(misc + 1);
      LDS_AS f32x4* ey = (LDS_AS f32x4*)(ring + L_EX + (wm ^ 1) * 3072) + lane;
#pragma unroll
      for (int i = 0; i < 3; i++) r[i] = ey[i * 64] + f32x4{oa[4 * i], oa[4 * i + 1], oa[4 * i + 2], oa[4 * i + 3]};
      if (attempt || !vote) break;
      lds_bar();
      ma = -INFINITY; mb = -INFINITY;
      for (int t = 0; t < 64; t++) {
        const unsigned kb_ = KV + (head * 16 + (t >> 2)) * KVC + (t & 3) * 1280;
        const u32x4 k1 = gl128<16>(rw, kb_ + lane * 16); const u32x2 k2 = gl64<16>(rw, kb_ + 1024 + (lane & 31) * 8);
        ma = fmaxf(ma, max16(scores(k1, k2, qa1, qa2))); mb = fmaxf(mb, max16(scores(k1, k2, qb1, qb2)));
      }
      raw_bar();
    }
  }
  {
    const unsigned lb = __float_as_uint(r[2][0]);
    const float il = __builtin_amdgcn_rcpf(__uint_as_float(__builtin_amdgcn_permlane32_swap(lb, lb, false, false)[1]));
    const unsigned sl8 = os & 255;
    st128<16>(rw, os + sl8 + h * 512, clean(pk8(r[0] * il, r[1] * il)));
    const u32x4 t = clean(pk8(r[2] * il, r[2] * il));
    st64<16>(rw, h ? WS_END : os + 1024, u32x2{t[0], t[1]});
  }
  const lptr wa = lds + L_A + wn * 35 * 1024 + lane * 16, wb = ring + lane * 16;
  constexpr int NPRE = 15;
  u32x4 bf[50];
  f32x4 acc[5] = {};
#pragma unroll
  for (int j = 0; j < NPRE; j++) bf[j] = ld128(wa + j * 1024);
#pragma unroll
  for (int j = 0; j < 5; j++) asm volatile("" : "+v"(acc[j]));
  if (FINAL) dc = __uint_as_float(gl32<16>(rw, o3[3]));
#pragma unroll 1
  for (int spins = 0; spins < (1 << 18); spins++) {
#pragma unroll
    for (int i = 0; i < 3; i++) x4v[i] = gl64<16>(rw, o3[i]);
    if (!FINAL || !__builtin_amdgcn_ballot_w64(umax4(max(x4v[0][0], x4v[0][1]), u32x4{x4v[1][0], x4v[1][1], x4v[2][0], x4v[2][1]}) == EMPTY)) break;
    nap<8>();
  }
  drain();
  {
    u32x4 af[10];
    auto ldf = [&](int i) {
      if (i < 8) af[i] = __builtin_amdgcn_raw_buffer_load_b128(rw, o1, i * 2 * 1280, 16);
      else { const u32x2 a = __builtin_amdgcn_raw_buffer_load_b64(rw, o2, (i - 8) * 8 * 1280, 16), b = __builtin_amdgcn_raw_buffer_load_b64(rw, o2, ((i - 8) * 8 + 1) * 1280, 16);
             af[i] = u32x4{a[0], a[1], b[0], b[1]}; }
    };
#pragma unroll
    for (int i = 0; i < 10; i++) ldf(i);
#pragma unroll 1
    for (int spins = 0; spins < (1 << 18); spins++) {
      unsigned m = 0;
#pragma unroll
      for (int i = 0; i < 10; i++) m = umax4(m, af[i]);
      if (!__builtin_amdgcn_ballot_w64(m == EMPTY)) break;
      nap<RSLEEP>();
#pragma unroll
      for (int i = 0; i < 10; i++) if (__builtin_amdgcn_ballot_w64(umax4(0, af[i]) == EMPTY)) ldf(i);
    }
#pragma unroll
    for (int i = 0; i < 10; i++) asm volatile("" : "+v"(af[i]));
    asm volatile("s_nop 1" : "+v"(acc[0]), "+v"(acc[1]), "+v"(acc[2]), "+v"(acc[3]), "+v"(acc[4]));
    SB();
#pragma unroll
    for (int i = 0; i < 10; i++) {
#pragma unroll
      for (int a = 0; a < 5; a++) {
        if (FINAL) mfma16v(acc[a], bf[5 * i + a], af[i]); else mfma16v(acc[a], af[i], bf[5 * i + a]);
        const int j = 5 * i + a + NPRE; if (j < 50) bf[j] = ld128(j < 35 ? wa + j * 1024 : wb + (j - 35) * 1024);
        SB();
      }
    }
  }
  asm volatile("s_nop 7\n\ts_nop 1" : "+v"(acc[0]), "+v"(acc[1]), "+v"(acc[2]), "+v"(acc[3]), "+v"(acc[4]) :: "memory");
  if (!FINAL) { col_load(kin, rw, true, true); SB(); }
  {
    LDS_AS f32x4* ex = (LDS_AS f32x4*)(ring + L_EX + 12288 + wm * 3072) + lane;
    LDS_AS f32x4* ey = (LDS_AS f32x4*)(lds + (wn ^ 1) * HR + L_EX + 12288 + wm * 3072) + lane + 64 * wn;
    ex[0] = acc[2]; ex[64] = acc[3]; ex[128] = acc[4];
    dc *= 0.039528470752104741f;
#pragma unroll
    for (int i = 0; i < 3; i++) { acc[i] += f32x4{bflo(x4v[i][0]), bfhi(x4v[i][0]), bflo(x4v[i][1]), bfhi(x4v[i][1])}; if (FINAL) acc[i] += dc; }
    lds_bar();
#pragma unroll
    for (int i = 0; i < 3; i++) acc[i] += ey[64 * i];
  }
  if (FINAL) {
    const rsrc_t ro = mk_rsrc(out, 2048 * 640 * 2);
    const unsigned op = (rowof(pblk + trow(wm & 1, r16)) * 640 + 80 * ct + 48 * wn + 4 * g) * 2;
#pragma unroll
    for (int i = 0; i < 3; i++)
      if (i < 2 || wn == 0) st64<0>(ro, op + 32 * i, pk4(acc[i]));
  } else {
    f32x4 sq = {0.f, 0.f, 0.f, 0.f};
#pragma unroll
    for (int i = 0; i < 3; i++)
      if (i < 2 || wn == 0) {
        const int j = 80 * ct + 48 * wn + 16 * i + r16;
        st128<16>(rw, r3t + j * 64 + g * 16, clean(asu(acc[i])));
        if (j != 1) sq += acc[i] * acc[i];
      }
#pragma unroll
    for (int e = 0; e < 4; e++) sq[e] = row16_sum(sq[e]);
    LDS_AS float* sl = (LDS_AS float*)(lds + L_MISC);
    if (r16 == 0) *(LDS_AS f32x4*)(sl + wn * 64 + 32 * (wm & 1) + 16 * (wm >> 1) + 4 * g) = sq;
    lds_bar();
    if (w == 0) {
      LDS_AS f32x4* s4 = (LDS_AS f32x4*)sl + 2 * (lane & 7);
      st128<16>(rw, WS_SSQ + (lane >> 3) * 32768 + (ct * 2048 + 1024 * ((lane >> 2) & 1) + 64 * qt + 32 * hf + 8 * (lane & 3)) * 2, clean(pk8(s4[0] + s4[16], s4[1] + s4[17])));
    }
  }
}

__global__ void __launch_bounds__(768, 3) k_main(const u16* x, const u16* wr, const u16* wi, const u16* wqkv, const u16* wo, char* ws, u16* out) {
  if (wave_id() >= 8) {
    extern __shared__ __attribute__((aligned(16))) char smem[];
    const unsigned long long t0 = __builtin_amdgcn_s_memrealtime();
    const int job = blockIdx.x;
    if (job < 144) raw_bar();
    const rsrc_t rw = mk_rsrc(ws, WS_END);
    __builtin_amdgcn_s_setprio(3);
    if (threadIdx.x < 528) ((LDS_AS unsigned*)((lptr)smem + L_MISC + 1024))[threadIdx.x - 512] = 0u;
    u32x2 tag = gl64<16>(rw, WS_EP);
    tag[0] += 1u;
    if (job < 228) raw_bar();
    if (job < 64) raw_bar();
    asm volatile("s_waitcnt vmcnt(0) lgkmcnt(0)\n\ts_barrier" : "+v"(tag) :: "memory");
    if (wave_id() == 8) {
      if (threadIdx.x < 520) st64<16>(rw, WS_F0 + (threadIdx.x - 512) * 2048 + job * 8, tag);
      if (job >= 248) {
        while (__builtin_amdgcn_s_memrealtime() - t0 < TPOLL) nap<2>();
        poll_tag(rw, tag, 0xFE0FFFF00000FFFFull);
        if (threadIdx.x < 544) st64<16>(rw, WS_GO + ((threadIdx.x - 512) * 8 + (job & 7)) * 64, tag);
      }
#pragma unroll 1
      for (int spins = 0; spins < (1 << 20); spins++) {
        const u32x2 v = gl64<16>(rw, WS_GO + job * 64);
        if (__builtin_amdgcn_readfirstlane((v[0] ^ tag[0]) | (v[1] ^ tag[1])) == 0) break;
        nap<GSLEEP>();
      }
    }
    if (wave_id() > 8) {
    while (__builtin_amdgcn_s_memrealtime() - t0 < TFILL) nap<2>();
    const int i0 = blockIdx.x * 192 + threadIdx.x - 576;
#pragma unroll
    for (int it = 0; it < 4; it++) {
      const int k = it * 49152 + i0, b = k / 1408;
      st128<16>(rw, k < 128 * 1408 ? WS_KV + b * 4 * KVC + (k - b * 1408) * 16 : WS_END, u32x4{EMPTY, EMPTY, EMPTY, EMPTY});
    }
    }
    raw_bar();
    loader<false>(ws, tag);
    loader<true>(ws, tag);
  } else {
    prep(x, wr, wi, wqkv, wo, ws);
    asm volatile("s_waitcnt vmcnt(0) lgkmcnt(0)\n\ts_barrier" ::: "memory");
    ColIn kin = {};
    compute<false>(ws, out, kin);
    compute<true>(ws, out, kin);
  }
}

extern "C" void kernel_launch(void* const* d_in, const int* in_sizes, int n_in, void* d_out, int out_size, void* d_ws, size_t ws_size,
                              hipStream_t stream) {
  const u16* x = (const u16*)d_in[0];
  const u16* wr = (const u16*)d_in[1];
  const u16* wi = (const u16*)d_in[2];
  const u16* wqkv = (const u16*)d_in[4];
  const u16* wo = (const u16*)d_in[5];
  char* ws = (char*)d_ws;
  (void)hipFuncSetAttribute((const void*)k_main, hipFuncAttributeMaxDynamicSharedMemorySize, LDS_FUSED);
  k_main<<<256, 768, LDS_FUSED, stream>>>(x, wr, wi, wqkv, wo, ws, (u16*)d_out);
}
```

```cpp
#include <hip/hip_runtime.h>
#include <hip/hip_bf16.h>

typedef unsigned short u16;
typedef __attribute__((ext_vector_type(16))) float f32x16;
typedef __attribute__((ext_vector_type(4))) float f32x4;
typedef __attribute__((ext_vector_type(2))) float f32x2;
typedef __attribute__((ext_vector_type(8))) __bf16 bf16x8;
typedef __attribute__((ext_vector_type(2))) __bf16 bf16x2;
typedef __attribute__((ext_vector_type(4))) short s16x4;
typedef __attribute__((ext_vector_type(4))) unsigned u32x4;
typedef __attribute__((ext_vector_type(2))) unsigned u32x2;
typedef __attribute__((ext_vector_type(8))) unsigned u32x8;
#define DI __device__ __forceinline__
#define LDS_AS __attribute__((address_space(3)))
typedef LDS_AS char* lptr;

constexpr double cx_cos_small(double x) { double x2 = x * x, t = 1, s = 1; for (int i = 1; i < 12; i++) { t *= -x2 / ((2 * i - 1) * (2 * i)); s += t; } return s; }
constexpr double cx_sin_small(double x) { double x2 = x * x, t = x, s = x; for (int i = 1; i < 12; i++) { t *= -x2 / ((2 * i) * (2 * i + 1)); s += t; } return s; }
constexpr double cx_cos640(int m) {
  m %= 640; if (m > 320) m = 640 - m; double sg = 1; if (m > 160) { m = 320 - m; sg = -1; }
  const double w = 6.283185307179586476925 / 640; return sg * (m <= 80 ? cx_cos_small(w * m) : cx_sin_small(w * (160 - m)));
}
constexpr u16 cx_bf16(double d) { float f = (float)d; unsigned u = __builtin_bit_cast(unsigned, f); u += 0x7FFFu + ((u >> 16) & 1u); return (u16)(u >> 16); }
struct Cos640 { double v[640]; };
constexpr Cos640 mk_cos() { Cos640 c{}; for (int m = 0; m < 640; m++) c.v[m] = cx_cos640(m); return c; }
constexpr Cos640 COS = mk_cos();
__host__ __device__ constexpr int kf_of(int G, int i) { return G ? G + 40 * i : (i == 0 ? 320 : i < 8 ? 40 * i : 40 * i - 300); }
__host__ __device__ constexpr int freq_of(int G, int i) { return kf_of(G, i) > 320 ? 640 - kf_of(G, i) : kf_of(G, i); }
constexpr double SA = 1.91318, SA0 = 1.9140625, S64 = 1.86954, S32 = 1.6245;
constexpr double a1val(int out, int a) {
  if (out >= 40 || a >= 40) return 0;
  const int G = out >> 1, part = out & 1;
  if (G == 0) return (part && (a & 1)) ? -SA0 : SA0;
  return SA * COS.v[(16 * a * G + 160 * part) % 640];
}
constexpr double m2val(int G, int o, int k) {
  const int i = o >> 1, po = o & 1, part = k >> 4, b = k & 15, kf = kf_of(G, i);
  const double c = COS.v[(b * kf) % 640], sn = COS.v[(b * kf + 480) % 640];
  if (G == 0) {
    if (part != (i >= 8)) return 0;
    if (i == 0) return 0.039528470752104741 / SA0 * (po ? 1.0 : c);
    return 0.055901699437494742 / SA0 * (po ? -sn : c);
  }
  const double v = po ? (part ? c : -sn) : (part ? sn : c);
  return 0.055901699437494742 / SA * ((po && kf > 320) ? -v : v);
}
struct DTab { u16 a1[3][64][8]; u16 a1b[3][64][4]; u16 pad[256]; u16 m2[20][2][64][8]; };
constexpr DTab mk_dtab() {
  DTab t{};
  for (int l = 0; l < 64; l++) {
    const int r = l & 15, g = l >> 4;
    for (int T = 0; T < 3; T++) {
      for (int e = 0; e < 8; e++) t.a1[T][l][e] = cx_bf16(a1val(16 * T + r, 16 * (e >> 2) + 4 * g + (e & 3)));
      for (int e = 0; e < 4; e++) t.a1b[T][l][e] = cx_bf16(a1val(16 * T + r, 32 + 4 * g + e));
    }
    for (int G = 0; G < 20; G++) for (int T = 0; T < 2; T++) for (int e = 0; e < 8; e++)
      t.m2[G][T][l][e] = cx_bf16(m2val(G, 8 * (r >> 2) + 4 * T + (r & 3), 8 * g + e));
  }
  return t;
}
static __device__ const __attribute__((aligned(1024))) DTab g_dt = mk_dtab();

struct ITab { u16 m2[20][2][64][8]; u16 a1[3][64][8]; u16 a1b[3][64][4]; u16 pad[1792]; };
constexpr ITab mk_itab() {
  ITab t{};
  for (int l = 0; l < 64; l++) {
    const int r = l & 15, g = l >> 4;
    for (int T = 0; T < 3; T++) {
      for (int e = 0; e < 8; e++) t.a1[T][l][e] = cx_bf16(a1val(16 * (e >> 2) + 4 * g + (e & 3), 16 * T + r));
      for (int e = 0; e < 4; e++) t.a1b[T][l][e] = cx_bf16(a1val(32 + 4 * g + e, 16 * T + r));
    }
    for (int G = 0; G < 20; G++) for (int P = 0; P < 2; P++) for (int e = 0; e < 8; e++) t.m2[G][P][l][e] = cx_bf16(m2val(G, 8 * g + e, 16 * P + r));
  }
  return t;
}
static __device__ const __attribute__((aligned(1024))) ITab g_it = mk_itab();

struct CTab { u16 v[10 * 512]; };
constexpr CTab mk_ctab() {
  CTab c{};
  for (int f = 0; f < 10; f++) for (int l = 0; l < 64; l++) for (int e = 0; e < 8; e++) {
    const int r = l & 15, g = l >> 4;
    const int m = f < 8 ? (8 * g + e) * (16 * (f >> 1) + r) : 2 * r * (16 * (e >> 2) + 4 * g + (e & 3));
    c.v[(f * 64 + l) * 8 + e] = cx_bf16((f < 8 ? S64 : S32) * COS.v[10 * ((m + (f & 1) * 48) & 63)]);
  }
  return c;
}
static __device__ const __attribute__((aligned(1024))) CTab g_ct = mk_ctab();
constexpr float RSK = (float)(2048.0 * S64 * S64 * S32 * S32 * 2048.0 * S64 * S64 * S32 * S32 / 640.0);

constexpr int RP = 2048 + 64, RBP = 2048 + 128;
constexpr unsigned RBSZ = 640 * RBP * 2;
constexpr unsigned WS_FL = 0;
constexpr unsigned WS_AUX = WS_FL + 3 * 32768;
constexpr unsigned WS_F0 = WS_AUX + 2048;
constexpr unsigned WS_GO = WS_F0 + 8 * 2048;
constexpr unsigned WS_EP = WS_GO + 256 * 64;
constexpr unsigned WS_RS1 = WS_EP + 256;
constexpr unsigned WS_RB0 = WS_RS1 + 8192;
constexpr unsigned WS_RB1 = WS_RB0 + RBSZ;
constexpr unsigned WS_WF = WS_RB1 + RBSZ;
constexpr unsigned WS_WQ = WS_WF + 320 * 8192;
constexpr unsigned WS_WOF = WS_WQ + 16 * 8 * 20 * 1024;
constexpr unsigned WS_WOP = WS_WOF + 40 * 20 * 1024;
constexpr int KVC = 11264;
constexpr unsigned WS_KV = WS_WOP + 40 * 20 * 1024;
constexpr unsigned WS_O = WS_KV + 32 * 16 * KVC;
constexpr unsigned LAYER2 = 32 * 16 * KVC + 32 * 64 * 1280;
constexpr unsigned WS_X4 = WS_KV + 2 * LAYER2;
constexpr unsigned WS_R3 = WS_X4 + 2048 * 640 * 2;
constexpr unsigned WS_RB2 = WS_R3 + 640 * RP * 4;
constexpr unsigned WS_SSQ = WS_RB2 + RBSZ;
constexpr unsigned EMPTY = 0xFFFFFFFFu;
constexpr unsigned WS_END = WS_SSQ + 8 * 2048 * 16;

constexpr int XDELAY = 20, K1DELAY = 40, YIDLE = 12, TPOLL = 250, TSLEEP = 1, GSLEEP = 2, TFILL = 400, YSLEEP = 4, XSLEEP = 1, RSLEEP = 2;

#define SB() __builtin_amdgcn_sched_barrier(0)
typedef __amdgpu_buffer_rsrc_t rsrc_t;
DI rsrc_t mk_rsrc(const void* p, unsigned bytes) { return __builtin_amdgcn_make_buffer_rsrc((void*)p, 0, bytes, 0x00020000); }
template <int AUX> DI void st128(rsrc_t r, unsigned off, u32x4 v) { __builtin_amdgcn_raw_buffer_store_b128(v, r, off, 0, AUX); }
template <int AUX> DI void st64(rsrc_t r, unsigned off, u32x2 v) { __builtin_amdgcn_raw_buffer_store_b64(v, r, off, 0, AUX); }
template <int AUX> DI void st32(rsrc_t r, unsigned off, unsigned v) { __builtin_amdgcn_raw_buffer_store_b32(v, r, off, 0, AUX); }
template <int AUX> DI u32x4 gl128(rsrc_t r, unsigned off) { return __builtin_amdgcn_raw_buffer_load_b128(r, off, 0, AUX); }
template <int AUX> DI u32x2 gl64(rsrc_t r, unsigned off) { return __builtin_amdgcn_raw_buffer_load_b64(r, off, 0, AUX); }
template <int AUX> DI unsigned gl32(rsrc_t r, unsigned off) { return __builtin_amdgcn_raw_buffer_load_b32(r, off, 0, AUX); }
DI u32x4 asu(f32x4 v) { return __builtin_bit_cast(u32x4, v); }
DI f32x4 asf(u32x4 v) { return __builtin_bit_cast(f32x4, v); }
DI unsigned pk2(float a, float b) { f32x2 v = {a, b}; return __builtin_bit_cast(unsigned, __builtin_convertvector(v, bf16x2)); }
DI u32x2 pk4(f32x4 a) { return u32x2{pk2(a[0], a[1]), pk2(a[2], a[3])}; }
DI u32x4 pk8(f32x4 a, f32x4 b) { return u32x4{pk2(a[0], a[1]), pk2(a[2], a[3]), pk2(b[0], b[1]), pk2(b[2], b[3])}; }
DI u32x4 clean(u32x4 v) { return __builtin_elementwise_min(v, u32x4{EMPTY - 1, EMPTY - 1, EMPTY - 1, EMPTY - 1}); }
DI unsigned umax4(unsigned m, u32x4 v) { return max(max(max(m, v[0]), v[1]), max(v[2], v[3])); }
DI float bflo(unsigned u) { return __uint_as_float(u << 16); }
DI float bfhi(unsigned u) { return __uint_as_float(u & 0xFFFF0000u); }
DI int rowof(int p) { return 32 * (((p >> 5) & 0x3C) + (p & 3)) + ((p << 2) & 16) + ((p >> 3) & 15); }
DI int rpos(int p) { return ((p << 8) & 1024) + ((p >> 3) << 2) + (p & 3); }
DI int trow(int T, int r) { return 8 * (r >> 2) + 4 * T + (r & 3); }
template <int CTRL> DI float dpp_add(float v) { return v + __int_as_float(__builtin_amdgcn_update_dpp(0, __float_as_int(v), CTRL, 0xF, 0xF, true)); }
DI float row16_sum(float v) { v = dpp_add<0x128>(v); v = dpp_add<0x124>(v); v = dpp_add<0x122>(v); return dpp_add<0x121>(v); }
DI f32x4 mfma16(u32x4 a, u32x4 b, f32x4 c) {
  return __builtin_amdgcn_mfma_f32_16x16x32_bf16(__builtin_bit_cast(bf16x8, a), __builtin_bit_cast(bf16x8, b), c, 0, 0, 0);
}
DI void mfma16v(f32x4& c, u32x4 a, u32x4 b) { asm volatile("v_mfma_f32_16x16x32_bf16 %0, %1, %2, %0" : "+v"(c) : "v"(a), "v"(b)); }
DI u32x2 ldtr(lptr p) { return __builtin_bit_cast(u32x2, __builtin_amdgcn_ds_read_tr16_b64_v4i16((LDS_AS s16x4*)p)); }
DI u32x4 ld128(lptr p) { return *(LDS_AS u32x4*)p; }
DI u32x2 ld64(lptr p) { return *(LDS_AS u32x2*)p; }
#define GL(v, s, o, x) "global_load_lds_dwordx4 " v ", " s " offset:" #o x "\n\t"
#define M0SET "s_mov_b32 %[sv], m0\n\ts_mov_b32 m0, %[l]\n\ts_nop 2\n\t"
#define M0ADD(x) "s_add_u32 m0, m0, " x "\n\ts_nop 0\n\t"
#define M0RST "s_mov_b32 m0, %[sv]"
DI void dma2(unsigned v, const char* sb, unsigned l) { unsigned sv;
  asm volatile(M0SET GL("%[v]", "%[s]", 0, " sc1") GL("%[v]", "%[s]", 1024, " sc1") M0RST : [sv] "=&s"(sv) : [v] "v"(v), [s] "s"(sb), [l] "s"(l) : "memory"); }
DI void dma4(unsigned v, const char* sb, unsigned l) { unsigned sv;
  asm volatile(M0SET GL("%[v]", "%[s]", 0, "") GL("%[v]", "%[s]", 1024, "") GL("%[v]", "%[s]", 2048, "") GL("%[v]", "%[s]", 3072, "") M0RST
               : [sv] "=&s"(sv) : [v] "v"(v), [s] "s"(sb), [l] "s"(l) : "memory"); }
DI void dma6_sc1(unsigned v, const char* sb, unsigned l) { unsigned sv;
  asm volatile(M0SET GL("%[v]", "%[s]", 0, " sc1") GL("%[v]", "%[s]", 1024, " sc1") GL("%[v]", "%[s]", 2048, " sc1") M0ADD("3072")
               GL("%[w]", "%[s]", 0, " sc1") GL("%[w]", "%[s]", 1024, " sc1") GL("%[w]", "%[s]", 2048, " sc1") M0RST
               : [sv] "=&s"(sv) : [v] "v"(v), [w] "v"(v + 3072), [s] "s"(sb), [l] "s"(l) : "memory", "scc"); }
template <int N> DI void vm_bar() { asm volatile("s_waitcnt vmcnt(%0)\n\ts_barrier" ::"i"(N) : "memory"); }
DI void lds_bar() { asm volatile("s_waitcnt lgkmcnt(0)\n\ts_barrier" ::: "memory"); }
DI void raw_bar() { asm volatile("s_barrier" ::: "memory"); }
template <int N> DI void nap() { __builtin_amdgcn_s_sleep(N); asm volatile("" ::: "memory"); }
DI void drain() { asm volatile("s_waitcnt vmcnt(0)" ::: "memory"); }
#define CODE_ADDR(sym) ({ unsigned lo_, hi_; \
    asm volatile("s_getpc_b64 vcc\n\ts_add_u32 %0, vcc_lo, " sym "@rel32@lo+4\n\ts_addc_u32 %1, vcc_hi, " sym "@rel32@hi+12" : "=s"(lo_), "=s"(hi_) :: "vcc", "scc"); \
    (const unsigned*)(((unsigned long long)hi_ << 32) | lo_); })
#define SYM_MAIN "_Z6k_mainPKtS0_S0_S0_S0_PcPt"
constexpr int CODE_LINES = 360;
DI int wave_id() { return __builtin_amdgcn_readfirstlane(threadIdx.x >> 6); }
DI unsigned lds_addr(lptr p) { return (unsigned)(size_t)p; }

DI int kappa_to_i(int kap) {
  int ks = kap >> 5, g = (kap >> 3) & 3, e = kap & 7;
  if (ks < 16) return (2 * ks + (g >> 1)) * 20 + 8 * (e >> 2) + 4 * (g & 1) + (e & 3);
  return (8 * (ks - 16) + 2 * g + (e >> 2)) * 20 + 16 + (e & 3);
}

DI unsigned wo_frag(int nt, int ks) {
  const int ct = nt / 5, c5 = nt - 5 * ct, kh = ks < 16 ? ks >> 3 : (ks - 16) >> 1, i = ks < 16 ? ks & 7 : 8 + (ks & 1);
  const int a = kh ? (c5 >= 3 ? c5 - 3 : c5 + 2) : c5;
  return ((ct * 2 + kh) * 50 + 5 * i + a) * 1024;
}

constexpr int SLOT32 = 32768;
DI void dma_a(const unsigned* vm, const char* mb, unsigned l) { unsigned sv;
  asm volatile(M0SET GL("%[a0]", "%[m]", 0, " sc1") GL("%[a1]", "%[m]", 1024, " sc1") GL("%[a2]", "%[m]", 2048, " sc1") GL("%[a3]", "%[m]", 3072, " sc1") M0RST
               : [sv] "=&s"(sv) : [a0] "v"(vm[0]), [a1] "v"(vm[1]), [a2] "v"(vm[2]), [a3] "v"(vm[3]), [m] "s"(mb), [l] "s"(l) : "memory"); }
DI void dma_s(unsigned v0, unsigned v1, const char* sb, unsigned l) { unsigned sv;
  asm volatile(M0SET GL("%[v0]", "%[s]", 0, " sc1") GL("%[v0]", "%[s]", 1024, " sc1") GL("%[v1]", "%[s]", 2048, " sc1") GL("%[v1]", "%[s]", 3072, " sc1") M0RST
               : [sv] "=&s"(sv) : [v0] "v"(v0), [v1] "v"(v1), [s] "s"(sb), [l] "s"(l) : "memory"); }
DI void dma1_sc1(unsigned v, const char* sb, unsigned l) { unsigned sv;
  asm volatile(M0SET GL("%[v]", "%[s]", 0, " sc1") M0RST : [sv] "=&s"(sv) : [v] "v"(v), [s] "s"(sb), [l] "s"(l) : "memory"); }
DI void dma1(unsigned v, const char* sb, unsigned l) { unsigned sv;
  asm volatile(M0SET GL("%[v]", "%[s]", 0, "") M0RST : [sv] "=&s"(sv) : [v] "v"(v), [s] "s"(sb), [l] "s"(l) : "memory"); }

constexpr int L_I2 = 40 * 1056, LDS_PREP = L_I2 + 20 * 2064 + 1024;
DI void prep(const u16* x, const u16* wr, const u16* wi, const u16* wqkv, const u16* wo, char* ws) {
  extern __shared__ __attribute__((aligned(16))) char smem[];
  const lptr lds = (lptr)smem;
  const int tid = threadIdx.x, lane = tid & 63, w = wave_id(), g = lane >> 4, r16 = lane & 15;
  const int job = blockIdx.x;
  if (__builtin_expect(job < 144, 1)) {
    const bool cols = job >= 64 && job < 124;
    const int cj = job - 64, cb = cj < 48 ? (cj & 48) + 2 * (cj & 7) + ((cj >> 3) & 1) : cj;
    u32x4 a1[3], m2[3][2]; u32x2 a1b[3];
    if (w < 5) dma1(tid * 16, (const char*)&g_dt, lds_addr(lds) + LDS_PREP + w * 1024);
    if (cols) {
#pragma unroll
      for (int i = 0; i < 5; i++) dma1((16 * (5 * w + i) + (lane >> 2)) * 3840 + cb * 64 + (lane & 3) * 16, (const char*)wqkv, lds_addr(lds) + (5 * w + i) * 1056);
    } else {
      const char* src = (const char*)(job < 64 ? x : wo);
      const int sl = 8 * (w & 3) + 4 * (w >> 2);
      src += (job < 64 ? rowof(32 * job + sl) : kappa_to_i(32 * (job - 124) + sl)) * 1280;
      const int st = (job < 64 ? 31 : 0) * 1280;
#pragma unroll
      for (int i = 0; i < 5; i++) dma1(1024 * i + lane * 16 + ((i == 4 || lane < 16 * i) ? i - 1 : i) * st, src, lds_addr(lds) + (5 * w + i) * 1024);
    }
    unsigned so[3][2]; float sc[2] = {1.f, 1.f};
    if (cols) {
#pragma unroll
      for (int m = 0; m < 2; m++) {
        const int c = 32 * cb + 16 * m + r16;
        const int which = c / 640, rem = c - which * 640, head = rem / 20, d = rem - head * 20, grp = d >> 2;
        const int T = grp < 4 ? (which == 2 ? 2 : 0) + (grp & 1) : (which == 1 ? 3 : 2), gs = grp < 4 ? (which == 1 ? 2 : 0) + (grp >> 1) : (which == 2 ? 3 : 2);
        if (which == 0) sc[m] = 0.22360679774997896f * 1.4426950408889634f;
#pragma unroll
        for (int i = 0; i < 3; i++) so[i][m] = WS_WQ + ((head * 4 + T) * 20 + w + 8 * i) * 1024 + (g * 16 + 4 * gs + (d & 3)) * 16;
      }
    } else if (job < 64) {
#pragma unroll
      for (int i = 0; i < 3; i++)
#pragma unroll
        for (int T = 0; T < 2; T++) so[i][T] = WS_RB0 + (job * 641 + (job & 1) + 32 * (w + 8 * i) + trow(T, r16)) * 64 + g * 16;
    } else {
#pragma unroll
      for (int i = 0; i < 3; i++) {
        const unsigned f = wo_frag(2 * (w + 8 * i) + (r16 >> 3), job - 124);
#pragma unroll
        for (int T = 0; T < 2; T++) so[i][T] = WS_WOF + f + (g * 16 + (trow(T, r16) & 15)) * 16;
      }
    }
    if (w >= 4) { so[2][0] = WS_END; so[2][1] = WS_END; }
    asm volatile("" : "+v"(so[0][0]), "+v"(so[0][1]), "+v"(so[1][0]), "+v"(so[1][1]), "+v"(so[2][0]), "+v"(so[2][1]), "+v"(sc[0]), "+v"(sc[1]));
    const f32x4 z4 = {0.f, 0.f, 0.f, 0.f};
    const lptr i2 = lds + L_I2;
    drain();
#pragma unroll
    for (int i = 0; i < 3; i++)
#pragma unroll
      for (int T = 0; T < 2; T++)
        asm volatile("global_load_dwordx4 %0, %1, %2" : "=v"(m2[i][T]) : "v"(((((i < 2 || w < 4) ? w + 8 * i : w) * 2 + T) * 64 + lane) * 16), "s"(&g_dt.m2[0][0][0][0]) : "memory");
    raw_bar();
#pragma unroll
    for (int T = 0; T < 3; T++) { a1[T] = ld128(lds + LDS_PREP + T * 1024 + lane * 16); a1b[T] = ld64(lds + LDS_PREP + 3072 + T * 512 + lane * 8); }
    f32x4 t1[4][3];
    if (cols) {
      const lptr src = lds + (4 * g + (r16 >> 2)) * 1056 + (w >> 1) * 256 + (w & 1) * 32 + (lane & 3) * 8, srb = src + (32 - 8 * (g >> 1)) * 1056;
#pragma unroll
      for (int i = 0; i < 4; i++) {
        const u32x2 f0 = ldtr(src + i * 64), f1 = ldtr(src + 16 * 1056 + i * 64), f2 = ldtr(srb + i * 64);
#pragma unroll
        for (int T = 0; T < 3; T++) t1[i][T] = mfma16(u32x4{a1b[T][0], a1b[T][1], 0u, 0u}, u32x4{f2[0], f2[1], 0u, 0u}, mfma16(a1[T], u32x4{f0[0], f0[1], f1[0], f1[1]}, z4));
      }
      const int xo = ((w >> 1) * 8) ^ (((r16 >> 1) & 3) * 16);
      const lptr dst = i2 + 2 * g * 2064 + (16 * (w & 1) + r16) * 64;
#pragma unroll
      for (int T = 0; T < 3; T++)
#pragma unroll
        for (int e = 0; e < 4; e++)
          if (T < 2 || g < 2) *(LDS_AS u32x2*)(dst + (8 * T + (e >> 1)) * 2064 + (xo ^ ((e & 1) * 32))) = u32x2{pk2(t1[0][T][e], t1[1][T][e]), pk2(t1[2][T][e], t1[3][T][e])};
    } else {
      const lptr src = lds + w * 5120 + lane * 8, srb = lds + w * 5120 + 1024 + (lane & 31) * 8;
#pragma unroll
      for (int i = 0; i < 4; i++) {
        const u32x2 f0 = ldtr(src + i * 1280), f1 = ldtr(src + i * 1280 + 512), f2 = ldtr(srb + i * 1280);
#pragma unroll
        for (int T = 0; T < 3; T++) t1[i][T] = mfma16(u32x4{f2[0], f2[1], 0u, 0u}, u32x4{a1b[T][0], a1b[T][1], 0u, 0u}, mfma16(u32x4{f0[0], f0[1], f1[0], f1[1]}, a1[T], z4));
      }
      const int xo = ((r16 & 1) * 32 + g * 8) ^ ((w & 1) * 32);
      const lptr dst = i2 + (r16 >> 1) * 2064 + w * 256;
#pragma unroll
      for (int i = 0; i < 4; i++)
#pragma unroll
        for (int T = 0; T < 3; T++)
          if (T < 2 || r16 < 8) *(LDS_AS u32x2*)(dst + T * 8 * 2064 + i * 64 + (xo ^ ((i >> 1) * 16))) = pk4(t1[i][T]);
    }
    lds_bar();
    const rsrc_t rw = mk_rsrc(ws, WS_END);
    asm volatile("s_waitcnt vmcnt(0)" : "+v"(m2[0][0]), "+v"(m2[0][1]), "+v"(m2[1][0]), "+v"(m2[1][1]), "+v"(m2[2][0]), "+v"(m2[2][1]));
    f32x4 sq[2] = {z4, z4};
    u32x4 f[3][2];
#pragma unroll
    for (int i = 0; i < 3; i++) {
      const lptr fp = i2 + ((i < 2 || w < 4) ? w + 8 * i : 0) * 2064 + r16 * 64 + ((g ^ ((r16 >> 1) & 3)) * 16);
      f[i][0] = ld128(fp); f[i][1] = ld128(fp + 1024);
    }
    if (cols) {
#pragma unroll
      for (int i = 0; i < 3; i++)
#pragma unroll
        for (int m = 0; m < 2; m++) st128<16>(rw, so[i][m], pk8(mfma16(m2[i][0], f[i][m], z4) * sc[m], mfma16(m2[i][1], f[i][m], z4) * sc[m]));
    } else {
#pragma unroll
      for (int i = 0; i < 3; i++)
#pragma unroll
        for (int T = 0; T < 2; T++) {
          const f32x4 d0 = mfma16(f[i][0], m2[i][T], z4), d1 = mfma16(f[i][1], m2[i][T], z4);
          st128<16>(rw, so[i][T], pk8(d0, d1));
          const float k = ((i == 2 && w >= 4) || (i == 0 && T == 0 && tid < 64 && r16 == 1)) ? 0.f : 1.f;
          sq[0] += d0 * d0 * k; sq[1] += d1 * d1 * k;
        }
    }
    LDS_AS float* sl = (LDS_AS float*)(lds + LDS_PREP - 1024);
    if (job < 64) {
#pragma unroll
      for (int m = 0; m < 2; m++) {
#pragma unroll
        for (int e = 0; e < 4; e++) sq[m][e] = row16_sum(sq[m][e]);
        if (r16 == 0) *(LDS_AS f32x4*)(sl + w * 32 + 8 * g + 4 * m) = sq[m];
      }
    }
    if (job < 64) {
      lds_bar();
      if (tid < 32) {
        float v = 0.f;
#pragma unroll
        for (int i = 0; i < 8; i++) v += sl[i * 32 + tid];
        st32<16>(rw, WS_RS1 + rpos(32 * job + tid) * 4, __float_as_uint(__builtin_amdgcn_rsqf((v + 640.f * 1e-5f) * RSK)));
      }
    }
    return;
  }
  const int mi = job - 144;
  const rsrc_t rw = mk_rsrc(ws, WS_END);
  if (mi < 64) {
    const int m = mi >> 3, oc = mi & 7;
    const char* src = (const char*)((w >> 2) ? wi : wr) + (64 * (4 * m + (w & 3)) + 8 * oc) * 642;
    dma4(lane * 16, src, lds_addr(lds) + w * 5152);
    dma1(4096 + lane * 16, src, lds_addr(lds) + w * 5152 + 4096);
    if (lane == 0) dma1(5120, src, lds_addr(lds) + w * 5152 + 5120);
    vm_bar<0>();
    LDS_AS u16* L = (LDS_AS u16*)lds + (tid & 7) * 321;
#pragma unroll
    for (int it = 0; it < 5; it++) {
      const int c = (tid >> 3) + 64 * it, col = freq_of(c >> 4, c & 15);
      u32x4 o;
#pragma unroll
      for (int e = 0; e < 4; e++) o[e] = (unsigned)L[e * 2576 + col] | ((unsigned)L[(4 + e) * 2576 + col] << 16);
      st128<16>(rw, WS_WF + c * 8192 + (m * 64 + 8 * oc + (tid & 7)) * 16, o);
    }
#pragma unroll
    for (int it = 0; it < 3; it++) st128<16>(rw, WS_RB1 + ((it * 64 + mi) * 512 + tid) * 16, u32x4{EMPTY, EMPTY, EMPTY, EMPTY});
  } else if (mi < 84) {
    unsigned pf0;
    {
      const unsigned* cm = CODE_ADDR(SYM_MAIN);
      const unsigned* p = mi >= 80 ? (const unsigned*)&g_it + min(tid, 359) * 32 : (mi & 8) ? (const unsigned*)&g_dt.m2[0][0][0][0] + min(tid, 319) * 32 : tid < 80 ? (const unsigned*)&g_ct + tid * 32 : cm + min(tid - 80, CODE_LINES - 1) * 32;
      asm volatile("global_load_dword %0, %1, off" : "=v"(pf0) : "v"(p) : "memory");
    }
    const char* src = (const char*)wo + kappa_to_i(32 * (mi - 64) + 8 * (w & 3) + 4 * (w >> 2)) * 1280;
    dma4(lane * 16, src, lds_addr(lds) + w * 5152);
    dma1(4096 + lane * 16, src, lds_addr(lds) + w * 5152 + 4096);
    asm volatile("s_waitcnt vmcnt(0)\n\ts_barrier" : "+v"(pf0) :: "memory");
#pragma unroll
    for (int i = 0; i < 5; i++) {
      const lptr a = lds + g * 5152 + (r16 >> 2) * 1280 + (5 * w + i) * 32 + (lane & 3) * 8;
      const u32x2 lo = ldtr(a), hi = ldtr(a + 4 * 5152);
      st128<16>(rw, WS_WOP + wo_frag(5 * w + i, mi - 64) + lane * 16, u32x4{lo[0], lo[1], hi[0], hi[1]});
    }
  } else {
    const int i0 = (mi - 84) * 512 + tid;
    nap<K1DELAY>();
    if (i0 < 6272) st128<16>(rw, WS_FL + i0 * 16, u32x4{0u, 0u, 0u, 0u});
#pragma unroll
    for (int it = 0; it < 6; it++) { const int i = 98304 + i0 + it * 14336; if (i < (int)RBSZ / 16) st128<16>(rw, WS_RB1 + i * 16, u32x4{EMPTY, EMPTY, EMPTY, EMPTY}); }
  }
}

#define PINF(x) asm volatile("" : "+v"(x))
DI void wave_sync(LDS_AS unsigned* c, unsigned n) {
  asm volatile("s_waitcnt lgkmcnt(0)" ::: "memory");
  if ((threadIdx.x & 63) == 0) __hip_atomic_fetch_add(c, 1u, __ATOMIC_RELAXED, __HIP_MEMORY_SCOPE_WORKGROUP);
  while (__builtin_amdgcn_readfirstlane(*(volatile LDS_AS unsigned*)c) < n) {}
  asm volatile("" ::: "memory");
}
DI void put_scale(lptr lds, int q, int tid, const u32x2* sp) {
  f32x4 r = {640.f * 1e-5f, 640.f * 1e-5f, 640.f * 1e-5f, 640.f * 1e-5f};
#pragma unroll
  for (int i = 0; i < 8; i++) r += f32x4{bflo(sp[i][0]), bfhi(sp[i][0]), bflo(sp[i][1]), bfhi(sp[i][1])};
#pragma unroll
  for (int e = 0; e < 4; e++) r[e] = __builtin_amdgcn_rsqf(r[e] * RSK);
  ((LDS_AS f32x4*)(lds + 2 * SLOT32))[q * 256 + tid] = r;
}
struct ColIn { u32x4 c64, s64, c32, s32, wf[2]; };
DI int col_of() { return blockIdx.x < 64 ? 192 + 2 * blockIdx.x + (wave_id() >> 2) : blockIdx.x - 64; }
DI void col_load(ColIn& k, rsrc_t rw, bool tab, bool filt) {
  int lane = threadIdx.x & 63; PINF(lane);
  const int w = wave_id() & 3;
  const u32x4* ct = (const u32x4*)&g_ct + lane;
  if (tab) { k.c64 = ct[(2 * w) * 64]; k.s64 = ct[(2 * w + 1) * 64]; k.c32 = ct[8 * 64]; k.s32 = ct[9 * 64]; }
  if (filt) {
#pragma unroll
    for (int t = 0; t < 2; t++) k.wf[t] = gl128<16>(rw, WS_WF + col_of() * 8192 + ((4 * t + (lane >> 4)) * 64 + 16 * w + (lane & 15)) * 16);
  }
}
template <bool FIRST>
DI void col_phase(rsrc_t rw, lptr lds, ColIn kin) {
  int lane = threadIdx.x & 63; PINF(lane);
  const int wv = wave_id(), w = wv & 3, q = wv >> 2, tid = w * 64 + lane, g = lane >> 4, r16 = lane & 15;
  const int c = col_of();
  const lptr img = lds + q * SLOT32;
  f32x4 xr[2], xi[2], rs[2]; u32x4 xb[2];
  u32x4 c64[2], s64[2], n64[2], c32[2], s32[2], n32[2];
  if (FIRST) col_load(kin, rw, true, false);
  SB();
  f32x4 t1c[2], t1s[2], t2c[2], t2s[2];
  {
    const float f1 = (float)(16 * w + r16) * (1.f / 2048.f), b1 = (float)(4 * g), f2 = (float)(16 * w + 4 * g) * (1.f / 2048.f), b2 = (float)r16;
#pragma unroll
    for (int t = 0; t < 2; t++) {
#pragma unroll
      for (int e = 0; e < 4; e++) {
        const float a1 = (b1 + (float)(16 * t + e)) * f1, a2 = (b2 + (float)(16 * t)) * (f2 + (float)e * (1.f / 2048.f));
        t1c[t][e] = __builtin_amdgcn_cosf(a1); t1s[t][e] = __builtin_amdgcn_sinf(-a1);
        t2c[t][e] = __builtin_amdgcn_cosf(a2); t2s[t][e] = __builtin_amdgcn_sinf(a2);
      }
      PINF(t1c[t]); PINF(t1s[t]); PINF(t2c[t]); PINF(t2s[t]);
    }
  }
  int wo_ = r16 * 128 + (((2 * w + (g >> 1)) ^ (r16 >> 1)) * 16) + (g & 1) * 8, ro_ = r16 * 128 + ((g ^ (r16 >> 1)) * 16), ro2 = ro_ ^ 64;
  PINF(wo_); PINF(ro_); PINF(ro2);
  {
    const unsigned par = (lane & 1) ? 0x80008000u : 0u;
    c64[0] = kin.c64; s64[0] = kin.s64; c32[0] = kin.c32; s32[0] = kin.s32;
    c64[1] = c64[0] ^ par; s64[1] = s64[0] ^ par; n64[0] = s64[0] ^ 0x80008000u; n64[1] = s64[1] ^ 0x80008000u;
    c32[1] = c32[0] ^ 0x80000000u; s32[1] = s32[0] ^ 0x80000000u; n32[0] = s32[0] ^ 0x80008000u; n32[1] = s32[0] ^ 0x00008000u;
    PINF(c64[1]); PINF(s64[1]); PINF(n64[0]); PINF(n64[1]); PINF(c32[1]); PINF(s32[1]); PINF(n32[0]); PINF(n32[1]);
  }
  SB();
  if (FIRST) {
    raw_bar();
#pragma unroll
    for (int t = 0; t < 2; t++) { xb[t] = gl128<16>(rw, WS_RB0 + ((tid >> 2) * 641 + ((tid >> 2) & 1) + 2 * c + t) * 64 + (tid & 3) * 16); rs[t] = asf(gl128<16>(rw, WS_RS1 + (1024 * t + 4 * tid) * 4)); }
    col_load(kin, rw, false, true);
    SB();
#pragma unroll
    for (int t = 0; t < 2; t++) {
      xr[t] = f32x4{bflo(xb[0][2 * t]), bfhi(xb[0][2 * t]), bflo(xb[0][2 * t + 1]), bfhi(xb[0][2 * t + 1])};
      xi[t] = f32x4{bflo(xb[1][2 * t]), bfhi(xb[1][2 * t]), bflo(xb[1][2 * t + 1]), bfhi(xb[1][2 * t + 1])};
    }
  } else {
    drain();
    u32x2 sp[8];
    const unsigned so = WS_SSQ + (blockIdx.x & 7) * 32768 + (1024 * q + 4 * tid) * 2, xo = WS_R3 + ((tid >> 2) * 642 + 2 * c) * 64 + (tid & 3) * 16;
    bool bs = true, bx = true;
#pragma unroll 1
    for (int spins = 0; spins < (1 << 18); spins++) {
      if (bx) {
#pragma unroll
        for (int t = 0; t < 2; t++) { xr[t] = asf(gl128<16>(rw, xo + 64 * 642 * 64 * t)); xi[t] = asf(gl128<16>(rw, xo + 64 * 642 * 64 * t + 64)); }
      }
      if (bs) {
#pragma unroll
        for (int i = 0; i < 8; i++) sp[i] = gl64<16>(rw, so + i * 4096);
      }
      unsigned m = 0;
#pragma unroll
      for (int t = 0; t < 2; t++) m = umax4(umax4(m, asu(xr[t])), asu(xi[t]));
      bx = __builtin_amdgcn_ballot_w64(m == EMPTY) != 0;
      m = 0;
#pragma unroll
      for (int i = 0; i < 8; i++) m = max(max(m, sp[i][0]), sp[i][1]);
      bs = __builtin_amdgcn_ballot_w64(m == EMPTY) != 0;
      if (!bs && !bx) break;
      nap<YSLEEP>();
    }
    put_scale(lds, q, tid, sp);
    LDS_AS f32x4* ex = (LDS_AS f32x4*)(lds + 2 * SLOT32) + tid;
    lds_bar();
    rs[0] = ex[0]; rs[1] = ex[256];
  }
#pragma unroll
  for (int t = 0; t < 2; t++) {
    u32x2 pb = pk4(xi[t] * rs[t]);
    if (c == 0) pb = u32x2{0u, 0u};
    *(LDS_AS u32x2*)(img + wo_ + t * 2048) = pk4(xr[t] * rs[t]);
    *(LDS_AS u32x2*)(img + 4096 + wo_ + t * 2048) = pb;
  }
  lds_bar();
  u32x4 ur[2][2], ui[2][2], br, bi;
  f32x4 ar[2], ai[2];
  const f32x4 z4 = {0.f, 0.f, 0.f, 0.f};
  auto rd_in = [&](int off) {
#pragma unroll
    for (int ks = 0; ks < 2; ks++)
#pragma unroll
      for (int t = 0; t < 2; t++) { const lptr p = img + off + (ks ? ro2 : ro_) + t * 2048; ur[t][ks] = ld128(p); ui[t][ks] = ld128(p + 4096); }
    SB();
  };
  auto cmul = [&](const f32x4* cc, const f32x4* sn) {
    br = pk8(ar[0] * cc[0] - ai[0] * sn[0], ar[1] * cc[1] - ai[1] * sn[1]);
    bi = pk8(ai[0] * cc[0] + ar[0] * sn[0], ai[1] * cc[1] + ar[1] * sn[1]);
  };
  rd_in(0);
#pragma unroll
  for (int ks = 0; ks < 2; ks++) {
#pragma unroll
    for (int t = 0; t < 2; t++) { ar[t] = mfma16(ur[t][ks], c64[ks], ks ? ar[t] : z4); ai[t] = mfma16(ui[t][ks], c64[ks], ks ? ai[t] : z4); }
#pragma unroll
    for (int t = 0; t < 2; t++) { ar[t] = mfma16(ui[t][ks], s64[ks], ar[t]); ai[t] = mfma16(ur[t][ks], n64[ks], ai[t]); }
  }
  const f32x4 fa[2] = {asf(kin.wf[0] << 16), asf(kin.wf[1] << 16)}, fb[2] = {asf(kin.wf[0] & 0xFFFF0000u), asf(kin.wf[1] & 0xFFFF0000u)};
  cmul(t1c, t1s);
#pragma unroll
  for (int t = 0; t < 2; t++) { ar[t] = mfma16(c32[t], br, z4); ai[t] = mfma16(c32[t], bi, z4); }
#pragma unroll
  for (int t = 0; t < 2; t++) { ar[t] = mfma16(s32[t], bi, ar[t]); ai[t] = mfma16(n32[t], br, ai[t]); }
  cmul(fa, fb);
#pragma unroll
  for (int t = 0; t < 2; t++) { ar[t] = mfma16(br, c32[t], z4); ai[t] = mfma16(bi, c32[t], z4); }
#pragma unroll
  for (int t = 0; t < 2; t++) { ar[t] = mfma16(bi, n32[t], ar[t]); ai[t] = mfma16(br, s32[t], ai[t]); }
  cmul(t2c, t2s);
#pragma unroll
  for (int t = 0; t < 2; t++) {
    *(LDS_AS u32x2*)(img + 8192 + wo_ + t * 2048) = u32x2{br[2 * t], br[2 * t + 1]};
    *(LDS_AS u32x2*)(img + 12288 + wo_ + t * 2048) = u32x2{bi[2 * t], bi[2 * t + 1]};
  }
  lds_bar();
  rd_in(8192);
#pragma unroll
  for (int ks = 0; ks < 2; ks++) {
#pragma unroll
    for (int t = 0; t < 2; t++) { ar[t] = mfma16(c64[ks], ur[t][ks], ks ? ar[t] : xr[t]); ai[t] = mfma16(c64[ks], ui[t][ks], ks ? ai[t] : xi[t]); }
#pragma unroll
    for (int t = 0; t < 2; t++) { ar[t] = mfma16(n64[ks], ui[t][ks], ar[t]); ai[t] = mfma16(s64[ks], ur[t][ks], ai[t]); }
  }
  const unsigned ob = (FIRST ? WS_RB1 : WS_RB2) + (2 * c * RBP + 8 * tid) * 2;
  st128<16>(rw, ob, clean(pk8(ar[0], ar[1])));
  st128<16>(rw, ob + RBP * 2, c == 0 ? u32x4{0u, 0u, 0u, 0u} : clean(pk8(ai[0], ai[1])));
}

DI f32x16 mfma32(u32x4 a, u32x4 b, f32x16 c) {
  return __builtin_amdgcn_mfma_f32_32x32x16_bf16(__builtin_bit_cast(bf16x8, a), __builtin_bit_cast(bf16x8, b), c, 0, 0, 0);
}
DI f32x16 mfma32k8(u32x2 a, u32x2 b, f32x16 c) {
  return __builtin_amdgcn_mfma_f32_32x32x8bf16_1k(__builtin_bit_cast(s16x4, a), __builtin_bit_cast(s16x4, b), c, 0, 0, 0);
}
DI f32x16 scores(u32x4 k1, u32x2 k2, u32x4 q1, u32x2 q2) {
  f32x16 z;
#pragma unroll
  for (int i = 0; i < 16; i++) z[i] = 0.f;
  f32x16 s = mfma32(k1, q1, z);
  asm volatile("s_nop 11" : "+v"(s));
  return mfma32k8(k2, q2, s);
}
DI float max16(const f32x16& s) {
  float m = fmaxf(s[0], s[1]);
#pragma unroll
  for (int i = 2; i < 16; i++) m = fmaxf(m, s[i]);
  return m;
}
#define STR_(x) #x
#define STR(x) STR_(x)
#define KOF(t) ((((t)&7)>>1)*11264+((t)&1)*1280)
#define VOF(t) ((((t)&7)>>1)*11264+5120+((t)&1)*1536)
#define EXP(d, s) "v_exp_f32 v" #d ", v" #s "\n\t"
#define CVT(d, s) "v_cvt_pk_bf16_f32 v" #d ", v" #s ", v[" #s "+1]\n\t"
#define STEPA(t) \
  "v_mfma_f32_32x32x16_bf16 v[48:63], v[80:83], v[136:139], v[120:135]\n\t" \
  "ds_read_b128 v[80:83], v100 offset:" STR(KOF(t+1)) "\n\t" \
  EXP(64,32) EXP(65,33) CVT(78,68) CVT(79,70) EXP(66,34) EXP(67,35) \
  "v_mfma_f32_32x32x16_bf16 v[104:119], v[90:93], v[76:79], v[104:119]\n\t" \
  "ds_read_b64_tr_b16 v[90:91], v102 offset:" STR(VOF(t)+768) "\n\t" \
  "ds_read_b64_tr_b16 v[92:93], v102 offset:" STR(VOF(t)+1152) "\n\t" \
  EXP(68,36) EXP(69,37) CVT(72,64) CVT(73,66) EXP(70,38) EXP(71,39) \
  "v_mfma_f32_32x32x8_bf16 v[48:63], v[84:85], v[140:141], v[48:63]\n\t" \
  "ds_read_b64 v[84:85], v101 offset:" STR(KOF(t+1)) "\n\t" \
  EXP(64,40) EXP(65,41) CVT(74,68) CVT(75,70) EXP(66,42) EXP(67,43) \
  "s_waitcnt lgkmcnt(4)\n\t" \
  "v_mfma_f32_32x32x16_bf16 v[0:15], v[86:89], v[72:75], v[0:15]\n\t" \
  EXP(68,44) EXP(69,45) CVT(76,64) CVT(77,66) EXP(70,46) EXP(71,47)
#define STEPB(t) \
  "s_waitcnt lgkmcnt(0)\n\t" \
  "v_mfma_f32_32x32x16_bf16 v[32:47], v[80:83], v[94:97], v[16:31]\n\t" \
  EXP(64,48) EXP(65,49) CVT(78,68) CVT(79,70) EXP(66,50) EXP(67,51) \
  "v_mfma_f32_32x32x16_bf16 v[0:15], v[90:93], v[76:79], v[0:15]\n\t" \
  EXP(68,52) EXP(69,53) CVT(72,64) CVT(73,66) EXP(70,54) EXP(71,55) \
  "v_mfma_f32_32x32x8_bf16 v[32:47], v[84:85], v[98:99], v[32:47]\n\t" \
  EXP(64,56) EXP(65,57) CVT(74,68) CVT(75,70) EXP(66,58) EXP(67,59) \
  "v_mfma_f32_32x32x16_bf16 v[104:119], v[86:89], v[72:75], v[104:119]\n\t" \
  "ds_read_b64_tr_b16 v[86:87], v102 offset:" STR(VOF(t+1)) "\n\t" \
  "ds_read_b64_tr_b16 v[88:89], v102 offset:" STR(VOF(t+1)+384) "\n\t" \
  EXP(68,60) EXP(69,61) CVT(76,64) CVT(77,66) EXP(70,62) EXP(71,63)
#define PR(n) "s_setprio " #n "\n\t"
#define CHUNK(i) PR(1) STEPA(2*(i)) PR(0) STEPB(2*(i)) "s_barrier\n\t" PR(2) STEPA(2*(i)+1) STEPB(2*(i)+1)
#define Z4(a) "v_mov_b64 v[" #a ":" #a "+1], 0\n\tv_mov_b64 v[" #a "+2:" #a "+3], 0\n\t"

#define R4(b) "v[" STR(b) ":" STR(b) "+3]"
#define MM(d, a, b) "v_mfma_f32_16x16x32_bf16 " R4(d) ", " R4(a) ", " R4(b) ", " R4(d) "\n\t"
#define GM(B, m, i) MM(16*m+4*i, B+8+4*i, B+4*m)
#define RT(B, m, hf, k) "ds_read_b64_tr_b16 v[" STR(B+4*m+2*hf) ":" STR(B+4*m+2*hf+1) "], v[84+" #m "] offset:" STR(k*8192+hf*1024) "\n\t"
#define RS(B, i, k) "ds_read_b128 " R4(B+8+4*i) ", v86 offset:" STR(i*2048+k*1024) "\n\t"
#define GSTEP(B, C, k) GM(B,0,0) RT(C,0,0,k) RT(C,0,1,k) GM(B,1,0) RS(C,0,k) RT(C,1,0,k) GM(B,0,1) RT(C,1,1,k) RS(C,1,k) GM(B,1,1) RS(C,2,k) \
  GM(B,0,2) RS(C,3,k) GM(B,1,2) GM(B,0,3) GM(B,1,3) MM(32,B,B)
#define GEMM_ASM(EXTRA) \
  Z4(0) Z4(4) Z4(8) Z4(12) Z4(16) Z4(20) Z4(24) Z4(28) Z4(32) "s_mov_b32 %[c], 0\n\ts_barrier\n\t" \
  RT(36,0,0,0) RT(36,0,1,0) RS(36,0,0) RT(36,1,0,0) RT(36,1,1,0) RS(36,1,0) RS(36,2,0) RS(36,3,0) EXTRA \
  "L_gemm_%=:\n\ts_add_u32 %[c], %[c], 1\n\ts_and_b32 %[t], %[c], 3\n\ts_cselect_b32 %[t], 1, -3\n\ts_lshl_b32 %[t], %[t], 15\n\ts_waitcnt lgkmcnt(0)\n\t" \
  GSTEP(36,60,1) \
  "v_add_u32 v84, %[t], v84\n\tv_add_u32 v85, %[t], v85\n\tv_add_u32 v86, %[t], v86\n\ts_waitcnt lgkmcnt(0)\n\ts_barrier\n\ts_setprio 1\n\t" \
  GSTEP(60,36,0) "s_setprio 0\n\t" \
  "s_cmp_lg_u32 %[c], 9\n\ts_cbranch_scc1 L_gemm_%=\n\ts_waitcnt lgkmcnt(0)\n\t" \
  GSTEP(36,60,1) "s_waitcnt lgkmcnt(0)\n\t" \
  MM(32,60,60) GM(60,0,0) GM(60,1,0) GM(60,0,1) GM(60,1,1) "s_nop 4\n\t" \
  "v_cndmask_b32 v36, v32, v33, %[m1]\n\tv_cndmask_b32 v37, v34, v35, %[m1]\n\tv_mov_b32 v38, 0x3727c5ac\n\tv_cndmask_b32 v36, v36, v37, %[m2]\n\t" \
  "v_fmac_f32 v38, 0x3acccccd, v36\n\tv_cmp_u_f32 vcc, v36, v36\n\tv_rsq_f32 v38, v38\n\ts_cbranch_vccz L_gok_%=\n\tds_write_b32 v90, v90\n\t" \
  "L_gok_%=:\n\tds_write_b32 v87, v38\n\ts_waitcnt lgkmcnt(0)\n\ts_barrier\n\t" \
  "ds_read_b32 v36, v88\n\tds_read_b32 v37, v89\n\tds_read_b32 v38, v90\n\t" \
  GM(60,0,2) GM(60,1,2) GM(60,0,3) GM(60,1,3) "s_waitcnt vmcnt(0) lgkmcnt(0)"
#define GEMM_OUT "={v[0:3]}"(acc[0][0]), "={v[4:7]}"(acc[0][1]), "={v[8:11]}"(acc[0][2]), "={v[12:15]}"(acc[0][3]), \
    "={v[16:19]}"(acc[1][0]), "={v[20:23]}"(acc[1][1]), "={v[24:27]}"(acc[1][2]), "={v[28:31]}"(acc[1][3]), \
    "={v36}"(rs[0]), "={v37}"(rs[1]), "={v38}"(vote), "+{v84}"(am0), "+{v85}"(am1), "+{v86}"(as), [c] "=&s"(c_), [t] "=&s"(t_)
#define GEMM_IN "{v87}"(a_w), "{v88}"(a_r[0]), "{v89}"(a_r[1]), "{v90}"(a_v), [m1] "s"(0xAAAAAAAAAAAAAAAAull), [m2] "s"(0xCCCCCCCCCCCCCCCCull)
#define GEMM_CLOB "memory", "scc", "vcc", "v32", "v33", "v34", "v35", "v39", "v40", "v41", "v42", "v43", "v44", "v45", "v46", "v47", "v48", "v49", "v50", \
    "v51", "v52", "v53", "v54", "v55", "v56", "v57", "v58", "v59", "v60", "v61", "v62", "v63", "v64", "v65", "v66", "v67", "v68", "v69", "v70", "v71", \
    "v72", "v73", "v74", "v75", "v76", "v77", "v78", "v79", "v80", "v81", "v82", "v83"
#define ITL(d, a) "global_load_dwordx4 " R4(d) ", v" #a ", %[it]\n\tglobal_load_dwordx4 " R4(d+4) ", v" #a ", %[it] offset:1024\n\t"

constexpr int HR = 4 * KVC;
constexpr int L_A = 2 * HR;
constexpr int L_EX = 15360;
constexpr int L_T = 4 * SLOT32, L_IA = L_T + 10240, L_U = L_A + 10240;
constexpr int L_MISC = L_A + 70 * 1024;
constexpr int LDS_FUSED = L_MISC + 2048;

DI bool poll16(rsrc_t rw, unsigned off, unsigned need) {
  const int lane = threadIdx.x & 63;
#pragma unroll 1
  for (int spins = 0; spins < (1 << 20); spins++) {
    const unsigned v = gl32<16>(rw, off + (lane & 15) * 4);
    const unsigned got = (unsigned)__builtin_amdgcn_ballot_w64(v != 0) & 0xFFFFu;
    if ((got & need) == need) return true;
    nap<1>();
  }
  return false;
}

DI void poll_aux(rsrc_t rw, unsigned off) {
#pragma unroll 1
  for (int spins = 0; spins < (1 << 20); spins++) {
    const u32x4 v = gl128<16>(rw, off + (threadIdx.x & 63) * 16);
    if (!__builtin_amdgcn_ballot_w64(min(min(v[0], v[1]), min(v[2], v[3])) == 0u)) break;
    nap<4>();
  }
}
DI void poll_tag(rsrc_t rw, u32x2 tag, unsigned long long need) {
  const unsigned o = WS_F0 + (blockIdx.x & 7) * 2048 + (threadIdx.x & 63) * 32;
#pragma unroll 1
  for (int spins = 0; spins < (1 << 20); spins++) {
    const u32x4 a = gl128<16>(rw, o), b = gl128<16>(rw, o + 16);
    const unsigned d = (a[0] ^ tag[0]) | (a[1] ^ tag[1]) | (a[2] ^ tag[0]) | (a[3] ^ tag[1]) | (b[0] ^ tag[0]) | (b[1] ^ tag[1]) | (b[2] ^ tag[0]) | (b[3] ^ tag[1]);
    if (!(__builtin_amdgcn_ballot_w64(d != 0u) & need)) break;
    nap<TSLEEP>();
  }
}
#define LAYER_VARS \
  extern __shared__ __attribute__((aligned(16))) char smem[]; \
  const lptr lds = (lptr)smem; \
  const rsrc_t rw = mk_rsrc(ws, WS_END); \
  int lane = threadIdx.x & 63; asm volatile("" : "+v"(lane)); \
  const int w = wave_id(), g = lane >> 4, r16 = lane & 15; \
  const int xcd = blockIdx.x & 7, bi = blockIdx.x >> 3, u = 8 * (xcd & 1) + (bi & 7), qt = 4 * (xcd >> 1) + (bi >> 3); \
  const int hf = u & 1, ct = u >> 1;     \
  constexpr unsigned FKV = WS_FL + (FINAL ? 32768 : 0), KV = WS_KV + (FINAL ? LAYER2 : 0), OB = WS_O + (FINAL ? LAYER2 : 0); \
  constexpr unsigned AUX = WS_AUX + (FINAL ? 1024 : 0), RB = FINAL ? WS_RB2 : WS_RB1; \
  (void)hf; (void)AUX; (void)RB; (void)FKV; \
  LDS_AS unsigned* misc = (LDS_AS unsigned*)(lds + L_MISC + 1024) + (FINAL ? 8 : 0);

template <bool FINAL>
DI void loader(char* ws, u32x2 tag) {
  LAYER_VARS
  {
    const int lw = w - 8;
    {
      unsigned vm[4];
#pragma unroll
      for (int r = 0; r < 4; r++) vm[r] = (4 * (4 * lw + r) + g) * (RBP * 2) + 256 * qt + (((r16 >> 1) ^ (g | ((r & 2) << 1))) * 32) + (r16 & 1) * 16 + 3072 - 1024 * r;
      const unsigned l0 = lds_addr(lds) + lw * 4096, v0 = 2 * lw * 20480 + lane * 16;
      constexpr int CNT = 8;
      auto lds_w = [&](int s) {
        const char* sb = ws + WS_WQ + u * 163840 + s * 2048;
        dma_s(v0, v0 + 20480 - 2048, sb, l0 + (s & 3) * SLOT32 + 16384);
      };
      auto lds_a = [&](int s) {
        dma_a(vm, ws + RB - 3072 + s * (64 * RBP * 2), l0 + (s & 3) * SLOT32); };
#pragma unroll 1
      for (int att = 0;; att++) {
        if (att == 0) { raw_bar(); if (FINAL) raw_bar(); else poll_tag(rw, tag, 0x7FFF0000ull); }
        lds_w(0); lds_w(1);
        if (att == 0) raw_bar();
        lds_w(2); lds_w(3);
        if (FINAL) dma2(40960 + lw * 2048 + lane * 16, (const char*)&g_it, lds_addr(lds) + L_IA + lw * 2048);
        auto probe = [&](int s0, int n) {
          const int pl = min(lane, n - 1);
          const unsigned po = RB + ((64 * (s0 + (pl >> 3)) + 16 * lw + 2 * (pl & 7) + 1) * RBP + 128 * qt + 126) * 2;
#pragma unroll 1
          for (int spins = 0; spins < (1 << 20); spins++) {
            if (!__builtin_amdgcn_ballot_w64(gl32<16>(rw, po) == EMPTY)) break;
            nap<XSLEEP>();
          }
        };
        if (att == 0) {
          raw_bar();
          if (blockIdx.x >= 64) nap<XDELAY>();
          probe(0, 48);
        }
        drain();
        unsigned nd = 0;
        if (att == 0 && lane == 0) nd = __hip_atomic_fetch_add(misc + 4, 1u, __ATOMIC_RELAXED, __HIP_MEMORY_SCOPE_WORKGROUP);
        if (nd == 11) st32<16>(rw, AUX + blockIdx.x * 4, 1u);
#pragma unroll 1
        for (int s = 0; s < 4; s++) lds_a(s);
        vm_bar<12>(); vm_bar<8>();
        if (att == 0) probe(6, 32);
        lds_a(4); lds_w(4);
        vm_bar<4 + CNT>();
#pragma unroll 1
        for (int s = 5; s < 10; s++) { lds_a(s); lds_w(s); vm_bar<2 * CNT>(); }
        vm_bar<CNT>();
        poll_aux(rw, AUX);
        if (!FINAL) { poll_tag(rw, tag, ~0ull); if (blockIdx.x == 0 && threadIdx.x == 512) st64<16>(rw, WS_EP, tag); }
        vm_bar<0>();
        if (FINAL) {
#pragma unroll
          for (int i = 0; i < 3; i++) { const int pc = min(3 * lw + i, 9); dma1_sc1(RB + ((64 * pc + lane) * RBP + 128 * qt + 8 * u) * 2, ws, lds_addr(lds) + L_T + pc * 1024); }
        }
        raw_bar();
        if (att || !*(volatile LDS_AS unsigned*)(misc + 2)) break;
      }
    }
    const int hh = lw >> 1, hp = lw & 1;
    const unsigned kvv = KV + (2 * u + hh) * 16 * KVC + hp * 5120 + lane * 16;
    const unsigned lkv = lds_addr(lds) + hh * HR + hp * 5120;
    const unsigned wv = (FINAL ? WS_WOP : WS_WOF) + (ct * 2 + hh) * 51200 + lane * 16;
    const unsigned myfl = FKV + (u * 16 + qt) * 128;
    vm_bar<0>();
    {
      u32x4 v[6];
#pragma unroll
      for (int i = 0; i < 6; i++) v[i] = ld128(lds + hh * HR + hp * 5120 + lane * 16 + i * 1024);
#pragma unroll
      for (int i = 0; i < 6; i++) st128<16>(rw, kvv + qt * KVC + i * 1024, clean(v[i]));
    }
    drain();
    unsigned cnt = 0;
    if (lane == 0) cnt = __hip_atomic_fetch_add(misc, 1u, __ATOMIC_RELAXED, __HIP_MEMORY_SCOPE_WORKGROUP);
    if (__builtin_amdgcn_readfirstlane(cnt) == 3 && lane < 16) st32<16>(rw, FKV + (u * 16 + lane) * 128 + qt * 4, 1u);
    auto ldkv = [&](int seq) {
      const int ch = seq == 0 ? qt : seq < 13 ? ((qt & 12) + 3 + seq) & 15 : (qt & 12) | ((qt + seq) & 3);
      dma6_sc1(kvv + ch * KVC, ws, lkv + (seq & 3) * KVC);
    };
    auto fetch = [&](int seq) {
      const unsigned src = kvv + ((((qt & 12) + 3 + seq) & 15)) * KVC;
      u32x4 v[6];
#pragma unroll 1
      for (int spins = 0; spins < (1 << 18); spins++) {
#pragma unroll
        for (int i = 0; i < 6; i++) v[i] = gl128<16>(rw, src + i * 1024);
        unsigned m = 0;
#pragma unroll
        for (int i = 0; i < 6; i++) m = umax4(m, v[i]);
        if (!__builtin_amdgcn_ballot_w64(m == EMPTY)) break;
        nap<2>();
      }
#pragma unroll
      for (int i = 0; i < 6; i++) *(LDS_AS u32x4*)(lds + hh * HR + hp * 5120 + seq * KVC + lane * 16 + i * 1024) = v[i];
    };
#pragma unroll 1
    for (int attempt = 0;; attempt++) {
      int c = -2;
      if (attempt) { ldkv(0); vm_bar<0>(); }
      else {
        fetch(1); lds_bar();
        fetch(2); poll16(rw, myfl, 12u << (((qt & 12) + 4) & 15)); ldkv(3); lds_bar();
        c = 1;
      }
#pragma unroll 1
      for (; c < 13; c++) {
        if (c == 2 && !attempt) poll16(rw, myfl, 0xFFFFu);
        if (!FINAL && !attempt) {
          if (c >= 2 && c < 9) {
            const u32x4 e = {EMPTY, EMPTY, EMPTY, EMPTY};
            const unsigned o = OB + LAYER2 + (((c - 2) * 256 + blockIdx.x) * 4 + lw) * 2048 + lane * 16;
            st128<16>(rw, o, e); st128<16>(rw, o + 1024, e);
            st128<16>(rw, ((qt & 3) < 2 && c < 8) ? kvv + LAYER2 + qt * KVC + (c - 2) * 1024 : WS_END, e);
            st128<16>(rw, c < 5 ? OB + ((4 * qt + lw) * 32 + 2 * u) * 1280 + min((c - 2) * 1024, 1536) + lane * 16 : WS_END, e);
          }
          if (c == 10) {
            drain();
            unsigned n = 0;
            if (lane == 0) n = __hip_atomic_fetch_add(misc + 3, 1u, __ATOMIC_RELAXED, __HIP_MEMORY_SCOPE_WORKGROUP);
            if (__builtin_amdgcn_readfirstlane(n) == 3 && lane < 32) st32<16>(rw, WS_FL + 65536 + (lane < 16 ? (u * 16 + lane) * 128 + qt * 4 : (r16 * 16 + qt) * 128 + 64 + u * 4), 1u);
          }
        }
        if (c == 11 && bi < (FINAL ? 2 : 4)) {
          const char* p = bi < 2 ? (const char*)CODE_ADDR(SYM_MAIN) : (const char*)&g_it;
          unsigned sv;
          asm volatile(M0SET "global_load_lds_dword %[v], %[s]\n\t" M0RST : [sv] "=&s"(sv)
                       : [v] "v"(min((bi & 1) * 256 + lw * 64 + lane, CODE_LINES - 1) * 128), [s] "s"(p), [l] "s"(lds_addr(lds) + L_MISC + 1536) : "memory");
        }
        if (c >= 2) { const int e = min(4 * (c - 2) + 2 * hp, 33); dma2(wv + e * 1024, ws, lds_addr(lds) + L_A + (hh * 35 + e) * 1024); }
        ldkv(c + 3);
        if (c == 0) vm_bar<12>();
        if (c >= 2) vm_bar<8>(); else if (c >= 0) vm_bar<6>();
      }
      if (!FINAL && !attempt) poll16(rw, myfl + 65536 + hp * 64, 0xFFFFu);
      vm_bar<0>();
      dma_s(wv + (35 + 7 * hp) * 1024, wv + (35 + 7 * hp) * 1024, ws, lds_addr(lds) + hh * HR + 7 * hp * 1024);
      dma_s(wv + (39 + 7 * hp) * 1024, wv + (39 + 7 * hp) * 1024, ws, lds_addr(lds) + hh * HR + (4 + 7 * hp) * 1024);
      raw_bar();
      vm_bar<0>();
      if (attempt || !*(volatile LDS_AS unsigned*)(misc + 1)) break;
      raw_bar();
    }
    if (!FINAL) { raw_bar(); raw_bar(); }
  }
}

template <bool FINAL>
DI void compute(char* ws, u16* out, ColIn& kin) {
  LAYER_VARS
  const int wm = w & 3, wn = w >> 2, h = lane >> 5;
  if (blockIdx.x < 64 || wn == 0) col_phase<!FINAL>(rw, lds, kin);
  else if (FINAL) {
    drain();
    nap<YIDLE>();
    u32x2 sp[8];
#pragma unroll 1
    for (int spins = 0; spins < (1 << 18); spins++) {
      unsigned m = 0;
#pragma unroll
      for (int i = 0; i < 8; i++) sp[i] = gl64<16>(rw, WS_SSQ + (blockIdx.x & 7) * 32768 + (1024 + 4 * (wm * 64 + lane)) * 2 + i * 4096);
#pragma unroll
      for (int i = 0; i < 8; i++) m = max(max(m, sp[i][0]), sp[i][1]);
      if (!__builtin_amdgcn_ballot_w64(m == EMPTY)) break;
      nap<YSLEEP>();
    }
    put_scale(lds, 1, wm * 64 + lane, sp);
    lds_bar(); raw_bar(); raw_bar();
  } else {
    raw_bar(); raw_bar(); raw_bar();
  }
  lds_bar();
  {
    drain();
    unsigned n = 0;
    if (lane == 0) n = __hip_atomic_fetch_add(misc + 4, 1u, __ATOMIC_RELAXED, __HIP_MEMORY_SCOPE_WORKGROUP);
    if (n == 11) st32<16>(rw, AUX + blockIdx.x * 4, 1u);
  }

  const int head = 2 * u + wn, kh = w & 1;
  const lptr ring = lds + wn * HR;
  const int pblk = 128 * qt + 64 * hf + 32 * (wm >> 1);
  const unsigned r3t = WS_R3 + (64 * (wm & 1) + (pblk >> 5)) * (642 * 64);
  u32x2 x4v[3]; float dc = 0.f;
  unsigned os = OB + ((4 * qt + wm) * 32 + head) * 1280 + (((lane << 2) & 16) + ((lane >> 1) & 12) + (lane & 3)) * 8;
  unsigned o1 = OB + ((pblk >> 5) * 32 + 16 * wn) * 1280 + (g >> 1) * 1280 + (g & 1) * 512 + (wm & 1) * 256 + r16 * 16;
  unsigned o2 = OB + ((pblk >> 5) * 32 + 16 * wn) * 1280 + 2 * g * 1280 + 1024 + (wm & 1) * 128 + r16 * 8;
  u32x4 o3 = {0u, 0u, 0u, 0u};
  if (!FINAL) {
#pragma unroll
    for (int i = 0; i < 3; i++) {
      const int j = 80 * ct + 48 * wn + 16 * (wn ? (i & 1) : i) + r16;
      o3[i] = (j == 1 ? WS_RB0 + ((pblk >> 5) * 641 + ((pblk >> 5) & 1) + 1) * 64 : WS_RB1 + (j * RBP + pblk) * 2) + 16 * g + 8 * (wm & 1);
    }
  } else {
    o3[3] = r3t + 64 + r16 * 4;
#pragma unroll
    for (int i = 0; i < 3; i++) o3[i] = WS_X4 + ((pblk + trow(wm & 1, r16)) * 640 + 80 * ct + 48 * wn + 16 * (wn ? (i & 1) : i) + 4 * g) * 2;
  }
  asm volatile("" : "+v"(os), "+v"(o1), "+v"(o2), "+v"(o3));
  unsigned ka = lds_addr(ring) + kh * 2560 + lane * 16, kb = lds_addr(ring) + kh * 2560 + 1024 + (lane & 31) * 8;
  unsigned va = lds_addr(ring) + kh * 3072 + (4 * h + (r16 >> 2)) * 48 + ((g & 1) ? ((lane & 3) ? 40 : 32) : (lane & 3) * 8);
  asm volatile("" : "+v"(ka), "+v"(kb), "+v"(va));
  {
    f32x4 acc[2][4]; float rs[2]; u32x8 im[3];
    unsigned mo[2], a0[2], a1[2], a2[2], a_r[2];
#pragma unroll
    for (int m = 0; m < 2; m++) {
      const int q = r16 >> 2, pr = 16 * (m ^ wn) + r16;
      mo[m] = lds_addr(lds) + (8 * g + q) * 256 + (((2 * wm + (m ^ wn)) ^ (q | ((g & 1) << 2))) * 32) + (lane & 3) * 8;
      const unsigned qb = L_A + (wn * 4 + wm) * 1280, kb = wn * HR + wm * 1280, vr = wn * HR + 5120 + (32 * wm + pr) * 48 + (g == 3 ? 32 : g * 16);
      a0[m] = ((g >> 1) ? kb : qb) + (g & 1) * 512 + pr * 16;
      a1[m] = g == 2 ? qb + 1024 + pr * 8 : vr; a2[m] = g == 2 ? kb + 1024 + pr * 8 : vr + 8;
      a_r[m] = lds_addr(lds) + L_MISC + (32 * wm + pr) * 4;
      asm volatile("" : "+v"(a0[m]), "+v"(a1[m]), "+v"(a2[m]));
    }
    const unsigned a_w = a_r[0] + (g == (r16 >> 2) ? 0 : 512), a_v = lds_addr((lptr)(misc + 2));
#pragma unroll 1
    for (int att = 0;; att++) {
      unsigned am0 = mo[0], am1 = mo[1], as = lds_addr(lds) + 16384 + wn * 8192 + lane * 16, vote; int c_, t_;
      if (!FINAL) asm volatile(GEMM_ASM("") : GEMM_OUT : GEMM_IN : GEMM_CLOB);
      else asm volatile(GEMM_ASM(ITL(96, 91) ITL(104, 92) ITL(112, 93)) : GEMM_OUT, "={v[96:103]}"(im[0]), "={v[104:111]}"(im[1]), "={v[112:119]}"(im[2])
                        : GEMM_IN, "{v91}"(w * 2048 + lane * 16), "{v92}"((w + 8) * 2048 + lane * 16), "{v93}"((w < 4 ? w + 16 : w) * 2048 + lane * 16), [it] "s"(&g_it) : GEMM_CLOB);
      if (att || !__builtin_amdgcn_readfirstlane(vote)) break;
    }
    SB();
#pragma unroll
    for (int m = 0; m < 2; m++) *(LDS_AS u32x4*)(lds + a0[m]) = pk8(acc[m][0] * rs[m], acc[m][1] * rs[m]);
    asm volatile("s_nop 1" : "+v"(acc[0][2]), "+v"(acc[1][2]), "+v"(acc[0][3]), "+v"(acc[1][3]) :: "memory");
#pragma unroll
    for (int m = 0; m < 2; m++) {
      u32x2 hi = pk4(acc[m][3] * rs[m]);
      if (g == 3) hi = u32x2{0x3F80u, 0u};
      *(LDS_AS u32x2*)(lds + a1[m]) = pk4(acc[m][2] * rs[m]);
      *(LDS_AS u32x2*)(lds + a2[m]) = hi;
    }
    lds_bar();
    if (FINAL) {
      u32x4 ia[3]; u32x2 ib[3];
#pragma unroll
      for (int T = 0; T < 3; T++) { ia[T] = ld128(lds + L_IA + T * 1024 + lane * 16); ib[T] = ld64(lds + L_IA + 3072 + T * 512 + lane * 8); }
      const f32x4 z4 = {0.f, 0.f, 0.f, 0.f};
#pragma unroll
      for (int i = 0; i < 3; i++) {
        const int G = (i < 2 || w < 4) ? w + 8 * i : w;
        const lptr tp = lds + L_T + (32 * G + 8 * g + (r16 >> 2)) * 16 + (lane & 1) * 8;
        const u32x2 f0 = ldtr(tp), f1 = ldtr(tp + 64);
#pragma unroll
        for (int P = 0; P < 2; P++) *(LDS_AS u32x2*)(lds + L_U + (r16 & 7) * 1296 + (2 * G + P) * 32 + g * 8) = pk4(mfma16(u32x4{im[i][4 * P], im[i][4 * P + 1], im[i][4 * P + 2], im[i][4 * P + 3]}, u32x4{f0[0], f0[1], f1[0], f1[1]}, z4));
      }
      wave_sync(misc + 5, 8);
      const lptr up = lds + L_U + w * 1296;
      const u32x2 f0 = ldtr(up + lane * 8), f1 = ldtr(up + 512 + lane * 8), f2 = ldtr(up + 1024 + (lane & 31) * 8);
#pragma unroll
      for (int T = 0; T < 3; T++) {
        const u32x2 v = pk4(mfma16(u32x4{f2[0], f2[1], 0u, 0u}, u32x4{ib[T][0], ib[T][1], 0u, 0u}, mfma16(u32x4{f0[0], f0[1], f1[0], f1[1]}, ia[T], z4)));
        st64<16>(rw, (T < 2 || r16 < 8) ? WS_X4 + ((128 * qt + 8 * u + w) * 640 + 16 * (16 * T + r16) + 4 * g) * 2 : WS_END, u32x2{min(v[0], EMPTY - 1), min(v[1], EMPTY - 1)});
      }
    }
  }
  f32x4 r[3];
  {
    const lptr qpa = lds + L_A + (wn * 4 + wm) * 1280, qpb = lds + L_A + (wn * 4 + (wm ^ 1)) * 1280;
    const u32x4 qa1 = ld128(qpa + lane * 16), qb1 = ld128(qpb + lane * 16);
    u32x2 qa2 = ld64(qpa + 1024 + (lane & 31) * 8), qb2 = ld64(qpb + 1024 + (lane & 31) * 8);
    if (h) { qa2 = u32x2{0u, 0u}; qb2 = qa2; }
    float ma, mb;
    {
      const u32x4 k1 = ld128(ring + lane * 16); const u32x2 k2 = ld64(ring + 1024 + (lane & 31) * 8);
      ma = max16(scores(k1, k2, qa1, qa2)); mb = max16(scores(k1, k2, qb1, qb2));
    }
    for (int attempt = 0;; attempt++) {
      ma = fmaxf(ma, __shfl_xor(ma, 32)); mb = fmaxf(mb, __shfl_xor(mb, 32));
      f32x16 cia, cib, oa, ob;
#pragma unroll
      for (int i = 0; i < 16; i++) { cia[i] = -ma; cib[i] = -mb; }
      int cnt = 4;
      asm volatile(
          "ds_read_b128 v[80:83], v100\n\t"
          "ds_read_b64 v[84:85], v101\n\t"
          "ds_read_b64_tr_b16 v[86:87], v102 offset:5120\n\t"
          "ds_read_b64_tr_b16 v[88:89], v102 offset:5504\n\t"
          Z4(0) Z4(68) Z4(76) Z4(90)
          "s_waitcnt lgkmcnt(0)\n\t"
          "v_mfma_f32_32x32x16_bf16 v[32:47], v[80:83], v[94:97], v[16:31]\n\t"
          Z4(4) Z4(8) Z4(12) Z4(104) Z4(108) Z4(112) Z4(116)
          "v_mfma_f32_32x32x8_bf16 v[32:47], v[84:85], v[98:99], v[32:47]\n\ts_nop 9\n\t"
          "L_attn_%=:\n\t"
          CHUNK(0) CHUNK(1) CHUNK(2) CHUNK(3)
          "s_sub_u32 %[cnt], %[cnt], 1\n\ts_cmp_lg_u32 %[cnt], 0\n\ts_cbranch_scc1 L_attn_%=\n\t"
          "s_setprio 0\n\t" CVT(78,68) CVT(79,70) "s_waitcnt lgkmcnt(0)\n\ts_nop 1\n\t"
          "v_mfma_f32_32x32x16_bf16 v[104:119], v[90:93], v[76:79], v[104:119]\n\ts_nop 15\n\t"
          : "={v[0:15]}"(oa), "={v[104:119]}"(ob), [cnt] "+s"(cnt)
          : "{v[16:31]}"(cia), "{v[120:135]}"(cib), "{v[94:97]}"(qa1), "{v[98:99]}"(qa2), "{v[136:139]}"(qb1), "{v[140:141]}"(qb2),
            "{v100}"(ka), "{v101}"(kb), "{v102}"(va)
          : "memory", "scc", "v32", "v33", "v34", "v35", "v36", "v37", "v38", "v39", "v40", "v41", "v42", "v43", "v44", "v45", "v46", "v47",
            "v48", "v49", "v50", "v51", "v52", "v53", "v54", "v55", "v56", "v57", "v58", "v59", "v60", "v61", "v62", "v63", "v64", "v65",
            "v66", "v67", "v68", "v69", "v70", "v71", "v72", "v73", "v74", "v75", "v76", "v77", "v78", "v79", "v80", "v81", "v82", "v83",
            "v84", "v85", "v86", "v87", "v88", "v89", "v90", "v91", "v92", "v93");
      LDS_AS f32x4* ex = (LDS_AS f32x4*)(ring + L_EX + wm * 3072) + lane;
#pragma unroll
      for (int i = 0; i < 3; i++) ex[i * 64] = f32x4{ob[4 * i], ob[4 * i + 1], ob[4 * i + 2], ob[4 * i + 3]};
      f32x4 cs = {0.f, 0.f, 0.f, 0.f};
#pragma unroll
      for (int i = 0; i < 3; i++) cs += f32x4{oa[4 * i], oa[4 * i + 1], oa[4 * i + 2], oa[4 * i + 3]} + f32x4{ob[4 * i], ob[4 * i + 1], ob[4 * i + 2], ob[4 * i + 3]};
      if (!(fabsf(cs[0] + cs[1] + cs[2] + cs[3]) < INFINITY) || (h && !(oa[8] + ob[8] < 0x1p60f))) misc[1] = 1u;
      lds_bar();
      const unsigned vote = *(volatile LDS_AS unsigned*)(misc + 1);
      LDS_AS f32x4* ey = (LDS_AS f32x4*)(ring + L_EX + (wm ^ 1) * 3072) + lane;
#pragma unroll
      for (int i = 0; i < 3; i++) r[i] = ey[i * 64] + f32x4{oa[4 * i], oa[4 * i + 1], oa[4 * i + 2], oa[4 * i + 3]};
      if (attempt || !vote) break;
      lds_bar();
      ma = -INFINITY; mb = -INFINITY;
      for (int t = 0; t < 64; t++) {
        const unsigned kb_ = KV + (head * 16 + (t >> 2)) * KVC + (t & 3) * 1280;
        const u32x4 k1 = gl128<16>(rw, kb_ + lane * 16); const u32x2 k2 = gl64<16>(rw, kb_ + 1024 + (lane & 31) * 8);
        ma = fmaxf(ma, max16(scores(k1, k2, qa1, qa2))); mb = fmaxf(mb, max16(scores(k1, k2, qb1, qb2)));
      }
      raw_bar();
    }
  }
  {
    const unsigned lb = __float_as_uint(r[2][0]);
    const float il = __builtin_amdgcn_rcpf(__uint_as_float(__builtin_amdgcn_permlane32_swap(lb, lb, false, false)[1]));
    const unsigned sl8 = os & 255;
    st128<16>(rw, os + sl8 + h * 512, clean(pk8(r[0] * il, r[1] * il)));
    const u32x4 t = clean(pk8(r[2] * il, r[2] * il));
    st64<16>(rw, h ? WS_END : os + 1024, u32x2{t[0], t[1]});
  }
  const lptr wa = lds + L_A + wn * 35 * 1024 + lane * 16, wb = ring + lane * 16;
  constexpr int NPRE = 15;
  u32x4 bf[50];
  f32x4 acc[5] = {};
#pragma unroll
  for (int j = 0; j < NPRE; j++) bf[j] = ld128(wa + j * 1024);
#pragma unroll
  for (int j = 0; j < 5; j++) asm volatile("" : "+v"(acc[j]));
  if (FINAL) dc = __uint_as_float(gl32<16>(rw, o3[3]));
#pragma unroll 1
  for (int spins = 0; spins < (1 << 18); spins++) {
#pragma unroll
    for (int i = 0; i < 3; i++) x4v[i] = gl64<16>(rw, o3[i]);
    if (!FINAL || !__builtin_amdgcn_ballot_w64(umax4(max(x4v[0][0], x4v[0][1]), u32x4{x4v[1][0], x4v[1][1], x4v[2][0], x4v[2][1]}) == EMPTY)) break;
    nap<8>();
  }
  drain();
  {
    u32x4 af[10];
    auto ldf = [&](int i) {
      if (i < 8) af[i] = __builtin_amdgcn_raw_buffer_load_b128(rw, o1, i * 2 * 1280, 16);
      else { const u32x2 a = __builtin_amdgcn_raw_buffer_load_b64(rw, o2, (i - 8) * 8 * 1280, 16), b = __builtin_amdgcn_raw_buffer_load_b64(rw, o2, ((i - 8) * 8 + 1) * 1280, 16);
             af[i] = u32x4{a[0], a[1], b[0], b[1]}; }
    };
#pragma unroll
    for (int i = 0; i < 10; i++) ldf(i);
#pragma unroll 1
    for (int spins = 0; spins < (1 << 18); spins++) {
      unsigned m = 0;
#pragma unroll
      for (int i = 0; i < 10; i++) m = umax4(m, af[i]);
      if (!__builtin_amdgcn_ballot_w64(m == EMPTY)) break;
      nap<RSLEEP>();
#pragma unroll
      for (int i = 0; i < 10; i++) if (__builtin_amdgcn_ballot_w64(umax4(0, af[i]) == EMPTY)) ldf(i);
    }
#pragma unroll
    for (int i = 0; i < 10; i++) asm volatile("" : "+v"(af[i]));
    asm volatile("s_nop 1" : "+v"(acc[0]), "+v"(acc[1]), "+v"(acc[2]), "+v"(acc[3]), "+v"(acc[4]));
    SB();
#pragma unroll
    for (int i = 0; i < 10; i++) {
#pragma unroll
      for (int a = 0; a < 5; a++) {
        if (FINAL) mfma16v(acc[a], bf[5 * i + a], af[i]); else mfma16v(acc[a], af[i], bf[5 * i + a]);
        const int j = 5 * i + a + NPRE; if (j < 50) bf[j] = ld128(j < 35 ? wa + j * 1024 : wb + (j - 35) * 1024);
        SB();
      }
    }
  }
  asm volatile("s_nop 7\n\ts_nop 1" : "+v"(acc[0]), "+v"(acc[1]), "+v"(acc[2]), "+v"(acc[3]), "+v"(acc[4]) :: "memory");
  if (!FINAL) { col_load(kin, rw, true, true); SB(); }
  {
    LDS_AS f32x4* ex = (LDS_AS f32x4*)(ring + L_EX + 12288 + wm * 3072) + lane;
    LDS_AS f32x4* ey = (LDS_AS f32x4*)(lds + (wn ^ 1) * HR + L_EX + 12288 + wm * 3072) + lane + 64 * wn;
    ex[0] = acc[2]; ex[64] = acc[3]; ex[128] = acc[4];
    dc *= 0.039528470752104741f;
#pragma unroll
    for (int i = 0; i < 3; i++) { acc[i] += f32x4{bflo(x4v[i][0]), bfhi(x4v[i][0]), bflo(x4v[i][1]), bfhi(x4v[i][1])}; if (FINAL) acc[i] += dc; }
    lds_bar();
#pragma unroll
    for (int i = 0; i < 3; i++) acc[i] += ey[64 * i];
  }
  if (FINAL) {
    const rsrc_t ro = mk_rsrc(out, 2048 * 640 * 2);
    const unsigned op = (rowof(pblk + trow(wm & 1, r16)) * 640 + 80 * ct + 48 * wn + 4 * g) * 2;
#pragma unroll
    for (int i = 0; i < 3; i++)
      if (i < 2 || wn == 0) st64<0>(ro, op + 32 * i, pk4(acc[i]));
  } else {
    f32x4 sq = {0.f, 0.f, 0.f, 0.f};
#pragma unroll
    for (int i = 0; i < 3; i++)
      if (i < 2 || wn == 0) {
        const int j = 80 * ct + 48 * wn + 16 * i + r16;
        st128<16>(rw, r3t + j * 64 + g * 16, clean(asu(acc[i])));
        if (j != 1) sq += acc[i] * acc[i];
      }
#pragma unroll
    for (int e = 0; e < 4; e++) sq[e] = row16_sum(sq[e]);
    LDS_AS float* sl = (LDS_AS float*)(lds + L_MISC);
    if (r16 == 0) *(LDS_AS f32x4*)(sl + wn * 64 + 32 * (wm & 1) + 16 * (wm >> 1) + 4 * g) = sq;
    lds_bar();
    if (w == 0) {
      LDS_AS f32x4* s4 = (LDS_AS f32x4*)sl + 2 * (lane & 7);
      st128<16>(rw, WS_SSQ + (lane >> 3) * 32768 + (ct * 2048 + 1024 * ((lane >> 2) & 1) + 64 * qt + 32 * hf + 8 * (lane & 3)) * 2, clean(pk8(s4[0] + s4[16], s4[1] + s4[17])));
    }
  }
}

__global__ void __launch_bounds__(768, 3) k_main(const u16* x, const u16* wr, const u16* wi, const u16* wqkv, const u16* wo, char* ws, u16* out) {
  if (wave_id() >= 8) {
    extern __shared__ __attribute__((aligned(16))) char smem[];
    const unsigned long long t0 = __builtin_amdgcn_s_memrealtime();
    const int job = blockIdx.x;
    if (job < 144) raw_bar();
    const rsrc_t rw = mk_rsrc(ws, WS_END);
    __builtin_amdgcn_s_setprio(3);
    if (threadIdx.x < 528) ((LDS_AS unsigned*)((lptr)smem + L_MISC + 1024))[threadIdx.x - 512] = 0u;
    u32x2 tag = gl64<16>(rw, WS_EP);
    tag[0] += 1u;
    if (job < 228) raw_bar();
    if (job < 64) raw_bar();
    asm volatile("s_waitcnt vmcnt(0) lgkmcnt(0)\n\ts_barrier" : "+v"(tag) :: "memory");
    if (wave_id() == 8) {
      if (threadIdx.x < 520) st64<16>(rw, WS_F0 + (threadIdx.x - 512) * 2048 + job * 8, tag);
      if (job >= 248) {
        while (__builtin_amdgcn_s_memrealtime() - t0 < TPOLL) nap<2>();
        poll_tag(rw, tag, 0xFE0FFFF00000FFFFull);
        if (threadIdx.x < 544) st64<16>(rw, WS_GO + ((threadIdx.x - 512) * 8 + (job & 7)) * 64, tag);
      }
#pragma unroll 1
      for (int spins = 0; spins < (1 << 20); spins++) {
        const u32x2 v = gl64<16>(rw, WS_GO + job * 64);
        if (__builtin_amdgcn_readfirstlane((v[0] ^ tag[0]) | (v[1] ^ tag[1])) == 0) break;
        nap<GSLEEP>();
      }
    }
    if (wave_id() > 8) {
    while (__builtin_amdgcn_s_memrealtime() - t0 < TFILL) nap<2>();
    const int i0 = blockIdx.x * 192 + threadIdx.x - 576;
#pragma unroll
    for (int it = 0; it < 4; it++) {
      const int k = it * 49152 + i0, b = k / 1408;
      st128<16>(rw, k < 128 * 1408 ? WS_KV + b * 4 * KVC + (k - b * 1408) * 16 : WS_END, u32x4{EMPTY, EMPTY, EMPTY, EMPTY});
    }
    }
    raw_bar();
    loader<false>(ws, tag);
    loader<true>(ws, tag);
  } else {
    prep(x, wr, wi, wqkv, wo, ws);
    asm volatile("s_waitcnt vmcnt(0) lgkmcnt(0)\n\ts_barrier" ::: "memory");
    ColIn kin = {};
    compute<false>(ws, out, kin);
    compute<true>(ws, out, kin);
  }
}

extern "C" void kernel_launch(void* const* d_in, const int* in_sizes, int n_in, void* d_out, int out_size, void* d_ws, size_t ws_size,
                              hipStream_t stream) {
  const u16* x = (const u16*)d_in[0];
  const u16* wr = (const u16*)d_in[1];
  const u16* wi = (const u16*)d_in[2];
  const u16* wqkv = (const u16*)d_in[4];
  const u16* wo = (const u16*)d_in[5];
  char* ws = (char*)d_ws;
  (void)hipFuncSetAttribute((const void*)k_main, hipFuncAttributeMaxDynamicSharedMemorySize, LDS_FUSED);
  k_main<<<256, 768, LDS_FUSED, stream>>>(x, wr, wi, wqkv, wo, ws, (u16*)d_out);
}
```

```cpp
#define WHICH 9
#include <hip/hip_runtime.h>
#include <hip/hip_bf16.h>

typedef unsigned short u16;
typedef __attribute__((ext_vector_type(16))) float f32x16;
typedef __attribute__((ext_vector_type(4))) float f32x4;
typedef __attribute__((ext_vector_type(2))) float f32x2;
typedef __attribute__((ext_vector_type(8))) __bf16 bf16x8;
typedef __attribute__((ext_vector_type(2))) __bf16 bf16x2;
typedef __attribute__((ext_vector_type(4))) short s16x4;
typedef __attribute__((ext_vector_type(4))) unsigned u32x4;
typedef __attribute__((ext_vector_type(2))) unsigned u32x2;
typedef __attribute__((ext_vector_type(8))) unsigned u32x8;
#define DI __device__ __forceinline__
#define LDS_AS __attribute__((address_space(3)))
typedef LDS_AS char* lptr;

constexpr double cx_cos_small(double x) { double x2 = x * x, t = 1, s = 1; for (int i = 1; i < 12; i++) { t *= -x2 / ((2 * i - 1) * (2 * i)); s += t; } return s; }
constexpr double cx_sin_small(double x) { double x2 = x * x, t = x, s = x; for (int i = 1; i < 12; i++) { t *= -x2 / ((2 * i) * (2 * i + 1)); s += t; } return s; }
constexpr double cx_cos640(int m) {
  m %= 640; if (m > 320) m = 640 - m; double sg = 1; if (m > 160) { m = 320 - m; sg = -1; }
  const double w = 6.283185307179586476925 / 640; return sg * (m <= 80 ? cx_cos_small(w * m) : cx_sin_small(w * (160 - m)));
}
constexpr u16 cx_bf16(double d) { float f = (float)d; unsigned u = __builtin_bit_cast(unsigned, f); u += 0x7FFFu + ((u >> 16) & 1u); return (u16)(u >> 16); }
struct Cos640 { double v[640]; };
constexpr Cos640 mk_cos() { Cos640 c{}; for (int m = 0; m < 640; m++) c.v[m] = cx_cos640(m); return c; }
constexpr Cos640 COS = mk_cos();
__host__ __device__ constexpr int kf_of(int G, int i) { return G ? G + 40 * i : (i == 0 ? 320 : i < 8 ? 40 * i : 40 * i - 300); }
__host__ __device__ constexpr int freq_of(int G, int i) { return kf_of(G, i) > 320 ? 640 - kf_of(G, i) : kf_of(G, i); }
constexpr double SA = 1.91318, SA0 = 1.9140625, S64 = 1.86954, S32 = 1.6245;
constexpr double a1val(int out, int a) {
  if (out >= 40 || a >= 40) return 0;
  const int G = out >> 1, part = out & 1;
  if (G == 0) return (part && (a & 1)) ? -SA0 : SA0;
  return SA * COS.v[(16 * a * G + 160 * part) % 640];
}
constexpr double m2val(int G, int o, int k) {
  const int i = o >> 1, po = o & 1, part = k >> 4, b = k & 15, kf = kf_of(G, i);
  const double c = COS.v[(b * kf) % 640], sn = COS.v[(b * kf + 480) % 640];
  if (G == 0) {
    if (part != (i >= 8)) return 0;
    if (i == 0) return 0.039528470752104741 / SA0 * (po ? 1.0 : c);
    return 0.055901699437494742 / SA0 * (po ? -sn : c);
  }
  const double v = po ? (part ? c : -sn) : (part ? sn : c);
  return 0.055901699437494742 / SA * ((po && kf > 320) ? -v : v);
}
struct DTab { u16 a1[3][64][8]; u16 a1b[3][64][4]; u16 pad[256]; u16 m2[20][2][64][8]; };
constexpr DTab mk_dtab() {
  DTab t{};
  for (int l = 0; l < 64; l++) {
    const int r = l & 15, g = l >> 4;
    for (int T = 0; T < 3; T++) {
      for (int e = 0; e < 8; e++) t.a1[T][l][e] = cx_bf16(a1val(16 * T + r, 16 * (e >> 2) + 4 * g + (e & 3)));
      for (int e = 0; e < 4; e++) t.a1b[T][l][e] = cx_bf16(a1val(16 * T + r, 32 + 4 * g + e));
    }
    for (int G = 0; G < 20; G++) for (int T = 0; T < 2; T++) for (int e = 0; e < 8; e++)
      t.m2[G][T][l][e] = cx_bf16(m2val(G, 8 * (r >> 2) + 4 * T + (r & 3), 8 * g + e));
  }
  return t;
}
static __device__ const __attribute__((aligned(1024))) DTab g_dt = mk_dtab();

struct ITab { u16 m2[20][2][64][8]; u16 a1[3][64][8]; u16 a1b[3][64][4]; u16 pad[1792]; };
constexpr ITab mk_itab() {
  ITab t{};
  for (int l = 0; l < 64; l++) {
    const int r = l & 15, g = l >> 4;
    for (int T = 0; T < 3; T++) {
      for (int e = 0; e < 8; e++) t.a1[T][l][e] = cx_bf16(a1val(16 * (e >> 2) + 4 * g + (e & 3), 16 * T + r));
      for (int e = 0; e < 4; e++) t.a1b[T][l][e] = cx_bf16(a1val(32 + 4 * g + e, 16 * T + r));
    }
    for (int G = 0; G < 20; G++) for (int P = 0; P < 2; P++) for (int e = 0; e < 8; e++) t.m2[G][P][l][e] = cx_bf16(m2val(G, 8 * g + e, 16 * P + r));
  }
  return t;
}
static __device__ const __attribute__((aligned(1024))) ITab g_it = mk_itab();

struct CTab { u16 v[10 * 512]; };
constexpr CTab mk_ctab() {
  CTab c{};
  for (int f = 0; f < 10; f++) for (int l = 0; l < 64; l++) for (int e = 0; e < 8; e++) {
    const int r = l & 15, g = l >> 4;
    const int m = f < 8 ? (8 * g + e) * (16 * (f >> 1) + r) : 2 * r * (16 * (e >> 2) + 4 * g + (e & 3));
    c.v[(f * 64 + l) * 8 + e] = cx_bf16((f < 8 ? S64 : S32) * COS.v[10 * ((m + (f & 1) * 48) & 63)]);
  }
  return c;
}
static __device__ const __attribute__((aligned(1024))) CTab g_ct = mk_ctab();
constexpr float RSK = (float)(2048.0 * S64 * S64 * S32 * S32 * 2048.0 * S64 * S64 * S32 * S32 / 640.0);

constexpr int RP = 2048 + 64, RBP = 2048 + 128;
constexpr unsigned RBSZ = 640 * RBP * 2;
constexpr unsigned WS_FL = 0;
constexpr unsigned WS_AUX = WS_FL + 3 * 32768;
constexpr unsigned WS_F0 = WS_AUX + 2048;
constexpr unsigned WS_GO = WS_F0 + 8 * 2048;
constexpr unsigned WS_EP = WS_GO + 256 * 64;
constexpr unsigned WS_RS1 = WS_EP + 256;
constexpr unsigned WS_RB0 = WS_RS1 + 8192;
constexpr unsigned WS_RB1 = WS_RB0 + RBSZ;
constexpr unsigned WS_WF = WS_RB1 + RBSZ;
constexpr unsigned WS_WQ = WS_WF + 320 * 8192;
constexpr unsigned WS_WOF = WS_WQ + 16 * 8 * 20 * 1024;
constexpr unsigned WS_WOP = WS_WOF + 40 * 20 * 1024;
constexpr int KVC = 11264;
constexpr unsigned WS_KV = WS_WOP + 40 * 20 * 1024;
constexpr unsigned WS_O = WS_KV + 32 * 16 * KVC;
constexpr unsigned LAYER2 = 32 * 16 * KVC + 32 * 64 * 1280;
constexpr unsigned WS_X4 = WS_KV + 2 * LAYER2;
constexpr unsigned WS_R3 = WS_X4 + 2048 * 640 * 2;
constexpr unsigned WS_RB2 = WS_R3 + 640 * RP * 4;
constexpr unsigned WS_SSQ = WS_RB2 + RBSZ;
constexpr unsigned EMPTY = 0xFFFFFFFFu;
constexpr unsigned WS_END = WS_SSQ + 8 * 2048 * 16;
#define FILLDELAY 10
#define OUTAUX 0
#define XDELAY 20
#define SW1 50
#define SW2 46
#define XNL 0
#define K1DELAY 40
#define YDELAY 0
#define ODELAY 0
#define WMODE 0
#define YIDLE 12
#define PROBE 1
#define TPOLL 250
#define TSLEEP 1
#define GSLEEP 2
#define LDELAY 30
#define TFILL 400
#define PFDELAY 40
#define YSLEEP 4
#define XSLEEP 1
#define RSLEEP 2
#define AUXB 16

#define STW 0
struct Stamp { unsigned long long r0, t; };
#define STAMP(k) Stamp st_; st_.r0 = __builtin_amdgcn_s_memrealtime(); st_.t = st_.r0
#define MARK(i) do { if ((i) == WHICH) st_.t = __builtin_amdgcn_s_memrealtime(); } while (0)
#define SARG , Stamp& st_
#define SPASS , st_
#define SB() __builtin_amdgcn_sched_barrier(0)
typedef __amdgpu_buffer_rsrc_t rsrc_t;
DI rsrc_t mk_rsrc(const void* p, unsigned bytes) { return __builtin_amdgcn_make_buffer_rsrc((void*)p, 0, bytes, 0x00020000); }
template <int AUX> DI void st128(rsrc_t r, unsigned off, u32x4 v) { __builtin_amdgcn_raw_buffer_store_b128(v, r, off, 0, AUX); }
template <int AUX> DI void st64(rsrc_t r, unsigned off, u32x2 v) { __builtin_amdgcn_raw_buffer_store_b64(v, r, off, 0, AUX); }
template <int AUX> DI void st32(rsrc_t r, unsigned off, unsigned v) { __builtin_amdgcn_raw_buffer_store_b32(v, r, off, 0, AUX); }
template <int AUX> DI u32x4 gl128(rsrc_t r, unsigned off) { return __builtin_amdgcn_raw_buffer_load_b128(r, off, 0, AUX); }
template <int AUX> DI u32x2 gl64(rsrc_t r, unsigned off) { return __builtin_amdgcn_raw_buffer_load_b64(r, off, 0, AUX); }
template <int AUX> DI unsigned gl32(rsrc_t r, unsigned off) { return __builtin_amdgcn_raw_buffer_load_b32(r, off, 0, AUX); }
DI u32x4 asu(f32x4 v) { return __builtin_bit_cast(u32x4, v); }
DI f32x4 asf(u32x4 v) { return __builtin_bit_cast(f32x4, v); }
DI unsigned pk2(float a, float b) { f32x2 v = {a, b}; return __builtin_bit_cast(unsigned, __builtin_convertvector(v, bf16x2)); }
DI u32x2 pk4(f32x4 a) { return u32x2{pk2(a[0], a[1]), pk2(a[2], a[3])}; }
DI u32x4 pk8(f32x4 a, f32x4 b) { return u32x4{pk2(a[0], a[1]), pk2(a[2], a[3]), pk2(b[0], b[1]), pk2(b[2], b[3])}; }
DI u32x4 clean(u32x4 v) { return __builtin_elementwise_min(v, u32x4{EMPTY - 1, EMPTY - 1, EMPTY - 1, EMPTY - 1}); }
DI unsigned umax4(unsigned m, u32x4 v) { return max(max(max(m, v[0]), v[1]), max(v[2], v[3])); }
DI float bflo(unsigned u) { return __uint_as_float(u << 16); }
DI float bfhi(unsigned u) { return __uint_as_float(u & 0xFFFF0000u); }
DI int rowof(int p) { return 32 * (((p >> 5) & 0x3C) + (p & 3)) + ((p << 2) & 16) + ((p >> 3) & 15); }
DI int rpos(int p) { return ((p << 8) & 1024) + ((p >> 3) << 2) + (p & 3); }
DI int trow(int T, int r) { return 8 * (r >> 2) + 4 * T + (r & 3); }
template <int CTRL> DI float dpp_add(float v) { return v + __int_as_float(__builtin_amdgcn_update_dpp(0, __float_as_int(v), CTRL, 0xF, 0xF, true)); }
DI float row16_sum(float v) { v = dpp_add<0x128>(v); v = dpp_add<0x124>(v); v = dpp_add<0x122>(v); return dpp_add<0x121>(v); }
DI f32x4 mfma16(u32x4 a, u32x4 b, f32x4 c) {
  return __builtin_amdgcn_mfma_f32_16x16x32_bf16(__builtin_bit_cast(bf16x8, a), __builtin_bit_cast(bf16x8, b), c, 0, 0, 0);
}
DI void mfma16v(f32x4& c, u32x4 a, u32x4 b) { asm volatile("v_mfma_f32_16x16x32_bf16 %0, %1, %2, %0" : "+v"(c) : "v"(a), "v"(b)); }
DI u32x2 ldtr(lptr p) { return __builtin_bit_cast(u32x2, __builtin_amdgcn_ds_read_tr16_b64_v4i16((LDS_AS s16x4*)p)); }
DI u32x4 ld128(lptr p) { return *(LDS_AS u32x4*)p; }
DI u32x2 ld64(lptr p) { return *(LDS_AS u32x2*)p; }
#define GL(v, s, o, x) "global_load_lds_dwordx4 " v ", " s " offset:" #o x "\n\t"
#define M0SET "s_mov_b32 %[sv], m0\n\ts_mov_b32 m0, %[l]\n\ts_nop 2\n\t"
#define M0ADD(x) "s_add_u32 m0, m0, " x "\n\ts_nop 0\n\t"
#define M0RST "s_mov_b32 m0, %[sv]"
DI void dma2(unsigned v, const char* sb, unsigned l) { unsigned sv;
  asm volatile(M0SET GL("%[v]", "%[s]", 0, " sc1") GL("%[v]", "%[s]", 1024, " sc1") M0RST : [sv] "=&s"(sv) : [v] "v"(v), [s] "s"(sb), [l] "s"(l) : "memory"); }
DI void dma4(unsigned v, const char* sb, unsigned l) { unsigned sv;
  asm volatile(M0SET GL("%[v]", "%[s]", 0, "") GL("%[v]", "%[s]", 1024, "") GL("%[v]", "%[s]", 2048, "") GL("%[v]", "%[s]", 3072, "") M0RST
               : [sv] "=&s"(sv) : [v] "v"(v), [s] "s"(sb), [l] "s"(l) : "memory"); }
DI void dma6_sc1(unsigned v, const char* sb, unsigned l) { unsigned sv;
  asm volatile(M0SET GL("%[v]", "%[s]", 0, " sc1") GL("%[v]", "%[s]", 1024, " sc1") GL("%[v]", "%[s]", 2048, " sc1") M0ADD("3072")
               GL("%[w]", "%[s]", 0, " sc1") GL("%[w]", "%[s]", 1024, " sc1") GL("%[w]", "%[s]", 2048, " sc1") M0RST
               : [sv] "=&s"(sv) : [v] "v"(v), [w] "v"(v + 3072), [s] "s"(sb), [l] "s"(l) : "memory", "scc"); }
template <int N> DI void vm_bar() { asm volatile("s_waitcnt vmcnt(%0)\n\ts_barrier" ::"i"(N) : "memory"); }
DI void lds_bar() { asm volatile("s_waitcnt lgkmcnt(0)\n\ts_barrier" ::: "memory"); }
DI void raw_bar() { asm volatile("s_barrier" ::: "memory"); }
template <int N> DI void nap() { __builtin_amdgcn_s_sleep(N); asm volatile("" ::: "memory"); }
DI void drain() { asm volatile("s_waitcnt vmcnt(0)" ::: "memory"); }
#define CODE_ADDR(sym) ({ unsigned lo_, hi_; \
    asm volatile("s_getpc_b64 vcc\n\ts_add_u32 %0, vcc_lo, " sym "@rel32@lo+4\n\ts_addc_u32 %1, vcc_hi, " sym "@rel32@hi+12" : "=s"(lo_), "=s"(hi_) :: "vcc", "scc"); \
    (const unsigned*)(((unsigned long long)hi_ << 32) | lo_); })
#define SYM_MAIN "_Z6k_mainPKtS0_S0_S0_S0_PcPt"
constexpr int CODE_LINES = 360;
DI int wave_id() { return __builtin_amdgcn_readfirstlane(threadIdx.x >> 6); }
DI unsigned lds_addr(lptr p) { return (unsigned)(size_t)p; }

DI int kappa_to_i(int kap) {
  int ks = kap >> 5, g = (kap >> 3) & 3, e = kap & 7;
  if (ks < 16) return (2 * ks + (g >> 1)) * 20 + 8 * (e >> 2) + 4 * (g & 1) + (e & 3);
  return (8 * (ks - 16) + 2 * g + (e >> 2)) * 20 + 16 + (e & 3);
}

DI unsigned wo_frag(int nt, int ks) {
  const int ct = nt / 5, c5 = nt - 5 * ct, kh = ks < 16 ? ks >> 3 : (ks - 16) >> 1, i = ks < 16 ? ks & 7 : 8 + (ks & 1);
  const int a = kh ? (c5 >= 3 ? c5 - 3 : c5 + 2) : c5;
  return ((ct * 2 + kh) * 50 + 5 * i + a) * 1024;
}

constexpr int SLOT32 = 32768;
DI void dma_a(const unsigned* vm, const char* mb, unsigned l) { unsigned sv;
  asm volatile(M0SET GL("%[a0]", "%[m]", 0, " sc1") GL("%[a1]", "%[m]", 1024, " sc1") GL("%[a2]", "%[m]", 2048, " sc1") GL("%[a3]", "%[m]", 3072, " sc1") M0RST
               : [sv] "=&s"(sv) : [a0] "v"(vm[0]), [a1] "v"(vm[1]), [a2] "v"(vm[2]), [a3] "v"(vm[3]), [m] "s"(mb), [l] "s"(l) : "memory"); }
DI void dma_s(unsigned v0, unsigned v1, const char* sb, unsigned l) { unsigned sv;
  asm volatile(M0SET GL("%[v0]", "%[s]", 0, " sc1") GL("%[v0]", "%[s]", 1024, " sc1") GL("%[v1]", "%[s]", 2048, " sc1") GL("%[v1]", "%[s]", 3072, " sc1") M0RST
               : [sv] "=&s"(sv) : [v0] "v"(v0), [v1] "v"(v1), [s] "s"(sb), [l] "s"(l) : "memory"); }
DI void dma1_sc1(unsigned v, const char* sb, unsigned l) { unsigned sv;
  asm volatile(M0SET GL("%[v]", "%[s]", 0, " sc1") M0RST : [sv] "=&s"(sv) : [v] "v"(v), [s] "s"(sb), [l] "s"(l) : "memory"); }
DI void dma1(unsigned v, const char* sb, unsigned l) { unsigned sv;
  asm volatile(M0SET GL("%[v]", "%[s]", 0, "") M0RST : [sv] "=&s"(sv) : [v] "v"(v), [s] "s"(sb), [l] "s"(l) : "memory"); }

constexpr int L_I2 = 40 * 1056, LDS_PREP = L_I2 + 20 * 2064 + 1024;
DI void prep(const u16* x, const u16* wr, const u16* wi, const u16* wqkv, const u16* wo, char* ws) {
  extern __shared__ __attribute__((aligned(16))) char smem[];
  const lptr lds = (lptr)smem;
  const int tid = threadIdx.x, lane = tid & 63, w = wave_id(), g = lane >> 4, r16 = lane & 15;
  const int job = blockIdx.x;
  if (__builtin_expect(job < 144, 1)) {
    const bool cols = job >= 64 && job < 124;
    const int cj = job - 64, cb = cj < 48 ? (cj & 48) + 2 * (cj & 7) + ((cj >> 3) & 1) : cj;
    u32x4 a1[3], m2[3][2]; u32x2 a1b[3];
    if (w < 5) dma1(tid * 16, (const char*)&g_dt, lds_addr(lds) + LDS_PREP + w * 1024);
    if (cols) {
#pragma unroll
      for (int i = 0; i < 5; i++) dma1((16 * (5 * w + i) + (lane >> 2)) * 3840 + cb * 64 + (lane & 3) * 16, (const char*)wqkv, lds_addr(lds) + (5 * w + i) * 1056);
    } else {
      const char* src = (const char*)(job < 64 ? x : wo);
      const int sl = 8 * (w & 3) + 4 * (w >> 2);
      src += (job < 64 ? rowof(32 * job + sl) : kappa_to_i(32 * (job - 124) + sl)) * 1280;
      const int st = (job < 64 ? 31 : 0) * 1280;
#pragma unroll
      for (int i = 0; i < 5; i++) dma1(1024 * i + lane * 16 + ((i == 4 || lane < 16 * i) ? i - 1 : i) * st, src, lds_addr(lds) + (5 * w + i) * 1024);
    }
    unsigned so[3][2]; float sc[2] = {1.f, 1.f};
    if (cols) {
#pragma unroll
      for (int m = 0; m < 2; m++) {
        const int c = 32 * cb + 16 * m + r16;
        const int which = c / 640, rem = c - which * 640, head = rem / 20, d = rem - head * 20, grp = d >> 2;
        const int T = grp < 4 ? (which == 2 ? 2 : 0) + (grp & 1) : (which == 1 ? 3 : 2), gs = grp < 4 ? (which == 1 ? 2 : 0) + (grp >> 1) : (which == 2 ? 3 : 2);
        if (which == 0) sc[m] = 0.22360679774997896f * 1.4426950408889634f;
#pragma unroll
        for (int i = 0; i < 3; i++) so[i][m] = WS_WQ + ((head * 4 + T) * 20 + w + 8 * i) * 1024 + (g * 16 + 4 * gs + (d & 3)) * 16;
      }
    } else if (job < 64) {
#pragma unroll
      for (int i = 0; i < 3; i++)
#pragma unroll
        for (int T = 0; T < 2; T++) so[i][T] = WS_RB0 + (job * 641 + (job & 1) + 32 * (w + 8 * i) + trow(T, r16)) * 64 + g * 16;
    } else {
#pragma unroll
      for (int i = 0; i < 3; i++) {
        const unsigned f = wo_frag(2 * (w + 8 * i) + (r16 >> 3), job - 124);
#pragma unroll
        for (int T = 0; T < 2; T++) so[i][T] = WS_WOF + f + (g * 16 + (trow(T, r16) & 15)) * 16;
      }
    }
    if (w >= 4) { so[2][0] = WS_END; so[2][1] = WS_END; }
    asm volatile("" : "+v"(so[0][0]), "+v"(so[0][1]), "+v"(so[1][0]), "+v"(so[1][1]), "+v"(so[2][0]), "+v"(so[2][1]), "+v"(sc[0]), "+v"(sc[1]));
    const f32x4 z4 = {0.f, 0.f, 0.f, 0.f};
    const lptr i2 = lds + L_I2;
    drain();
#pragma unroll
    for (int i = 0; i < 3; i++)
#pragma unroll
      for (int T = 0; T < 2; T++)
        asm volatile("global_load_dwordx4 %0, %1, %2" : "=v"(m2[i][T]) : "v"(((((i < 2 || w < 4) ? w + 8 * i : w) * 2 + T) * 64 + lane) * 16), "s"(&g_dt.m2[0][0][0][0]) : "memory");
    raw_bar();
#pragma unroll
    for (int T = 0; T < 3; T++) { a1[T] = ld128(lds + LDS_PREP + T * 1024 + lane * 16); a1b[T] = ld64(lds + LDS_PREP + 3072 + T * 512 + lane * 8); }
    f32x4 t1[4][3];
    if (cols) {
      const lptr src = lds + (4 * g + (r16 >> 2)) * 1056 + (w >> 1) * 256 + (w & 1) * 32 + (lane & 3) * 8, srb = src + (32 - 8 * (g >> 1)) * 1056;
#pragma unroll
      for (int i = 0; i < 4; i++) {
        const u32x2 f0 = ldtr(src + i * 64), f1 = ldtr(src + 16 * 1056 + i * 64), f2 = ldtr(srb + i * 64);
#pragma unroll
        for (int T = 0; T < 3; T++) t1[i][T] = mfma16(u32x4{a1b[T][0], a1b[T][1], 0u, 0u}, u32x4{f2[0], f2[1], 0u, 0u}, mfma16(a1[T], u32x4{f0[0], f0[1], f1[0], f1[1]}, z4));
      }
      const int xo = ((w >> 1) * 8) ^ (((r16 >> 1) & 3) * 16);
      const lptr dst = i2 + 2 * g * 2064 + (16 * (w & 1) + r16) * 64;
#pragma unroll
      for (int T = 0; T < 3; T++)
#pragma unroll
        for (int e = 0; e < 4; e++)
          if (T < 2 || g < 2) *(LDS_AS u32x2*)(dst + (8 * T + (e >> 1)) * 2064 + (xo ^ ((e & 1) * 32))) = u32x2{pk2(t1[0][T][e], t1[1][T][e]), pk2(t1[2][T][e], t1[3][T][e])};
    } else {
      const lptr src = lds + w * 5120 + lane * 8, srb = lds + w * 5120 + 1024 + (lane & 31) * 8;
#pragma unroll
      for (int i = 0; i < 4; i++) {
        const u32x2 f0 = ldtr(src + i * 1280), f1 = ldtr(src + i * 1280 + 512), f2 = ldtr(srb + i * 1280);
#pragma unroll
        for (int T = 0; T < 3; T++) t1[i][T] = mfma16(u32x4{f2[0], f2[1], 0u, 0u}, u32x4{a1b[T][0], a1b[T][1], 0u, 0u}, mfma16(u32x4{f0[0], f0[1], f1[0], f1[1]}, a1[T], z4));
      }
      const int xo = ((r16 & 1) * 32 + g * 8) ^ ((w & 1) * 32);
      const lptr dst = i2 + (r16 >> 1) * 2064 + w * 256;
#pragma unroll
      for (int i = 0; i < 4; i++)
#pragma unroll
        for (int T = 0; T < 3; T++)
          if (T < 2 || r16 < 8) *(LDS_AS u32x2*)(dst + T * 8 * 2064 + i * 64 + (xo ^ ((i >> 1) * 16))) = pk4(t1[i][T]);
    }
    lds_bar();
    const rsrc_t rw = mk_rsrc(ws, WS_END);
    asm volatile("s_waitcnt vmcnt(0)" : "+v"(m2[0][0]), "+v"(m2[0][1]), "+v"(m2[1][0]), "+v"(m2[1][1]), "+v"(m2[2][0]), "+v"(m2[2][1]));
    f32x4 sq[2] = {z4, z4};
    u32x4 f[3][2];
#pragma unroll
    for (int i = 0; i < 3; i++) {
      const lptr fp = i2 + ((i < 2 || w < 4) ? w + 8 * i : 0) * 2064 + r16 * 64 + ((g ^ ((r16 >> 1) & 3)) * 16);
      f[i][0] = ld128(fp); f[i][1] = ld128(fp + 1024);
    }
    if (cols) {
#pragma unroll
      for (int i = 0; i < 3; i++)
#pragma unroll
        for (int m = 0; m < 2; m++) st128<16>(rw, so[i][m], pk8(mfma16(m2[i][0], f[i][m], z4) * sc[m], mfma16(m2[i][1], f[i][m], z4) * sc[m]));
    } else {
#pragma unroll
      for (int i = 0; i < 3; i++)
#pragma unroll
        for (int T = 0; T < 2; T++) {
          const f32x4 d0 = mfma16(f[i][0], m2[i][T], z4), d1 = mfma16(f[i][1], m2[i][T], z4);
          st128<16>(rw, so[i][T], pk8(d0, d1));
          const float k = ((i == 2 && w >= 4) || (i == 0 && T == 0 && tid < 64 && r16 == 1)) ? 0.f : 1.f;
          sq[0] += d0 * d0 * k; sq[1] += d1 * d1 * k;
        }
    }
    LDS_AS float* sl = (LDS_AS float*)(lds + LDS_PREP - 1024);
    if (job < 64) {
#pragma unroll
      for (int m = 0; m < 2; m++) {
#pragma unroll
        for (int e = 0; e < 4; e++) sq[m][e] = row16_sum(sq[m][e]);
        if (r16 == 0) *(LDS_AS f32x4*)(sl + w * 32 + 8 * g + 4 * m) = sq[m];
      }
    }
    if (job < 64) {
      lds_bar();
      if (tid < 32) {
        float v = 0.f;
#pragma unroll
        for (int i = 0; i < 8; i++) v += sl[i * 32 + tid];
        st32<16>(rw, WS_RS1 + rpos(32 * job + tid) * 4, __float_as_uint(__builtin_amdgcn_rsqf((v + 640.f * 1e-5f) * RSK)));
      }
    }
    return;
  }
  const int mi = job - 144;
  const rsrc_t rw = mk_rsrc(ws, WS_END);
  if (mi < 64) {
    const int m = mi >> 3, oc = mi & 7;
    const char* src = (const char*)((w >> 2) ? wi : wr) + (64 * (4 * m + (w & 3)) + 8 * oc) * 642;
    dma4(lane * 16, src, lds_addr(lds) + w * 5152);
    dma1(4096 + lane * 16, src, lds_addr(lds) + w * 5152 + 4096);
    if (lane == 0) dma1(5120, src, lds_addr(lds) + w * 5152 + 5120);
    vm_bar<0>();
    LDS_AS u16* L = (LDS_AS u16*)lds + (tid & 7) * 321;
#pragma unroll
    for (int it = 0; it < 5; it++) {
      const int c = (tid >> 3) + 64 * it, col = freq_of(c >> 4, c & 15);
      u32x4 o;
#pragma unroll
      for (int e = 0; e < 4; e++) o[e] = (unsigned)L[e * 2576 + col] | ((unsigned)L[(4 + e) * 2576 + col] << 16);
      st128<16>(rw, WS_WF + c * 8192 + (m * 64 + 8 * oc + (tid & 7)) * 16, o);
    }
#pragma unroll
    for (int it = 0; it < 3; it++) st128<16>(rw, WS_RB1 + ((it * 64 + mi) * 512 + tid) * 16, u32x4{EMPTY, EMPTY, EMPTY, EMPTY});
  } else if (mi < 84) {
    unsigned pf0;
    {
      const unsigned* cm = CODE_ADDR(SYM_MAIN);
      const unsigned* p = mi >= 80 ? (const unsigned*)&g_it + min(tid, 359) * 32 : (mi & 8) ? (const unsigned*)&g_dt.m2[0][0][0][0] + min(tid, 319) * 32 : tid < 80 ? (const unsigned*)&g_ct + tid * 32 : cm + min(tid - 80, CODE_LINES - 1) * 32;
      asm volatile("global_load_dword %0, %1, off" : "=v"(pf0) : "v"(p) : "memory");
    }
    const char* src = (const char*)wo + kappa_to_i(32 * (mi - 64) + 8 * (w & 3) + 4 * (w >> 2)) * 1280;
    dma4(lane * 16, src, lds_addr(lds) + w * 5152);
    dma1(4096 + lane * 16, src, lds_addr(lds) + w * 5152 + 4096);
    asm volatile("s_waitcnt vmcnt(0)\n\ts_barrier" : "+v"(pf0) :: "memory");
#pragma unroll
    for (int i = 0; i < 5; i++) {
      const lptr a = lds + g * 5152 + (r16 >> 2) * 1280 + (5 * w + i) * 32 + (lane & 3) * 8;
      const u32x2 lo = ldtr(a), hi = ldtr(a + 4 * 5152);
      st128<16>(rw, WS_WOP + wo_frag(5 * w + i, mi - 64) + lane * 16, u32x4{lo[0], lo[1], hi[0], hi[1]});
    }
  } else {
    const int i0 = (mi - 84) * 512 + tid;
    nap<K1DELAY>();
    if (i0 < 6272) st128<16>(rw, WS_FL + i0 * 16, u32x4{0u, 0u, 0u, 0u});
#pragma unroll
    for (int it = 0; it < 6; it++) { const int i = 98304 + i0 + it * 14336; if (i < (int)RBSZ / 16) st128<16>(rw, WS_RB1 + i * 16, u32x4{EMPTY, EMPTY, EMPTY, EMPTY}); }
  }
}

#define PINF(x) asm volatile("" : "+v"(x))
DI void wave_sync(LDS_AS unsigned* c, unsigned n) {
  asm volatile("s_waitcnt lgkmcnt(0)" ::: "memory");
  if ((threadIdx.x & 63) == 0) __hip_atomic_fetch_add(c, 1u, __ATOMIC_RELAXED, __HIP_MEMORY_SCOPE_WORKGROUP);
  while (__builtin_amdgcn_readfirstlane(*(volatile LDS_AS unsigned*)c) < n) {}
  asm volatile("" ::: "memory");
}
DI void put_scale(lptr lds, int q, int tid, const u32x2* sp) {
  f32x4 r = {640.f * 1e-5f, 640.f * 1e-5f, 640.f * 1e-5f, 640.f * 1e-5f};
#pragma unroll
  for (int i = 0; i < 8; i++) r += f32x4{bflo(sp[i][0]), bfhi(sp[i][0]), bflo(sp[i][1]), bfhi(sp[i][1])};
#pragma unroll
  for (int e = 0; e < 4; e++) r[e] = __builtin_amdgcn_rsqf(r[e] * RSK);
  ((LDS_AS f32x4*)(lds + 2 * SLOT32))[q * 256 + tid] = r;
}
struct ColIn { u32x4 c64, s64, c32, s32, wf[2]; };
DI int col_of() { return blockIdx.x < 64 ? 192 + 2 * blockIdx.x + (wave_id() >> 2) : blockIdx.x - 64; }
DI void col_load(ColIn& k, rsrc_t rw, bool tab, bool filt) {
  int lane = threadIdx.x & 63; PINF(lane);
  const int w = wave_id() & 3;
  const u32x4* ct = (const u32x4*)&g_ct + lane;
  if (tab) { k.c64 = ct[(2 * w) * 64]; k.s64 = ct[(2 * w + 1) * 64]; k.c32 = ct[8 * 64]; k.s32 = ct[9 * 64]; }
  if (filt) {
#pragma unroll
    for (int t = 0; t < 2; t++) k.wf[t] = gl128<16>(rw, WS_WF + col_of() * 8192 + ((4 * t + (lane >> 4)) * 64 + 16 * w + (lane & 15)) * 16);
  }
}
template <bool FIRST>
DI void col_phase(rsrc_t rw, lptr lds, ColIn kin SARG) {
  int lane = threadIdx.x & 63; PINF(lane);
  const int wv = wave_id(), w = wv & 3, q = wv >> 2, tid = w * 64 + lane, g = lane >> 4, r16 = lane & 15;
  const int c = col_of();
  const lptr img = lds + q * SLOT32;
  f32x4 xr[2], xi[2], rs[2]; u32x4 xb[2];
  u32x4 c64[2], s64[2], n64[2], c32[2], s32[2], n32[2];
  if (FIRST) col_load(kin, rw, true, false);
  SB();
  f32x4 t1c[2], t1s[2], t2c[2], t2s[2];
  {
    const float f1 = (float)(16 * w + r16) * (1.f / 2048.f), b1 = (float)(4 * g), f2 = (float)(16 * w + 4 * g) * (1.f / 2048.f), b2 = (float)r16;
#pragma unroll
    for (int t = 0; t < 2; t++) {
#pragma unroll
      for (int e = 0; e < 4; e++) {
        const float a1 = (b1 + (float)(16 * t + e)) * f1, a2 = (b2 + (float)(16 * t)) * (f2 + (float)e * (1.f / 2048.f));
        t1c[t][e] = __builtin_amdgcn_cosf(a1); t1s[t][e] = __builtin_amdgcn_sinf(-a1);
        t2c[t][e] = __builtin_amdgcn_cosf(a2); t2s[t][e] = __builtin_amdgcn_sinf(a2);
      }
      PINF(t1c[t]); PINF(t1s[t]); PINF(t2c[t]); PINF(t2s[t]);
    }
  }
  int wo_ = r16 * 128 + (((2 * w + (g >> 1)) ^ (r16 >> 1)) * 16) + (g & 1) * 8, ro_ = r16 * 128 + ((g ^ (r16 >> 1)) * 16), ro2 = ro_ ^ 64;
  PINF(wo_); PINF(ro_); PINF(ro2);
  {
    const unsigned par = (lane & 1) ? 0x80008000u : 0u;
    c64[0] = kin.c64; s64[0] = kin.s64; c32[0] = kin.c32; s32[0] = kin.s32;
    c64[1] = c64[0] ^ par; s64[1] = s64[0] ^ par; n64[0] = s64[0] ^ 0x80008000u; n64[1] = s64[1] ^ 0x80008000u;
    c32[1] = c32[0] ^ 0x80000000u; s32[1] = s32[0] ^ 0x80000000u; n32[0] = s32[0] ^ 0x80008000u; n32[1] = s32[0] ^ 0x00008000u;
    PINF(c64[1]); PINF(s64[1]); PINF(n64[0]); PINF(n64[1]); PINF(c32[1]); PINF(s32[1]); PINF(n32[0]); PINF(n32[1]);
  }
  SB();
  if (FIRST) {
    raw_bar();
#pragma unroll
    for (int t = 0; t < 2; t++) { xb[t] = gl128<16>(rw, WS_RB0 + ((tid >> 2) * 641 + ((tid >> 2) & 1) + 2 * c + t) * 64 + (tid & 3) * 16); rs[t] = asf(gl128<16>(rw, WS_RS1 + (1024 * t + 4 * tid) * 4)); }
    col_load(kin, rw, false, true);
    SB();
#pragma unroll
    for (int t = 0; t < 2; t++) {
      xr[t] = f32x4{bflo(xb[0][2 * t]), bfhi(xb[0][2 * t]), bflo(xb[0][2 * t + 1]), bfhi(xb[0][2 * t + 1])};
      xi[t] = f32x4{bflo(xb[1][2 * t]), bfhi(xb[1][2 * t]), bflo(xb[1][2 * t + 1]), bfhi(xb[1][2 * t + 1])};
    }
  } else {
    MARK(23);
    drain();
    MARK(11);
    u32x2 sp[8];
    const unsigned so = WS_SSQ + (blockIdx.x & 7) * 32768 + (1024 * q + 4 * tid) * 2, xo = WS_R3 + ((tid >> 2) * 642 + 2 * c) * 64 + (tid & 3) * 16;
    bool bs = true, bx = true;
#pragma unroll 1
    for (int spins = 0; spins < (1 << 18); spins++) {
      if (bx) {
#pragma unroll
        for (int t = 0; t < 2; t++) { xr[t] = asf(gl128<16>(rw, xo + 64 * 642 * 64 * t)); xi[t] = asf(gl128<16>(rw, xo + 64 * 642 * 64 * t + 64)); }
      }
      if (bs) {
#pragma unroll
        for (int i = 0; i < 8; i++) sp[i] = gl64<16>(rw, so + i * 4096);
      }
      unsigned m = 0;
#pragma unroll
      for (int t = 0; t < 2; t++) m = umax4(umax4(m, asu(xr[t])), asu(xi[t]));
      bx = __builtin_amdgcn_ballot_w64(m == EMPTY) != 0;
      m = 0;
#pragma unroll
      for (int i = 0; i < 8; i++) m = max(max(m, sp[i][0]), sp[i][1]);
      bs = __builtin_amdgcn_ballot_w64(m == EMPTY) != 0;
      if (!bs && !bx) break;
      nap<YSLEEP>();
    }
    put_scale(lds, q, tid, sp);
    LDS_AS f32x4* ex = (LDS_AS f32x4*)(lds + 2 * SLOT32) + tid;
    lds_bar();
    rs[0] = ex[0]; rs[1] = ex[256];
  }
  MARK(FIRST ? 9 : 21);
#pragma unroll
  for (int t = 0; t < 2; t++) {
    u32x2 pb = pk4(xi[t] * rs[t]);
    if (c == 0) pb = u32x2{0u, 0u};
    *(LDS_AS u32x2*)(img + wo_ + t * 2048) = pk4(xr[t] * rs[t]);
    *(LDS_AS u32x2*)(img + 4096 + wo_ + t * 2048) = pb;
  }
  lds_bar();
  u32x4 ur[2][2], ui[2][2], br, bi;
  f32x4 ar[2], ai[2];
  const f32x4 z4 = {0.f, 0.f, 0.f, 0.f};
  auto rd_in = [&](int off) {
#pragma unroll
    for (int ks = 0; ks < 2; ks++)
#pragma unroll
      for (int t = 0; t < 2; t++) { const lptr p = img + off + (ks ? ro2 : ro_) + t * 2048; ur[t][ks] = ld128(p); ui[t][ks] = ld128(p + 4096); }
    SB();
  };
  auto cmul = [&](const f32x4* cc, const f32x4* sn) {
    br = pk8(ar[0] * cc[0] - ai[0] * sn[0], ar[1] * cc[1] - ai[1] * sn[1]);
    bi = pk8(ai[0] * cc[0] + ar[0] * sn[0], ai[1] * cc[1] + ar[1] * sn[1]);
  };
  rd_in(0);
#pragma unroll
  for (int ks = 0; ks < 2; ks++) {
#pragma unroll
    for (int t = 0; t < 2; t++) { ar[t] = mfma16(ur[t][ks], c64[ks], ks ? ar[t] : z4); ai[t] = mfma16(ui[t][ks], c64[ks], ks ? ai[t] : z4); }
#pragma unroll
    for (int t = 0; t < 2; t++) { ar[t] = mfma16(ui[t][ks], s64[ks], ar[t]); ai[t] = mfma16(ur[t][ks], n64[ks], ai[t]); }
  }
  const f32x4 fa[2] = {asf(kin.wf[0] << 16), asf(kin.wf[1] << 16)}, fb[2] = {asf(kin.wf[0] & 0xFFFF0000u), asf(kin.wf[1] & 0xFFFF0000u)};
  cmul(t1c, t1s);
#pragma unroll
  for (int t = 0; t < 2; t++) { ar[t] = mfma16(c32[t], br, z4); ai[t] = mfma16(c32[t], bi, z4); }
#pragma unroll
  for (int t = 0; t < 2; t++) { ar[t] = mfma16(s32[t], bi, ar[t]); ai[t] = mfma16(n32[t], br, ai[t]); }
  cmul(fa, fb);
#pragma unroll
  for (int t = 0; t < 2; t++) { ar[t] = mfma16(br, c32[t], z4); ai[t] = mfma16(bi, c32[t], z4); }
#pragma unroll
  for (int t = 0; t < 2; t++) { ar[t] = mfma16(bi, n32[t], ar[t]); ai[t] = mfma16(br, s32[t], ai[t]); }
  cmul(t2c, t2s);
#pragma unroll
  for (int t = 0; t < 2; t++) {
    *(LDS_AS u32x2*)(img + 8192 + wo_ + t * 2048) = u32x2{br[2 * t], br[2 * t + 1]};
    *(LDS_AS u32x2*)(img + 12288 + wo_ + t * 2048) = u32x2{bi[2 * t], bi[2 * t + 1]};
  }
  lds_bar();
  rd_in(8192);
#pragma unroll
  for (int ks = 0; ks < 2; ks++) {
#pragma unroll
    for (int t = 0; t < 2; t++) { ar[t] = mfma16(c64[ks], ur[t][ks], ks ? ar[t] : xr[t]); ai[t] = mfma16(c64[ks], ui[t][ks], ks ? ai[t] : xi[t]); }
#pragma unroll
    for (int t = 0; t < 2; t++) { ar[t] = mfma16(n64[ks], ui[t][ks], ar[t]); ai[t] = mfma16(s64[ks], ur[t][ks], ai[t]); }
  }
  const unsigned ob = (FIRST ? WS_RB1 : WS_RB2) + (2 * c * RBP + 8 * tid) * 2;
  st128<16>(rw, ob, clean(pk8(ar[0], ar[1])));
  st128<16>(rw, ob + RBP * 2, c == 0 ? u32x4{0u, 0u, 0u, 0u} : clean(pk8(ai[0], ai[1])));
}

DI f32x16 mfma32(u32x4 a, u32x4 b, f32x16 c) {
  return __builtin_amdgcn_mfma_f32_32x32x16_bf16(__builtin_bit_cast(bf16x8, a), __builtin_bit_cast(bf16x8, b), c, 0, 0, 0);
}
DI f32x16 mfma32k8(u32x2 a, u32x2 b, f32x16 c) {
  return __builtin_amdgcn_mfma_f32_32x32x8bf16_1k(__builtin_bit_cast(s16x4, a), __builtin_bit_cast(s16x4, b), c, 0, 0, 0);
}
DI f32x16 scores(u32x4 k1, u32x2 k2, u32x4 q1, u32x2 q2) {
  f32x16 z;
#pragma unroll
  for (int i = 0; i < 16; i++) z[i] = 0.f;
  f32x16 s = mfma32(k1, q1, z);
  asm volatile("s_nop 11" : "+v"(s));
  return mfma32k8(k2, q2, s);
}
DI float max16(const f32x16& s) {
  float m = fmaxf(s[0], s[1]);
#pragma unroll
  for (int i = 2; i < 16; i++) m = fmaxf(m, s[i]);
  return m;
}
#define STR_(x) #x
#define STR(x) STR_(x)
#define KOF(t) ((((t)&7)>>1)*11264+((t)&1)*1280)
#define VOF(t) ((((t)&7)>>1)*11264+5120+((t)&1)*1536)
#define EXP(d, s) "v_exp_f32 v" #d ", v" #s "\n\t"
#define CVT(d, s) "v_cvt_pk_bf16_f32 v" #d ", v" #s ", v[" #s "+1]\n\t"
#define STEPA(t) \
  "v_mfma_f32_32x32x16_bf16 v[48:63], v[80:83], v[136:139], v[120:135]\n\t" \
  "ds_read_b128 v[80:83], v100 offset:" STR(KOF(t+1)) "\n\t" \
  EXP(64,32) EXP(65,33) CVT(78,68) CVT(79,70) EXP(66,34) EXP(67,35) \
  "v_mfma_f32_32x32x16_bf16 v[104:119], v[90:93], v[76:79], v[104:119]\n\t" \
  "ds_read_b64_tr_b16 v[90:91], v102 offset:" STR(VOF(t)+768) "\n\t" \
  "ds_read_b64_tr_b16 v[92:93], v102 offset:" STR(VOF(t)+1152) "\n\t" \
  EXP(68,36) EXP(69,37) CVT(72,64) CVT(73,66) EXP(70,38) EXP(71,39) \
  "v_mfma_f32_32x32x8_bf16 v[48:63], v[84:85], v[140:141], v[48:63]\n\t" \
  "ds_read_b64 v[84:85], v101 offset:" STR(KOF(t+1)) "\n\t" \
  EXP(64,40) EXP(65,41) CVT(74,68) CVT(75,70) EXP(66,42) EXP(67,43) \
  "s_waitcnt lgkmcnt(4)\n\t" \
  "v_mfma_f32_32x32x16_bf16 v[0:15], v[86:89], v[72:75], v[0:15]\n\t" \
  EXP(68,44) EXP(69,45) CVT(76,64) CVT(77,66) EXP(70,46) EXP(71,47)
#define STEPB(t) \
  "s_waitcnt lgkmcnt(0)\n\t" \
  "v_mfma_f32_32x32x16_bf16 v[32:47], v[80:83], v[94:97], v[16:31]\n\t" \
  EXP(64,48) EXP(65,49) CVT(78,68) CVT(79,70) EXP(66,50) EXP(67,51) \
  "v_mfma_f32_32x32x16_bf16 v[0:15], v[90:93], v[76:79], v[0:15]\n\t" \
  EXP(68,52) EXP(69,53) CVT(72,64) CVT(73,66) EXP(70,54) EXP(71,55) \
  "v_mfma_f32_32x32x8_bf16 v[32:47], v[84:85], v[98:99], v[32:47]\n\t" \
  EXP(64,56) EXP(65,57) CVT(74,68) CVT(75,70) EXP(66,58) EXP(67,59) \
  "v_mfma_f32_32x32x16_bf16 v[104:119], v[86:89], v[72:75], v[104:119]\n\t" \
  "ds_read_b64_tr_b16 v[86:87], v102 offset:" STR(VOF(t+1)) "\n\t" \
  "ds_read_b64_tr_b16 v[88:89], v102 offset:" STR(VOF(t+1)+384) "\n\t" \
  EXP(68,60) EXP(69,61) CVT(76,64) CVT(77,66) EXP(70,62) EXP(71,63)
#define PR(n) "s_setprio " #n "\n\t"
#define CHUNK(i) PR(1) STEPA(2*(i)) PR(0) STEPB(2*(i)) "s_barrier\n\t" PR(2) STEPA(2*(i)+1) STEPB(2*(i)+1)
#define Z4(a) "v_mov_b64 v[" #a ":" #a "+1], 0\n\tv_mov_b64 v[" #a "+2:" #a "+3], 0\n\t"

#define R4(b) "v[" STR(b) ":" STR(b) "+3]"
#define MM(d, a, b) "v_mfma_f32_16x16x32_bf16 " R4(d) ", " R4(a) ", " R4(b) ", " R4(d) "\n\t"
#define GM(B, m, i) MM(16*m+4*i, B+8+4*i, B+4*m)
#define RT(B, m, hf, k) "ds_read_b64_tr_b16 v[" STR(B+4*m+2*hf) ":" STR(B+4*m+2*hf+1) "], v[84+" #m "] offset:" STR(k*8192+hf*1024) "\n\t"
#define RS(B, i, k) "ds_read_b128 " R4(B+8+4*i) ", v86 offset:" STR(i*2048+k*1024) "\n\t"
#define GSTEP(B, C, k) GM(B,0,0) RT(C,0,0,k) RT(C,0,1,k) GM(B,1,0) RS(C,0,k) RT(C,1,0,k) GM(B,0,1) RT(C,1,1,k) RS(C,1,k) GM(B,1,1) RS(C,2,k) \
  GM(B,0,2) RS(C,3,k) GM(B,1,2) GM(B,0,3) GM(B,1,3) MM(32,B,B)
#define GEMM_ASM(EXTRA) \
  Z4(0) Z4(4) Z4(8) Z4(12) Z4(16) Z4(20) Z4(24) Z4(28) Z4(32) "s_mov_b32 %[c], 0\n\ts_barrier\n\t" \
  RT(36,0,0,0) RT(36,0,1,0) RS(36,0,0) RT(36,1,0,0) RT(36,1,1,0) RS(36,1,0) RS(36,2,0) RS(36,3,0) EXTRA \
  "L_gemm_%=:\n\ts_add_u32 %[c], %[c], 1\n\ts_and_b32 %[t], %[c], 3\n\ts_cselect_b32 %[t], 1, -3\n\ts_lshl_b32 %[t], %[t], 15\n\ts_waitcnt lgkmcnt(0)\n\t" \
  GSTEP(36,60,1) \
  "v_add_u32 v84, %[t], v84\n\tv_add_u32 v85, %[t], v85\n\tv_add_u32 v86, %[t], v86\n\ts_waitcnt lgkmcnt(0)\n\ts_barrier\n\ts_setprio 1\n\t" \
  GSTEP(60,36,0) "s_setprio 0\n\t" \
  "s_cmp_lg_u32 %[c], 9\n\ts_cbranch_scc1 L_gemm_%=\n\ts_waitcnt lgkmcnt(0)\n\t" \
  GSTEP(36,60,1) "s_waitcnt lgkmcnt(0)\n\t" \
  MM(32,60,60) GM(60,0,0) GM(60,1,0) GM(60,0,1) GM(60,1,1) "s_nop 4\n\t" \
  "v_cndmask_b32 v36, v32, v33, %[m1]\n\tv_cndmask_b32 v37, v34, v35, %[m1]\n\tv_mov_b32 v38, 0x3727c5ac\n\tv_cndmask_b32 v36, v36, v37, %[m2]\n\t" \
  "v_fmac_f32 v38, 0x3acccccd, v36\n\tv_cmp_u_f32 vcc, v36, v36\n\tv_rsq_f32 v38, v38\n\ts_cbranch_vccz L_gok_%=\n\tds_write_b32 v90, v90\n\t" \
  "L_gok_%=:\n\tds_write_b32 v87, v38\n\ts_waitcnt lgkmcnt(0)\n\ts_barrier\n\t" \
  "ds_read_b32 v36, v88\n\tds_read_b32 v37, v89\n\tds_read_b32 v38, v90\n\t" \
  GM(60,0,2) GM(60,1,2) GM(60,0,3) GM(60,1,3) "s_waitcnt vmcnt(0) lgkmcnt(0)"
#define GEMM_OUT "={v[0:3]}"(acc[0][0]), "={v[4:7]}"(acc[0][1]), "={v[8:11]}"(acc[0][2]), "={v[12:15]}"(acc[0][3]), \
    "={v[16:19]}"(acc[1][0]), "={v[20:23]}"(acc[1][1]), "={v[24:27]}"(acc[1][2]), "={v[28:31]}"(acc[1][3]), \
    "={v36}"(rs[0]), "={v37}"(rs[1]), "={v38}"(vote), "+{v84}"(am0), "+{v85}"(am1), "+{v86}"(as), [c] "=&s"(c_), [t] "=&s"(t_)
#define GEMM_IN "{v87}"(a_w), "{v88}"(a_r[0]), "{v89}"(a_r[1]), "{v90}"(a_v), [m1] "s"(0xAAAAAAAAAAAAAAAAull), [m2] "s"(0xCCCCCCCCCCCCCCCCull)
#define GEMM_CLOB "memory", "scc", "vcc", "v32", "v33", "v34", "v35", "v39", "v40", "v41", "v42", "v43", "v44", "v45", "v46", "v47", "v48", "v49", "v50", \
    "v51", "v52", "v53", "v54", "v55", "v56", "v57", "v58", "v59", "v60", "v61", "v62", "v63", "v64", "v65", "v66", "v67", "v68", "v69", "v70", "v71", \
    "v72", "v73", "v74", "v75", "v76", "v77", "v78", "v79", "v80", "v81", "v82", "v83"
#define ITL(d, a) "global_load_dwordx4 " R4(d) ", v" #a ", %[it]\n\tglobal_load_dwordx4 " R4(d+4) ", v" #a ", %[it] offset:1024\n\t"

constexpr int HR = 4 * KVC;
constexpr int L_A = 2 * HR;
constexpr int L_EX = 15360;
constexpr int L_T = 4 * SLOT32, L_IA = L_T + 10240, L_U = L_A + 10240;
constexpr int L_MISC = L_A + 70 * 1024;
constexpr int LDS_FUSED = L_MISC + 2048;

DI bool poll16(rsrc_t rw, unsigned off, unsigned need) {
  const int lane = threadIdx.x & 63;
#pragma unroll 1
  for (int spins = 0; spins < (1 << 20); spins++) {
    const unsigned v = gl32<16>(rw, off + (lane & 15) * 4);
    const unsigned got = (unsigned)__builtin_amdgcn_ballot_w64(v != 0) & 0xFFFFu;
    if ((got & need) == need) return true;
    nap<1>();
  }
  return false;
}

DI void poll_aux(rsrc_t rw, unsigned off) {
#pragma unroll 1
  for (int spins = 0; spins < (1 << 20); spins++) {
    const u32x4 v = gl128<16>(rw, off + (threadIdx.x & 63) * 16);
    if (!__builtin_amdgcn_ballot_w64(min(min(v[0], v[1]), min(v[2], v[3])) == 0u)) break;
    nap<4>();
  }
}
DI void poll_tag(rsrc_t rw, u32x2 tag, unsigned long long need) {
  const unsigned o = WS_F0 + (blockIdx.x & 7) * 2048 + (threadIdx.x & 63) * 32;
#pragma unroll 1
  for (int spins = 0; spins < (1 << 20); spins++) {
    const u32x4 a = gl128<16>(rw, o), b = gl128<16>(rw, o + 16);
    const unsigned d = (a[0] ^ tag[0]) | (a[1] ^ tag[1]) | (a[2] ^ tag[0]) | (a[3] ^ tag[1]) | (b[0] ^ tag[0]) | (b[1] ^ tag[1]) | (b[2] ^ tag[0]) | (b[3] ^ tag[1]);
    if (!(__builtin_amdgcn_ballot_w64(d != 0u) & need)) break;
    nap<TSLEEP>();
  }
}
#define LAYER_VARS \
  extern __shared__ __attribute__((aligned(16))) char smem[]; \
  const lptr lds = (lptr)smem; \
  const rsrc_t rw = mk_rsrc(ws, WS_END); \
  int lane = threadIdx.x & 63; asm volatile("" : "+v"(lane)); \
  const int w = wave_id(), g = lane >> 4, r16 = lane & 15; \
  const int xcd = blockIdx.x & 7, bi = blockIdx.x >> 3, u = 8 * (xcd & 1) + (bi & 7), qt = 4 * (xcd >> 1) + (bi >> 3); \
  const int hf = u & 1, ct = u >> 1;     \
  constexpr unsigned FKV = WS_FL + (FINAL ? 32768 : 0), KV = WS_KV + (FINAL ? LAYER2 : 0), OB = WS_O + (FINAL ? LAYER2 : 0); \
  constexpr unsigned AUX = WS_AUX + (FINAL ? 1024 : 0), RB = FINAL ? WS_RB2 : WS_RB1; \
  constexpr int MK = FINAL ? 12 : 0; (void)MK; (void)hf; (void)AUX; (void)RB; (void)FKV; \
  LDS_AS unsigned* misc = (LDS_AS unsigned*)(lds + L_MISC + 1024) + (FINAL ? 8 : 0);

template <bool FINAL>
DI void loader(char* ws, u32x2 tag SARG) {
  LAYER_VARS
  {
    const int lw = w - 8;
    {
      unsigned vm[4];
#pragma unroll
      for (int r = 0; r < 4; r++) vm[r] = (4 * (4 * lw + r) + g) * (RBP * 2) + 256 * qt + (((r16 >> 1) ^ (g | ((r & 2) << 1))) * 32) + (r16 & 1) * 16 + 3072 - 1024 * r;
      const unsigned l0 = lds_addr(lds) + lw * 4096, v0 = 2 * lw * 20480 + lane * 16;
      constexpr int CNT = 8;
      auto lds_w = [&](int s) {
        const char* sb = ws + WS_WQ + u * 163840 + s * 2048;
        dma_s(v0, v0 + 20480 - 2048, sb, l0 + (s & 3) * SLOT32 + 16384);
      };
      auto lds_a = [&](int s) {
        dma_a(vm, ws + RB - 3072 + s * (64 * RBP * 2), l0 + (s & 3) * SLOT32); };
#pragma unroll 1
      for (int att = 0;; att++) {
        if (att == 0) { raw_bar(); if (FINAL) raw_bar(); else poll_tag(rw, tag, 0x7FFF0000ull); }
        lds_w(0); lds_w(1);
        if (att == 0) raw_bar();
        lds_w(2); lds_w(3);
        if (FINAL) dma2(40960 + lw * 2048 + lane * 16, (const char*)&g_it, lds_addr(lds) + L_IA + lw * 2048);
        auto probe = [&](int s0, int n) {
          const int pl = min(lane, n - 1);
          const unsigned po = RB + ((64 * (s0 + (pl >> 3)) + 16 * lw + 2 * (pl & 7) + 1) * RBP + 128 * qt + 126) * 2;
#pragma unroll 1
          for (int spins = 0; spins < (1 << 20); spins++) {
            if (!__builtin_amdgcn_ballot_w64(gl32<16>(rw, po) == EMPTY)) break;
            nap<XSLEEP>();
          }
        };
        if (att == 0) {
          raw_bar();
          if (blockIdx.x >= 64) nap<XDELAY>();
          MARK(MK + 7);
          probe(0, 48);
        }
        drain();
        MARK(MK + 8);
        unsigned nd = 0;
        if (att == 0 && lane == 0) nd = __hip_atomic_fetch_add(misc + 4, 1u, __ATOMIC_RELAXED, __HIP_MEMORY_SCOPE_WORKGROUP);
        if (nd == 11) st32<16>(rw, AUX + blockIdx.x * 4, 1u);
#pragma unroll 1
        for (int s = 0; s < 4; s++) lds_a(s);
        vm_bar<12>(); vm_bar<8>();
        if (att == 0) probe(6, 32);
        lds_a(4); lds_w(4);
        vm_bar<4 + CNT>();
#pragma unroll 1
        for (int s = 5; s < 10; s++) { lds_a(s); lds_w(s); vm_bar<2 * CNT>(); }
        vm_bar<CNT>();
        poll_aux(rw, AUX);
        if (!FINAL) { poll_tag(rw, tag, ~0ull); if (blockIdx.x == 0 && threadIdx.x == 512) st64<16>(rw, WS_EP, tag); }
        vm_bar<0>();
        if (FINAL) {
#pragma unroll
          for (int i = 0; i < 3; i++) { const int pc = min(3 * lw + i, 9); dma1_sc1(RB + ((64 * pc + lane) * RBP + 128 * qt + 8 * u) * 2, ws, lds_addr(lds) + L_T + pc * 1024); }
        }
        raw_bar();
        if (XNL || att || !*(volatile LDS_AS unsigned*)(misc + 2)) break;
      }
    }
    MARK(MK + 0);
    const int hh = lw >> 1, hp = lw & 1;
    const unsigned kvv = KV + (2 * u + hh) * 16 * KVC + hp * 5120 + lane * 16;
    const unsigned lkv = lds_addr(lds) + hh * HR + hp * 5120;
    const unsigned wv = (FINAL ? WS_WOP : WS_WOF) + (ct * 2 + hh) * 51200 + lane * 16;
    const unsigned myfl = FKV + (u * 16 + qt) * 128;
    vm_bar<0>();
    MARK(MK + 1);
    {
      u32x4 v[6];
#pragma unroll
      for (int i = 0; i < 6; i++) v[i] = ld128(lds + hh * HR + hp * 5120 + lane * 16 + i * 1024);
#pragma unroll
      for (int i = 0; i < 6; i++) st128<16>(rw, kvv + qt * KVC + i * 1024, clean(v[i]));
    }
    drain();
    unsigned cnt = 0;
    if (lane == 0) cnt = __hip_atomic_fetch_add(misc, 1u, __ATOMIC_RELAXED, __HIP_MEMORY_SCOPE_WORKGROUP);
    if (__builtin_amdgcn_readfirstlane(cnt) == 3 && lane < 16) st32<16>(rw, FKV + (u * 16 + lane) * 128 + qt * 4, 1u);
    MARK(MK + 2);
    auto ldkv = [&](int seq) {
      const int ch = seq == 0 ? qt : seq < 13 ? ((qt & 12) + 3 + seq) & 15 : (qt & 12) | ((qt + seq) & 3);
      dma6_sc1(kvv + ch * KVC, ws, lkv + (seq & 3) * KVC);
    };
    auto fetch = [&](int seq) {
      const unsigned src = kvv + ((((qt & 12) + 3 + seq) & 15)) * KVC;
      u32x4 v[6];
#pragma unroll 1
      for (int spins = 0; spins < (1 << 18); spins++) {
#pragma unroll
        for (int i = 0; i < 6; i++) v[i] = gl128<16>(rw, src + i * 1024);
        unsigned m = 0;
#pragma unroll
        for (int i = 0; i < 6; i++) m = umax4(m, v[i]);
        if (!__builtin_amdgcn_ballot_w64(m == EMPTY)) break;
        nap<2>();
      }
#pragma unroll
      for (int i = 0; i < 6; i++) *(LDS_AS u32x4*)(lds + hh * HR + hp * 5120 + seq * KVC + lane * 16 + i * 1024) = v[i];
    };
#pragma unroll 1
    for (int attempt = 0;; attempt++) {
      int c = -2;
      if (attempt) { ldkv(0); vm_bar<0>(); }
      else {
        fetch(1); MARK(MK + 3); lds_bar();
        MARK(MK + 4);
        fetch(2); poll16(rw, myfl, 12u << (((qt & 12) + 4) & 15)); ldkv(3); MARK(MK + 5); lds_bar();
        c = 1;
      }
#pragma unroll 1
      for (; c < 13; c++) {
        if (c == 2 && !attempt) poll16(rw, myfl, 0xFFFFu);
        if (!FINAL && !attempt) {
          if (c >= 2 && c < 9) {
            const u32x4 e = {EMPTY, EMPTY, EMPTY, EMPTY};
            const unsigned o = OB + LAYER2 + (((c - 2) * 256 + blockIdx.x) * 4 + lw) * 2048 + lane * 16;
            st128<16>(rw, o, e); st128<16>(rw, o + 1024, e);
            st128<16>(rw, ((qt & 3) < 2 && c < 8) ? kvv + LAYER2 + qt * KVC + (c - 2) * 1024 : WS_END, e);
            st128<16>(rw, c < 5 ? OB + ((4 * qt + lw) * 32 + 2 * u) * 1280 + min((c - 2) * 1024, 1536) + lane * 16 : WS_END, e);
          }
          if (c == 10) {
            drain();
            unsigned n = 0;
            if (lane == 0) n = __hip_atomic_fetch_add(misc + 3, 1u, __ATOMIC_RELAXED, __HIP_MEMORY_SCOPE_WORKGROUP);
            if (__builtin_amdgcn_readfirstlane(n) == 3 && lane < 32) st32<16>(rw, WS_FL + 65536 + (lane < 16 ? (u * 16 + lane) * 128 + qt * 4 : (r16 * 16 + qt) * 128 + 64 + u * 4), 1u);
          }
        }
        if (c >= 2) { const int e = min(4 * (c - 2) + 2 * hp, 33); dma2(wv + e * 1024, ws, lds_addr(lds) + L_A + (hh * 35 + e) * 1024); }
        ldkv(c + 3);
        if (c == 0) vm_bar<12>();
        if (c >= 2) vm_bar<8>(); else if (c >= 0) vm_bar<6>();
      }
      if (!FINAL && !attempt) poll16(rw, myfl + 65536 + hp * 64, 0xFFFFu);
      vm_bar<0>();
      dma_s(wv + (35 + 7 * hp) * 1024, wv + (35 + 7 * hp) * 1024, ws, lds_addr(lds) + hh * HR + 7 * hp * 1024);
      dma_s(wv + (39 + 7 * hp) * 1024, wv + (39 + 7 * hp) * 1024, ws, lds_addr(lds) + hh * HR + (4 + 7 * hp) * 1024);
      raw_bar();
      vm_bar<0>();
      if (attempt || !*(volatile LDS_AS unsigned*)(misc + 1)) break;
      raw_bar();
    }
    if (!FINAL) { raw_bar(); raw_bar(); }
  }
}

template <bool FINAL>
DI void compute(char* ws, u16* out, ColIn& kin SARG) {
  LAYER_VARS
  const int wm = w & 3, wn = w >> 2, h = lane >> 5;
  if (blockIdx.x < 64 || wn == 0) col_phase<!FINAL>(rw, lds, kin SPASS);
  else if (FINAL) {
    drain();
    nap<YIDLE>();
    u32x2 sp[8];
#pragma unroll 1
    for (int spins = 0; spins < (1 << 18); spins++) {
      unsigned m = 0;
#pragma unroll
      for (int i = 0; i < 8; i++) sp[i] = gl64<16>(rw, WS_SSQ + (blockIdx.x & 7) * 32768 + (1024 + 4 * (wm * 64 + lane)) * 2 + i * 4096);
#pragma unroll
      for (int i = 0; i < 8; i++) m = max(max(m, sp[i][0]), sp[i][1]);
      if (!__builtin_amdgcn_ballot_w64(m == EMPTY)) break;
      nap<YSLEEP>();
    }
    put_scale(lds, 1, wm * 64 + lane, sp);
    lds_bar(); raw_bar(); raw_bar();
  } else {
    raw_bar(); raw_bar(); raw_bar();
  }
  MARK(MK + 10);
  lds_bar();
  {
    drain();
    unsigned n = 0;
    if (lane == 0) n = __hip_atomic_fetch_add(misc + 4, 1u, __ATOMIC_RELAXED, __HIP_MEMORY_SCOPE_WORKGROUP);
    if (n == 11) st32<16>(rw, AUX + blockIdx.x * 4, 1u);
  }

  const int head = 2 * u + wn, kh = w & 1;
  const lptr ring = lds + wn * HR;
  const int pblk = 128 * qt + 64 * hf + 32 * (wm >> 1);
  const unsigned r3t = WS_R3 + (64 * (wm & 1) + (pblk >> 5)) * (642 * 64);
  u32x2 x4v[3]; float dc = 0.f;
  unsigned os = OB + ((4 * qt + wm) * 32 + head) * 1280 + (((lane << 2) & 16) + ((lane >> 1) & 12) + (lane & 3)) * 8;
  unsigned o1 = OB + ((pblk >> 5) * 32 + 16 * wn) * 1280 + (g >> 1) * 1280 + (g & 1) * 512 + (wm & 1) * 256 + r16 * 16;
  unsigned o2 = OB + ((pblk >> 5) * 32 + 16 * wn) * 1280 + 2 * g * 1280 + 1024 + (wm & 1) * 128 + r16 * 8;
  u32x4 o3 = {0u, 0u, 0u, 0u};
  if (!FINAL) {
#pragma unroll
    for (int i = 0; i < 3; i++) {
      const int j = 80 * ct + 48 * wn + 16 * (wn ? (i & 1) : i) + r16;
      o3[i] = (j == 1 ? WS_RB0 + ((pblk >> 5) * 641 + ((pblk >> 5) & 1) + 1) * 64 : WS_RB1 + (j * RBP + pblk) * 2) + 16 * g + 8 * (wm & 1);
    }
  } else {
    o3[3] = r3t + 64 + r16 * 4;
#pragma unroll
    for (int i = 0; i < 3; i++) o3[i] = WS_X4 + ((pblk + trow(wm & 1, r16)) * 640 + 80 * ct + 48 * wn + 16 * (wn ? (i & 1) : i) + 4 * g) * 2;
  }
  asm volatile("" : "+v"(os), "+v"(o1), "+v"(o2), "+v"(o3));
  unsigned ka = lds_addr(ring) + kh * 2560 + lane * 16, kb = lds_addr(ring) + kh * 2560 + 1024 + (lane & 31) * 8;
  unsigned va = lds_addr(ring) + kh * 3072 + (4 * h + (r16 >> 2)) * 48 + ((g & 1) ? ((lane & 3) ? 40 : 32) : (lane & 3) * 8);
  asm volatile("" : "+v"(ka), "+v"(kb), "+v"(va));
  {
    f32x4 acc[2][4]; float rs[2]; u32x8 im[3];
    unsigned mo[2], a0[2], a1[2], a2[2], a_r[2];
#pragma unroll
    for (int m = 0; m < 2; m++) {
      const int q = r16 >> 2, pr = 16 * (m ^ wn) + r16;
      mo[m] = lds_addr(lds) + (8 * g + q) * 256 + (((2 * wm + (m ^ wn)) ^ (q | ((g & 1) << 2))) * 32) + (lane & 3) * 8;
      const unsigned qb = L_A + (wn * 4 + wm) * 1280, kb = wn * HR + wm * 1280, vr = wn * HR + 5120 + (32 * wm + pr) * 48 + (g == 3 ? 32 : g * 16);
      a0[m] = ((g >> 1) ? kb : qb) + (g & 1) * 512 + pr * 16;
      a1[m] = g == 2 ? qb + 1024 + pr * 8 : vr; a2[m] = g == 2 ? kb + 1024 + pr * 8 : vr + 8;
      a_r[m] = lds_addr(lds) + L_MISC + (32 * wm + pr) * 4;
      asm volatile("" : "+v"(a0[m]), "+v"(a1[m]), "+v"(a2[m]));
    }
    const unsigned a_w = a_r[0] + (g == (r16 >> 2) ? 0 : 512), a_v = lds_addr((lptr)(misc + 2));
#pragma unroll 1
    for (int att = 0;; att++) {
      unsigned am0 = mo[0], am1 = mo[1], as = lds_addr(lds) + 16384 + wn * 8192 + lane * 16, vote; int c_, t_;
      if (!FINAL) asm volatile(GEMM_ASM("") : GEMM_OUT : GEMM_IN : GEMM_CLOB);
      else asm volatile(GEMM_ASM(ITL(96, 91) ITL(104, 92) ITL(112, 93)) : GEMM_OUT, "={v[96:103]}"(im[0]), "={v[104:111]}"(im[1]), "={v[112:119]}"(im[2])
                        : GEMM_IN, "{v91}"(w * 2048 + lane * 16), "{v92}"((w + 8) * 2048 + lane * 16), "{v93}"((w < 4 ? w + 16 : w) * 2048 + lane * 16), [it] "s"(&g_it) : GEMM_CLOB);
      if (XNL || att || !__builtin_amdgcn_readfirstlane(vote)) break;
    }
    SB();
    MARK(MK + 0);
#pragma unroll
    for (int m = 0; m < 2; m++) *(LDS_AS u32x4*)(lds + a0[m]) = pk8(acc[m][0] * rs[m], acc[m][1] * rs[m]);
    asm volatile("s_nop 1" : "+v"(acc[0][2]), "+v"(acc[1][2]), "+v"(acc[0][3]), "+v"(acc[1][3]) :: "memory");
#pragma unroll
    for (int m = 0; m < 2; m++) {
      u32x2 hi = pk4(acc[m][3] * rs[m]);
      if (g == 3) hi = u32x2{0x3F80u, 0u};
      *(LDS_AS u32x2*)(lds + a1[m]) = pk4(acc[m][2] * rs[m]);
      *(LDS_AS u32x2*)(lds + a2[m]) = hi;
    }
    lds_bar();
    if (FINAL) {
      u32x4 ia[3]; u32x2 ib[3];
#pragma unroll
      for (int T = 0; T < 3; T++) { ia[T] = ld128(lds + L_IA + T * 1024 + lane * 16); ib[T] = ld64(lds + L_IA + 3072 + T * 512 + lane * 8); }
      const f32x4 z4 = {0.f, 0.f, 0.f, 0.f};
#pragma unroll
      for (int i = 0; i < 3; i++) {
        const int G = (i < 2 || w < 4) ? w + 8 * i : w;
        const lptr tp = lds + L_T + (32 * G + 8 * g + (r16 >> 2)) * 16 + (lane & 1) * 8;
        const u32x2 f0 = ldtr(tp), f1 = ldtr(tp + 64);
#pragma unroll
        for (int P = 0; P < 2; P++) *(LDS_AS u32x2*)(lds + L_U + (r16 & 7) * 1296 + (2 * G + P) * 32 + g * 8) = pk4(mfma16(u32x4{im[i][4 * P], im[i][4 * P + 1], im[i][4 * P + 2], im[i][4 * P + 3]}, u32x4{f0[0], f0[1], f1[0], f1[1]}, z4));
      }
      wave_sync(misc + 5, 8);
      const lptr up = lds + L_U + w * 1296;
      const u32x2 f0 = ldtr(up + lane * 8), f1 = ldtr(up + 512 + lane * 8), f2 = ldtr(up + 1024 + (lane & 31) * 8);
#pragma unroll
      for (int T = 0; T < 3; T++) {
        const u32x2 v = pk4(mfma16(u32x4{f2[0], f2[1], 0u, 0u}, u32x4{ib[T][0], ib[T][1], 0u, 0u}, mfma16(u32x4{f0[0], f0[1], f1[0], f1[1]}, ia[T], z4)));
        st64<16>(rw, (T < 2 || r16 < 8) ? WS_X4 + ((128 * qt + 8 * u + w) * 640 + 16 * (16 * T + r16) + 4 * g) * 2 : WS_END, u32x2{min(v[0], EMPTY - 1), min(v[1], EMPTY - 1)});
      }
    }
  }
  MARK(MK + 1);
  f32x4 r[3];
  {
    const lptr qpa = lds + L_A + (wn * 4 + wm) * 1280, qpb = lds + L_A + (wn * 4 + (wm ^ 1)) * 1280;
    const u32x4 qa1 = ld128(qpa + lane * 16), qb1 = ld128(qpb + lane * 16);
    u32x2 qa2 = ld64(qpa + 1024 + (lane & 31) * 8), qb2 = ld64(qpb + 1024 + (lane & 31) * 8);
    if (h) { qa2 = u32x2{0u, 0u}; qb2 = qa2; }
    float ma, mb;
    {
      const u32x4 k1 = ld128(ring + lane * 16); const u32x2 k2 = ld64(ring + 1024 + (lane & 31) * 8);
      ma = max16(scores(k1, k2, qa1, qa2)); mb = max16(scores(k1, k2, qb1, qb2));
    }
    for (int attempt = 0;; attempt++) {
      ma = fmaxf(ma, __shfl_xor(ma, 32)); mb = fmaxf(mb, __shfl_xor(mb, 32));
      f32x16 cia, cib, oa, ob;
#pragma unroll
      for (int i = 0; i < 16; i++) { cia[i] = -ma; cib[i] = -mb; }
      int cnt = 4;
      MARK(MK + 2);
      asm volatile(
          "ds_read_b128 v[80:83], v100\n\t"
          "ds_read_b64 v[84:85], v101\n\t"
          "ds_read_b64_tr_b16 v[86:87], v102 offset:5120\n\t"
          "ds_read_b64_tr_b16 v[88:89], v102 offset:5504\n\t"
          Z4(0) Z4(68) Z4(76) Z4(90)
          "s_waitcnt lgkmcnt(0)\n\t"
          "v_mfma_f32_32x32x16_bf16 v[32:47], v[80:83], v[94:97], v[16:31]\n\t"
          Z4(4) Z4(8) Z4(12) Z4(104) Z4(108) Z4(112) Z4(116)
          "v_mfma_f32_32x32x8_bf16 v[32:47], v[84:85], v[98:99], v[32:47]\n\ts_nop 9\n\t"
          "L_attn_%=:\n\t"
          CHUNK(0) CHUNK(1) CHUNK(2) CHUNK(3)
          "s_sub_u32 %[cnt], %[cnt], 1\n\ts_cmp_lg_u32 %[cnt], 0\n\ts_cbranch_scc1 L_attn_%=\n\t"
          "s_setprio 0\n\t" CVT(78,68) CVT(79,70) "s_waitcnt lgkmcnt(0)\n\ts_nop 1\n\t"
          "v_mfma_f32_32x32x16_bf16 v[104:119], v[90:93], v[76:79], v[104:119]\n\ts_nop 15\n\t"
          : "={v[0:15]}"(oa), "={v[104:119]}"(ob), [cnt] "+s"(cnt)
          : "{v[16:31]}"(cia), "{v[120:135]}"(cib), "{v[94:97]}"(qa1), "{v[98:99]}"(qa2), "{v[136:139]}"(qb1), "{v[140:141]}"(qb2),
            "{v100}"(ka), "{v101}"(kb), "{v102}"(va)
          : "memory", "scc", "v32", "v33", "v34", "v35", "v36", "v37", "v38", "v39", "v40", "v41", "v42", "v43", "v44", "v45", "v46", "v47",
            "v48", "v49", "v50", "v51", "v52", "v53", "v54", "v55", "v56", "v57", "v58", "v59", "v60", "v61", "v62", "v63", "v64", "v65",
            "v66", "v67", "v68", "v69", "v70", "v71", "v72", "v73", "v74", "v75", "v76", "v77", "v78", "v79", "v80", "v81", "v82", "v83",
            "v84", "v85", "v86", "v87", "v88", "v89", "v90", "v91", "v92", "v93");
      MARK(MK + 3);
      LDS_AS f32x4* ex = (LDS_AS f32x4*)(ring + L_EX + wm * 3072) + lane;
#pragma unroll
      for (int i = 0; i < 3; i++) ex[i * 64] = f32x4{ob[4 * i], ob[4 * i + 1], ob[4 * i + 2], ob[4 * i + 3]};
      f32x4 cs = {0.f, 0.f, 0.f, 0.f};
#pragma unroll
      for (int i = 0; i < 3; i++) cs += f32x4{oa[4 * i], oa[4 * i + 1], oa[4 * i + 2], oa[4 * i + 3]} + f32x4{ob[4 * i], ob[4 * i + 1], ob[4 * i + 2], ob[4 * i + 3]};
      if (!(fabsf(cs[0] + cs[1] + cs[2] + cs[3]) < INFINITY) || (h && !(oa[8] + ob[8] < 0x1p60f))) misc[1] = 1u;
      lds_bar();
      const unsigned vote = *(volatile LDS_AS unsigned*)(misc + 1);
      LDS_AS f32x4* ey = (LDS_AS f32x4*)(ring + L_EX + (wm ^ 1) * 3072) + lane;
#pragma unroll
      for (int i = 0; i < 3; i++) r[i] = ey[i * 64] + f32x4{oa[4 * i], oa[4 * i + 1], oa[4 * i + 2], oa[4 * i + 3]};
      if (attempt || !vote) break;
      lds_bar();
      ma = -INFINITY; mb = -INFINITY;
      for (int t = 0; t < 64; t++) {
        const unsigned kb_ = KV + (head * 16 + (t >> 2)) * KVC + (t & 3) * 1280;
        const u32x4 k1 = gl128<16>(rw, kb_ + lane * 16); const u32x2 k2 = gl64<16>(rw, kb_ + 1024 + (lane & 31) * 8);
        ma = fmaxf(ma, max16(scores(k1, k2, qa1, qa2))); mb = fmaxf(mb, max16(scores(k1, k2, qb1, qb2)));
      }
      raw_bar();
    }
  }
  {
    const unsigned lb = __float_as_uint(r[2][0]);
    const float il = __builtin_amdgcn_rcpf(__uint_as_float(__builtin_amdgcn_permlane32_swap(lb, lb, false, false)[1]));
    const unsigned sl8 = os & 255;
    st128<16>(rw, os + sl8 + h * 512, clean(pk8(r[0] * il, r[1] * il)));
    const u32x4 t = clean(pk8(r[2] * il, r[2] * il));
    st64<16>(rw, h ? WS_END : os + 1024, u32x2{t[0], t[1]});
  }
  MARK(MK + 4);
  const lptr wa = lds + L_A + wn * 35 * 1024 + lane * 16, wb = ring + lane * 16;
  constexpr int NPRE = 15;
  u32x4 bf[50];
  f32x4 acc[5] = {};
#pragma unroll
  for (int j = 0; j < NPRE; j++) bf[j] = ld128(wa + j * 1024);
#pragma unroll
  for (int j = 0; j < 5; j++) asm volatile("" : "+v"(acc[j]));
  if (FINAL) dc = __uint_as_float(gl32<16>(rw, o3[3]));
#pragma unroll 1
  for (int spins = 0; spins < (1 << 18); spins++) {
#pragma unroll
    for (int i = 0; i < 3; i++) x4v[i] = gl64<16>(rw, o3[i]);
    if (!FINAL || !__builtin_amdgcn_ballot_w64(umax4(max(x4v[0][0], x4v[0][1]), u32x4{x4v[1][0], x4v[1][1], x4v[2][0], x4v[2][1]}) == EMPTY)) break;
    nap<8>();
  }
  drain();
  MARK(MK + 5);
  {
    u32x4 af[10];
    auto ldf = [&](int i) {
      if (i < 8) af[i] = __builtin_amdgcn_raw_buffer_load_b128(rw, o1, i * 2 * 1280, 16);
      else { const u32x2 a = __builtin_amdgcn_raw_buffer_load_b64(rw, o2, (i - 8) * 8 * 1280, 16), b = __builtin_amdgcn_raw_buffer_load_b64(rw, o2, ((i - 8) * 8 + 1) * 1280, 16);
             af[i] = u32x4{a[0], a[1], b[0], b[1]}; }
    };
#pragma unroll
    for (int i = 0; i < 10; i++) ldf(i);
#pragma unroll 1
    for (int spins = 0; spins < (1 << 18); spins++) {
      unsigned m = 0;
#pragma unroll
      for (int i = 0; i < 10; i++) m = umax4(m, af[i]);
      if (!__builtin_amdgcn_ballot_w64(m == EMPTY)) break;
      nap<RSLEEP>();
#pragma unroll
      for (int i = 0; i < 10; i++) if (__builtin_amdgcn_ballot_w64(umax4(0, af[i]) == EMPTY)) ldf(i);
    }
    MARK(MK + 8);
#pragma unroll
    for (int i = 0; i < 10; i++) asm volatile("" : "+v"(af[i]));
    asm volatile("s_nop 1" : "+v"(acc[0]), "+v"(acc[1]), "+v"(acc[2]), "+v"(acc[3]), "+v"(acc[4]));
    SB();
#pragma unroll
    for (int i = 0; i < 10; i++) {
#pragma unroll
      for (int a = 0; a < 5; a++) {
        if (FINAL) mfma16v(acc[a], bf[5 * i + a], af[i]); else mfma16v(acc[a], af[i], bf[5 * i + a]);
        const int j = 5 * i + a + NPRE; if (j < 50) bf[j] = ld128(j < 35 ? wa + j * 1024 : wb + (j - 35) * 1024);
        SB();
      }
    }
  }
  asm volatile("s_nop 7\n\ts_nop 1" : "+v"(acc[0]), "+v"(acc[1]), "+v"(acc[2]), "+v"(acc[3]), "+v"(acc[4]) :: "memory");
  MARK(MK + 6);
  if (!FINAL) { col_load(kin, rw, true, true); SB(); }
  {
    LDS_AS f32x4* ex = (LDS_AS f32x4*)(ring + L_EX + 12288 + wm * 3072) + lane;
    LDS_AS f32x4* ey = (LDS_AS f32x4*)(lds + (wn ^ 1) * HR + L_EX + 12288 + wm * 3072) + lane + 64 * wn;
    ex[0] = acc[2]; ex[64] = acc[3]; ex[128] = acc[4];
    dc *= 0.039528470752104741f;
#pragma unroll
    for (int i = 0; i < 3; i++) { acc[i] += f32x4{bflo(x4v[i][0]), bfhi(x4v[i][0]), bflo(x4v[i][1]), bfhi(x4v[i][1])}; if (FINAL) acc[i] += dc; }
    lds_bar();
#pragma unroll
    for (int i = 0; i < 3; i++) acc[i] += ey[64 * i];
  }
  if (FINAL) {
    const rsrc_t ro = mk_rsrc(out, 2048 * 640 * 2);
    const unsigned op = (rowof(pblk + trow(wm & 1, r16)) * 640 + 80 * ct + 48 * wn + 4 * g) * 2;
#pragma unroll
    for (int i = 0; i < 3; i++)
      if (i < 2 || wn == 0) st64<0>(ro, op + 32 * i, pk4(acc[i]));
  } else {
    f32x4 sq = {0.f, 0.f, 0.f, 0.f};
#pragma unroll
    for (int i = 0; i < 3; i++)
      if (i < 2 || wn == 0) {
        const int j = 80 * ct + 48 * wn + 16 * i + r16;
        st128<16>(rw, r3t + j * 64 + g * 16, clean(asu(acc[i])));
        if (j != 1) sq += acc[i] * acc[i];
      }
#pragma unroll
    for (int e = 0; e < 4; e++) sq[e] = row16_sum(sq[e]);
    LDS_AS float* sl = (LDS_AS float*)(lds + L_MISC);
    if (r16 == 0) *(LDS_AS f32x4*)(sl + wn * 64 + 32 * (wm & 1) + 16 * (wm >> 1) + 4 * g) = sq;
    lds_bar();
    if (w == 0) {
      LDS_AS f32x4* s4 = (LDS_AS f32x4*)sl + 2 * (lane & 7);
      st128<16>(rw, WS_SSQ + (lane >> 3) * 32768 + (ct * 2048 + 1024 * ((lane >> 2) & 1) + 64 * qt + 32 * hf + 8 * (lane & 3)) * 2, clean(pk8(s4[0] + s4[16], s4[1] + s4[17])));
    }
  }
  MARK(MK + 7);
}

__global__ void __launch_bounds__(768, 3) k_main(const u16* x, const u16* wr, const u16* wi, const u16* wqkv, const u16* wo, char* ws, u16* out) {
  STAMP(1);
  if (wave_id() >= 8) {
    extern __shared__ __attribute__((aligned(16))) char smem[];
    const unsigned long long t0 = __builtin_amdgcn_s_memrealtime();
    const int job = blockIdx.x;
    if (job < 144) raw_bar();
    const rsrc_t rw = mk_rsrc(ws, WS_END);
    __builtin_amdgcn_s_setprio(3);
    if (threadIdx.x < 528) ((LDS_AS unsigned*)((lptr)smem + L_MISC + 1024))[threadIdx.x - 512] = 0u;
    u32x2 tag = gl64<16>(rw, WS_EP);
    tag[0] += 1u;
    if (job < 228) raw_bar();
    if (job < 64) raw_bar();
    asm volatile("s_waitcnt vmcnt(0) lgkmcnt(0)\n\ts_barrier" : "+v"(tag) :: "memory");
    if (wave_id() == 8) {
      if (threadIdx.x < 520) st64<16>(rw, WS_F0 + (threadIdx.x - 512) * 2048 + job * 8, tag);
      MARK(9);
      if (job >= 248) {
        while (__builtin_amdgcn_s_memrealtime() - t0 < TPOLL) nap<2>();
        poll_tag(rw, tag, 0xFE0FFFF00000FFFFull);
        if (threadIdx.x < 544) st64<16>(rw, WS_GO + ((threadIdx.x - 512) * 8 + (job & 7)) * 64, tag);
      }
#pragma unroll 1
      for (int spins = 0; spins < (1 << 20); spins++) {
        const u32x2 v = gl64<16>(rw, WS_GO + job * 64);
        if (__builtin_amdgcn_readfirstlane((v[0] ^ tag[0]) | (v[1] ^ tag[1])) == 0) break;
        nap<GSLEEP>();
      }
      MARK(10);
    }
    if (wave_id() > 8) {
    while (__builtin_amdgcn_s_memrealtime() - t0 < TFILL) nap<2>();
    const int i0 = blockIdx.x * 192 + threadIdx.x - 576;
#pragma unroll
    for (int it = 0; it < 4; it++) {
      const int k = it * 49152 + i0, b = k / 1408;
      st128<16>(rw, k < 128 * 1408 ? WS_KV + b * 4 * KVC + (k - b * 1408) * 16 : WS_END, u32x4{EMPTY, EMPTY, EMPTY, EMPTY});
    }
    }
    raw_bar();
    loader<false>(ws, tag SPASS);
    loader<true>(ws, tag SPASS);
  } else {
    prep(x, wr, wi, wqkv, wo, ws);
    asm volatile("s_waitcnt vmcnt(0) lgkmcnt(0)\n\ts_barrier" ::: "memory");
    ColIn kin = {};
    compute<false>(ws, out, kin SPASS);
    compute<true>(ws, out, kin SPASS);
    if (blockIdx.x == 100 && threadIdx.x < 64) { const unsigned long long until = st_.r0 + 10000 + 4 * (st_.t - st_.r0); while (__builtin_amdgcn_s_memrealtime() < until) __builtin_amdgcn_s_sleep(1); }
  }
}

#define STOPAT(n)
extern "C" void kernel_launch(void* const* d_in, const int* in_sizes, int n_in, void* d_out, int out_size, void* d_ws, size_t ws_size,
                              hipStream_t stream) {
  const u16* x = (const u16*)d_in[0];
  const u16* wr = (const u16*)d_in[1];
  const u16* wi = (const u16*)d_in[2];
  const u16* wqkv = (const u16*)d_in[4];
  const u16* wo = (const u16*)d_in[5];
  char* ws = (char*)d_ws;
  (void)hipFuncSetAttribute((const void*)k_main, hipFuncAttributeMaxDynamicSharedMemorySize, LDS_FUSED);
  k_main<<<256, 768, LDS_FUSED, stream>>>(x, wr, wi, wqkv, wo, ws, (u16*)d_out);
}
```

```cpp
#include <hip/hip_runtime.h>
#include <hip/hip_bf16.h>

typedef unsigned short u16;
typedef __attribute__((ext_vector_type(16))) float f32x16;
typedef __attribute__((ext_vector_type(4))) float f32x4;
typedef __attribute__((ext_vector_type(2))) float f32x2;
typedef __attribute__((ext_vector_type(8))) __bf16 bf16x8;
typedef __attribute__((ext_vector_type(2))) __bf16 bf16x2;
typedef __attribute__((ext_vector_type(4))) short s16x4;
typedef __attribute__((ext_vector_type(4))) unsigned u32x4;
typedef __attribute__((ext_vector_type(2))) unsigned u32x2;
typedef __attribute__((ext_vector_type(8))) unsigned u32x8;
#define DI __device__ __forceinline__
#define LDS_AS __attribute__((address_space(3)))
typedef LDS_AS char* lptr;

constexpr double cx_cos_small(double x) { double x2 = x * x, t = 1, s = 1; for (int i = 1; i < 12; i++) { t *= -x2 / ((2 * i - 1) * (2 * i)); s += t; } return s; }
constexpr double cx_sin_small(double x) { double x2 = x * x, t = x, s = x; for (int i = 1; i < 12; i++) { t *= -x2 / ((2 * i) * (2 * i + 1)); s += t; } return s; }
constexpr double cx_cos640(int m) {
  m %= 640; if (m > 320) m = 640 - m; double sg = 1; if (m > 160) { m = 320 - m; sg = -1; }
  const double w = 6.283185307179586476925 / 640; return sg * (m <= 80 ? cx_cos_small(w * m) : cx_sin_small(w * (160 - m)));
}
constexpr u16 cx_bf16(double d) { float f = (float)d; unsigned u = __builtin_bit_cast(unsigned, f); u += 0x7FFFu + ((u >> 16) & 1u); return (u16)(u >> 16); }
struct Cos640 { double v[640]; };
constexpr Cos640 mk_cos() { Cos640 c{}; for (int m = 0; m < 640; m++) c.v[m] = cx_cos640(m); return c; }
constexpr Cos640 COS = mk_cos();
__host__ __device__ constexpr int kf_of(int G, int i) { return G ? G + 40 * i : (i == 0 ? 320 : i < 8 ? 40 * i : 40 * i - 300); }
__host__ __device__ constexpr int freq_of(int G, int i) { return kf_of(G, i) > 320 ? 640 - kf_of(G, i) : kf_of(G, i); }
constexpr double SA = 1.91318, SA0 = 1.9140625, S64 = 1.86954, S32 = 1.6245;
constexpr double a1val(int out, int a) {
  if (out >= 40 || a >= 40) return 0;
  const int G = out >> 1, part = out & 1;
  if (G == 0) return (part && (a & 1)) ? -SA0 : SA0;
  return SA * COS.v[(16 * a * G + 160 * part) % 640];
}
constexpr double m2val(int G, int o, int k) {
  const int i = o >> 1, po = o & 1, part = k >> 4, b = k & 15, kf = kf_of(G, i);
  const double c = COS.v[(b * kf) % 640], sn = COS.v[(b * kf + 480) % 640];
  if (G == 0) {
    if (part != (i >= 8)) return 0;
    if (i == 0) return 0.039528470752104741 / SA0 * (po ? 1.0 : c);
    return 0.055901699437494742 / SA0 * (po ? -sn : c);
  }
  const double v = po ? (part ? c : -sn) : (part ? sn : c);
  return 0.055901699437494742 / SA * ((po && kf > 320) ? -v : v);
}
struct DTab { u16 a1[3][64][8]; u16 a1b[3][64][4]; u16 pad[256]; u16 m2[20][2][64][8]; };
constexpr DTab mk_dtab() {
  DTab t{};
  for (int l = 0; l < 64; l++) {
    const int r = l & 15, g = l >> 4;
    for (int T = 0; T < 3; T++) {
      for (int e = 0; e < 8; e++) t.a1[T][l][e] = cx_bf16(a1val(16 * T + r, 16 * (e >> 2) + 4 * g + (e & 3)));
      for (int e = 0; e < 4; e++) t.a1b[T][l][e] = cx_bf16(a1val(16 * T + r, 32 + 4 * g + e));
    }
    for (int G = 0; G < 20; G++) for (int T = 0; T < 2; T++) for (int e = 0; e < 8; e++)
      t.m2[G][T][l][e] = cx_bf16(m2val(G, 8 * (r >> 2) + 4 * T + (r & 3), 8 * g + e));
  }
  return t;
}
static __device__ const __attribute__((aligned(1024))) DTab g_dt = mk_dtab();

struct ITab { u16 m2[20][2][64][8]; u16 a1[3][64][8]; u16 a1b[3][64][4]; u16 pad[1792]; };
constexpr ITab mk_itab() {
  ITab t{};
  for (int l = 0; l < 64; l++) {
    const int r = l & 15, g = l >> 4;
    for (int T = 0; T < 3; T++) {
      for (int e = 0; e < 8; e++) t.a1[T][l][e] = cx_bf16(a1val(16 * (e >> 2) + 4 * g + (e & 3), 16 * T + r));
      for (int e = 0; e < 4; e++) t.a1b[T][l][e] = cx_bf16(a1val(32 + 4 * g + e, 16 * T + r));
    }
    for (int G = 0; G < 20; G++) for (int P = 0; P < 2; P++) for (int e = 0; e < 8; e++) t.m2[G][P][l][e] = cx_bf16(m2val(G, 8 * g + e, 16 * P + r));
  }
  return t;
}
static __device__ const __attribute__((aligned(1024))) ITab g_it = mk_itab();

struct CTab { u16 v[10 * 512]; };
constexpr CTab mk_ctab() {
  CTab c{};
  for (int f = 0; f < 10; f++) for (int l = 0; l < 64; l++) for (int e = 0; e < 8; e++) {
    const int r = l & 15, g = l >> 4;
    const int m = f < 8 ? (8 * g + e) * (16 * (f >> 1) + r) : 2 * r * (16 * (e >> 2) + 4 * g + (e & 3));
    c.v[(f * 64 + l) * 8 + e] = cx_bf16((f < 8 ? S64 : S32) * COS.v[10 * ((m + (f & 1) * 48) & 63)]);
  }
  return c;
}
static __device__ const __attribute__((aligned(1024))) CTab g_ct = mk_ctab();
constexpr float RSK = (float)(2048.0 * S64 * S64 * S32 * S32 * 2048.0 * S64 * S64 * S32 * S32 / 640.0);

constexpr int RP = 2048 + 64, RBP = 2048 + 128;
constexpr unsigned RBSZ = 640 * RBP * 2;
constexpr unsigned WS_FL = 0;
constexpr unsigned WS_AUX = WS_FL + 3 * 32768;
constexpr unsigned WS_F0 = WS_AUX + 2048;
constexpr unsigned WS_GO = WS_F0 + 8 * 2048;
constexpr unsigned WS_EP = WS_GO + 256 * 64;
constexpr unsigned WS_RS1 = WS_EP + 256;
constexpr unsigned WS_RB0 = WS_RS1 + 8192;
constexpr unsigned WS_RB1 = WS_RB0 + RBSZ;
constexpr unsigned WS_WF = WS_RB1 + RBSZ;
constexpr unsigned WS_WQ = WS_WF + 320 * 8192;
constexpr unsigned WS_WOF = WS_WQ + 16 * 8 * 20 * 1024;
constexpr unsigned WS_WOP = WS_WOF + 40 * 20 * 1024;
constexpr int KVC = 11264;
constexpr unsigned WS_KV = WS_WOP + 40 * 20 * 1024;
constexpr unsigned WS_O = WS_KV + 32 * 16 * KVC;
constexpr unsigned LAYER2 = 32 * 16 * KVC + 32 * 64 * 1280;
constexpr unsigned WS_X4 = WS_KV + 2 * LAYER2;
constexpr unsigned WS_R3 = WS_X4 + 2048 * 640 * 2;
constexpr unsigned WS_RB2 = WS_R3 + 640 * RP * 4;
constexpr unsigned WS_SSQ = WS_RB2 + RBSZ;
constexpr unsigned EMPTY = 0xFFFFFFFFu;
constexpr unsigned WS_END = WS_SSQ + 8 * 2048 * 16;

constexpr int XDELAY = 20, K1DELAY = 40, YIDLE = 12, TPOLL = 250, TSLEEP = 1, GSLEEP = 2, TFILL = 400, YSLEEP = 4, XSLEEP = 1, RSLEEP = 2;

#define SB() __builtin_amdgcn_sched_barrier(0)
typedef __amdgpu_buffer_rsrc_t rsrc_t;
DI rsrc_t mk_rsrc(const void* p, unsigned bytes) { return __builtin_amdgcn_make_buffer_rsrc((void*)p, 0, bytes, 0x00020000); }
template <int AUX> DI void st128(rsrc_t r, unsigned off, u32x4 v) { __builtin_amdgcn_raw_buffer_store_b128(v, r, off, 0, AUX); }
template <int AUX> DI void st64(rsrc_t r, unsigned off, u32x2 v) { __builtin_amdgcn_raw_buffer_store_b64(v, r, off, 0, AUX); }
template <int AUX> DI void st32(rsrc_t r, unsigned off, unsigned v) { __builtin_amdgcn_raw_buffer_store_b32(v, r, off, 0, AUX); }
template <int AUX> DI u32x4 gl128(rsrc_t r, unsigned off) { return __builtin_amdgcn_raw_buffer_load_b128(r, off, 0, AUX); }
template <int AUX> DI u32x2 gl64(rsrc_t r, unsigned off) { return __builtin_amdgcn_raw_buffer_load_b64(r, off, 0, AUX); }
template <int AUX> DI unsigned gl32(rsrc_t r, unsigned off) { return __builtin_amdgcn_raw_buffer_load_b32(r, off, 0, AUX); }
DI u32x4 asu(f32x4 v) { return __builtin_bit_cast(u32x4, v); }
DI f32x4 asf(u32x4 v) { return __builtin_bit_cast(f32x4, v); }
DI unsigned pk2(float a, float b) { f32x2 v = {a, b}; return __builtin_bit_cast(unsigned, __builtin_convertvector(v, bf16x2)); }
DI u32x2 pk4(f32x4 a) { return u32x2{pk2(a[0], a[1]), pk2(a[2], a[3])}; }
DI u32x4 pk8(f32x4 a, f32x4 b) { return u32x4{pk2(a[0], a[1]), pk2(a[2], a[3]), pk2(b[0], b[1]), pk2(b[2], b[3])}; }
DI u32x4 clean(u32x4 v) { return __builtin_elementwise_min(v, u32x4{EMPTY - 1, EMPTY - 1, EMPTY - 1, EMPTY - 1}); }
DI unsigned umax4(unsigned m, u32x4 v) { return max(max(max(m, v[0]), v[1]), max(v[2], v[3])); }
DI float bflo(unsigned u) { return __uint_as_float(u << 16); }
DI float bfhi(unsigned u) { return __uint_as_float(u & 0xFFFF0000u); }
DI int rowof(int p) { return 32 * (((p >> 5) & 0x3C) + (p & 3)) + ((p << 2) & 16) + ((p >> 3) & 15); }
DI int rpos(int p) { return ((p << 8) & 1024) + ((p >> 3) << 2) + (p & 3); }
DI int trow(int T, int r) { return 8 * (r >> 2) + 4 * T + (r & 3); }
template <int CTRL> DI float dpp_add(float v) { return v + __int_as_float(__builtin_amdgcn_update_dpp(0, __float_as_int(v), CTRL, 0xF, 0xF, true)); }
DI float row16_sum(float v) { v = dpp_add<0x128>(v); v = dpp_add<0x124>(v); v = dpp_add<0x122>(v); return dpp_add<0x121>(v); }
DI f32x4 mfma16(u32x4 a, u32x4 b, f32x4 c) {
  return __builtin_amdgcn_mfma_f32_16x16x32_bf16(__builtin_bit_cast(bf16x8, a), __builtin_bit_cast(bf16x8, b), c, 0, 0, 0);
}
DI void mfma16v(f32x4& c, u32x4 a, u32x4 b) { asm volatile("v_mfma_f32_16x16x32_bf16 %0, %1, %2, %0" : "+v"(c) : "v"(a), "v"(b)); }
DI u32x2 ldtr(lptr p) { return __builtin_bit_cast(u32x2, __builtin_amdgcn_ds_read_tr16_b64_v4i16((LDS_AS s16x4*)p)); }
DI u32x4 ld128(lptr p) { return *(LDS_AS u32x4*)p; }
DI u32x2 ld64(lptr p) { return *(LDS_AS u32x2*)p; }
#define GL(v, s, o, x) "global_load_lds_dwordx4 " v ", " s " offset:" #o x "\n\t"
#define M0SET "s_mov_b32 %[sv], m0\n\ts_mov_b32 m0, %[l]\n\ts_nop 2\n\t"
#define M0ADD(x) "s_add_u32 m0, m0, " x "\n\ts_nop 0\n\t"
#define M0RST "s_mov_b32 m0, %[sv]"
DI void dma2(unsigned v, const char* sb, unsigned l) { unsigned sv;
  asm volatile(M0SET GL("%[v]", "%[s]", 0, " sc1") GL("%[v]", "%[s]", 1024, " sc1") M0RST : [sv] "=&s"(sv) : [v] "v"(v), [s] "s"(sb), [l] "s"(l) : "memory"); }
DI void dma4(unsigned v, const char* sb, unsigned l) { unsigned sv;
  asm volatile(M0SET GL("%[v]", "%[s]", 0, "") GL("%[v]", "%[s]", 1024, "") GL("%[v]", "%[s]", 2048, "") GL("%[v]", "%[s]", 3072, "") M0RST
               : [sv] "=&s"(sv) : [v] "v"(v), [s] "s"(sb), [l] "s"(l) : "memory"); }
DI void dma6_sc1(unsigned v, const char* sb, unsigned l) { unsigned sv;
  asm volatile(M0SET GL("%[v]", "%[s]", 0, " sc1") GL("%[v]", "%[s]", 1024, " sc1") GL("%[v]", "%[s]", 2048, " sc1") M0ADD("3072")
               GL("%[w]", "%[s]", 0, " sc1") GL("%[w]", "%[s]", 1024, " sc1") GL("%[w]", "%[s]", 2048, " sc1") M0RST
               : [sv] "=&s"(sv) : [v] "v"(v), [w] "v"(v + 3072), [s] "s"(sb), [l] "s"(l) : "memory", "scc"); }
template <int N> DI void vm_bar() { asm volatile("s_waitcnt vmcnt(%0)\n\ts_barrier" ::"i"(N) : "memory"); }
DI void lds_bar() { asm volatile("s_waitcnt lgkmcnt(0)\n\ts_barrier" ::: "memory"); }
DI void raw_bar() { asm volatile("s_barrier" ::: "memory"); }
template <int N> DI void nap() { __builtin_amdgcn_s_sleep(N); asm volatile("" ::: "memory"); }
DI void drain() { asm volatile("s_waitcnt vmcnt(0)" ::: "memory"); }
#define CODE_ADDR(sym) ({ unsigned lo_, hi_; \
    asm volatile("s_getpc_b64 vcc\n\ts_add_u32 %0, vcc_lo, " sym "@rel32@lo+4\n\ts_addc_u32 %1, vcc_hi, " sym "@rel32@hi+12" : "=s"(lo_), "=s"(hi_) :: "vcc", "scc"); \
    (const unsigned*)(((unsigned long long)hi_ << 32) | lo_); })
#define SYM_MAIN "_Z6k_mainPKtS0_S0_S0_S0_PcPt"
constexpr int CODE_LINES = 360;
DI int wave_id() { return __builtin_amdgcn_readfirstlane(threadIdx.x >> 6); }
DI unsigned lds_addr(lptr p) { return (unsigned)(size_t)p; }

DI int kappa_to_i(int kap) {
  int ks = kap >> 5, g = (kap >> 3) & 3, e = kap & 7;
  if (ks < 16) return (2 * ks + (g >> 1)) * 20 + 8 * (e >> 2) + 4 * (g & 1) + (e & 3);
  return (8 * (ks - 16) + 2 * g + (e >> 2)) * 20 + 16 + (e & 3);
}

DI unsigned wo_frag(int nt, int ks) {
  const int ct = nt / 5, c5 = nt - 5 * ct, kh = ks < 16 ? ks >> 3 : (ks - 16) >> 1, i = ks < 16 ? ks & 7 : 8 + (ks & 1);
  const int a = kh ? (c5 >= 3 ? c5 - 3 : c5 + 2) : c5;
  return ((ct * 2 + kh) * 50 + 5 * i + a) * 1024;
}

constexpr int SLOT32 = 32768;
DI void dma_a(const unsigned* vm, const char* mb, unsigned l) { unsigned sv;
  asm volatile(M0SET GL("%[a0]", "%[m]", 0, " sc1") GL("%[a1]", "%[m]", 1024, " sc1") GL("%[a2]", "%[m]", 2048, " sc1") GL("%[a3]", "%[m]", 3072, " sc1") M0RST
               : [sv] "=&s"(sv) : [a0] "v"(vm[0]), [a1] "v"(vm[1]), [a2] "v"(vm[2]), [a3] "v"(vm[3]), [m] "s"(mb), [l] "s"(l) : "memory"); }
DI void dma_s(unsigned v0, unsigned v1, const char* sb, unsigned l) { unsigned sv;
  asm volatile(M0SET GL("%[v0]", "%[s]", 0, " sc1") GL("%[v0]", "%[s]", 1024, " sc1") GL("%[v1]", "%[s]", 2048, " sc1") GL("%[v1]", "%[s]", 3072, " sc1") M0RST
               : [sv] "=&s"(sv) : [v0] "v"(v0), [v1] "v"(v1), [s] "s"(sb), [l] "s"(l) : "memory"); }
DI void dma1_sc1(unsigned v, const char* sb, unsigned l) { unsigned sv;
  asm volatile(M0SET GL("%[v]", "%[s]", 0, " sc1") M0RST : [sv] "=&s"(sv) : [v] "v"(v), [s] "s"(sb), [l] "s"(l) : "memory"); }
DI void dma1(unsigned v, const char* sb, unsigned l) { unsigned sv;
  asm volatile(M0SET GL("%[v]", "%[s]", 0, "") M0RST : [sv] "=&s"(sv) : [v] "v"(v), [s] "s"(sb), [l] "s"(l) : "memory"); }

constexpr int L_I2 = 40 * 1056, LDS_PREP = L_I2 + 20 * 2064 + 1024;
DI void prep(const u16* x, const u16* wr, const u16* wi, const u16* wqkv, const u16* wo, char* ws) {
  extern __shared__ __attribute__((aligned(16))) char smem[];
  const lptr lds = (lptr)smem;
  const int tid = threadIdx.x, lane = tid & 63, w = wave_id(), g = lane >> 4, r16 = lane & 15;
  const int job = blockIdx.x;
  if (__builtin_expect(job < 144, 1)) {
    const bool cols = job >= 64 && job < 124;
    const int cj = job - 64, cb = cj < 48 ? (cj & 48) + 2 * (cj & 7) + ((cj >> 3) & 1) : cj;
    u32x4 a1[3], m2[3][2]; u32x2 a1b[3];
    if (w < 5) dma1(tid * 16, (const char*)&g_dt, lds_addr(lds) + LDS_PREP + w * 1024);
    if (cols) {
#pragma unroll
      for (int i = 0; i < 5; i++) dma1((16 * (5 * w + i) + (lane >> 2)) * 3840 + cb * 64 + (lane & 3) * 16, (const char*)wqkv, lds_addr(lds) + (5 * w + i) * 1056);
    } else {
      const char* src = (const char*)(job < 64 ? x : wo);
      const int sl = 8 * (w & 3) + 4 * (w >> 2);
      src += (job < 64 ? rowof(32 * job + sl) : kappa_to_i(32 * (job - 124) + sl)) * 1280;
      const int st = (job < 64 ? 31 : 0) * 1280;
#pragma unroll
      for (int i = 0; i < 5; i++) dma1(1024 * i + lane * 16 + ((i == 4 || lane < 16 * i) ? i - 1 : i) * st, src, lds_addr(lds) + (5 * w + i) * 1024);
    }
    unsigned so[3][2]; float sc[2] = {1.f, 1.f};
    if (cols) {
#pragma unroll
      for (int m = 0; m < 2; m++) {
        const int c = 32 * cb + 16 * m + r16;
        const int which = c / 640, rem = c - which * 640, head = rem / 20, d = rem - head * 20, grp = d >> 2;
        const int T = grp < 4 ? (which == 2 ? 2 : 0) + (grp & 1) : (which == 1 ? 3 : 2), gs = grp < 4 ? (which == 1 ? 2 : 0) + (grp >> 1) : (which == 2 ? 3 : 2);
        if (which == 0) sc[m] = 0.22360679774997896f * 1.4426950408889634f;
#pragma unroll
        for (int i = 0; i < 3; i++) so[i][m] = WS_WQ + ((head * 4 + T) * 20 + w + 8 * i) * 1024 + (g * 16 + 4 * gs + (d & 3)) * 16;
      }
    } else if (job < 64) {
#pragma unroll
      for (int i = 0; i < 3; i++)
#pragma unroll
        for (int T = 0; T < 2; T++) so[i][T] = WS_RB0 + (job * 641 + (job & 1) + 32 * (w + 8 * i) + trow(T, r16)) * 64 + g * 16;
    } else {
#pragma unroll
      for (int i = 0; i < 3; i++) {
        const unsigned f = wo_frag(2 * (w + 8 * i) + (r16 >> 3), job - 124);
#pragma unroll
        for (int T = 0; T < 2; T++) so[i][T] = WS_WOF + f + (g * 16 + (trow(T, r16) & 15)) * 16;
      }
    }
    if (w >= 4) { so[2][0] = WS_END; so[2][1] = WS_END; }
    asm volatile("" : "+v"(so[0][0]), "+v"(so[0][1]), "+v"(so[1][0]), "+v"(so[1][1]), "+v"(so[2][0]), "+v"(so[2][1]), "+v"(sc[0]), "+v"(sc[1]));
    const f32x4 z4 = {0.f, 0.f, 0.f, 0.f};
    const lptr i2 = lds + L_I2;
    drain();
#pragma unroll
    for (int i = 0; i < 3; i++)
#pragma unroll
      for (int T = 0; T < 2; T++)
        asm volatile("global_load_dwordx4 %0, %1, %2" : "=v"(m2[i][T]) : "v"(((((i < 2 || w < 4) ? w + 8 * i : w) * 2 + T) * 64 + lane) * 16), "s"(&g_dt.m2[0][0][0][0]) : "memory");
    raw_bar();
#pragma unroll
    for (int T = 0; T < 3; T++) { a1[T] = ld128(lds + LDS_PREP + T * 1024 + lane * 16); a1b[T] = ld64(lds + LDS_PREP + 3072 + T * 512 + lane * 8); }
    f32x4 t1[4][3];
    if (cols) {
      const lptr src = lds + (4 * g + (r16 >> 2)) * 1056 + (w >> 1) * 256 + (w & 1) * 32 + (lane & 3) * 8, srb = src + (32 - 8 * (g >> 1)) * 1056;
#pragma unroll
      for (int i = 0; i < 4; i++) {
        const u32x2 f0 = ldtr(src + i * 64), f1 = ldtr(src + 16 * 1056 + i * 64), f2 = ldtr(srb + i * 64);
#pragma unroll
        for (int T = 0; T < 3; T++) t1[i][T] = mfma16(u32x4{a1b[T][0], a1b[T][1], 0u, 0u}, u32x4{f2[0], f2[1], 0u, 0u}, mfma16(a1[T], u32x4{f0[0], f0[1], f1[0], f1[1]}, z4));
      }
      const int xo = ((w >> 1) * 8) ^ (((r16 >> 1) & 3) * 16);
      const lptr dst = i2 + 2 * g * 2064 + (16 * (w & 1) + r16) * 64;
#pragma unroll
      for (int T = 0; T < 3; T++)
#pragma unroll
        for (int e = 0; e < 4; e++)
          if (T < 2 || g < 2) *(LDS_AS u32x2*)(dst + (8 * T + (e >> 1)) * 2064 + (xo ^ ((e & 1) * 32))) = u32x2{pk2(t1[0][T][e], t1[1][T][e]), pk2(t1[2][T][e], t1[3][T][e])};
    } else {
      const lptr src = lds + w * 5120 + lane * 8, srb = lds + w * 5120 + 1024 + (lane & 31) * 8;
#pragma unroll
      for (int i = 0; i < 4; i++) {
        const u32x2 f0 = ldtr(src + i * 1280), f1 = ldtr(src + i * 1280 + 512), f2 = ldtr(srb + i * 1280);
#pragma unroll
        for (int T = 0; T < 3; T++) t1[i][T] = mfma16(u32x4{f2[0], f2[1], 0u, 0u}, u32x4{a1b[T][0], a1b[T][1], 0u, 0u}, mfma16(u32x4{f0[0], f0[1], f1[0], f1[1]}, a1[T], z4));
      }
      const int xo = ((r16 & 1) * 32 + g * 8) ^ ((w & 1) * 32);
      const lptr dst = i2 + (r16 >> 1) * 2064 + w * 256;
#pragma unroll
      for (int i = 0; i < 4; i++)
#pragma unroll
        for (int T = 0; T < 3; T++)
          if (T < 2 || r16 < 8) *(LDS_AS u32x2*)(dst + T * 8 * 2064 + i * 64 + (xo ^ ((i >> 1) * 16))) = pk4(t1[i][T]);
    }
    lds_bar();
    const rsrc_t rw = mk_rsrc(ws, WS_END);
    asm volatile("s_waitcnt vmcnt(0)" : "+v"(m2[0][0]), "+v"(m2[0][1]), "+v"(m2[1][0]), "+v"(m2[1][1]), "+v"(m2[2][0]), "+v"(m2[2][1]));
    f32x4 sq[2] = {z4, z4};
    u32x4 f[3][2];
#pragma unroll
    for (int i = 0; i < 3; i++) {
      const lptr fp = i2 + ((i < 2 || w < 4) ? w + 8 * i : 0) * 2064 + r16 * 64 + ((g ^ ((r16 >> 1) & 3)) * 16);
      f[i][0] = ld128(fp); f[i][1] = ld128(fp + 1024);
    }
    if (cols) {
#pragma unroll
      for (int i = 0; i < 3; i++)
#pragma unroll
        for (int m = 0; m < 2; m++) st128<16>(rw, so[i][m], pk8(mfma16(m2[i][0], f[i][m], z4) * sc[m], mfma16(m2[i][1], f[i][m], z4) * sc[m]));
    } else {
#pragma unroll
      for (int i = 0; i < 3; i++)
#pragma unroll
        for (int T = 0; T < 2; T++) {
          const f32x4 d0 = mfma16(f[i][0], m2[i][T], z4), d1 = mfma16(f[i][1], m2[i][T], z4);
          st128<16>(rw, so[i][T], pk8(d0, d1));
          const float k = ((i == 2 && w >= 4) || (i == 0 && T == 0 && tid < 64 && r16 == 1)) ? 0.f : 1.f;
          sq[0] += d0 * d0 * k; sq[1] += d1 * d1 * k;
        }
    }
    LDS_AS float* sl = (LDS_AS float*)(lds + LDS_PREP - 1024);
    if (job < 64) {
#pragma unroll
      for (int m = 0; m < 2; m++) {
#pragma unroll
        for (int e = 0; e < 4; e++) sq[m][e] = row16_sum(sq[m][e]);
        if (r16 == 0) *(LDS_AS f32x4*)(sl + w * 32 + 8 * g + 4 * m) = sq[m];
      }
    }
    if (job < 64) {
      lds_bar();
      if (tid < 32) {
        float v = 0.f;
#pragma unroll
        for (int i = 0; i < 8; i++) v += sl[i * 32 + tid];
        st32<16>(rw, WS_RS1 + rpos(32 * job + tid) * 4, __float_as_uint(__builtin_amdgcn_rsqf((v + 640.f * 1e-5f) * RSK)));
      }
    }
    return;
  }
  const int mi = job - 144;
  const rsrc_t rw = mk_rsrc(ws, WS_END);
  if (mi < 64) {
    const int m = mi >> 3, oc = mi & 7;
    const char* src = (const char*)((w >> 2) ? wi : wr) + (64 * (4 * m + (w & 3)) + 8 * oc) * 642;
    dma4(lane * 16, src, lds_addr(lds) + w * 5152);
    dma1(4096 + lane * 16, src, lds_addr(lds) + w * 5152 + 4096);
    if (lane == 0) dma1(5120, src, lds_addr(lds) + w * 5152 + 5120);
    vm_bar<0>();
    LDS_AS u16* L = (LDS_AS u16*)lds + (tid & 7) * 321;
#pragma unroll
    for (int it = 0; it < 5; it++) {
      const int c = (tid >> 3) + 64 * it, col = freq_of(c >> 4, c & 15);
      u32x4 o;
#pragma unroll
      for (int e = 0; e < 4; e++) o[e] = (unsigned)L[e * 2576 + col] | ((unsigned)L[(4 + e) * 2576 + col] << 16);
      st128<16>(rw, WS_WF + c * 8192 + (m * 64 + 8 * oc + (tid & 7)) * 16, o);
    }
#pragma unroll
    for (int it = 0; it < 3; it++) st128<16>(rw, WS_RB1 + ((it * 64 + mi) * 512 + tid) * 16, u32x4{EMPTY, EMPTY, EMPTY, EMPTY});
  } else if (mi < 84) {
    unsigned pf0;
    {
      const unsigned* cm = CODE_ADDR(SYM_MAIN);
      const unsigned* p = mi >= 80 ? (const unsigned*)&g_it + min(tid, 359) * 32 : (mi & 8) ? (const unsigned*)&g_dt.m2[0][0][0][0] + min(tid, 319) * 32 : tid < 80 ? (const unsigned*)&g_ct + tid * 32 : cm + min(tid - 80, CODE_LINES - 1) * 32;
      asm volatile("global_load_dword %0, %1, off" : "=v"(pf0) : "v"(p) : "memory");
    }
    const char* src = (const char*)wo + kappa_to_i(32 * (mi - 64) + 8 * (w & 3) + 4 * (w >> 2)) * 1280;
    dma4(lane * 16, src, lds_addr(lds) + w * 5152);
    dma1(4096 + lane * 16, src, lds_addr(lds) + w * 5152 + 4096);
    asm volatile("s_waitcnt vmcnt(0)\n\ts_barrier" : "+v"(pf0) :: "memory");
#pragma unroll
    for (int i = 0; i < 5; i++) {
      const lptr a = lds + g * 5152 + (r16 >> 2) * 1280 + (5 * w + i) * 32 + (lane & 3) * 8;
      const u32x2 lo = ldtr(a), hi = ldtr(a + 4 * 5152);
      st128<16>(rw, WS_WOP + wo_frag(5 * w + i, mi - 64) + lane * 16, u32x4{lo[0], lo[1], hi[0], hi[1]});
    }
  } else {
    const int i0 = (mi - 84) * 512 + tid;
    nap<K1DELAY>();
    if (i0 < 6272) st128<16>(rw, WS_FL + i0 * 16, u32x4{0u, 0u, 0u, 0u});
    if (i0 < 10240) st128<16>(rw, WS_WQ + (((i0 / 320) * 4 + 3) * 80 + (i0 % 320 >> 2)) * 256 + (12 + (i0 & 3)) * 16, u32x4{0u, 0u, 0u, 0u});
#pragma unroll
    for (int it = 0; it < 6; it++) { const int i = 98304 + i0 + it * 14336; if (i < (int)RBSZ / 16) st128<16>(rw, WS_RB1 + i * 16, u32x4{EMPTY, EMPTY, EMPTY, EMPTY}); }
  }
}

#define PINF(x) asm volatile("" : "+v"(x))
DI void wave_sync(LDS_AS unsigned* c, unsigned n) {
  asm volatile("s_waitcnt lgkmcnt(0)" ::: "memory");
  if ((threadIdx.x & 63) == 0) __hip_atomic_fetch_add(c, 1u, __ATOMIC_RELAXED, __HIP_MEMORY_SCOPE_WORKGROUP);
  while (__builtin_amdgcn_readfirstlane(*(volatile LDS_AS unsigned*)c) < n) {}
  asm volatile("" ::: "memory");
}
DI void put_scale(lptr lds, int q, int tid, const u32x2* sp) {
  f32x4 r = {640.f * 1e-5f, 640.f * 1e-5f, 640.f * 1e-5f, 640.f * 1e-5f};
#pragma unroll
  for (int i = 0; i < 8; i++) r += f32x4{bflo(sp[i][0]), bfhi(sp[i][0]), bflo(sp[i][1]), bfhi(sp[i][1])};
#pragma unroll
  for (int e = 0; e < 4; e++) r[e] = __builtin_amdgcn_rsqf(r[e] * RSK);
  ((LDS_AS f32x4*)(lds + 2 * SLOT32))[q * 256 + tid] = r;
}
struct ColIn { u32x4 c64, s64, c32, s32, wf[2]; };
DI int col_of() { return blockIdx.x < 64 ? 192 + 2 * blockIdx.x + (wave_id() >> 2) : blockIdx.x - 64; }
DI void col_load(ColIn& k, rsrc_t rw, bool tab, bool filt) {
  int lane = threadIdx.x & 63; PINF(lane);
  const int w = wave_id() & 3;
  const u32x4* ct = (const u32x4*)&g_ct + lane;
  if (tab) { k.c64 = ct[(2 * w) * 64]; k.s64 = ct[(2 * w + 1) * 64]; k.c32 = ct[8 * 64]; k.s32 = ct[9 * 64]; }
  if (filt) {
#pragma unroll
    for (int t = 0; t < 2; t++) k.wf[t] = gl128<16>(rw, WS_WF + col_of() * 8192 + ((4 * t + (lane >> 4)) * 64 + 16 * w + (lane & 15)) * 16);
  }
}
template <bool FIRST>
DI void col_phase(rsrc_t rw, lptr lds, ColIn kin) {
  int lane = threadIdx.x & 63; PINF(lane);
  const int wv = wave_id(), w = wv & 3, q = wv >> 2, tid = w * 64 + lane, g = lane >> 4, r16 = lane & 15;
  const int c = col_of();
  const lptr img = lds + q * SLOT32;
  f32x4 xr[2], xi[2], rs[2]; u32x4 xb[2];
  u32x4 c64[2], s64[2], n64[2], c32[2], s32[2], n32[2];
  if (FIRST) col_load(kin, rw, true, false);
  SB();
  f32x4 t1c[2], t1s[2], t2c[2], t2s[2];
  {
    const float f1 = (float)(16 * w + r16) * (1.f / 2048.f), b1 = (float)(4 * g), f2 = (float)(16 * w + 4 * g) * (1.f / 2048.f), b2 = (float)r16;
#pragma unroll
    for (int t = 0; t < 2; t++) {
#pragma unroll
      for (int e = 0; e < 4; e++) {
        const float a1 = (b1 + (float)(16 * t + e)) * f1, a2 = (b2 + (float)(16 * t)) * (f2 + (float)e * (1.f / 2048.f));
        t1c[t][e] = __builtin_amdgcn_cosf(a1); t1s[t][e] = __builtin_amdgcn_sinf(-a1);
        t2c[t][e] = __builtin_amdgcn_cosf(a2); t2s[t][e] = __builtin_amdgcn_sinf(a2);
      }
      PINF(t1c[t]); PINF(t1s[t]); PINF(t2c[t]); PINF(t2s[t]);
    }
  }
  int wo_ = r16 * 128 + (((2 * w + (g >> 1)) ^ (r16 >> 1)) * 16) + (g & 1) * 8, ro_ = r16 * 128 + ((g ^ (r16 >> 1)) * 16), ro2 = ro_ ^ 64;
  PINF(wo_); PINF(ro_); PINF(ro2);
  {
    const unsigned par = (lane & 1) ? 0x80008000u : 0u;
    c64[0] = kin.c64; s64[0] = kin.s64; c32[0] = kin.c32; s32[0] = kin.s32;
    c64[1] = c64[0] ^ par; s64[1] = s64[0] ^ par; n64[0] = s64[0] ^ 0x80008000u; n64[1] = s64[1] ^ 0x80008000u;
    c32[1] = c32[0] ^ 0x80000000u; s32[1] = s32[0] ^ 0x80000000u; n32[0] = s32[0] ^ 0x80008000u; n32[1] = s32[0] ^ 0x00008000u;
    PINF(c64[1]); PINF(s64[1]); PINF(n64[0]); PINF(n64[1]); PINF(c32[1]); PINF(s32[1]); PINF(n32[0]); PINF(n32[1]);
  }
  SB();
  if (FIRST) {
    raw_bar();
#pragma unroll
    for (int t = 0; t < 2; t++) { xb[t] = gl128<16>(rw, WS_RB0 + ((tid >> 2) * 641 + ((tid >> 2) & 1) + 2 * c + t) * 64 + (tid & 3) * 16); rs[t] = asf(gl128<16>(rw, WS_RS1 + (1024 * t + 4 * tid) * 4)); }
    col_load(kin, rw, false, true);
    SB();
#pragma unroll
    for (int t = 0; t < 2; t++) {
      xr[t] = f32x4{bflo(xb[0][2 * t]), bfhi(xb[0][2 * t]), bflo(xb[0][2 * t + 1]), bfhi(xb[0][2 * t + 1])};
      xi[t] = f32x4{bflo(xb[1][2 * t]), bfhi(xb[1][2 * t]), bflo(xb[1][2 * t + 1]), bfhi(xb[1][2 * t + 1])};
    }
  } else {
    drain();
    u32x2 sp[8];
    const unsigned so = WS_SSQ + (blockIdx.x & 7) * 32768 + (1024 * q + 4 * tid) * 2, xo = WS_R3 + ((tid >> 2) * 642 + 2 * c) * 64 + (tid & 3) * 16;
    bool bs = true, bx = true;
#pragma unroll 1
    for (int spins = 0; spins < (1 << 18); spins++) {
      if (bx) {
#pragma unroll
        for (int t = 0; t < 2; t++) { xr[t] = asf(gl128<16>(rw, xo + 64 * 642 * 64 * t)); xi[t] = asf(gl128<16>(rw, xo + 64 * 642 * 64 * t + 64)); }
      }
      if (bs) {
#pragma unroll
        for (int i = 0; i < 8; i++) sp[i] = gl64<16>(rw, so + i * 4096);
      }
      unsigned m = 0;
#pragma unroll
      for (int t = 0; t < 2; t++) m = umax4(umax4(m, asu(xr[t])), asu(xi[t]));
      bx = __builtin_amdgcn_ballot_w64(m == EMPTY) != 0;
      m = 0;
#pragma unroll
      for (int i = 0; i < 8; i++) m = max(max(m, sp[i][0]), sp[i][1]);
      bs = __builtin_amdgcn_ballot_w64(m == EMPTY) != 0;
      if (!bs && !bx) break;
      nap<YSLEEP>();
    }
    put_scale(lds, q, tid, sp);
    LDS_AS f32x4* ex = (LDS_AS f32x4*)(lds + 2 * SLOT32) + tid;
    lds_bar();
    rs[0] = ex[0]; rs[1] = ex[256];
  }
#pragma unroll
  for (int t = 0; t < 2; t++) {
    u32x2 pb = pk4(xi[t] * rs[t]);
    if (c == 0) pb = u32x2{0u, 0u};
    *(LDS_AS u32x2*)(img + wo_ + t * 2048) = pk4(xr[t] * rs[t]);
    *(LDS_AS u32x2*)(img + 4096 + wo_ + t * 2048) = pb;
  }
  lds_bar();
  u32x4 ur[2][2], ui[2][2], br, bi;
  f32x4 ar[2], ai[2];
  const f32x4 z4 = {0.f, 0.f, 0.f, 0.f};
  auto rd_in = [&](int off) {
#pragma unroll
    for (int ks = 0; ks < 2; ks++)
#pragma unroll
      for (int t = 0; t < 2; t++) { const lptr p = img + off + (ks ? ro2 : ro_) + t * 2048; ur[t][ks] = ld128(p); ui[t][ks] = ld128(p + 4096); }
    SB();
  };
  auto cmul = [&](const f32x4* cc, const f32x4* sn) {
    br = pk8(ar[0] * cc[0] - ai[0] * sn[0], ar[1] * cc[1] - ai[1] * sn[1]);
    bi = pk8(ai[0] * cc[0] + ar[0] * sn[0], ai[1] * cc[1] + ar[1] * sn[1]);
  };
  rd_in(0);
#pragma unroll
  for (int ks = 0; ks < 2; ks++) {
#pragma unroll
    for (int t = 0; t < 2; t++) { ar[t] = mfma16(ur[t][ks], c64[ks], ks ? ar[t] : z4); ai[t] = mfma16(ui[t][ks], c64[ks], ks ? ai[t] : z4); }
#pragma unroll
    for (int t = 0; t < 2; t++) { ar[t] = mfma16(ui[t][ks], s64[ks], ar[t]); ai[t] = mfma16(ur[t][ks], n64[ks], ai[t]); }
  }
  const f32x4 fa[2] = {asf(kin.wf[0] << 16), asf(kin.wf[1] << 16)}, fb[2] = {asf(kin.wf[0] & 0xFFFF0000u), asf(kin.wf[1] & 0xFFFF0000u)};
  cmul(t1c, t1s);
#pragma unroll
  for (int t = 0; t < 2; t++) { ar[t] = mfma16(c32[t], br, z4); ai[t] = mfma16(c32[t], bi, z4); }
#pragma unroll
  for (int t = 0; t < 2; t++) { ar[t] = mfma16(s32[t], bi, ar[t]); ai[t] = mfma16(n32[t], br, ai[t]); }
  cmul(fa, fb);
#pragma unroll
  for (int t = 0; t < 2; t++) { ar[t] = mfma16(br, c32[t], z4); ai[t] = mfma16(bi, c32[t], z4); }
#pragma unroll
  for (int t = 0; t < 2; t++) { ar[t] = mfma16(bi, n32[t], ar[t]); ai[t] = mfma16(br, s32[t], ai[t]); }
  cmul(t2c, t2s);
#pragma unroll
  for (int t = 0; t < 2; t++) {
    *(LDS_AS u32x2*)(img + 8192 + wo_ + t * 2048) = u32x2{br[2 * t], br[2 * t + 1]};
    *(LDS_AS u32x2*)(img + 12288 + wo_ + t * 2048) = u32x2{bi[2 * t], bi[2 * t + 1]};
  }
  lds_bar();
  rd_in(8192);
#pragma unroll
  for (int ks = 0; ks < 2; ks++) {
#pragma unroll
    for (int t = 0; t < 2; t++) { ar[t] = mfma16(c64[ks], ur[t][ks], ks ? ar[t] : xr[t]); ai[t] = mfma16(c64[ks], ui[t][ks], ks ? ai[t] : xi[t]); }
#pragma unroll
    for (int t = 0; t < 2; t++) { ar[t] = mfma16(n64[ks], ui[t][ks], ar[t]); ai[t] = mfma16(s64[ks], ur[t][ks], ai[t]); }
  }
  const unsigned ob = (FIRST ? WS_RB1 : WS_RB2) + (2 * c * RBP + 8 * tid) * 2;
  st128<16>(rw, ob, clean(pk8(ar[0], ar[1])));
  st128<16>(rw, ob + RBP * 2, c == 0 ? u32x4{0u, 0u, 0u, 0u} : clean(pk8(ai[0], ai[1])));
}

DI f32x16 mfma32(u32x4 a, u32x4 b, f32x16 c) {
  return __builtin_amdgcn_mfma_f32_32x32x16_bf16(__builtin_bit_cast(bf16x8, a), __builtin_bit_cast(bf16x8, b), c, 0, 0, 0);
}
DI f32x16 mfma32k8(u32x2 a, u32x2 b, f32x16 c) {
  return __builtin_amdgcn_mfma_f32_32x32x8bf16_1k(__builtin_bit_cast(s16x4, a), __builtin_bit_cast(s16x4, b), c, 0, 0, 0);
}
DI f32x16 scores(u32x4 k1, u32x2 k2, u32x4 q1, u32x2 q2) {
  f32x16 z;
#pragma unroll
  for (int i = 0; i < 16; i++) z[i] = 0.f;
  f32x16 s = mfma32(k1, q1, z);
  asm volatile("s_nop 11" : "+v"(s));
  return mfma32k8(k2, q2, s);
}
DI float max16(const f32x16& s) {
  float m = fmaxf(s[0], s[1]);
#pragma unroll
  for (int i = 2; i < 16; i++) m = fmaxf(m, s[i]);
  return m;
}
#define STR_(x) #x
#define STR(x) STR_(x)
#define KOF(t) ((((t)&7)>>1)*11264+((t)&1)*1280)
#define VOF(t) ((((t)&7)>>1)*11264+5120+((t)&1)*1536)
#define EXP(d, s) "v_exp_f32 v" #d ", v" #s "\n\t"
#define CVT(d, s) "v_cvt_pk_bf16_f32 v" #d ", v" #s ", v[" #s "+1]\n\t"
#define STEPA(t) \
  "v_mfma_f32_32x32x16_bf16 v[48:63], v[80:83], v[136:139], v[120:135]\n\t" \
  "ds_read_b128 v[80:83], v100 offset:" STR(KOF(t+1)) "\n\t" \
  EXP(64,32) EXP(65,33) CVT(78,68) CVT(79,70) EXP(66,34) EXP(67,35) \
  "v_mfma_f32_32x32x16_bf16 v[104:119], v[90:93], v[76:79], v[104:119]\n\t" \
  "ds_read_b64_tr_b16 v[90:91], v102 offset:" STR(VOF(t)+768) "\n\t" \
  "ds_read_b64_tr_b16 v[92:93], v102 offset:" STR(VOF(t)+1152) "\n\t" \
  EXP(68,36) EXP(69,37) CVT(72,64) CVT(73,66) EXP(70,38) EXP(71,39) \
  "v_mfma_f32_32x32x8_bf16 v[48:63], v[84:85], v[140:141], v[48:63]\n\t" \
  "ds_read_b64 v[84:85], v101 offset:" STR(KOF(t+1)) "\n\t" \
  EXP(64,40) EXP(65,41) CVT(74,68) CVT(75,70) EXP(66,42) EXP(67,43) \
  "s_waitcnt lgkmcnt(4)\n\t" \
  "v_mfma_f32_32x32x16_bf16 v[0:15], v[86:89], v[72:75], v[0:15]\n\t" \
  EXP(68,44) EXP(69,45) CVT(76,64) CVT(77,66) EXP(70,46) EXP(71,47)
#define STEPB(t) \
  "s_waitcnt lgkmcnt(0)\n\t" \
  "v_mfma_f32_32x32x16_bf16 v[32:47], v[80:83], v[94:97], v[16:31]\n\t" \
  EXP(64,48) EXP(65,49) CVT(78,68) CVT(79,70) EXP(66,50) EXP(67,51) \
  "v_mfma_f32_32x32x16_bf16 v[0:15], v[90:93], v[76:79], v[0:15]\n\t" \
  EXP(68,52) EXP(69,53) CVT(72,64) CVT(73,66) EXP(70,54) EXP(71,55) \
  "v_mfma_f32_32x32x8_bf16 v[32:47], v[84:85], v[98:99], v[32:47]\n\t" \
  EXP(64,56) EXP(65,57) CVT(74,68) CVT(75,70) EXP(66,58) EXP(67,59) \
  "v_mfma_f32_32x32x16_bf16 v[104:119], v[86:89], v[72:75], v[104:119]\n\t" \
  "ds_read_b64_tr_b16 v[86:87], v102 offset:" STR(VOF(t+1)) "\n\t" \
  "ds_read_b64_tr_b16 v[88:89], v102 offset:" STR(VOF(t+1)+384) "\n\t" \
  EXP(68,60) EXP(69,61) CVT(76,64) CVT(77,66) EXP(70,62) EXP(71,63)
#define PR(n) "s_setprio " #n "\n\t"
#define CHUNK(i) PR(1) STEPA(2*(i)) PR(0) STEPB(2*(i)) "s_barrier\n\t" PR(2) STEPA(2*(i)+1) STEPB(2*(i)+1)
#define Z4(a) "v_mov_b64 v[" #a ":" #a "+1], 0\n\tv_mov_b64 v[" #a "+2:" #a "+3], 0\n\t"

#define R4(b) "v[" STR(b) ":" STR(b) "+3]"
#define MM(d, a, b) "v_mfma_f32_16x16x32_bf16 " R4(d) ", " R4(a) ", " R4(b) ", " R4(d) "\n\t"
#define GM(B, m, i) MM(16*m+4*i, B+8+4*i, B+4*m)
#define RT(B, m, hf, k) "ds_read_b64_tr_b16 v[" STR(B+4*m+2*hf) ":" STR(B+4*m+2*hf+1) "], v[84+" #m "] offset:" STR(k*8192+hf*1024) "\n\t"
#define RS(B, i, k) "ds_read_b128 " R4(B+8+4*i) ", v86 offset:" STR(i*2048+k*1024) "\n\t"
#define GSTEP(B, C, k) GM(B,0,0) RT(C,0,0,k) RT(C,0,1,k) GM(B,1,0) RS(C,0,k) RT(C,1,0,k) GM(B,0,1) RT(C,1,1,k) RS(C,1,k) GM(B,1,1) RS(C,2,k) \
  GM(B,0,2) RS(C,3,k) GM(B,1,2) GM(B,0,3) GM(B,1,3) MM(32,B,B)
#define GEMM_ASM(EXTRA) \
  Z4(0) Z4(4) Z4(8) Z4(12) Z4(16) Z4(20) Z4(24) Z4(28) Z4(32) "s_mov_b32 %[c], 0\n\ts_barrier\n\t" \
  RT(36,0,0,0) RT(36,0,1,0) RS(36,0,0) RT(36,1,0,0) RT(36,1,1,0) RS(36,1,0) RS(36,2,0) RS(36,3,0) EXTRA \
  "L_gemm_%=:\n\ts_add_u32 %[c], %[c], 1\n\ts_and_b32 %[t], %[c], 3\n\ts_cselect_b32 %[t], 1, -3\n\ts_lshl_b32 %[t], %[t], 15\n\ts_waitcnt lgkmcnt(0)\n\t" \
  GSTEP(36,60,1) \
  "v_add_u32 v84, %[t], v84\n\tv_add_u32 v85, %[t], v85\n\tv_add_u32 v86, %[t], v86\n\ts_waitcnt lgkmcnt(0)\n\ts_barrier\n\ts_setprio 1\n\t" \
  GSTEP(60,36,0) "s_setprio 0\n\t" \
  "s_cmp_lg_u32 %[c], 9\n\ts_cbranch_scc1 L_gemm_%=\n\ts_waitcnt lgkmcnt(0)\n\t" \
  GSTEP(36,60,1) "s_waitcnt lgkmcnt(0)\n\t" \
  MM(32,60,60) GM(60,0,0) GM(60,1,0) GM(60,0,1) GM(60,1,1) "s_nop 4\n\t" \
  "v_cndmask_b32 v36, v32, v33, %[m1]\n\tv_cndmask_b32 v37, v34, v35, %[m1]\n\tv_mov_b32 v38, 0x3727c5ac\n\tv_cndmask_b32 v36, v36, v37, %[m2]\n\t" \
  "v_fmac_f32 v38, 0x3acccccd, v36\n\tv_cmp_u_f32 vcc, v36, v36\n\tv_rsq_f32 v38, v38\n\ts_cbranch_vccz L_gok_%=\n\tds_write_b32 v90, v90\n\t" \
  "L_gok_%=:\n\tds_write_b32 v87, v38\n\ts_waitcnt lgkmcnt(0)\n\ts_barrier\n\t" \
  "ds_read_b32 v36, v88\n\tds_read_b32 v37, v89\n\tds_read_b32 v38, v90\n\t" \
  GM(60,0,2) GM(60,1,2) GM(60,0,3) GM(60,1,3) "s_waitcnt vmcnt(0) lgkmcnt(0)"
#define GEMM_OUT "={v[0:3]}"(acc[0][0]), "={v[4:7]}"(acc[0][1]), "={v[8:11]}"(acc[0][2]), "={v[12:15]}"(acc[0][3]), \
    "={v[16:19]}"(acc[1][0]), "={v[20:23]}"(acc[1][1]), "={v[24:27]}"(acc[1][2]), "={v[28:31]}"(acc[1][3]), \
    "={v36}"(rs[0]), "={v37}"(rs[1]), "={v38}"(vote), "+{v84}"(am0), "+{v85}"(am1), "+{v86}"(as), [c] "=&s"(c_), [t] "=&s"(t_)
#define GEMM_IN "{v87}"(a_w), "{v88}"(a_r[0]), "{v89}"(a_r[1]), "{v90}"(a_v), [m1] "s"(0xAAAAAAAAAAAAAAAAull), [m2] "s"(0xCCCCCCCCCCCCCCCCull)
#define GEMM_CLOB "memory", "scc", "vcc", "v32", "v33", "v34", "v35", "v39", "v40", "v41", "v42", "v43", "v44", "v45", "v46", "v47", "v48", "v49", "v50", \
    "v51", "v52", "v53", "v54", "v55", "v56", "v57", "v58", "v59", "v60", "v61", "v62", "v63", "v64", "v65", "v66", "v67", "v68", "v69", "v70", "v71", \
    "v72", "v73", "v74", "v75", "v76", "v77", "v78", "v79", "v80", "v81", "v82", "v83"
#define ITL(d, a) "global_load_dwordx4 " R4(d) ", v" #a ", %[it]\n\tglobal_load_dwordx4 " R4(d+4) ", v" #a ", %[it] offset:1024\n\t"

constexpr int HR = 4 * KVC;
constexpr int L_A = 2 * HR;
constexpr int L_EX = 15360;
constexpr int L_T = 4 * SLOT32, L_IA = L_T + 10240, L_U = L_A + 10240;
constexpr int L_MISC = L_A + 70 * 1024;
constexpr int LDS_FUSED = L_MISC + 2048;

DI bool poll16(rsrc_t rw, unsigned off, unsigned need) {
  const int lane = threadIdx.x & 63;
#pragma unroll 1
  for (int spins = 0; spins < (1 << 20); spins++) {
    const unsigned v = gl32<16>(rw, off + (lane & 15) * 4);
    const unsigned got = (unsigned)__builtin_amdgcn_ballot_w64(v != 0) & 0xFFFFu;
    if ((got & need) == need) return true;
    nap<1>();
  }
  return false;
}

DI void poll_aux(rsrc_t rw, unsigned off) {
#pragma unroll 1
  for (int spins = 0; spins < (1 << 20); spins++) {
    const u32x4 v = gl128<16>(rw, off + (threadIdx.x & 63) * 16);
    if (!__builtin_amdgcn_ballot_w64(min(min(v[0], v[1]), min(v[2], v[3])) == 0u)) break;
    nap<4>();
  }
}
DI void poll_tag(rsrc_t rw, u32x2 tag, unsigned long long need) {
  const unsigned o = WS_F0 + (blockIdx.x & 7) * 2048 + (threadIdx.x & 63) * 32;
#pragma unroll 1
  for (int spins = 0; spins < (1 << 20); spins++) {
    const u32x4 a = gl128<16>(rw, o), b = gl128<16>(rw, o + 16);
    const unsigned d = (a[0] ^ tag[0]) | (a[1] ^ tag[1]) | (a[2] ^ tag[0]) | (a[3] ^ tag[1]) | (b[0] ^ tag[0]) | (b[1] ^ tag[1]) | (b[2] ^ tag[0]) | (b[3] ^ tag[1]);
    if (!(__builtin_amdgcn_ballot_w64(d != 0u) & need)) break;
    nap<TSLEEP>();
  }
}
#define LAYER_VARS \
  extern __shared__ __attribute__((aligned(16))) char smem[]; \
  const lptr lds = (lptr)smem; \
  const rsrc_t rw = mk_rsrc(ws, WS_END); \
  int lane = threadIdx.x & 63; asm volatile("" : "+v"(lane)); \
  const int w = wave_id(), g = lane >> 4, r16 = lane & 15; \
  const int xcd = blockIdx.x & 7, bi = blockIdx.x >> 3, u = 8 * (xcd & 1) + (bi & 7), qt = 4 * (xcd >> 1) + (bi >> 3); \
  const int hf = u & 1, ct = u >> 1;     \
  constexpr unsigned FKV = WS_FL + (FINAL ? 32768 : 0), KV = WS_KV + (FINAL ? LAYER2 : 0), OB = WS_O + (FINAL ? LAYER2 : 0); \
  constexpr unsigned AUX = WS_AUX + (FINAL ? 1024 : 0), RB = FINAL ? WS_RB2 : WS_RB1; \
  (void)hf; (void)AUX; (void)RB; (void)FKV; \
  LDS_AS unsigned* misc = (LDS_AS unsigned*)(lds + L_MISC + 1024) + (FINAL ? 8 : 0);

template <bool FINAL>
DI void loader(char* ws, u32x2 tag) {
  LAYER_VARS
  {
    const int lw = w - 8;
    {
      unsigned vm[4];
#pragma unroll
      for (int r = 0; r < 4; r++) vm[r] = (4 * (4 * lw + r) + g) * (RBP * 2) + 256 * qt + (((r16 >> 1) ^ (g | ((r & 2) << 1))) * 32) + (r16 & 1) * 16 + 3072 - 1024 * r;
      const unsigned l0 = lds_addr(lds) + lw * 4096, v0 = 2 * lw * 20480 + lane * 16;
      constexpr int CNT = 8;
      auto lds_w = [&](int s) {
        const char* sb = ws + WS_WQ + u * 163840 + s * 2048;
        dma_s(v0, v0 + 20480 - 2048, sb, l0 + (s & 3) * SLOT32 + 16384);
      };
      auto lds_a = [&](int s) {
        dma_a(vm, ws + RB - 3072 + s * (64 * RBP * 2), l0 + (s & 3) * SLOT32); };
#pragma unroll 1
      for (int att = 0;; att++) {
        if (att == 0) { raw_bar(); if (FINAL) raw_bar(); else poll_tag(rw, tag, 0x7FFF0000ull); }
        lds_w(0); lds_w(1);
        if (att == 0) raw_bar();
        lds_w(2); lds_w(3);
        if (FINAL) dma2(40960 + lw * 2048 + lane * 16, (const char*)&g_it, lds_addr(lds) + L_IA + lw * 2048);
        auto probe = [&](int s0, int n) {
          const int pl = min(lane, n - 1);
          const unsigned po = RB + ((64 * (s0 + (pl >> 3)) + 16 * lw + 2 * (pl & 7) + 1) * RBP + 128 * qt + 126) * 2;
#pragma unroll 1
          for (int spins = 0; spins < (1 << 20); spins++) {
            if (!__builtin_amdgcn_ballot_w64(gl32<16>(rw, po) == EMPTY)) break;
            nap<XSLEEP>();
          }
        };
        if (att == 0) {
          raw_bar();
          if (blockIdx.x >= 64) nap<XDELAY>();
          probe(0, 48);
        }
        drain();
        unsigned nd = 0;
        if (att == 0 && lane == 0) nd = __hip_atomic_fetch_add(misc + 4, 1u, __ATOMIC_RELAXED, __HIP_MEMORY_SCOPE_WORKGROUP);
        if (nd == 11) st32<16>(rw, AUX + blockIdx.x * 4, 1u);
#pragma unroll 1
        for (int s = 0; s < 4; s++) lds_a(s);
        vm_bar<12>(); vm_bar<8>();
        if (att == 0) probe(6, 32);
        lds_a(4); lds_w(4);
        vm_bar<4 + CNT>();
#pragma unroll 1
        for (int s = 5; s < 10; s++) { lds_a(s); lds_w(s); vm_bar<2 * CNT>(); }
        vm_bar<CNT>();
        poll_aux(rw, AUX);
        if (!FINAL) { poll_tag(rw, tag, ~0ull); if (blockIdx.x == 0 && threadIdx.x == 512) st64<16>(rw, WS_EP, tag); }
        vm_bar<0>();
        if (FINAL) {
#pragma unroll
          for (int i = 0; i < 3; i++) { const int pc = min(3 * lw + i, 9); dma1_sc1(RB + ((64 * pc + lane) * RBP + 128 * qt + 8 * u) * 2, ws, lds_addr(lds) + L_T + pc * 1024); }
        }
        raw_bar();
        if (att || !*(volatile LDS_AS unsigned*)(misc + 2)) break;
      }
    }
    const int hh = lw >> 1, hp = lw & 1;
    const unsigned kvv = KV + (2 * u + hh) * 16 * KVC + hp * 5120 + lane * 16;
    const unsigned lkv = lds_addr(lds) + hh * HR + hp * 5120;
    const unsigned wv = (FINAL ? WS_WOP : WS_WOF) + (ct * 2 + hh) * 51200 + lane * 16;
    const unsigned myfl = FKV + (u * 16 + qt) * 128;
    vm_bar<0>();
    {
      u32x4 v[6];
#pragma unroll
      for (int i = 0; i < 6; i++) v[i] = ld128(lds + hh * HR + hp * 5120 + lane * 16 + i * 1024);
#pragma unroll
      for (int i = 0; i < 6; i++) st128<16>(rw, kvv + qt * KVC + i * 1024, clean(v[i]));
    }
    drain();
    unsigned cnt = 0;
    if (lane == 0) cnt = __hip_atomic_fetch_add(misc, 1u, __ATOMIC_RELAXED, __HIP_MEMORY_SCOPE_WORKGROUP);
    if (__builtin_amdgcn_readfirstlane(cnt) == 3 && lane < 16) st32<16>(rw, FKV + (u * 16 + lane) * 128 + qt * 4, 1u);
    auto ldkv = [&](int seq) {
      const int ch = seq == 0 ? qt : seq < 13 ? ((qt & 12) + 3 + seq) & 15 : (qt & 12) | ((qt + seq) & 3);
      dma6_sc1(kvv + ch * KVC, ws, lkv + (seq & 3) * KVC);
    };
    auto fetch = [&](int seq) {
      const unsigned src = kvv + ((((qt & 12) + 3 + seq) & 15)) * KVC;
      u32x4 v[6];
#pragma unroll 1
      for (int spins = 0; spins < (1 << 18); spins++) {
#pragma unroll
        for (int i = 0; i < 6; i++) v[i] = gl128<16>(rw, src + i * 1024);
        unsigned m = 0;
#pragma unroll
        for (int i = 0; i < 6; i++) m = umax4(m, v[i]);
        if (!__builtin_amdgcn_ballot_w64(m == EMPTY)) break;
        nap<2>();
      }
#pragma unroll
      for (int i = 0; i < 6; i++) *(LDS_AS u32x4*)(lds + hh * HR + hp * 5120 + seq * KVC + lane * 16 + i * 1024) = v[i];
    };
#pragma unroll 1
    for (int attempt = 0;; attempt++) {
      int c = -2;
      if (attempt) { ldkv(0); vm_bar<0>(); }
      else {
        fetch(1); lds_bar();
        fetch(2); poll16(rw, myfl, 12u << (((qt & 12) + 4) & 15)); ldkv(3); lds_bar();
        c = 1;
      }
#pragma unroll 1
      for (; c < 13; c++) {
        if (c == 2 && !attempt) poll16(rw, myfl, 0xFFFFu);
        if (!FINAL && !attempt) {
          if (c >= 2 && c < 9) {
            const u32x4 e = {EMPTY, EMPTY, EMPTY, EMPTY};
            const unsigned o = OB + LAYER2 + (((c - 2) * 256 + blockIdx.x) * 4 + lw) * 2048 + lane * 16;
            st128<16>(rw, o, e); st128<16>(rw, o + 1024, e);
            st128<16>(rw, ((qt & 3) < 2 && c < 8) ? kvv + LAYER2 + qt * KVC + (c - 2) * 1024 : WS_END, e);
            st128<16>(rw, c < 5 ? OB + ((4 * qt + lw) * 32 + 2 * u) * 1280 + min((c - 2) * 1024, 1536) + lane * 16 : WS_END, e);
          }
          if (c == 10) {
            drain();
            unsigned n = 0;
            if (lane == 0) n = __hip_atomic_fetch_add(misc + 3, 1u, __ATOMIC_RELAXED, __HIP_MEMORY_SCOPE_WORKGROUP);
            if (__builtin_amdgcn_readfirstlane(n) == 3 && lane < 32) st32<16>(rw, WS_FL + 65536 + (lane < 16 ? (u * 16 + lane) * 128 + qt * 4 : (r16 * 16 + qt) * 128 + 64 + u * 4), 1u);
          }
        }
        if (c >= 2) { const int e = min(4 * (c - 2) + 2 * hp, 33); dma2(wv + e * 1024, ws, lds_addr(lds) + L_A + (hh * 35 + e) * 1024); }
        ldkv(c + 3);
        if (c == 0) vm_bar<12>();
        if (c >= 2) vm_bar<8>(); else if (c >= 0) vm_bar<6>();
      }
      if (!FINAL && !attempt) poll16(rw, myfl + 65536 + hp * 64, 0xFFFFu);
      vm_bar<0>();
      dma_s(wv + (35 + 7 * hp) * 1024, wv + (35 + 7 * hp) * 1024, ws, lds_addr(lds) + hh * HR + 7 * hp * 1024);
      dma_s(wv + (39 + 7 * hp) * 1024, wv + (39 + 7 * hp) * 1024, ws, lds_addr(lds) + hh * HR + (4 + 7 * hp) * 1024);
      if (FINAL && lw == 0 && lane < 9)
        st64<16>(rw, lane < 8 ? WS_F0 + lane * 2048 + blockIdx.x * 8 : WS_GO + blockIdx.x * 64, u32x2{tag[0], ~tag[1]});
      raw_bar();
      vm_bar<0>();
      if (attempt || !*(volatile LDS_AS unsigned*)(misc + 1)) break;
      raw_bar();
    }
    if (!FINAL) { raw_bar(); raw_bar(); }
  }
}

template <bool FINAL>
DI void compute(char* ws, u16* out, ColIn& kin) {
  LAYER_VARS
  const int wm = w & 3, wn = w >> 2, h = lane >> 5;
  if (blockIdx.x < 64 || wn == 0) col_phase<!FINAL>(rw, lds, kin);
  else if (FINAL) {
    drain();
    nap<YIDLE>();
    u32x2 sp[8];
#pragma unroll 1
    for (int spins = 0; spins < (1 << 18); spins++) {
      unsigned m = 0;
#pragma unroll
      for (int i = 0; i < 8; i++) sp[i] = gl64<16>(rw, WS_SSQ + (blockIdx.x & 7) * 32768 + (1024 + 4 * (wm * 64 + lane)) * 2 + i * 4096);
#pragma unroll
      for (int i = 0; i < 8; i++) m = max(max(m, sp[i][0]), sp[i][1]);
      if (!__builtin_amdgcn_ballot_w64(m == EMPTY)) break;
      nap<YSLEEP>();
    }
    put_scale(lds, 1, wm * 64 + lane, sp);
    lds_bar(); raw_bar(); raw_bar();
  } else {
    raw_bar(); raw_bar(); raw_bar();
  }
  lds_bar();
  {
    drain();
    unsigned n = 0;
    if (lane == 0) n = __hip_atomic_fetch_add(misc + 4, 1u, __ATOMIC_RELAXED, __HIP_MEMORY_SCOPE_WORKGROUP);
    if (n == 11) st32<16>(rw, AUX + blockIdx.x * 4, 1u);
  }

  const int head = 2 * u + wn, kh = w & 1;
  const lptr ring = lds + wn * HR;
  const int pblk = 128 * qt + 64 * hf + 32 * (wm >> 1);
  const unsigned r3t = WS_R3 + (64 * (wm & 1) + (pblk >> 5)) * (642 * 64);
  u32x2 x4v[3]; float dc = 0.f;
  unsigned os = OB + ((4 * qt + wm) * 32 + head) * 1280 + (((lane << 2) & 16) + ((lane >> 1) & 12) + (lane & 3)) * 8;
  unsigned o1 = OB + ((pblk >> 5) * 32 + 16 * wn) * 1280 + (g >> 1) * 1280 + (g & 1) * 512 + (wm & 1) * 256 + r16 * 16;
  unsigned o2 = OB + ((pblk >> 5) * 32 + 16 * wn) * 1280 + 2 * g * 1280 + 1024 + (wm & 1) * 128 + r16 * 8;
  u32x4 o3 = {0u, 0u, 0u, 0u};
  if (!FINAL) {
#pragma unroll
    for (int i = 0; i < 3; i++) {
      const int j = 80 * ct + 48 * wn + 16 * (wn ? (i & 1) : i) + r16;
      o3[i] = (j == 1 ? WS_RB0 + ((pblk >> 5) * 641 + ((pblk >> 5) & 1) + 1) * 64 : WS_RB1 + (j * RBP + pblk) * 2) + 16 * g + 8 * (wm & 1);
    }
  } else {
    o3[3] = r3t + 64 + r16 * 4;
#pragma unroll
    for (int i = 0; i < 3; i++) o3[i] = WS_X4 + ((pblk + trow(wm & 1, r16)) * 640 + 80 * ct + 48 * wn + 16 * (wn ? (i & 1) : i) + 4 * g) * 2;
  }
  asm volatile("" : "+v"(os), "+v"(o1), "+v"(o2), "+v"(o3));
  unsigned ka = lds_addr(ring) + kh * 2560 + lane * 16, kb = lds_addr(ring) + kh * 2560 + 1024 + (lane & 31) * 8;
  unsigned va = lds_addr(ring) + kh * 3072 + (4 * h + (r16 >> 2)) * 48 + ((g & 1) ? ((lane & 3) ? 40 : 32) : (lane & 3) * 8);
  asm volatile("" : "+v"(ka), "+v"(kb), "+v"(va));
  {
    f32x4 acc[2][4]; float rs[2]; u32x8 im[3];
    unsigned mo[2], a0[2], a1[2], a2[2], a_r[2];
#pragma unroll
    for (int m = 0; m < 2; m++) {
      const int q = r16 >> 2, pr = 16 * (m ^ wn) + r16;
      mo[m] = lds_addr(lds) + (8 * g + q) * 256 + (((2 * wm + (m ^ wn)) ^ (q | ((g & 1) << 2))) * 32) + (lane & 3) * 8;
      const unsigned qb = L_A + (wn * 4 + wm) * 1280, kb = wn * HR + wm * 1280, vr = wn * HR + 5120 + (32 * wm + pr) * 48 + (g == 3 ? 32 : g * 16);
      a0[m] = ((g >> 1) ? kb : qb) + (g & 1) * 512 + pr * 16;
      a1[m] = g == 2 ? qb + 1024 + pr * 8 : vr; a2[m] = g == 2 ? kb + 1024 + pr * 8 : vr + 8;
      a_r[m] = lds_addr(lds) + L_MISC + (32 * wm + pr) * 4;
      asm volatile("" : "+v"(a0[m]), "+v"(a1[m]), "+v"(a2[m]));
    }
    const unsigned a_w = a_r[0] + (g == (r16 >> 2) ? 0 : 512), a_v = lds_addr((lptr)(misc + 2));
#pragma unroll 1
    for (int att = 0;; att++) {
      unsigned am0 = mo[0], am1 = mo[1], as = lds_addr(lds) + 16384 + wn * 8192 + lane * 16, vote; int c_, t_;
      if (!FINAL) asm volatile(GEMM_ASM("") : GEMM_OUT : GEMM_IN : GEMM_CLOB);
      else asm volatile(GEMM_ASM(ITL(96, 91) ITL(104, 92) ITL(112, 93)) : GEMM_OUT, "={v[96:103]}"(im[0]), "={v[104:111]}"(im[1]), "={v[112:119]}"(im[2])
                        : GEMM_IN, "{v91}"(w * 2048 + lane * 16), "{v92}"((w + 8) * 2048 + lane * 16), "{v93}"((w < 4 ? w + 16 : w) * 2048 + lane * 16), [it] "s"(&g_it) : GEMM_CLOB);
      if (att || !__builtin_amdgcn_readfirstlane(vote)) break;
    }
    SB();
#pragma unroll
    for (int m = 0; m < 2; m++) *(LDS_AS u32x4*)(lds + a0[m]) = pk8(acc[m][0] * rs[m], acc[m][1] * rs[m]);
    asm volatile("s_nop 1" : "+v"(acc[0][2]), "+v"(acc[1][2]), "+v"(acc[0][3]), "+v"(acc[1][3]) :: "memory");
#pragma unroll
    for (int m = 0; m < 2; m++) {
      u32x2 hi = pk4(acc[m][3] * rs[m]);
      if (g == 3) hi = u32x2{0x3F80u, 0u};
      *(LDS_AS u32x2*)(lds + a1[m]) = pk4(acc[m][2] * rs[m]);
      *(LDS_AS u32x2*)(lds + a2[m]) = hi;
    }
    lds_bar();
    if (FINAL) {
      u32x4 ia[3]; u32x2 ib[3];
#pragma unroll
      for (int T = 0; T < 3; T++) { ia[T] = ld128(lds + L_IA + T * 1024 + lane * 16); ib[T] = ld64(lds + L_IA + 3072 + T * 512 + lane * 8); }
      const f32x4 z4 = {0.f, 0.f, 0.f, 0.f};
#pragma unroll
      for (int i = 0; i < 3; i++) {
        const int G = (i < 2 || w < 4) ? w + 8 * i : w;
        const lptr tp = lds + L_T + (32 * G + 8 * g + (r16 >> 2)) * 16 + (lane & 1) * 8;
        const u32x2 f0 = ldtr(tp), f1 = ldtr(tp + 64);
#pragma unroll
        for (int P = 0; P < 2; P++) *(LDS_AS u32x2*)(lds + L_U + (r16 & 7) * 1296 + (2 * G + P) * 32 + g * 8) = pk4(mfma16(u32x4{im[i][4 * P], im[i][4 * P + 1], im[i][4 * P + 2], im[i][4 * P + 3]}, u32x4{f0[0], f0[1], f1[0], f1[1]}, z4));
      }
      wave_sync(misc + 5, 8);
      const lptr up = lds + L_U + w * 1296;
      const u32x2 f0 = ldtr(up + lane * 8), f1 = ldtr(up + 512 + lane * 8), f2 = ldtr(up + 1024 + (lane & 31) * 8);
#pragma unroll
      for (int T = 0; T < 3; T++) {
        const u32x2 v = pk4(mfma16(u32x4{f2[0], f2[1], 0u, 0u}, u32x4{ib[T][0], ib[T][1], 0u, 0u}, mfma16(u32x4{f0[0], f0[1], f1[0], f1[1]}, ia[T], z4)));
        st64<16>(rw, (T < 2 || r16 < 8) ? WS_X4 + ((128 * qt + 8 * u + w) * 640 + 16 * (16 * T + r16) + 4 * g) * 2 : WS_END, u32x2{min(v[0], EMPTY - 1), min(v[1], EMPTY - 1)});
      }
    }
  }
  f32x4 r[3];
  {
    const lptr qpa = lds + L_A + (wn * 4 + wm) * 1280, qpb = lds + L_A + (wn * 4 + (wm ^ 1)) * 1280;
    const u32x4 qa1 = ld128(qpa + lane * 16), qb1 = ld128(qpb + lane * 16);
    u32x2 qa2 = ld64(qpa + 1024 + (lane & 31) * 8), qb2 = ld64(qpb + 1024 + (lane & 31) * 8);
    if (h) { qa2 = u32x2{0u, 0u}; qb2 = qa2; }
    float ma, mb;
    {
      const u32x4 k1 = ld128(ring + lane * 16); const u32x2 k2 = ld64(ring + 1024 + (lane & 31) * 8);
      ma = max16(scores(k1, k2, qa1, qa2)); mb = max16(scores(k1, k2, qb1, qb2));
    }
    for (int attempt = 0;; attempt++) {
      ma = fmaxf(ma, __shfl_xor(ma, 32)); mb = fmaxf(mb, __shfl_xor(mb, 32));
      f32x16 cia, cib, oa, ob;
#pragma unroll
      for (int i = 0; i < 16; i++) { cia[i] = -ma; cib[i] = -mb; }
      int cnt = 4;
      asm volatile(
          "ds_read_b128 v[80:83], v100\n\t"
          "ds_read_b64 v[84:85], v101\n\t"
          "ds_read_b64_tr_b16 v[86:87], v102 offset:5120\n\t"
          "ds_read_b64_tr_b16 v[88:89], v102 offset:5504\n\t"
          Z4(0) Z4(68) Z4(76) Z4(90)
          "s_waitcnt lgkmcnt(0)\n\t"
          "v_mfma_f32_32x32x16_bf16 v[32:47], v[80:83], v[94:97], v[16:31]\n\t"
          Z4(4) Z4(8) Z4(12) Z4(104) Z4(108) Z4(112) Z4(116)
          "v_mfma_f32_32x32x8_bf16 v[32:47], v[84:85], v[98:99], v[32:47]\n\ts_nop 9\n\t"
          "L_attn_%=:\n\t"
          CHUNK(0) CHUNK(1) CHUNK(2) CHUNK(3)
          "s_sub_u32 %[cnt], %[cnt], 1\n\ts_cmp_lg_u32 %[cnt], 0\n\ts_cbranch_scc1 L_attn_%=\n\t"
          "s_setprio 0\n\t" CVT(78,68) CVT(79,70) "s_waitcnt lgkmcnt(0)\n\ts_nop 1\n\t"
          "v_mfma_f32_32x32x16_bf16 v[104:119], v[90:93], v[76:79], v[104:119]\n\ts_nop 15\n\t"
          : "={v[0:15]}"(oa), "={v[104:119]}"(ob), [cnt] "+s"(cnt)
          : "{v[16:31]}"(cia), "{v[120:135]}"(cib), "{v[94:97]}"(qa1), "{v[98:99]}"(qa2), "{v[136:139]}"(qb1), "{v[140:141]}"(qb2),
            "{v100}"(ka), "{v101}"(kb), "{v102}"(va)
          : "memory", "scc", "v32", "v33", "v34", "v35", "v36", "v37", "v38", "v39", "v40", "v41", "v42", "v43", "v44", "v45", "v46", "v47",
            "v48", "v49", "v50", "v51", "v52", "v53", "v54", "v55", "v56", "v57", "v58", "v59", "v60", "v61", "v62", "v63", "v64", "v65",
            "v66", "v67", "v68", "v69", "v70", "v71", "v72", "v73", "v74", "v75", "v76", "v77", "v78", "v79", "v80", "v81", "v82", "v83",
            "v84", "v85", "v86", "v87", "v88", "v89", "v90", "v91", "v92", "v93");
      LDS_AS f32x4* ex = (LDS_AS f32x4*)(ring + L_EX + wm * 3072) + lane;
#pragma unroll
      for (int i = 0; i < 3; i++) ex[i * 64] = f32x4{ob[4 * i], ob[4 * i + 1], ob[4 * i + 2], ob[4 * i + 3]};
      f32x4 cs = {0.f, 0.f, 0.f, 0.f};
#pragma unroll
      for (int i = 0; i < 3; i++) cs += f32x4{oa[4 * i], oa[4 * i + 1], oa[4 * i + 2], oa[4 * i + 3]} + f32x4{ob[4 * i], ob[4 * i + 1], ob[4 * i + 2], ob[4 * i + 3]};
      if (!(fabsf(cs[0] + cs[1] + cs[2] + cs[3]) < INFINITY) || (h && !(oa[8] + ob[8] < 0x1p60f))) misc[1] = 1u;
      lds_bar();
      const unsigned vote = *(volatile LDS_AS unsigned*)(misc + 1);
      LDS_AS f32x4* ey = (LDS_AS f32x4*)(ring + L_EX + (wm ^ 1) * 3072) + lane;
#pragma unroll
      for (int i = 0; i < 3; i++) r[i] = ey[i * 64] + f32x4{oa[4 * i], oa[4 * i + 1], oa[4 * i + 2], oa[4 * i + 3]};
      if (attempt || !vote) break;
      lds_bar();
      ma = -INFINITY; mb = -INFINITY;
      for (int t = 0; t < 64; t++) {
        const unsigned kb_ = KV + (head * 16 + (t >> 2)) * KVC + (t & 3) * 1280;
        const u32x4 k1 = gl128<16>(rw, kb_ + lane * 16); const u32x2 k2 = gl64<16>(rw, kb_ + 1024 + (lane & 31) * 8);
        ma = fmaxf(ma, max16(scores(k1, k2, qa1, qa2))); mb = fmaxf(mb, max16(scores(k1, k2, qb1, qb2)));
      }
      raw_bar();
    }
  }
  {
    const unsigned lb = __float_as_uint(r[2][0]);
    const float il = __builtin_amdgcn_rcpf(__uint_as_float(__builtin_amdgcn_permlane32_swap(lb, lb, false, false)[1]));
    const unsigned sl8 = os & 255;
    st128<16>(rw, os + sl8 + h * 512, clean(pk8(r[0] * il, r[1] * il)));
    const u32x4 t = clean(pk8(r[2] * il, r[2] * il));
    st64<16>(rw, h ? WS_END : os + 1024, u32x2{t[0], t[1]});
  }
  const lptr wa = lds + L_A + wn * 35 * 1024 + lane * 16, wb = ring + lane * 16;
  constexpr int NPRE = 15;
  u32x4 bf[50];
  f32x4 acc[5] = {};
#pragma unroll
  for (int j = 0; j < NPRE; j++) bf[j] = ld128(wa + j * 1024);
#pragma unroll
  for (int j = 0; j < 5; j++) asm volatile("" : "+v"(acc[j]));
  if (FINAL) dc = __uint_as_float(gl32<16>(rw, o3[3]));
#pragma unroll 1
  for (int spins = 0; spins < (1 << 18); spins++) {
#pragma unroll
    for (int i = 0; i < 3; i++) x4v[i] = gl64<16>(rw, o3[i]);
    if (!FINAL || !__builtin_amdgcn_ballot_w64(umax4(max(x4v[0][0], x4v[0][1]), u32x4{x4v[1][0], x4v[1][1], x4v[2][0], x4v[2][1]}) == EMPTY)) break;
    nap<8>();
  }
  drain();
  {
    u32x4 af[10];
    auto ldf = [&](int i) {
      if (i < 8) af[i] = __builtin_amdgcn_raw_buffer_load_b128(rw, o1, i * 2 * 1280, 16);
      else { const u32x2 a = __builtin_amdgcn_raw_buffer_load_b64(rw, o2, (i - 8) * 8 * 1280, 16), b = __builtin_amdgcn_raw_buffer_load_b64(rw, o2, ((i - 8) * 8 + 1) * 1280, 16);
             af[i] = u32x4{a[0], a[1], b[0], b[1]}; }
    };
#pragma unroll
    for (int i = 0; i < 10; i++) ldf(i);
#pragma unroll 1
    for (int spins = 0; spins < (1 << 18); spins++) {
      unsigned m = 0;
#pragma unroll
      for (int i = 0; i < 10; i++) m = umax4(m, af[i]);
      if (!__builtin_amdgcn_ballot_w64(m == EMPTY)) break;
      nap<RSLEEP>();
#pragma unroll
      for (int i = 0; i < 10; i++) if (__builtin_amdgcn_ballot_w64(umax4(0, af[i]) == EMPTY)) ldf(i);
    }
#pragma unroll
    for (int i = 0; i < 10; i++) asm volatile("" : "+v"(af[i]));
    asm volatile("s_nop 1" : "+v"(acc[0]), "+v"(acc[1]), "+v"(acc[2]), "+v"(acc[3]), "+v"(acc[4]));
    SB();
#pragma unroll
    for (int i = 0; i < 10; i++) {
#pragma unroll
      for (int a = 0; a < 5; a++) {
        if (FINAL) mfma16v(acc[a], bf[5 * i + a], af[i]); else mfma16v(acc[a], af[i], bf[5 * i + a]);
        const int j = 5 * i + a + NPRE; if (j < 50) bf[j] = ld128(j < 35 ? wa + j * 1024 : wb + (j - 35) * 1024);
        SB();
      }
    }
  }
  asm volatile("s_nop 7\n\ts_nop 1" : "+v"(acc[0]), "+v"(acc[1]), "+v"(acc[2]), "+v"(acc[3]), "+v"(acc[4]) :: "memory");
  if (!FINAL) { col_load(kin, rw, true, true); SB(); }
  {
    LDS_AS f32x4* ex = (LDS_AS f32x4*)(ring + L_EX + 12288 + wm * 3072) + lane;
    LDS_AS f32x4* ey = (LDS_AS f32x4*)(lds + (wn ^ 1) * HR + L_EX + 12288 + wm * 3072) + lane + 64 * wn;
    ex[0] = acc[2]; ex[64] = acc[3]; ex[128] = acc[4];
    dc *= 0.039528470752104741f;
#pragma unroll
    for (int i = 0; i < 3; i++) { acc[i] += f32x4{bflo(x4v[i][0]), bfhi(x4v[i][0]), bflo(x4v[i][1]), bfhi(x4v[i][1])}; if (FINAL) acc[i] += dc; }
    lds_bar();
#pragma unroll
    for (int i = 0; i < 3; i++) acc[i] += ey[64 * i];
  }
  if (FINAL) {
    const rsrc_t ro = mk_rsrc(out, 2048 * 640 * 2);
    const unsigned op = (rowof(pblk + trow(wm & 1, r16)) * 640 + 80 * ct + 48 * wn + 4 * g) * 2;
#pragma unroll
    for (int i = 0; i < 3; i++)
      if (i < 2 || wn == 0) st64<0>(ro, op + 32 * i, pk4(acc[i]));
  } else {
    f32x4 sq = {0.f, 0.f, 0.f, 0.f};
#pragma unroll
    for (int i = 0; i < 3; i++)
      if (i < 2 || wn == 0) {
        const int j = 80 * ct + 48 * wn + 16 * i + r16;
        st128<16>(rw, r3t + j * 64 + g * 16, clean(asu(acc[i])));
        if (j != 1) sq += acc[i] * acc[i];
      }
#pragma unroll
    for (int e = 0; e < 4; e++) sq[e] = row16_sum(sq[e]);
    LDS_AS float* sl = (LDS_AS float*)(lds + L_MISC);
    if (r16 == 0) *(LDS_AS f32x4*)(sl + wn * 64 + 32 * (wm & 1) + 16 * (wm >> 1) + 4 * g) = sq;
    lds_bar();
    if (w == 0) {
      LDS_AS f32x4* s4 = (LDS_AS f32x4*)sl + 2 * (lane & 7);
      st128<16>(rw, WS_SSQ + (lane >> 3) * 32768 + (ct * 2048 + 1024 * ((lane >> 2) & 1) + 64 * qt + 32 * hf + 8 * (lane & 3)) * 2, clean(pk8(s4[0] + s4[16], s4[1] + s4[17])));
    }
  }
}

__global__ void __launch_bounds__(768, 3) k_main(const u16* x, const u16* wr, const u16* wi, const u16* wqkv, const u16* wo, char* ws, u16* out) {
  if (wave_id() >= 8) {
    extern __shared__ __attribute__((aligned(16))) char smem[];
    const unsigned long long t0 = __builtin_amdgcn_s_memrealtime();
    const int job = blockIdx.x;
    if (job < 144) raw_bar();
    const rsrc_t rw = mk_rsrc(ws, WS_END);
    __builtin_amdgcn_s_setprio(3);
    if (threadIdx.x < 528) ((LDS_AS unsigned*)((lptr)smem + L_MISC + 1024))[threadIdx.x - 512] = 0u;
    u32x2 tag = gl64<16>(rw, WS_EP);
    tag[0] += 1u;
    if (job < 228) raw_bar();
    if (job < 64) raw_bar();
    asm volatile("s_waitcnt vmcnt(0) lgkmcnt(0)\n\ts_barrier" : "+v"(tag) :: "memory");
    if (wave_id() == 8) {
      if (threadIdx.x < 520) st64<16>(rw, WS_F0 + (threadIdx.x - 512) * 2048 + job * 8, tag);
      if (job >= 248) {
        while (__builtin_amdgcn_s_memrealtime() - t0 < TPOLL) nap<2>();
        poll_tag(rw, tag, 0xFE0FFFF00000FFFFull);
        if (threadIdx.x < 544) st64<16>(rw, WS_GO + ((threadIdx.x - 512) * 8 + (job & 7)) * 64, tag);
      }
#pragma unroll 1
      for (int spins = 0; spins < (1 << 20); spins++) {
        const u32x2 v = gl64<16>(rw, WS_GO + job * 64);
        if (__builtin_amdgcn_readfirstlane((v[0] ^ tag[0]) | (v[1] ^ tag[1])) == 0) break;
        nap<GSLEEP>();
      }
    }
    if (wave_id() > 8) {
    while (__builtin_amdgcn_s_memrealtime() - t0 < TFILL) nap<2>();
    const int i0 = blockIdx.x * 192 + threadIdx.x - 576;
#pragma unroll
    for (int it = 0; it < 4; it++) {
      const int k = it * 49152 + i0, b = k / 1408;
      st128<16>(rw, k < 128 * 1408 ? WS_KV + b * 4 * KVC + (k - b * 1408) * 16 : WS_END, u32x4{EMPTY, EMPTY, EMPTY, EMPTY});
    }
    }
    raw_bar();
    loader<false>(ws, tag);
    loader<true>(ws, tag);
  } else {
    prep(x, wr, wi, wqkv, wo, ws);
    asm volatile("s_waitcnt vmcnt(0) lgkmcnt(0)\n\ts_barrier" ::: "memory");
    ColIn kin = {};
    compute<false>(ws, out, kin);
    compute<true>(ws, out, kin);
  }
}

extern "C" void kernel_launch(void* const* d_in, const int* in_sizes, int n_in, void* d_out, int out_size, void* d_ws, size_t ws_size,
                              hipStream_t stream) {
  const u16* x = (const u16*)d_in[0];
  const u16* wr = (const u16*)d_in[1];
  const u16* wi = (const u16*)d_in[2];
  const u16* wqkv = (const u16*)d_in[4];
  const u16* wo = (const u16*)d_in[5];
  char* ws = (char*)d_ws;
  (void)hipFuncSetAttribute((const void*)k_main, hipFuncAttributeMaxDynamicSharedMemorySize, LDS_FUSED);
  k_main<<<256, 768, LDS_FUSED, stream>>>(x, wr, wi, wqkv, wo, ws, (u16*)d_out);
}
```

```cpp
#include <hip/hip_runtime.h>
#include <hip/hip_bf16.h>

typedef unsigned short u16;
typedef __attribute__((ext_vector_type(16))) float f32x16;
typedef __attribute__((ext_vector_type(4))) float f32x4;
typedef __attribute__((ext_vector_type(2))) float f32x2;
typedef __attribute__((ext_vector_type(8))) __bf16 bf16x8;
typedef __attribute__((ext_vector_type(2))) __bf16 bf16x2;
typedef __attribute__((ext_vector_type(4))) short s16x4;
typedef __attribute__((ext_vector_type(4))) unsigned u32x4;
typedef __attribute__((ext_vector_type(2))) unsigned u32x2;
typedef __attribute__((ext_vector_type(8))) unsigned u32x8;
#define DI __device__ __forceinline__
#define LDS_AS __attribute__((address_space(3)))
typedef LDS_AS char* lptr;

constexpr double cx_cos_small(double x) { double x2 = x * x, t = 1, s = 1; for (int i = 1; i < 12; i++) { t *= -x2 / ((2 * i - 1) * (2 * i)); s += t; } return s; }
constexpr double cx_sin_small(double x) { double x2 = x * x, t = x, s = x; for (int i = 1; i < 12; i++) { t *= -x2 / ((2 * i) * (2 * i + 1)); s += t; } return s; }
constexpr double cx_cos640(int m) {
  m %= 640; if (m > 320) m = 640 - m; double sg = 1; if (m > 160) { m = 320 - m; sg = -1; }
  const double w = 6.283185307179586476925 / 640; return sg * (m <= 80 ? cx_cos_small(w * m) : cx_sin_small(w * (160 - m)));
}
constexpr u16 cx_bf16(double d) { float f = (float)d; unsigned u = __builtin_bit_cast(unsigned, f); u += 0x7FFFu + ((u >> 16) & 1u); return (u16)(u >> 16); }
struct Cos640 { double v[640]; };
constexpr Cos640 mk_cos() { Cos640 c{}; for (int m = 0; m < 640; m++) c.v[m] = cx_cos640(m); return c; }
constexpr Cos640 COS = mk_cos();
__host__ __device__ constexpr int kf_of(int G, int i) { return G ? G + 40 * i : (i == 0 ? 320 : i < 8 ? 40 * i : 40 * i - 300); }
__host__ __device__ constexpr int freq_of(int G, int i) { return kf_of(G, i) > 320 ? 640 - kf_of(G, i) : kf_of(G, i); }
constexpr double SA = 1.91318, SA0 = 1.9140625, S64 = 1.86954, S32 = 1.6245;
constexpr double a1val(int out, int a) {
  if (out >= 40 || a >= 40) return 0;
  const int G = out >> 1, part = out & 1;
  if (G == 0) return (part && (a & 1)) ? -SA0 : SA0;
  return SA * COS.v[(16 * a * G + 160 * part) % 640];
}
constexpr double m2val(int G, int o, int k) {
  const int i = o >> 1, po = o & 1, part = k >> 4, b = k & 15, kf = kf_of(G, i);
  const double c = COS.v[(b * kf) % 640], sn = COS.v[(b * kf + 480) % 640];
  if (G == 0) {
    if (part != (i >= 8)) return 0;
    if (i == 0) return 0.039528470752104741 / SA0 * (po ? 1.0 : c);
    return 0.055901699437494742 / SA0 * (po ? -sn : c);
  }
  const double v = po ? (part ? c : -sn) : (part ? sn : c);
  return 0.055901699437494742 / SA * ((po && kf > 320) ? -v : v);
}
struct DTab { u16 a1[3][64][8]; u16 a1b[3][64][4]; u16 pad[256]; u16 m2[20][2][64][8]; };
constexpr DTab mk_dtab() {
  DTab t{};
  for (int l = 0; l < 64; l++) {
    const int r = l & 15, g = l >> 4;
    for (int T = 0; T < 3; T++) {
      for (int e = 0; e < 8; e++) t.a1[T][l][e] = cx_bf16(a1val(16 * T + r, 16 * (e >> 2) + 4 * g + (e & 3)));
      for (int e = 0; e < 4; e++) t.a1b[T][l][e] = cx_bf16(a1val(16 * T + r, 32 + 4 * g + e));
    }
    for (int G = 0; G < 20; G++) for (int T = 0; T < 2; T++) for (int e = 0; e < 8; e++)
      t.m2[G][T][l][e] = cx_bf16(m2val(G, 8 * (r >> 2) + 4 * T + (r & 3), 8 * g + e));
  }
  return t;
}
static __device__ const __attribute__((aligned(1024))) DTab g_dt = mk_dtab();

struct ITab { u16 m2[20][2][64][8]; u16 a1[3][64][8]; u16 a1b[3][64][4]; u16 pad[1792]; };
constexpr ITab mk_itab() {
  ITab t{};
  for (int l = 0; l < 64; l++) {
    const int r = l & 15, g = l >> 4;
    for (int T = 0; T < 3; T++) {
      for (int e = 0; e < 8; e++) t.a1[T][l][e] = cx_bf16(a1val(16 * (e >> 2) + 4 * g + (e & 3), 16 * T + r));
      for (int e = 0; e < 4; e++) t.a1b[T][l][e] = cx_bf16(a1val(32 + 4 * g + e, 16 * T + r));
    }
    for (int G = 0; G < 20; G++) for (int P = 0; P < 2; P++) for (int e = 0; e < 8; e++) t.m2[G][P][l][e] = cx_bf16(m2val(G, 8 * g + e, 16 * P + r));
  }
  return t;
}
static __device__ const __attribute__((aligned(1024))) ITab g_it = mk_itab();

struct CTab { u16 v[10 * 512]; };
constexpr CTab mk_ctab() {
  CTab c{};
  for (int f = 0; f < 10; f++) for (int l = 0; l < 64; l++) for (int e = 0; e < 8; e++) {
    const int r = l & 15, g = l >> 4;
    const int m = f < 8 ? (8 * g + e) * (16 * (f >> 1) + r) : 2 * r * (16 * (e >> 2) + 4 * g + (e & 3));
    c.v[(f * 64 + l) * 8 + e] = cx_bf16((f < 8 ? S64 : S32) * COS.v[10 * ((m + (f & 1) * 48) & 63)]);
  }
  return c;
}
static __device__ const __attribute__((aligned(1024))) CTab g_ct = mk_ctab();
constexpr float RSK = (float)(2048.0 * S64 * S64 * S32 * S32 * 2048.0 * S64 * S64 * S32 * S32 / 640.0);

constexpr int RP = 2048 + 64, RBP = 2048 + 128;
constexpr unsigned RBSZ = 640 * RBP * 2;
constexpr unsigned WS_FL = 0;
constexpr unsigned WS_AUX = WS_FL + 3 * 32768;
constexpr unsigned WS_F0 = WS_AUX + 2048;
constexpr unsigned WS_GO = WS_F0 + 8 * 2048;
constexpr unsigned WS_FX = WS_GO + 256 * 64;
constexpr unsigned WS_EP = WS_FX + 64 * 512;
constexpr unsigned WS_RS1 = WS_EP + 256;
constexpr unsigned WS_RB0 = WS_RS1 + 8192;
constexpr unsigned WS_RB1 = WS_RB0 + RBSZ;
constexpr unsigned WS_WF = WS_RB1 + RBSZ;
constexpr unsigned WS_WQ = WS_WF + 320 * 8192;
constexpr unsigned WS_WOF = WS_WQ + 16 * 8 * 20 * 1024;
constexpr unsigned WS_WOP = WS_WOF + 40 * 20 * 1024;
constexpr int KVC = 11264;
constexpr unsigned WS_KV = WS_WOP + 40 * 20 * 1024;
constexpr unsigned WS_O = WS_KV + 32 * 16 * KVC;
constexpr unsigned LAYER2 = 32 * 16 * KVC + 32 * 64 * 1280;
constexpr unsigned WS_X4 = WS_KV + 2 * LAYER2;
constexpr unsigned WS_R3 = WS_X4 + 2048 * 640 * 2;
constexpr unsigned WS_RB2 = WS_R3 + 640 * RP * 4;
constexpr unsigned WS_SSQ = WS_RB2 + RBSZ;
constexpr unsigned EMPTY = 0xFFFFFFFFu;
constexpr unsigned WS_END = WS_SSQ + 8 * 2048 * 16;

constexpr int XDELAY = 20, K1DELAY = 40, YIDLE = 12, TPOLL = 250, TSLEEP = 1, GSLEEP = 2, TFILL = 400, TLOOK = 450, YSLEEP = 4, XSLEEP = 1, RSLEEP = 2;

#define SB() __builtin_amdgcn_sched_barrier(0)
typedef __amdgpu_buffer_rsrc_t rsrc_t;
DI rsrc_t mk_rsrc(const void* p, unsigned bytes) { return __builtin_amdgcn_make_buffer_rsrc((void*)p, 0, bytes, 0x00020000); }
template <int AUX> DI void st128(rsrc_t r, unsigned off, u32x4 v) { __builtin_amdgcn_raw_buffer_store_b128(v, r, off, 0, AUX); }
template <int AUX> DI void st64(rsrc_t r, unsigned off, u32x2 v) { __builtin_amdgcn_raw_buffer_store_b64(v, r, off, 0, AUX); }
template <int AUX> DI void st32(rsrc_t r, unsigned off, unsigned v) { __builtin_amdgcn_raw_buffer_store_b32(v, r, off, 0, AUX); }
template <int AUX> DI u32x4 gl128(rsrc_t r, unsigned off) { return __builtin_amdgcn_raw_buffer_load_b128(r, off, 0, AUX); }
template <int AUX> DI u32x2 gl64(rsrc_t r, unsigned off) { return __builtin_amdgcn_raw_buffer_load_b64(r, off, 0, AUX); }
template <int AUX> DI unsigned gl32(rsrc_t r, unsigned off) { return __builtin_amdgcn_raw_buffer_load_b32(r, off, 0, AUX); }
DI u32x4 asu(f32x4 v) { return __builtin_bit_cast(u32x4, v); }
DI f32x4 asf(u32x4 v) { return __builtin_bit_cast(f32x4, v); }
DI unsigned pk2(float a, float b) { f32x2 v = {a, b}; return __builtin_bit_cast(unsigned, __builtin_convertvector(v, bf16x2)); }
DI u32x2 pk4(f32x4 a) { return u32x2{pk2(a[0], a[1]), pk2(a[2], a[3])}; }
DI u32x4 pk8(f32x4 a, f32x4 b) { return u32x4{pk2(a[0], a[1]), pk2(a[2], a[3]), pk2(b[0], b[1]), pk2(b[2], b[3])}; }
DI u32x4 clean(u32x4 v) { return __builtin_elementwise_min(v, u32x4{EMPTY - 1, EMPTY - 1, EMPTY - 1, EMPTY - 1}); }
DI unsigned umax4(unsigned m, u32x4 v) { return max(max(max(m, v[0]), v[1]), max(v[2], v[3])); }
DI float bflo(unsigned u) { return __uint_as_float(u << 16); }
DI float bfhi(unsigned u) { return __uint_as_float(u & 0xFFFF0000u); }
DI int rowof(int p) { return 32 * (((p >> 5) & 0x3C) + (p & 3)) + ((p << 2) & 16) + ((p >> 3) & 15); }
DI int rpos(int p) { return ((p << 8) & 1024) + ((p >> 3) << 2) + (p & 3); }
DI int trow(int T, int r) { return 8 * (r >> 2) + 4 * T + (r & 3); }
template <int CTRL> DI float dpp_add(float v) { return v + __int_as_float(__builtin_amdgcn_update_dpp(0, __float_as_int(v), CTRL, 0xF, 0xF, true)); }
DI float row16_sum(float v) { v = dpp_add<0x128>(v); v = dpp_add<0x124>(v); v = dpp_add<0x122>(v); return dpp_add<0x121>(v); }
DI f32x4 mfma16(u32x4 a, u32x4 b, f32x4 c) {
  return __builtin_amdgcn_mfma_f32_16x16x32_bf16(__builtin_bit_cast(bf16x8, a), __builtin_bit_cast(bf16x8, b), c, 0, 0, 0);
}
DI void mfma16v(f32x4& c, u32x4 a, u32x4 b) { asm volatile("v_mfma_f32_16x16x32_bf16 %0, %1, %2, %0" : "+v"(c) : "v"(a), "v"(b)); }
DI u32x2 ldtr(lptr p) { return __builtin_bit_cast(u32x2, __builtin_amdgcn_ds_read_tr16_b64_v4i16((LDS_AS s16x4*)p)); }
DI u32x4 ld128(lptr p) { return *(LDS_AS u32x4*)p; }
DI u32x2 ld64(lptr p) { return *(LDS_AS u32x2*)p; }
#define GL(v, s, o, x) "global_load_lds_dwordx4 " v ", " s " offset:" #o x "\n\t"
#define M0SET "s_mov_b32 %[sv], m0\n\ts_mov_b32 m0, %[l]\n\ts_nop 2\n\t"
#define M0ADD(x) "s_add_u32 m0, m0, " x "\n\ts_nop 0\n\t"
#define M0RST "s_mov_b32 m0, %[sv]"
DI void dma2(unsigned v, const char* sb, unsigned l) { unsigned sv;
  asm volatile(M0SET GL("%[v]", "%[s]", 0, " sc1") GL("%[v]", "%[s]", 1024, " sc1") M0RST : [sv] "=&s"(sv) : [v] "v"(v), [s] "s"(sb), [l] "s"(l) : "memory"); }
DI void dma4(unsigned v, const char* sb, unsigned l) { unsigned sv;
  asm volatile(M0SET GL("%[v]", "%[s]", 0, "") GL("%[v]", "%[s]", 1024, "") GL("%[v]", "%[s]", 2048, "") GL("%[v]", "%[s]", 3072, "") M0RST
               : [sv] "=&s"(sv) : [v] "v"(v), [s] "s"(sb), [l] "s"(l) : "memory"); }
DI void dma6_sc1(unsigned v, const char* sb, unsigned l) { unsigned sv;
  asm volatile(M0SET GL("%[v]", "%[s]", 0, " sc1") GL("%[v]", "%[s]", 1024, " sc1") GL("%[v]", "%[s]", 2048, " sc1") M0ADD("3072")
               GL("%[w]", "%[s]", 0, " sc1") GL("%[w]", "%[s]", 1024, " sc1") GL("%[w]", "%[s]", 2048, " sc1") M0RST
               : [sv] "=&s"(sv) : [v] "v"(v), [w] "v"(v + 3072), [s] "s"(sb), [l] "s"(l) : "memory", "scc"); }
template <int N> DI void vm_bar() { asm volatile("s_waitcnt vmcnt(%0)\n\ts_barrier" ::"i"(N) : "memory"); }
DI void lds_bar() { asm volatile("s_waitcnt lgkmcnt(0)\n\ts_barrier" ::: "memory"); }
DI void raw_bar() { asm volatile("s_barrier" ::: "memory"); }
template <int N> DI void nap() { __builtin_amdgcn_s_sleep(N); asm volatile("" ::: "memory"); }
DI void drain() { asm volatile("s_waitcnt vmcnt(0)" ::: "memory"); }
#define CODE_ADDR(sym) ({ unsigned lo_, hi_; \
    asm volatile("s_getpc_b64 vcc\n\ts_add_u32 %0, vcc_lo, " sym "@rel32@lo+4\n\ts_addc_u32 %1, vcc_hi, " sym "@rel32@hi+12" : "=s"(lo_), "=s"(hi_) :: "vcc", "scc"); \
    (const unsigned*)(((unsigned long long)hi_ << 32) | lo_); })
#define SYM_MAIN "_Z6k_mainPKtS0_S0_S0_S0_PcPt"
constexpr int CODE_LINES = 360;
DI int wave_id() { return __builtin_amdgcn_readfirstlane(threadIdx.x >> 6); }
DI unsigned lds_addr(lptr p) { return (unsigned)(size_t)p; }

DI int kappa_to_i(int kap) {
  int ks = kap >> 5, g = (kap >> 3) & 3, e = kap & 7;
  if (ks < 16) return (2 * ks + (g >> 1)) * 20 + 8 * (e >> 2) + 4 * (g & 1) + (e & 3);
  return (8 * (ks - 16) + 2 * g + (e >> 2)) * 20 + 16 + (e & 3);
}

DI unsigned wo_frag(int nt, int ks) {
  const int ct = nt / 5, c5 = nt - 5 * ct, kh = ks < 16 ? ks >> 3 : (ks - 16) >> 1, i = ks < 16 ? ks & 7 : 8 + (ks & 1);
  const int a = kh ? (c5 >= 3 ? c5 - 3 : c5 + 2) : c5;
  return ((ct * 2 + kh) * 50 + 5 * i + a) * 1024;
}

constexpr int SLOT32 = 32768;
DI void dma_a(const unsigned* vm, const char* mb, unsigned l) { unsigned sv;
  asm volatile(M0SET GL("%[a0]", "%[m]", 0, " sc1") GL("%[a1]", "%[m]", 1024, " sc1") GL("%[a2]", "%[m]", 2048, " sc1") GL("%[a3]", "%[m]", 3072, " sc1") M0RST
               : [sv] "=&s"(sv) : [a0] "v"(vm[0]), [a1] "v"(vm[1]), [a2] "v"(vm[2]), [a3] "v"(vm[3]), [m] "s"(mb), [l] "s"(l) : "memory"); }
DI void dma_s(unsigned v0, unsigned v1, const char* sb, unsigned l) { unsigned sv;
  asm volatile(M0SET GL("%[v0]", "%[s]", 0, " sc1") GL("%[v0]", "%[s]", 1024, " sc1") GL("%[v1]", "%[s]", 2048, " sc1") GL("%[v1]", "%[s]", 3072, " sc1") M0RST
               : [sv] "=&s"(sv) : [v0] "v"(v0), [v1] "v"(v1), [s] "s"(sb), [l] "s"(l) : "memory"); }
DI void dma1_sc1(unsigned v, const char* sb, unsigned l) { unsigned sv;
  asm volatile(M0SET GL("%[v]", "%[s]", 0, " sc1") M0RST : [sv] "=&s"(sv) : [v] "v"(v), [s] "s"(sb), [l] "s"(l) : "memory"); }
DI void dma1(unsigned v, const char* sb, unsigned l) { unsigned sv;
  asm volatile(M0SET GL("%[v]", "%[s]", 0, "") M0RST : [sv] "=&s"(sv) : [v] "v"(v), [s] "s"(sb), [l] "s"(l) : "memory"); }

constexpr int L_I2 = 40 * 1056, LDS_PREP = L_I2 + 20 * 2064 + 1024;
DI void prep(const u16* x, const u16* wr, const u16* wi, const u16* wqkv, const u16* wo, char* ws) {
  extern __shared__ __attribute__((aligned(16))) char smem[];
  const lptr lds = (lptr)smem;
  const int tid = threadIdx.x, lane = tid & 63, w = wave_id(), g = lane >> 4, r16 = lane & 15;
  const int job = blockIdx.x;
  if (__builtin_expect(job < 144, 1)) {
    const bool cols = job >= 64 && job < 124;
    const int cj = job - 64, cb = cj < 48 ? (cj & 48) + 2 * (cj & 7) + ((cj >> 3) & 1) : cj;
    u32x4 a1[3], m2[3][2]; u32x2 a1b[3];
    if (__builtin_expect(cols, 0)) {
#pragma unroll
      for (int i = 0; i < 5; i++) dma1((16 * (5 * w + i) + (lane >> 2)) * 3840 + cb * 64 + (lane & 3) * 16, (const char*)wqkv, lds_addr(lds) + (5 * w + i) * 1056);
    } else {
      const char* src = (const char*)(job < 64 ? x : wo);
      const int sl = 8 * (w & 3) + 4 * (w >> 2);
      src += (__builtin_expect(job < 64, 1) ? rowof(32 * job + sl) : kappa_to_i(32 * (job - 124) + sl)) * 1280;
      const int st = (job < 64 ? 31 : 0) * 1280;
      const unsigned b0 = lane * 16, vr[4] = {b0, b0 + (lane < 16 ? 0 : st), b0 + (lane < 32 ? st : 2 * st), b0 + (lane < 48 ? 2 * st : 3 * st)};
      unsigned sv;
      asm volatile(M0SET GL("%[a0]", "%[s]", 0, "") GL("%[a1]", "%[s]", 1024, "") GL("%[a2]", "%[s]", 2048, "") GL("%[a3]", "%[s]", 3072, "") M0ADD("4096") GL("%[a4]", "%[s]", 0, "") M0RST
                   : [sv] "=&s"(sv) : [a0] "v"(vr[0]), [a1] "v"(vr[1]), [a2] "v"(vr[2]), [a3] "v"(vr[3]), [a4] "v"(b0 + 4096 + 3 * st), [s] "s"(src), [l] "s"(lds_addr(lds) + w * 5120) : "memory", "scc");
    }
    if (w < 5) dma1(tid * 16, (const char*)&g_dt, lds_addr(lds) + LDS_PREP + w * 1024);
    unsigned so[3][2]; float sc[2] = {1.f, 1.f};
    if (cols) {
#pragma unroll
      for (int m = 0; m < 2; m++) {
        const int c = 32 * cb + 16 * m + r16;
        const int which = c / 640, rem = c - which * 640, head = rem / 20, d = rem - head * 20, grp = d >> 2;
        const int T = grp < 4 ? (which == 2 ? 2 : 0) + (grp & 1) : (which == 1 ? 3 : 2), gs = grp < 4 ? (which == 1 ? 2 : 0) + (grp >> 1) : (which == 2 ? 3 : 2);
        if (which == 0) sc[m] = 0.22360679774997896f * 1.4426950408889634f;
#pragma unroll
        for (int i = 0; i < 3; i++) so[i][m] = WS_WQ + ((head * 4 + T) * 20 + w + 8 * i) * 1024 + (g * 16 + 4 * gs + (d & 3)) * 16;
      }
    } else if (job < 64) {
#pragma unroll
      for (int i = 0; i < 3; i++)
#pragma unroll
        for (int T = 0; T < 2; T++) so[i][T] = WS_RB0 + (job * 641 + (job & 1) + 32 * (w + 8 * i) + trow(T, r16)) * 64 + g * 16;
    } else {
#pragma unroll
      for (int i = 0; i < 3; i++) {
        const unsigned f = wo_frag(2 * (w + 8 * i) + (r16 >> 3), job - 124);
#pragma unroll
        for (int T = 0; T < 2; T++) so[i][T] = WS_WOF + f + (g * 16 + (trow(T, r16) & 15)) * 16;
      }
    }
    if (w >= 4) { so[2][0] = WS_END; so[2][1] = WS_END; }
    asm volatile("" : "+v"(so[0][0]), "+v"(so[0][1]), "+v"(so[1][0]), "+v"(so[1][1]), "+v"(so[2][0]), "+v"(so[2][1]), "+v"(sc[0]), "+v"(sc[1]));
    const f32x4 z4 = {0.f, 0.f, 0.f, 0.f};
    const lptr i2 = lds + L_I2;
    drain();
#pragma unroll
    for (int i = 0; i < 3; i++)
#pragma unroll
      for (int T = 0; T < 2; T++)
        asm volatile("global_load_dwordx4 %0, %1, %2" : "=v"(m2[i][T]) : "v"(((((i < 2 || w < 4) ? w + 8 * i : w) * 2 + T) * 64 + lane) * 16), "s"(&g_dt.m2[0][0][0][0]) : "memory");
    raw_bar();
#pragma unroll
    for (int T = 0; T < 3; T++) { a1[T] = ld128(lds + LDS_PREP + T * 1024 + lane * 16); a1b[T] = ld64(lds + LDS_PREP + 3072 + T * 512 + lane * 8); }
    f32x4 t1[4][3];
    if (cols) {
      const lptr src = lds + (4 * g + (r16 >> 2)) * 1056 + (w >> 1) * 256 + (w & 1) * 32 + (lane & 3) * 8, srb = src + (32 - 8 * (g >> 1)) * 1056;
#pragma unroll
      for (int i = 0; i < 4; i++) {
        const u32x2 f0 = ldtr(src + i * 64), f1 = ldtr(src + 16 * 1056 + i * 64), f2 = ldtr(srb + i * 64);
#pragma unroll
        for (int T = 0; T < 3; T++) t1[i][T] = mfma16(u32x4{a1b[T][0], a1b[T][1], 0u, 0u}, u32x4{f2[0], f2[1], 0u, 0u}, mfma16(a1[T], u32x4{f0[0], f0[1], f1[0], f1[1]}, z4));
      }
      const int xo = ((w >> 1) * 8) ^ (((r16 >> 1) & 3) * 16);
      const lptr dst = i2 + 2 * g * 2064 + (16 * (w & 1) + r16) * 64;
#pragma unroll
      for (int T = 0; T < 3; T++)
#pragma unroll
        for (int e = 0; e < 4; e++)
          if (T < 2 || g < 2) *(LDS_AS u32x2*)(dst + (8 * T + (e >> 1)) * 2064 + (xo ^ ((e & 1) * 32))) = u32x2{pk2(t1[0][T][e], t1[1][T][e]), pk2(t1[2][T][e], t1[3][T][e])};
    } else {
      const lptr src = lds + w * 5120 + lane * 8, srb = lds + w * 5120 + 1024 + (lane & 31) * 8;
#pragma unroll
      for (int i = 0; i < 4; i++) {
        const u32x2 f0 = ldtr(src + i * 1280), f1 = ldtr(src + i * 1280 + 512), f2 = ldtr(srb + i * 1280);
#pragma unroll
        for (int T = 0; T < 3; T++) t1[i][T] = mfma16(u32x4{f2[0], f2[1], 0u, 0u}, u32x4{a1b[T][0], a1b[T][1], 0u, 0u}, mfma16(u32x4{f0[0], f0[1], f1[0], f1[1]}, a1[T], z4));
      }
      const int xo = ((r16 & 1) * 32 + g * 8) ^ ((w & 1) * 32);
      const lptr dst = i2 + (r16 >> 1) * 2064 + w * 256;
#pragma unroll
      for (int i = 0; i < 4; i++)
#pragma unroll
        for (int T = 0; T < 3; T++)
          if (T < 2 || r16 < 8) *(LDS_AS u32x2*)(dst + T * 8 * 2064 + i * 64 + (xo ^ ((i >> 1) * 16))) = pk4(t1[i][T]);
    }
    lds_bar();
    const rsrc_t rw = mk_rsrc(ws, WS_END);
    asm volatile("s_waitcnt vmcnt(0)" : "+v"(m2[0][0]), "+v"(m2[0][1]), "+v"(m2[1][0]), "+v"(m2[1][1]), "+v"(m2[2][0]), "+v"(m2[2][1]));
    f32x4 sq[2] = {z4, z4};
    u32x4 f[3][2];
#pragma unroll
    for (int i = 0; i < 3; i++) {
      const lptr fp = i2 + ((i < 2 || w < 4) ? w + 8 * i : 0) * 2064 + r16 * 64 + ((g ^ ((r16 >> 1) & 3)) * 16);
      f[i][0] = ld128(fp); f[i][1] = ld128(fp + 1024);
    }
    if (cols) {
#pragma unroll
      for (int i = 0; i < 3; i++)
#pragma unroll
        for (int m = 0; m < 2; m++) st128<16>(rw, so[i][m], pk8(mfma16(m2[i][0], f[i][m], z4) * sc[m], mfma16(m2[i][1], f[i][m], z4) * sc[m]));
    } else {
#pragma unroll
      for (int i = 0; i < 3; i++)
#pragma unroll
        for (int T = 0; T < 2; T++) {
          const f32x4 d0 = mfma16(f[i][0], m2[i][T], z4), d1 = mfma16(f[i][1], m2[i][T], z4);
          st128<16>(rw, so[i][T], pk8(d0, d1));
          const float k = ((i == 2 && w >= 4) || (i == 0 && T == 0 && tid < 64 && r16 == 1)) ? 0.f : 1.f;
          sq[0] += d0 * d0 * k; sq[1] += d1 * d1 * k;
        }
    }
    LDS_AS float* sl = (LDS_AS float*)(lds + LDS_PREP - 1024);
    if (job < 64) {
#pragma unroll
      for (int m = 0; m < 2; m++) {
#pragma unroll
        for (int e = 0; e < 4; e++) sq[m][e] = row16_sum(sq[m][e]);
        if (r16 == 0) *(LDS_AS f32x4*)(sl + w * 32 + 8 * g + 4 * m) = sq[m];
      }
    }
    if (job < 64) {
      lds_bar();
      if (tid < 32) {
        float v = 0.f;
#pragma unroll
        for (int i = 0; i < 8; i++) v += sl[i * 32 + tid];
        st32<16>(rw, WS_RS1 + rpos(32 * job + tid) * 4, __float_as_uint(__builtin_amdgcn_rsqf((v + 640.f * 1e-5f) * RSK)));
      }
    }
    return;
  }
  const int mi = job - 144;
  const rsrc_t rw = mk_rsrc(ws, WS_END);
  if (mi < 64) {
    const int m = mi >> 3, oc = mi & 7;
    const char* src = (const char*)((w >> 2) ? wi : wr) + (64 * (4 * m + (w & 3)) + 8 * oc) * 642;
    dma4(lane * 16, src, lds_addr(lds) + w * 5152);
    dma1(4096 + lane * 16, src, lds_addr(lds) + w * 5152 + 4096);
    if (lane == 0) dma1(5120, src, lds_addr(lds) + w * 5152 + 5120);
    vm_bar<0>();
    LDS_AS u16* L = (LDS_AS u16*)lds + (tid & 7) * 321;
#pragma unroll
    for (int it = 0; it < 5; it++) {
      const int c = (tid >> 3) + 64 * it, col = freq_of(c >> 4, c & 15);
      u32x4 o;
#pragma unroll
      for (int e = 0; e < 4; e++) o[e] = (unsigned)L[e * 2576 + col] | ((unsigned)L[(4 + e) * 2576 + col] << 16);
      st128<16>(rw, WS_WF + c * 8192 + (m * 64 + 8 * oc + (tid & 7)) * 16, o);
    }
#pragma unroll
    for (int it = 0; it < 3; it++) st128<16>(rw, WS_RB1 + ((it * 64 + mi) * 512 + tid) * 16, u32x4{EMPTY, EMPTY, EMPTY, EMPTY});
  } else if (mi < 84) {
    unsigned pf0;
    {
      const unsigned* cm = CODE_ADDR(SYM_MAIN);
      const unsigned* p = mi >= 80 ? (const unsigned*)&g_it + min(tid, 359) * 32 : (mi & 8) ? (const unsigned*)&g_dt.m2[0][0][0][0] + min(tid, 319) * 32 : tid < 80 ? (const unsigned*)&g_ct + tid * 32 : cm + min(tid - 80, CODE_LINES - 1) * 32;
      asm volatile("global_load_dword %0, %1, off" : "=v"(pf0) : "v"(p) : "memory");
    }
    const char* src = (const char*)wo + kappa_to_i(32 * (mi - 64) + 8 * (w & 3) + 4 * (w >> 2)) * 1280;
    dma4(lane * 16, src, lds_addr(lds) + w * 5152);
    dma1(4096 + lane * 16, src, lds_addr(lds) + w * 5152 + 4096);
    asm volatile("s_waitcnt vmcnt(0)\n\ts_barrier" : "+v"(pf0) :: "memory");
#pragma unroll
    for (int i = 0; i < 5; i++) {
      const lptr a = lds + g * 5152 + (r16 >> 2) * 1280 + (5 * w + i) * 32 + (lane & 3) * 8;
      const u32x2 lo = ldtr(a), hi = ldtr(a + 4 * 5152);
      st128<16>(rw, WS_WOP + wo_frag(5 * w + i, mi - 64) + lane * 16, u32x4{lo[0], lo[1], hi[0], hi[1]});
    }
  } else {
    const int i0 = (mi - 84) * 512 + tid;
    nap<K1DELAY>();
    if (i0 < 6272) st128<16>(rw, WS_FL + i0 * 16, u32x4{0u, 0u, 0u, 0u});
    if (i0 < 10240) st128<16>(rw, WS_WQ + (((i0 / 320) * 4 + 3) * 80 + (i0 % 320 >> 2)) * 256 + (12 + (i0 & 3)) * 16, u32x4{0u, 0u, 0u, 0u});
#pragma unroll
    for (int it = 0; it < 6; it++) { const int i = 98304 + i0 + it * 14336; if (i < (int)RBSZ / 16) st128<16>(rw, WS_RB1 + i * 16, u32x4{EMPTY, EMPTY, EMPTY, EMPTY}); }
  }
}

#define PINF(x) asm volatile("" : "+v"(x))
DI void wave_sync(LDS_AS unsigned* c, unsigned n) {
  asm volatile("s_waitcnt lgkmcnt(0)" ::: "memory");
  if ((threadIdx.x & 63) == 0) __hip_atomic_fetch_add(c, 1u, __ATOMIC_RELAXED, __HIP_MEMORY_SCOPE_WORKGROUP);
  while (__builtin_amdgcn_readfirstlane(*(volatile LDS_AS unsigned*)c) < n) {}
  asm volatile("" ::: "memory");
}
DI void put_scale(lptr lds, int q, int tid, const u32x2* sp) {
  f32x4 r = {640.f * 1e-5f, 640.f * 1e-5f, 640.f * 1e-5f, 640.f * 1e-5f};
#pragma unroll
  for (int i = 0; i < 8; i++) r += f32x4{bflo(sp[i][0]), bfhi(sp[i][0]), bflo(sp[i][1]), bfhi(sp[i][1])};
#pragma unroll
  for (int e = 0; e < 4; e++) r[e] = __builtin_amdgcn_rsqf(r[e] * RSK);
  ((LDS_AS f32x4*)(lds + 2 * SLOT32))[q * 256 + tid] = r;
}
struct ColIn { u32x4 c64, s64, c32, s32, wf[2]; };
DI int col_of() { return blockIdx.x < 64 ? 192 + 2 * blockIdx.x + (wave_id() >> 2) : blockIdx.x < 124 ? blockIdx.x + 68 : blockIdx.x - 124; }
DI void col_load(ColIn& k, rsrc_t rw, bool tab, bool filt) {
  int lane = threadIdx.x & 63; PINF(lane);
  const int w = wave_id() & 3;
  const u32x4* ct = (const u32x4*)&g_ct + lane;
  if (tab) { k.c64 = ct[(2 * w) * 64]; k.s64 = ct[(2 * w + 1) * 64]; k.c32 = ct[8 * 64]; k.s32 = ct[9 * 64]; }
  if (filt) {
#pragma unroll
    for (int t = 0; t < 2; t++) k.wf[t] = gl128<16>(rw, WS_WF + col_of() * 8192 + ((4 * t + (lane >> 4)) * 64 + 16 * w + (lane & 15)) * 16);
  }
}
template <bool FIRST>
DI void col_phase(rsrc_t rw, lptr lds, ColIn kin) {
  int lane = threadIdx.x & 63; PINF(lane);
  const int wv = wave_id(), w = wv & 3, q = wv >> 2, tid = w * 64 + lane, g = lane >> 4, r16 = lane & 15;
  const int c = col_of();
  const lptr img = lds + q * SLOT32;
  f32x4 xr[2], xi[2], rs[2]; u32x4 xb[2];
  u32x4 c64[2], s64[2], n64[2], c32[2], s32[2], n32[2];
  if (FIRST) col_load(kin, rw, true, false);
  SB();
  f32x4 t1c[2], t1s[2], t2c[2], t2s[2];
  {
    const float f1 = (float)(16 * w + r16) * (1.f / 2048.f), b1 = (float)(4 * g), f2 = (float)(16 * w + 4 * g) * (1.f / 2048.f), b2 = (float)r16;
#pragma unroll
    for (int t = 0; t < 2; t++) {
#pragma unroll
      for (int e = 0; e < 4; e++) {
        const float a1 = (b1 + (float)(16 * t + e)) * f1, a2 = (b2 + (float)(16 * t)) * (f2 + (float)e * (1.f / 2048.f));
        t1c[t][e] = __builtin_amdgcn_cosf(a1); t1s[t][e] = __builtin_amdgcn_sinf(-a1);
        t2c[t][e] = __builtin_amdgcn_cosf(a2); t2s[t][e] = __builtin_amdgcn_sinf(a2);
      }
      PINF(t1c[t]); PINF(t1s[t]); PINF(t2c[t]); PINF(t2s[t]);
    }
  }
  int wo_ = r16 * 128 + (((2 * w + (g >> 1)) ^ (r16 >> 1)) * 16) + (g & 1) * 8, ro_ = r16 * 128 + ((g ^ (r16 >> 1)) * 16), ro2 = ro_ ^ 64;
  PINF(wo_); PINF(ro_); PINF(ro2);
  {
    const unsigned par = (lane & 1) ? 0x80008000u : 0u;
    c64[0] = kin.c64; s64[0] = kin.s64; c32[0] = kin.c32; s32[0] = kin.s32;
    c64[1] = c64[0] ^ par; s64[1] = s64[0] ^ par; n64[0] = s64[0] ^ 0x80008000u; n64[1] = s64[1] ^ 0x80008000u;
    c32[1] = c32[0] ^ 0x80000000u; s32[1] = s32[0] ^ 0x80000000u; n32[0] = s32[0] ^ 0x80008000u; n32[1] = s32[0] ^ 0x00008000u;
    PINF(c64[1]); PINF(s64[1]); PINF(n64[0]); PINF(n64[1]); PINF(c32[1]); PINF(s32[1]); PINF(n32[0]); PINF(n32[1]);
  }
  SB();
  if (FIRST) {
    raw_bar();
#pragma unroll
    for (int t = 0; t < 2; t++) { xb[t] = gl128<16>(rw, WS_RB0 + ((tid >> 2) * 641 + ((tid >> 2) & 1) + 2 * c + t) * 64 + (tid & 3) * 16); rs[t] = asf(gl128<16>(rw, WS_RS1 + (1024 * t + 4 * tid) * 4)); }
    col_load(kin, rw, false, true);
    SB();
#pragma unroll
    for (int t = 0; t < 2; t++) {
      xr[t] = f32x4{bflo(xb[0][2 * t]), bfhi(xb[0][2 * t]), bflo(xb[0][2 * t + 1]), bfhi(xb[0][2 * t + 1])};
      xi[t] = f32x4{bflo(xb[1][2 * t]), bfhi(xb[1][2 * t]), bflo(xb[1][2 * t + 1]), bfhi(xb[1][2 * t + 1])};
    }
  } else {
    drain();
    u32x2 sp[8];
    const unsigned so = WS_SSQ + (blockIdx.x & 7) * 32768 + (1024 * q + 4 * tid) * 2, xo = WS_R3 + ((tid >> 2) * 642 + 2 * c) * 64 + (tid & 3) * 16;
    bool bs = true, bx = true;
#pragma unroll 1
    for (int spins = 0; spins < (1 << 18); spins++) {
      if (bx) {
#pragma unroll
        for (int t = 0; t < 2; t++) { xr[t] = asf(gl128<16>(rw, xo + 64 * 642 * 64 * t)); xi[t] = asf(gl128<16>(rw, xo + 64 * 642 * 64 * t + 64)); }
      }
      if (bs) {
#pragma unroll
        for (int i = 0; i < 8; i++) sp[i] = gl64<16>(rw, so + i * 4096);
      }
      unsigned m = 0;
#pragma unroll
      for (int t = 0; t < 2; t++) m = umax4(umax4(m, asu(xr[t])), asu(xi[t]));
      bx = __builtin_amdgcn_ballot_w64(m == EMPTY) != 0;
      m = 0;
#pragma unroll
      for (int i = 0; i < 8; i++) m = max(max(m, sp[i][0]), sp[i][1]);
      bs = __builtin_amdgcn_ballot_w64(m == EMPTY) != 0;
      if (!bs && !bx) break;
      nap<YSLEEP>();
    }
    put_scale(lds, q, tid, sp);
    LDS_AS f32x4* ex = (LDS_AS f32x4*)(lds + 2 * SLOT32) + tid;
    lds_bar();
    rs[0] = ex[0]; rs[1] = ex[256];
  }
#pragma unroll
  for (int t = 0; t < 2; t++) {
    u32x2 pb = pk4(xi[t] * rs[t]);
    if (c == 0) pb = u32x2{0u, 0u};
    *(LDS_AS u32x2*)(img + wo_ + t * 2048) = pk4(xr[t] * rs[t]);
    *(LDS_AS u32x2*)(img + 4096 + wo_ + t * 2048) = pb;
  }
  lds_bar();
  u32x4 ur[2][2], ui[2][2], br, bi;
  f32x4 ar[2], ai[2];
  const f32x4 z4 = {0.f, 0.f, 0.f, 0.f};
  auto rd_in = [&](int off) {
#pragma unroll
    for (int ks = 0; ks < 2; ks++)
#pragma unroll
      for (int t = 0; t < 2; t++) { const lptr p = img + off + (ks ? ro2 : ro_) + t * 2048; ur[t][ks] = ld128(p); ui[t][ks] = ld128(p + 4096); }
    SB();
  };
  auto cmul = [&](const f32x4* cc, const f32x4* sn) {
    br = pk8(ar[0] * cc[0] - ai[0] * sn[0], ar[1] * cc[1] - ai[1] * sn[1]);
    bi = pk8(ai[0] * cc[0] + ar[0] * sn[0], ai[1] * cc[1] + ar[1] * sn[1]);
  };
  rd_in(0);
#pragma unroll
  for (int ks = 0; ks < 2; ks++) {
#pragma unroll
    for (int t = 0; t < 2; t++) { ar[t] = mfma16(ur[t][ks], c64[ks], ks ? ar[t] : z4); ai[t] = mfma16(ui[t][ks], c64[ks], ks ? ai[t] : z4); }
#pragma unroll
    for (int t = 0; t < 2; t++) { ar[t] = mfma16(ui[t][ks], s64[ks], ar[t]); ai[t] = mfma16(ur[t][ks], n64[ks], ai[t]); }
  }
  const f32x4 fa[2] = {asf(kin.wf[0] << 16), asf(kin.wf[1] << 16)}, fb[2] = {asf(kin.wf[0] & 0xFFFF0000u), asf(kin.wf[1] & 0xFFFF0000u)};
  cmul(t1c, t1s);
#pragma unroll
  for (int t = 0; t < 2; t++) { ar[t] = mfma16(c32[t], br, z4); ai[t] = mfma16(c32[t], bi, z4); }
#pragma unroll
  for (int t = 0; t < 2; t++) { ar[t] = mfma16(s32[t], bi, ar[t]); ai[t] = mfma16(n32[t], br, ai[t]); }
  cmul(fa, fb);
#pragma unroll
  for (int t = 0; t < 2; t++) { ar[t] = mfma16(br, c32[t], z4); ai[t] = mfma16(bi, c32[t], z4); }
#pragma unroll
  for (int t = 0; t < 2; t++) { ar[t] = mfma16(bi, n32[t], ar[t]); ai[t] = mfma16(br, s32[t], ai[t]); }
  cmul(t2c, t2s);
#pragma unroll
  for (int t = 0; t < 2; t++) {
    *(LDS_AS u32x2*)(img + 8192 + wo_ + t * 2048) = u32x2{br[2 * t], br[2 * t + 1]};
    *(LDS_AS u32x2*)(img + 12288 + wo_ + t * 2048) = u32x2{bi[2 * t], bi[2 * t + 1]};
  }
  lds_bar();
  rd_in(8192);
#pragma unroll
  for (int ks = 0; ks < 2; ks++) {
#pragma unroll
    for (int t = 0; t < 2; t++) { ar[t] = mfma16(c64[ks], ur[t][ks], ks ? ar[t] : xr[t]); ai[t] = mfma16(c64[ks], ui[t][ks], ks ? ai[t] : xi[t]); }
#pragma unroll
    for (int t = 0; t < 2; t++) { ar[t] = mfma16(n64[ks], ui[t][ks], ar[t]); ai[t] = mfma16(s64[ks], ur[t][ks], ai[t]); }
  }
  const unsigned ob = (FIRST ? WS_RB1 : WS_RB2) + (2 * c * RBP + 8 * tid) * 2;
  st128<16>(rw, ob, clean(pk8(ar[0], ar[1])));
  st128<16>(rw, ob + RBP * 2, c == 0 ? u32x4{0u, 0u, 0u, 0u} : clean(pk8(ai[0], ai[1])));
}

DI f32x16 mfma32(u32x4 a, u32x4 b, f32x16 c) {
  return __builtin_amdgcn_mfma_f32_32x32x16_bf16(__builtin_bit_cast(bf16x8, a), __builtin_bit_cast(bf16x8, b), c, 0, 0, 0);
}
DI f32x16 mfma32k8(u32x2 a, u32x2 b, f32x16 c) {
  return __builtin_amdgcn_mfma_f32_32x32x8bf16_1k(__builtin_bit_cast(s16x4, a), __builtin_bit_cast(s16x4, b), c, 0, 0, 0);
}
DI f32x16 scores(u32x4 k1, u32x2 k2, u32x4 q1, u32x2 q2) {
  f32x16 z;
#pragma unroll
  for (int i = 0; i < 16; i++) z[i] = 0.f;
  f32x16 s = mfma32(k1, q1, z);
  asm volatile("s_nop 11" : "+v"(s));
  return mfma32k8(k2, q2, s);
}
DI float max16(const f32x16& s) {
  float m = fmaxf(s[0], s[1]);
#pragma unroll
  for (int i = 2; i < 16; i++) m = fmaxf(m, s[i]);
  return m;
}
#define STR_(x) #x
#define STR(x) STR_(x)
#define KOF(t) ((((t)&7)>>1)*11264+((t)&1)*1280)
#define VOF(t) ((((t)&7)>>1)*11264+5120+((t)&1)*1536)
#define EXP(d, s) "v_exp_f32 v" #d ", v" #s "\n\t"
#define CVT(d, s) "v_cvt_pk_bf16_f32 v" #d ", v" #s ", v[" #s "+1]\n\t"
#define STEPA(t) \
  "v_mfma_f32_32x32x16_bf16 v[48:63], v[80:83], v[136:139], v[120:135]\n\t" \
  "ds_read_b128 v[80:83], v100 offset:" STR(KOF(t+1)) "\n\t" \
  EXP(64,32) EXP(65,33) CVT(78,68) CVT(79,70) EXP(66,34) EXP(67,35) \
  "v_mfma_f32_32x32x16_bf16 v[104:119], v[90:93], v[76:79], v[104:119]\n\t" \
  "ds_read_b64_tr_b16 v[90:91], v102 offset:" STR(VOF(t)+768) "\n\t" \
  "ds_read_b64_tr_b16 v[92:93], v102 offset:" STR(VOF(t)+1152) "\n\t" \
  EXP(68,36) EXP(69,37) CVT(72,64) CVT(73,66) EXP(70,38) EXP(71,39) \
  "v_mfma_f32_32x32x8_bf16 v[48:63], v[84:85], v[140:141], v[48:63]\n\t" \
  "ds_read_b64 v[84:85], v101 offset:" STR(KOF(t+1)) "\n\t" \
  EXP(64,40) EXP(65,41) CVT(74,68) CVT(75,70) EXP(66,42) EXP(67,43) \
  "s_waitcnt lgkmcnt(4)\n\t" \
  "v_mfma_f32_32x32x16_bf16 v[0:15], v[86:89], v[72:75], v[0:15]\n\t" \
  EXP(68,44) EXP(69,45) CVT(76,64) CVT(77,66) EXP(70,46) EXP(71,47)
#define STEPB(t) \
  "s_waitcnt lgkmcnt(0)\n\t" \
  "v_mfma_f32_32x32x16_bf16 v[32:47], v[80:83], v[94:97], v[16:31]\n\t" \
  EXP(64,48) EXP(65,49) CVT(78,68) CVT(79,70) EXP(66,50) EXP(67,51) \
  "v_mfma_f32_32x32x16_bf16 v[0:15], v[90:93], v[76:79], v[0:15]\n\t" \
  EXP(68,52) EXP(69,53) CVT(72,64) CVT(73,66) EXP(70,54) EXP(71,55) \
  "v_mfma_f32_32x32x8_bf16 v[32:47], v[84:85], v[98:99], v[32:47]\n\t" \
  EXP(64,56) EXP(65,57) CVT(74,68) CVT(75,70) EXP(66,58) EXP(67,59) \
  "v_mfma_f32_32x32x16_bf16 v[104:119], v[86:89], v[72:75], v[104:119]\n\t" \
  "ds_read_b64_tr_b16 v[86:87], v102 offset:" STR(VOF(t+1)) "\n\t" \
  "ds_read_b64_tr_b16 v[88:89], v102 offset:" STR(VOF(t+1)+384) "\n\t" \
  EXP(68,60) EXP(69,61) CVT(76,64) CVT(77,66) EXP(70,62) EXP(71,63)
#define PR(n) "s_setprio " #n "\n\t"
#define CHUNK(i) PR(1) STEPA(2*(i)) PR(0) STEPB(2*(i)) "s_barrier\n\t" PR(2) STEPA(2*(i)+1) STEPB(2*(i)+1)
#define Z4(a) "v_mov_b64 v[" #a ":" #a "+1], 0\n\tv_mov_b64 v[" #a "+2:" #a "+3], 0\n\t"

#define R4(b) "v[" STR(b) ":" STR(b) "+3]"
#define MM(d, a, b) "v_mfma_f32_16x16x32_bf16 " R4(d) ", " R4(a) ", " R4(b) ", " R4(d) "\n\t"
#define GM(B, m, i) MM(16*m+4*i, B+8+4*i, B+4*m)
#define RT(B, m, hf, k) "ds_read_b64_tr_b16 v[" STR(B+4*m+2*hf) ":" STR(B+4*m+2*hf+1) "], v[84+" #m "] offset:" STR(k*8192+hf*1024) "\n\t"
#define RS(B, i, k) "ds_read_b128 " R4(B+8+4*i) ", v86 offset:" STR(i*2048+k*1024) "\n\t"
#define GSTEP(B, C, k) GM(B,0,0) RT(C,0,0,k) RT(C,0,1,k) GM(B,1,0) RS(C,0,k) RT(C,1,0,k) GM(B,0,1) RT(C,1,1,k) RS(C,1,k) GM(B,1,1) RS(C,2,k) \
  GM(B,0,2) RS(C,3,k) GM(B,1,2) GM(B,0,3) GM(B,1,3) MM(32,B,B)
#define GEMM_ASM(EXTRA) \
  Z4(0) Z4(4) Z4(8) Z4(12) Z4(16) Z4(20) Z4(24) Z4(28) Z4(32) "s_mov_b32 %[c], 0\n\ts_barrier\n\t" \
  RT(36,0,0,0) RT(36,0,1,0) RS(36,0,0) RT(36,1,0,0) RT(36,1,1,0) RS(36,1,0) RS(36,2,0) RS(36,3,0) EXTRA \
  "L_gemm_%=:\n\ts_add_u32 %[c], %[c], 1\n\ts_and_b32 %[t], %[c], 3\n\ts_cselect_b32 %[t], 1, -3\n\ts_lshl_b32 %[t], %[t], 15\n\ts_waitcnt lgkmcnt(0)\n\t" \
  GSTEP(36,60,1) \
  "v_add_u32 v84, %[t], v84\n\tv_add_u32 v85, %[t], v85\n\tv_add_u32 v86, %[t], v86\n\ts_waitcnt lgkmcnt(0)\n\ts_barrier\n\ts_setprio 1\n\t" \
  GSTEP(60,36,0) "s_setprio 0\n\t" \
  "s_cmp_lg_u32 %[c], 9\n\ts_cbranch_scc1 L_gemm_%=\n\ts_waitcnt lgkmcnt(0)\n\t" \
  GSTEP(36,60,1) "s_waitcnt lgkmcnt(0)\n\t" \
  MM(32,60,60) GM(60,0,0) GM(60,1,0) GM(60,0,1) GM(60,1,1) "s_nop 4\n\t" \
  "v_cndmask_b32 v36, v32, v33, %[m1]\n\tv_cndmask_b32 v37, v34, v35, %[m1]\n\tv_mov_b32 v38, 0x3727c5ac\n\tv_cndmask_b32 v36, v36, v37, %[m2]\n\t" \
  "v_fmac_f32 v38, 0x3acccccd, v36\n\tv_cmp_u_f32 vcc, v36, v36\n\tv_rsq_f32 v38, v38\n\ts_cbranch_vccz L_gok_%=\n\tds_write_b32 v90, v90\n\t" \
  "L_gok_%=:\n\tds_write_b32 v87, v38\n\ts_waitcnt lgkmcnt(0)\n\ts_barrier\n\t" \
  "ds_read_b32 v36, v88\n\tds_read_b32 v37, v89\n\tds_read_b32 v38, v90\n\t" \
  GM(60,0,2) GM(60,1,2) GM(60,0,3) GM(60,1,3) "s_waitcnt vmcnt(0) lgkmcnt(0)"
#define GEMM_OUT "={v[0:3]}"(acc[0][0]), "={v[4:7]}"(acc[0][1]), "={v[8:11]}"(acc[0][2]), "={v[12:15]}"(acc[0][3]), \
    "={v[16:19]}"(acc[1][0]), "={v[20:23]}"(acc[1][1]), "={v[24:27]}"(acc[1][2]), "={v[28:31]}"(acc[1][3]), \
    "={v36}"(rs[0]), "={v37}"(rs[1]), "={v38}"(vote), "+{v84}"(am0), "+{v85}"(am1), "+{v86}"(as), [c] "=&s"(c_), [t] "=&s"(t_)
#define GEMM_IN "{v87}"(a_w), "{v88}"(a_r[0]), "{v89}"(a_r[1]), "{v90}"(a_v), [m1] "s"(0xAAAAAAAAAAAAAAAAull), [m2] "s"(0xCCCCCCCCCCCCCCCCull)
#define GEMM_CLOB "memory", "scc", "vcc", "v32", "v33", "v34", "v35", "v39", "v40", "v41", "v42", "v43", "v44", "v45", "v46", "v47", "v48", "v49", "v50", \
    "v51", "v52", "v53", "v54", "v55", "v56", "v57", "v58", "v59", "v60", "v61", "v62", "v63", "v64", "v65", "v66", "v67", "v68", "v69", "v70", "v71", \
    "v72", "v73", "v74", "v75", "v76", "v77", "v78", "v79", "v80", "v81", "v82", "v83"
#define ITL(d, a) "global_load_dwordx4 " R4(d) ", v" #a ", %[it]\n\tglobal_load_dwordx4 " R4(d+4) ", v" #a ", %[it] offset:1024\n\t"

constexpr int HR = 4 * KVC;
constexpr int L_A = 2 * HR;
constexpr int L_EX = 15360;
constexpr int L_T = 4 * SLOT32, L_IA = L_T + 10240, L_U = L_A + 10240;
constexpr int L_MISC = L_A + 70 * 1024;
constexpr int LDS_FUSED = L_MISC + 2048;

DI bool poll16(rsrc_t rw, unsigned off, unsigned need) {
  const int lane = threadIdx.x & 63;
#pragma unroll 1
  for (int spins = 0; spins < (1 << 20); spins++) {
    const unsigned v = gl32<16>(rw, off + (lane & 15) * 4);
    const unsigned got = (unsigned)__builtin_amdgcn_ballot_w64(v != 0) & 0xFFFFu;
    if ((got & need) == need) return true;
    nap<1>();
  }
  return false;
}

DI void poll_aux(rsrc_t rw, unsigned off) {
#pragma unroll 1
  for (int spins = 0; spins < (1 << 20); spins++) {
    const u32x4 v = gl128<16>(rw, off + (threadIdx.x & 63) * 16);
    if (!__builtin_amdgcn_ballot_w64(min(min(v[0], v[1]), min(v[2], v[3])) == 0u)) break;
    nap<4>();
  }
}
DI void poll_tag(rsrc_t rw, u32x2 tag, unsigned long long need) {
  const unsigned o = WS_F0 + (blockIdx.x & 7) * 2048 + (threadIdx.x & 63) * 32;
#pragma unroll 1
  for (int spins = 0; spins < (1 << 20); spins++) {
    const u32x4 a = gl128<16>(rw, o), b = gl128<16>(rw, o + 16);
    const unsigned d = (a[0] ^ tag[0]) | (a[1] ^ tag[1]) | (a[2] ^ tag[0]) | (a[3] ^ tag[1]) | (b[0] ^ tag[0]) | (b[1] ^ tag[1]) | (b[2] ^ tag[0]) | (b[3] ^ tag[1]);
    if (!(__builtin_amdgcn_ballot_w64(d != 0u) & need)) break;
    nap<TSLEEP>();
  }
}
#define LAYER_VARS \
  extern __shared__ __attribute__((aligned(16))) char smem[]; \
  const lptr lds = (lptr)smem; \
  const rsrc_t rw = mk_rsrc(ws, WS_END); \
  int lane = threadIdx.x & 63; asm volatile("" : "+v"(lane)); \
  const int w = wave_id(), g = lane >> 4, r16 = lane & 15; \
  const int xcd = blockIdx.x & 7, bi = blockIdx.x >> 3, u = 8 * (xcd & 1) + (bi & 7), qt = 4 * (xcd >> 1) + (bi >> 3); \
  const int hf = u & 1, ct = u >> 1;     \
  constexpr unsigned FKV = WS_FL + (FINAL ? 32768 : 0), KV = WS_KV + (FINAL ? LAYER2 : 0), OB = WS_O + (FINAL ? LAYER2 : 0); \
  constexpr unsigned AUX = WS_AUX + (FINAL ? 1024 : 0), RB = FINAL ? WS_RB2 : WS_RB1; \
  (void)hf; (void)AUX; (void)RB; (void)FKV; \
  LDS_AS unsigned* misc = (LDS_AS unsigned*)(lds + L_MISC + 1024) + (FINAL ? 8 : 0);

template <bool FINAL>
DI void loader(char* ws, u32x2 tag) {
  LAYER_VARS
  {
    const int lw = w - 8;
    {
      unsigned vm[4];
#pragma unroll
      for (int r = 0; r < 4; r++) vm[r] = (4 * (4 * lw + r) + g) * (RBP * 2) + 256 * qt + (((r16 >> 1) ^ (g | ((r & 2) << 1))) * 32) + (r16 & 1) * 16 + 3072 - 1024 * r;
      const unsigned l0 = lds_addr(lds) + lw * 4096, v0 = 2 * lw * 20480 + lane * 16;
      constexpr int CNT = 8;
      auto lds_w = [&](int s) {
        const char* sb = ws + WS_WQ + u * 163840 + s * 2048;
        dma_s(v0, v0 + 20480 - 2048, sb, l0 + (s & 3) * SLOT32 + 16384);
      };
      auto lds_a = [&](int s) {
        dma_a(vm, ws + RB - 3072 + s * (64 * RBP * 2), l0 + (s & 3) * SLOT32); };
#pragma unroll 1
      for (int att = 0;; att++) {
        if (att == 0) { raw_bar(); if (FINAL) raw_bar(); else poll_tag(rw, tag, 0x7FFF0000ull); }
        lds_w(0); lds_w(1);
        if (att == 0) raw_bar();
        lds_w(2); lds_w(3);
        if (FINAL) dma2(40960 + lw * 2048 + lane * 16, (const char*)&g_it, lds_addr(lds) + L_IA + lw * 2048);
        auto probe = [&](int s0, int n) {
          const int pl = min(lane, n - 1);
          const unsigned po = RB + ((64 * (s0 + (pl >> 3)) + 16 * lw + 2 * (pl & 7) + 1) * RBP + 128 * qt + 126) * 2;
#pragma unroll 1
          for (int spins = 0; spins < (1 << 20); spins++) {
            if (!__builtin_amdgcn_ballot_w64(gl32<16>(rw, po) == EMPTY)) break;
            nap<XSLEEP>();
          }
        };
        if (att == 0) {
          raw_bar();
          if (blockIdx.x >= 64) nap<XDELAY>();
          probe(0, 32);
        }
        drain();
        unsigned nd = 0;
        if (att == 0 && lane == 0) nd = __hip_atomic_fetch_add(misc + 4, 1u, __ATOMIC_RELAXED, __HIP_MEMORY_SCOPE_WORKGROUP);
        if (nd == 11) st32<16>(rw, AUX + blockIdx.x * 4, 1u);
#pragma unroll 1
        for (int s = 0; s < 4; s++) lds_a(s);
        vm_bar<12>(); vm_bar<8>();
        if (att == 0) probe(4, 48);
        lds_a(4); lds_w(4);
        vm_bar<4 + CNT>();
#pragma unroll 1
        for (int s = 5; s < 10; s++) { lds_a(s); lds_w(s); vm_bar<2 * CNT>(); }
        vm_bar<CNT>();
        poll_aux(rw, AUX);
        if (!FINAL) { poll_tag(rw, tag, ~0ull); if (blockIdx.x == 0 && threadIdx.x == 512) st64<16>(rw, WS_EP, tag); }
        vm_bar<0>();
        if (FINAL) {
#pragma unroll
          for (int i = 0; i < 3; i++) { const int pc = min(3 * lw + i, 9); dma1_sc1(RB + ((64 * pc + lane) * RBP + 128 * qt + 8 * u) * 2, ws, lds_addr(lds) + L_T + pc * 1024); }
        }
        raw_bar();
        if (att || !*(volatile LDS_AS unsigned*)(misc + 2)) break;
      }
    }
    const int hh = lw >> 1, hp = lw & 1;
    const unsigned kvv = KV + (2 * u + hh) * 16 * KVC + hp * 5120 + lane * 16;
    const unsigned lkv = lds_addr(lds) + hh * HR + hp * 5120;
    const unsigned wv = (FINAL ? WS_WOP : WS_WOF) + (ct * 2 + hh) * 51200 + lane * 16;
    const unsigned myfl = FKV + (u * 16 + qt) * 128;
    vm_bar<0>();
    {
      u32x4 v[6];
#pragma unroll
      for (int i = 0; i < 6; i++) v[i] = ld128(lds + hh * HR + hp * 5120 + lane * 16 + i * 1024);
#pragma unroll
      for (int i = 0; i < 6; i++) st128<16>(rw, kvv + qt * KVC + i * 1024, clean(v[i]));
    }
    drain();
    unsigned cnt = 0;
    if (lane == 0) cnt = __hip_atomic_fetch_add(misc, 1u, __ATOMIC_RELAXED, __HIP_MEMORY_SCOPE_WORKGROUP);
    if (__builtin_amdgcn_readfirstlane(cnt) == 3 && lane < 16) st32<16>(rw, FKV + (u * 16 + lane) * 128 + qt * 4, 1u);
    auto ldkv = [&](int seq) {
      const int ch = seq == 0 ? qt : seq < 13 ? ((qt & 12) + 3 + seq) & 15 : (qt & 12) | ((qt + seq) & 3);
      dma6_sc1(kvv + ch * KVC, ws, lkv + (seq & 3) * KVC);
    };
    auto fetch = [&](int seq) {
      const unsigned src = kvv + ((((qt & 12) + 3 + seq) & 15)) * KVC;
      u32x4 v[6];
#pragma unroll 1
      for (int spins = 0; spins < (1 << 18); spins++) {
#pragma unroll
        for (int i = 0; i < 6; i++) v[i] = gl128<16>(rw, src + i * 1024);
        unsigned m = 0;
#pragma unroll
        for (int i = 0; i < 6; i++) m = umax4(m, v[i]);
        if (!__builtin_amdgcn_ballot_w64(m == EMPTY)) break;
        nap<2>();
      }
#pragma unroll
      for (int i = 0; i < 6; i++) *(LDS_AS u32x4*)(lds + hh * HR + hp * 5120 + seq * KVC + lane * 16 + i * 1024) = v[i];
    };
#pragma unroll 1
    for (int attempt = 0;; attempt++) {
      int c = -2;
      if (attempt) { ldkv(0); vm_bar<0>(); }
      else {
        fetch(1); lds_bar();
        fetch(2); poll16(rw, myfl, 12u << (((qt & 12) + 4) & 15)); ldkv(3); lds_bar();
        c = 1;
      }
#pragma unroll 1
      for (; c < 13; c++) {
        if (c == 2 && !attempt) poll16(rw, myfl, 0xFFFFu);
        if (!FINAL && !attempt) {
          if (c >= 2 && c < 9) {
            const u32x4 e = {EMPTY, EMPTY, EMPTY, EMPTY};
            const unsigned o = OB + LAYER2 + (((c - 2) * 256 + blockIdx.x) * 4 + lw) * 2048 + lane * 16;
            st128<16>(rw, o, e); st128<16>(rw, o + 1024, e);
            st128<16>(rw, ((qt & 3) < 2 && c < 8) ? kvv + LAYER2 + qt * KVC + (c - 2) * 1024 : WS_END, e);
            st128<16>(rw, c < 5 ? OB + ((4 * qt + lw) * 32 + 2 * u) * 1280 + min((c - 2) * 1024, 1536) + lane * 16 : WS_END, e);
          }
          if (c == 10) {
            drain();
            unsigned n = 0;
            if (lane == 0) n = __hip_atomic_fetch_add(misc + 3, 1u, __ATOMIC_RELAXED, __HIP_MEMORY_SCOPE_WORKGROUP);
            if (__builtin_amdgcn_readfirstlane(n) == 3 && lane < 32) st32<16>(rw, WS_FL + 65536 + (lane < 16 ? (u * 16 + lane) * 128 + qt * 4 : (r16 * 16 + qt) * 128 + 64 + u * 4), 1u);
          }
        }
        if (c >= 2) { const int e = min(4 * (c - 2) + 2 * hp, 33); dma2(wv + e * 1024, ws, lds_addr(lds) + L_A + (hh * 35 + e) * 1024); }
        ldkv(c + 3);
        if (c == 0) vm_bar<12>();
        if (c >= 2) vm_bar<8>(); else if (c >= 0) vm_bar<6>();
      }
      if (!FINAL && !attempt) poll16(rw, myfl + 65536 + hp * 64, 0xFFFFu);
      vm_bar<0>();
      dma_s(wv + (35 + 7 * hp) * 1024, wv + (35 + 7 * hp) * 1024, ws, lds_addr(lds) + hh * HR + 7 * hp * 1024);
      dma_s(wv + (39 + 7 * hp) * 1024, wv + (39 + 7 * hp) * 1024, ws, lds_addr(lds) + hh * HR + (4 + 7 * hp) * 1024);
      if (FINAL && lw == 0 && lane < 9)
        st64<16>(rw, lane < 8 ? WS_F0 + lane * 2048 + blockIdx.x * 8 : WS_GO + blockIdx.x * 64, u32x2{tag[0], ~tag[1]});
      if (FINAL && lw == 1 && blockIdx.x < 64) st64<16>(rw, WS_FX + lane * 512 + blockIdx.x * 8, u32x2{tag[0], ~tag[1]});
      raw_bar();
      vm_bar<0>();
      if (attempt || !*(volatile LDS_AS unsigned*)(misc + 1)) break;
      raw_bar();
    }
    if (!FINAL) { raw_bar(); raw_bar(); }
  }
}

template <bool FINAL>
DI void compute(char* ws, u16* out, ColIn& kin) {
  LAYER_VARS
  const int wm = w & 3, wn = w >> 2, h = lane >> 5;
  if (blockIdx.x < 64 || wn == 0) col_phase<!FINAL>(rw, lds, kin);
  else if (FINAL) {
    drain();
    nap<YIDLE>();
    u32x2 sp[8];
#pragma unroll 1
    for (int spins = 0; spins < (1 << 18); spins++) {
      unsigned m = 0;
#pragma unroll
      for (int i = 0; i < 8; i++) sp[i] = gl64<16>(rw, WS_SSQ + (blockIdx.x & 7) * 32768 + (1024 + 4 * (wm * 64 + lane)) * 2 + i * 4096);
#pragma unroll
      for (int i = 0; i < 8; i++) m = max(max(m, sp[i][0]), sp[i][1]);
      if (!__builtin_amdgcn_ballot_w64(m == EMPTY)) break;
      nap<YSLEEP>();
    }
    put_scale(lds, 1, wm * 64 + lane, sp);
    lds_bar(); raw_bar(); raw_bar();
  } else {
    raw_bar(); raw_bar(); raw_bar();
  }
  lds_bar();
  {
    drain();
    unsigned n = 0;
    if (lane == 0) n = __hip_atomic_fetch_add(misc + 4, 1u, __ATOMIC_RELAXED, __HIP_MEMORY_SCOPE_WORKGROUP);
    if (n == 11) st32<16>(rw, AUX + blockIdx.x * 4, 1u);
  }

  const int head = 2 * u + wn, kh = w & 1;
  const lptr ring = lds + wn * HR;
  const int pblk = 128 * qt + 64 * hf + 32 * (wm >> 1);
  const unsigned r3t = WS_R3 + (64 * (wm & 1) + (pblk >> 5)) * (642 * 64);
  u32x2 x4v[3]; float dc = 0.f;
  unsigned os = OB + ((4 * qt + wm) * 32 + head) * 1280 + (((lane << 2) & 16) + ((lane >> 1) & 12) + (lane & 3)) * 8;
  unsigned o1 = OB + ((pblk >> 5) * 32 + 16 * wn) * 1280 + (g >> 1) * 1280 + (g & 1) * 512 + (wm & 1) * 256 + r16 * 16;
  unsigned o2 = OB + ((pblk >> 5) * 32 + 16 * wn) * 1280 + 2 * g * 1280 + 1024 + (wm & 1) * 128 + r16 * 8;
  u32x4 o3 = {0u, 0u, 0u, 0u};
  if (!FINAL) {
#pragma unroll
    for (int i = 0; i < 3; i++) {
      const int j = 80 * ct + 48 * wn + 16 * (wn ? (i & 1) : i) + r16;
      o3[i] = (j == 1 ? WS_RB0 + ((pblk >> 5) * 641 + ((pblk >> 5) & 1) + 1) * 64 : WS_RB1 + (j * RBP + pblk) * 2) + 16 * g + 8 * (wm & 1);
    }
  } else {
    o3[3] = r3t + 64 + r16 * 4;
#pragma unroll
    for (int i = 0; i < 3; i++) o3[i] = WS_X4 + ((pblk + trow(wm & 1, r16)) * 640 + 80 * ct + 48 * wn + 16 * (wn ? (i & 1) : i) + 4 * g) * 2;
  }
  asm volatile("" : "+v"(os), "+v"(o1), "+v"(o2), "+v"(o3));
  unsigned ka = lds_addr(ring) + kh * 2560 + lane * 16, kb = lds_addr(ring) + kh * 2560 + 1024 + (lane & 31) * 8;
  unsigned va = lds_addr(ring) + kh * 3072 + (4 * h + (r16 >> 2)) * 48 + ((g & 1) ? ((lane & 3) ? 40 : 32) : (lane & 3) * 8);
  asm volatile("" : "+v"(ka), "+v"(kb), "+v"(va));
  {
    f32x4 acc[2][4]; float rs[2]; u32x8 im[3];
    unsigned mo[2], a0[2], a1[2], a2[2], a_r[2];
#pragma unroll
    for (int m = 0; m < 2; m++) {
      const int q = r16 >> 2, pr = 16 * (m ^ wn) + r16;
      mo[m] = lds_addr(lds) + (8 * g + q) * 256 + (((2 * wm + (m ^ wn)) ^ (q | ((g & 1) << 2))) * 32) + (lane & 3) * 8;
      const unsigned qb = L_A + (wn * 4 + wm) * 1280, kb = wn * HR + wm * 1280, vr = wn * HR + 5120 + (32 * wm + pr) * 48 + (g == 3 ? 32 : g * 16);
      a0[m] = ((g >> 1) ? kb : qb) + (g & 1) * 512 + pr * 16;
      a1[m] = g == 2 ? qb + 1024 + pr * 8 : vr; a2[m] = g == 2 ? kb + 1024 + pr * 8 : vr + 8;
      a_r[m] = lds_addr(lds) + L_MISC + (32 * wm + pr) * 4;
      asm volatile("" : "+v"(a0[m]), "+v"(a1[m]), "+v"(a2[m]));
    }
    const unsigned a_w = a_r[0] + (g == (r16 >> 2) ? 0 : 512), a_v = lds_addr((lptr)(misc + 2));
#pragma unroll 1
    for (int att = 0;; att++) {
      unsigned am0 = mo[0], am1 = mo[1], as = lds_addr(lds) + 16384 + wn * 8192 + lane * 16, vote; int c_, t_;
      if (!FINAL) asm volatile(GEMM_ASM("") : GEMM_OUT : GEMM_IN : GEMM_CLOB);
      else asm volatile(GEMM_ASM(ITL(96, 91) ITL(104, 92) ITL(112, 93)) : GEMM_OUT, "={v[96:103]}"(im[0]), "={v[104:111]}"(im[1]), "={v[112:119]}"(im[2])
                        : GEMM_IN, "{v91}"(w * 2048 + lane * 16), "{v92}"((w + 8) * 2048 + lane * 16), "{v93}"((w < 4 ? w + 16 : w) * 2048 + lane * 16), [it] "s"(&g_it) : GEMM_CLOB);
      if (att || !__builtin_amdgcn_readfirstlane(vote)) break;
    }
    SB();
#pragma unroll
    for (int m = 0; m < 2; m++) *(LDS_AS u32x4*)(lds + a0[m]) = pk8(acc[m][0] * rs[m], acc[m][1] * rs[m]);
    asm volatile("s_nop 1" : "+v"(acc[0][2]), "+v"(acc[1][2]), "+v"(acc[0][3]), "+v"(acc[1][3]) :: "memory");
#pragma unroll
    for (int m = 0; m < 2; m++) {
      u32x2 hi = pk4(acc[m][3] * rs[m]);
      if (g == 3) hi = u32x2{0x3F80u, 0u};
      *(LDS_AS u32x2*)(lds + a1[m]) = pk4(acc[m][2] * rs[m]);
      *(LDS_AS u32x2*)(lds + a2[m]) = hi;
    }
    lds_bar();
    if (FINAL) {
      u32x4 ia[3]; u32x2 ib[3];
#pragma unroll
      for (int T = 0; T < 3; T++) { ia[T] = ld128(lds + L_IA + T * 1024 + lane * 16); ib[T] = ld64(lds + L_IA + 3072 + T * 512 + lane * 8); }
      const f32x4 z4 = {0.f, 0.f, 0.f, 0.f};
#pragma unroll
      for (int i = 0; i < 3; i++) {
        const int G = (i < 2 || w < 4) ? w + 8 * i : w;
        const lptr tp = lds + L_T + (32 * G + 8 * g + (r16 >> 2)) * 16 + (lane & 1) * 8;
        const u32x2 f0 = ldtr(tp), f1 = ldtr(tp + 64);
#pragma unroll
        for (int P = 0; P < 2; P++) *(LDS_AS u32x2*)(lds + L_U + (r16 & 7) * 1296 + (2 * G + P) * 32 + g * 8) = pk4(mfma16(u32x4{im[i][4 * P], im[i][4 * P + 1], im[i][4 * P + 2], im[i][4 * P + 3]}, u32x4{f0[0], f0[1], f1[0], f1[1]}, z4));
      }
      wave_sync(misc + 5, 8);
      const lptr up = lds + L_U + w * 1296;
      const u32x2 f0 = ldtr(up + lane * 8), f1 = ldtr(up + 512 + lane * 8), f2 = ldtr(up + 1024 + (lane & 31) * 8);
#pragma unroll
      for (int T = 0; T < 3; T++) {
        const u32x2 v = pk4(mfma16(u32x4{f2[0], f2[1], 0u, 0u}, u32x4{ib[T][0], ib[T][1], 0u, 0u}, mfma16(u32x4{f0[0], f0[1], f1[0], f1[1]}, ia[T], z4)));
        st64<16>(rw, (T < 2 || r16 < 8) ? WS_X4 + ((128 * qt + 8 * u + w) * 640 + 16 * (16 * T + r16) + 4 * g) * 2 : WS_END, u32x2{min(v[0], EMPTY - 1), min(v[1], EMPTY - 1)});
      }
    }
  }
  f32x4 r[3];
  {
    const lptr qpa = lds + L_A + (wn * 4 + wm) * 1280, qpb = lds + L_A + (wn * 4 + (wm ^ 1)) * 1280;
    const u32x4 qa1 = ld128(qpa + lane * 16), qb1 = ld128(qpb + lane * 16);
    u32x2 qa2 = ld64(qpa + 1024 + (lane & 31) * 8), qb2 = ld64(qpb + 1024 + (lane & 31) * 8);
    if (h) { qa2 = u32x2{0u, 0u}; qb2 = qa2; }
    float ma, mb;
    {
      const u32x4 k1 = ld128(ring + lane * 16); const u32x2 k2 = ld64(ring + 1024 + (lane & 31) * 8);
      ma = max16(scores(k1, k2, qa1, qa2)); mb = max16(scores(k1, k2, qb1, qb2));
    }
    for (int attempt = 0;; attempt++) {
      ma = fmaxf(ma, __shfl_xor(ma, 32)); mb = fmaxf(mb, __shfl_xor(mb, 32));
      f32x16 cia, cib, oa, ob;
#pragma unroll
      for (int i = 0; i < 16; i++) { cia[i] = -ma; cib[i] = -mb; }
      int cnt = 4;
      asm volatile(
          "ds_read_b128 v[80:83], v100\n\t"
          "ds_read_b64 v[84:85], v101\n\t"
          "ds_read_b64_tr_b16 v[86:87], v102 offset:5120\n\t"
          "ds_read_b64_tr_b16 v[88:89], v102 offset:5504\n\t"
          Z4(0) Z4(68) Z4(76) Z4(90)
          "s_waitcnt lgkmcnt(0)\n\t"
          "v_mfma_f32_32x32x16_bf16 v[32:47], v[80:83], v[94:97], v[16:31]\n\t"
          Z4(4) Z4(8) Z4(12) Z4(104) Z4(108) Z4(112) Z4(116)
          "v_mfma_f32_32x32x8_bf16 v[32:47], v[84:85], v[98:99], v[32:47]\n\ts_nop 9\n\t"
          "L_attn_%=:\n\t"
          CHUNK(0) CHUNK(1) CHUNK(2) CHUNK(3)
          "s_sub_u32 %[cnt], %[cnt], 1\n\ts_cmp_lg_u32 %[cnt], 0\n\ts_cbranch_scc1 L_attn_%=\n\t"
          "s_setprio 0\n\t" CVT(78,68) CVT(79,70) "s_waitcnt lgkmcnt(0)\n\ts_nop 1\n\t"
          "v_mfma_f32_32x32x16_bf16 v[104:119], v[90:93], v[76:79], v[104:119]\n\ts_nop 15\n\t"
          : "={v[0:15]}"(oa), "={v[104:119]}"(ob), [cnt] "+s"(cnt)
          : "{v[16:31]}"(cia), "{v[120:135]}"(cib), "{v[94:97]}"(qa1), "{v[98:99]}"(qa2), "{v[136:139]}"(qb1), "{v[140:141]}"(qb2),
            "{v100}"(ka), "{v101}"(kb), "{v102}"(va)
          : "memory", "scc", "v32", "v33", "v34", "v35", "v36", "v37", "v38", "v39", "v40", "v41", "v42", "v43", "v44", "v45", "v46", "v47",
            "v48", "v49", "v50", "v51", "v52", "v53", "v54", "v55", "v56", "v57", "v58", "v59", "v60", "v61", "v62", "v63", "v64", "v65",
            "v66", "v67", "v68", "v69", "v70", "v71", "v72", "v73", "v74", "v75", "v76", "v77", "v78", "v79", "v80", "v81", "v82", "v83",
            "v84", "v85", "v86", "v87", "v88", "v89", "v90", "v91", "v92", "v93");
      LDS_AS f32x4* ex = (LDS_AS f32x4*)(ring + L_EX + wm * 3072) + lane;
#pragma unroll
      for (int i = 0; i < 3; i++) ex[i * 64] = f32x4{ob[4 * i], ob[4 * i + 1], ob[4 * i + 2], ob[4 * i + 3]};
      f32x4 cs = {0.f, 0.f, 0.f, 0.f};
#pragma unroll
      for (int i = 0; i < 3; i++) cs += f32x4{oa[4 * i], oa[4 * i + 1], oa[4 * i + 2], oa[4 * i + 3]} + f32x4{ob[4 * i], ob[4 * i + 1], ob[4 * i + 2], ob[4 * i + 3]};
      if (!(fabsf(cs[0] + cs[1] + cs[2] + cs[3]) < INFINITY) || (h && !(oa[8] + ob[8] < 0x1p60f))) misc[1] = 1u;
      lds_bar();
      const unsigned vote = *(volatile LDS_AS unsigned*)(misc + 1);
      LDS_AS f32x4* ey = (LDS_AS f32x4*)(ring + L_EX + (wm ^ 1) * 3072) + lane;
#pragma unroll
      for (int i = 0; i < 3; i++) r[i] = ey[i * 64] + f32x4{oa[4 * i], oa[4 * i + 1], oa[4 * i + 2], oa[4 * i + 3]};
      if (attempt || !vote) break;
      lds_bar();
      ma = -INFINITY; mb = -INFINITY;
      for (int t = 0; t < 64; t++) {
        const unsigned kb_ = KV + (head * 16 + (t >> 2)) * KVC + (t & 3) * 1280;
        const u32x4 k1 = gl128<16>(rw, kb_ + lane * 16); const u32x2 k2 = gl64<16>(rw, kb_ + 1024 + (lane & 31) * 8);
        ma = fmaxf(ma, max16(scores(k1, k2, qa1, qa2))); mb = fmaxf(mb, max16(scores(k1, k2, qb1, qb2)));
      }
      raw_bar();
    }
  }
  {
    const unsigned lb = __float_as_uint(r[2][0]);
    const float il = __builtin_amdgcn_rcpf(__uint_as_float(__builtin_amdgcn_permlane32_swap(lb, lb, false, false)[1]));
    const unsigned sl8 = os & 255;
    st128<16>(rw, os + sl8 + h * 512, clean(pk8(r[0] * il, r[1] * il)));
    const u32x4 t = clean(pk8(r[2] * il, r[2] * il));
    st64<16>(rw, h ? WS_END : os + 1024, u32x2{t[0], t[1]});
  }
  const lptr wa = lds + L_A + wn * 35 * 1024 + lane * 16, wb = ring + lane * 16;
  constexpr int NPRE = 15;
  u32x4 bf[50];
  f32x4 acc[5] = {};
#pragma unroll
  for (int j = 0; j < NPRE; j++) bf[j] = ld128(wa + j * 1024);
#pragma unroll
  for (int j = 0; j < 5; j++) asm volatile("" : "+v"(acc[j]));
  if (FINAL) dc = __uint_as_float(gl32<16>(rw, o3[3]));
#pragma unroll 1
  for (int spins = 0; spins < (1 << 18); spins++) {
#pragma unroll
    for (int i = 0; i < 3; i++) x4v[i] = gl64<16>(rw, o3[i]);
    if (!FINAL || !__builtin_amdgcn_ballot_w64(umax4(max(x4v[0][0], x4v[0][1]), u32x4{x4v[1][0], x4v[1][1], x4v[2][0], x4v[2][1]}) == EMPTY)) break;
    nap<8>();
  }
  drain();
  {
    u32x4 af[10];
    auto ldf = [&](int i) {
      if (i < 8) af[i] = __builtin_amdgcn_raw_buffer_load_b128(rw, o1, i * 2 * 1280, 16);
      else { const u32x2 a = __builtin_amdgcn_raw_buffer_load_b64(rw, o2, (i - 8) * 8 * 1280, 16), b = __builtin_amdgcn_raw_buffer_load_b64(rw, o2, ((i - 8) * 8 + 1) * 1280, 16);
             af[i] = u32x4{a[0], a[1], b[0], b[1]}; }
    };
#pragma unroll
    for (int i = 0; i < 10; i++) ldf(i);
#pragma unroll 1
    for (int spins = 0; spins < (1 << 18); spins++) {
      unsigned m = 0;
#pragma unroll
      for (int i = 0; i < 10; i++) m = umax4(m, af[i]);
      if (!__builtin_amdgcn_ballot_w64(m == EMPTY)) break;
      nap<RSLEEP>();
#pragma unroll
      for (int i = 0; i < 10; i++) if (__builtin_amdgcn_ballot_w64(umax4(0, af[i]) == EMPTY)) ldf(i);
    }
#pragma unroll
    for (int i = 0; i < 10; i++) asm volatile("" : "+v"(af[i]));
    asm volatile("s_nop 1" : "+v"(acc[0]), "+v"(acc[1]), "+v"(acc[2]), "+v"(acc[3]), "+v"(acc[4]));
    SB();
#pragma unroll
    for (int i = 0; i < 10; i++) {
#pragma unroll
      for (int a = 0; a < 5; a++) {
        if (FINAL) mfma16v(acc[a], bf[5 * i + a], af[i]); else mfma16v(acc[a], af[i], bf[5 * i + a]);
        const int j = 5 * i + a + NPRE; if (j < 50) bf[j] = ld128(j < 35 ? wa + j * 1024 : wb + (j - 35) * 1024);
        SB();
      }
    }
  }
  asm volatile("s_nop 7\n\ts_nop 1" : "+v"(acc[0]), "+v"(acc[1]), "+v"(acc[2]), "+v"(acc[3]), "+v"(acc[4]) :: "memory");
  if (!FINAL) { col_load(kin, rw, true, true); SB(); }
  {
    LDS_AS f32x4* ex = (LDS_AS f32x4*)(ring + L_EX + 12288 + wm * 3072) + lane;
    LDS_AS f32x4* ey = (LDS_AS f32x4*)(lds + (wn ^ 1) * HR + L_EX + 12288 + wm * 3072) + lane + 64 * wn;
    ex[0] = acc[2]; ex[64] = acc[3]; ex[128] = acc[4];
    dc *= 0.039528470752104741f;
#pragma unroll
    for (int i = 0; i < 3; i++) { acc[i] += f32x4{bflo(x4v[i][0]), bfhi(x4v[i][0]), bflo(x4v[i][1]), bfhi(x4v[i][1])}; if (FINAL) acc[i] += dc; }
    lds_bar();
#pragma unroll
    for (int i = 0; i < 3; i++) acc[i] += ey[64 * i];
  }
  if (FINAL) {
    const rsrc_t ro = mk_rsrc(out, 2048 * 640 * 2);
    const unsigned op = (rowof(pblk + trow(wm & 1, r16)) * 640 + 80 * ct + 48 * wn + 4 * g) * 2;
#pragma unroll
    for (int i = 0; i < 3; i++)
      if (i < 2 || wn == 0) st64<0>(ro, op + 32 * i, pk4(acc[i]));
  } else {
    f32x4 sq = {0.f, 0.f, 0.f, 0.f};
#pragma unroll
    for (int i = 0; i < 3; i++)
      if (i < 2 || wn == 0) {
        const int j = 80 * ct + 48 * wn + 16 * i + r16;
        st128<16>(rw, r3t + j * 64 + g * 16, clean(asu(acc[i])));
        if (j != 1) sq += acc[i] * acc[i];
      }
#pragma unroll
    for (int e = 0; e < 4; e++) sq[e] = row16_sum(sq[e]);
    LDS_AS float* sl = (LDS_AS float*)(lds + L_MISC);
    if (r16 == 0) *(LDS_AS f32x4*)(sl + wn * 64 + 32 * (wm & 1) + 16 * (wm >> 1) + 4 * g) = sq;
    lds_bar();
    if (w == 0) {
      LDS_AS f32x4* s4 = (LDS_AS f32x4*)sl + 2 * (lane & 7);
      st128<16>(rw, WS_SSQ + (lane >> 3) * 32768 + (ct * 2048 + 1024 * ((lane >> 2) & 1) + 64 * qt + 32 * hf + 8 * (lane & 3)) * 2, clean(pk8(s4[0] + s4[16], s4[1] + s4[17])));
    }
  }
}

__global__ void __launch_bounds__(768, 3) k_main(const u16* x, const u16* wr, const u16* wi, const u16* wqkv, const u16* wo, char* ws, u16* out) {
  if (wave_id() >= 8) {
    extern __shared__ __attribute__((aligned(16))) char smem[];
    const unsigned long long t0 = __builtin_amdgcn_s_memrealtime();
    const int job = blockIdx.x;
    if (job < 144) raw_bar();
    const rsrc_t rw = mk_rsrc(ws, WS_END);
    __builtin_amdgcn_s_setprio(3);
    if (threadIdx.x < 528) ((LDS_AS unsigned*)((lptr)smem + L_MISC + 1024))[threadIdx.x - 512] = 0u;
    u32x2 tag = gl64<16>(rw, WS_EP);
    tag[0] += 1u;
    if (job < 228) raw_bar();
    if (job < 64) raw_bar();
    asm volatile("s_waitcnt vmcnt(0) lgkmcnt(0)\n\ts_barrier" : "+v"(tag) :: "memory");
    if (wave_id() == 8) {
      if (job < 64) st64<16>(rw, WS_FX + (threadIdx.x - 512) * 512 + job * 8, tag);
      if (threadIdx.x < 520) st64<16>(rw, WS_F0 + (threadIdx.x - 512) * 2048 + job * 8, tag);
      if (job >= 248) {
        while (__builtin_amdgcn_s_memrealtime() - t0 < TPOLL) nap<2>();
        poll_tag(rw, tag, 0xFE0FFFF000000000ull);
        if (threadIdx.x < 544) st64<16>(rw, WS_GO + ((threadIdx.x - 512) * 8 + (job & 7)) * 64, tag);
      }
      bool go = false;
#pragma unroll 1
      for (int spins = 0; spins < (1 << 20); spins++) {
        const bool look = go || job < 144 || __builtin_amdgcn_s_memrealtime() - t0 > TLOOK;
        const u32x2 x_ = gl64<16>(rw, look ? WS_FX + (job & 63) * 512 + (threadIdx.x - 512) * 8 : WS_END), v = gl64<16>(rw, go ? WS_END : WS_GO + job * 64);
        go = go || __builtin_amdgcn_readfirstlane((v[0] ^ tag[0]) | (v[1] ^ tag[1])) == 0;
        if (go && look && !__builtin_amdgcn_ballot_w64(((x_[0] ^ tag[0]) | (x_[1] ^ tag[1])) != 0u)) break;
        nap<GSLEEP>();
      }
    }
    if (wave_id() > 8) {
    while (__builtin_amdgcn_s_memrealtime() - t0 < TFILL) nap<2>();
    const int i0 = blockIdx.x * 192 + threadIdx.x - 576;
#pragma unroll
    for (int it = 0; it < 4; it++) {
      const int k = it * 49152 + i0, b = k / 1408;
      st128<16>(rw, k < 128 * 1408 ? WS_KV + b * 4 * KVC + (k - b * 1408) * 16 : WS_END, u32x4{EMPTY, EMPTY, EMPTY, EMPTY});
    }
    }
    raw_bar();
    loader<false>(ws, tag);
    loader<true>(ws, tag);
  } else {
    prep(x, wr, wi, wqkv, wo, ws);
    asm volatile("s_waitcnt vmcnt(0) lgkmcnt(0)\n\ts_barrier" ::: "memory");
    ColIn kin = {};
    compute<false>(ws, out, kin);
    compute<true>(ws, out, kin);
  }
}

extern "C" void kernel_launch(void* const* d_in, const int* in_sizes, int n_in, void* d_out, int out_size, void* d_ws, size_t ws_size,
                              hipStream_t stream) {
  const u16* x = (const u16*)d_in[0];
  const u16* wr = (const u16*)d_in[1];
  const u16* wi = (const u16*)d_in[2];
  const u16* wqkv = (const u16*)d_in[4];
  const u16* wo = (const u16*)d_in[5];
  char* ws = (char*)d_ws;
  (void)hipFuncSetAttribute((const void*)k_main, hipFuncAttributeMaxDynamicSharedMemorySize, LDS_FUSED);
  k_main<<<256, 768, LDS_FUSED, stream>>>(x, wr, wi, wqkv, wo, ws, (u16*)d_out);
}
```

```cpp
#include <hip/hip_runtime.h>
#include <hip/hip_bf16.h>

typedef unsigned short u16;
typedef __attribute__((ext_vector_type(16))) float f32x16;
typedef __attribute__((ext_vector_type(4))) float f32x4;
typedef __attribute__((ext_vector_type(2))) float f32x2;
typedef __attribute__((ext_vector_type(8))) __bf16 bf16x8;
typedef __attribute__((ext_vector_type(2))) __bf16 bf16x2;
typedef __attribute__((ext_vector_type(4))) short s16x4;
typedef __attribute__((ext_vector_type(4))) unsigned u32x4;
typedef __attribute__((ext_vector_type(2))) unsigned u32x2;
typedef __attribute__((ext_vector_type(8))) unsigned u32x8;
#define DI __device__ __forceinline__
#define LDS_AS __attribute__((address_space(3)))
typedef LDS_AS char* lptr;

constexpr double cx_cos_small(double x) { double x2 = x * x, t = 1, s = 1; for (int i = 1; i < 12; i++) { t *= -x2 / ((2 * i - 1) * (2 * i)); s += t; } return s; }
constexpr double cx_sin_small(double x) { double x2 = x * x, t = x, s = x; for (int i = 1; i < 12; i++) { t *= -x2 / ((2 * i) * (2 * i + 1)); s += t; } return s; }
constexpr double cx_cos640(int m) {
  m %= 640; if (m > 320) m = 640 - m; double sg = 1; if (m > 160) { m = 320 - m; sg = -1; }
  const double w = 6.283185307179586476925 / 640; return sg * (m <= 80 ? cx_cos_small(w * m) : cx_sin_small(w * (160 - m)));
}
constexpr u16 cx_bf16(double d) { float f = (float)d; unsigned u = __builtin_bit_cast(unsigned, f); u += 0x7FFFu + ((u >> 16) & 1u); return (u16)(u >> 16); }
struct Cos640 { double v[640]; };
constexpr Cos640 mk_cos() { Cos640 c{}; for (int m = 0; m < 640; m++) c.v[m] = cx_cos640(m); return c; }
constexpr Cos640 COS = mk_cos();
__host__ __device__ constexpr int kf_of(int G, int i) { return G ? G + 40 * i : (i == 0 ? 320 : i < 8 ? 40 * i : 40 * i - 300); }
__host__ __device__ constexpr int freq_of(int G, int i) { return kf_of(G, i) > 320 ? 640 - kf_of(G, i) : kf_of(G, i); }
constexpr double SA = 1.91318, SA0 = 1.9140625, S64 = 1.86954, S32 = 1.6245;
constexpr double a1val(int out, int a) {
  if (out >= 40 || a >= 40) return 0;
  const int G = out >> 1, part = out & 1;
  if (G == 0) return (part && (a & 1)) ? -SA0 : SA0;
  return SA * COS.v[(16 * a * G + 160 * part) % 640];
}
constexpr double m2val(int G, int o, int k) {
  const int i = o >> 1, po = o & 1, part = k >> 4, b = k & 15, kf = kf_of(G, i);
  const double c = COS.v[(b * kf) % 640], sn = COS.v[(b * kf + 480) % 640];
  if (G == 0) {
    if (part != (i >= 8)) return 0;
    if (i == 0) return 0.039528470752104741 / SA0 * (po ? 1.0 : c);
    return 0.055901699437494742 / SA0 * (po ? -sn : c);
  }
  const double v = po ? (part ? c : -sn) : (part ? sn : c);
  return 0.055901699437494742 / SA * ((po && kf > 320) ? -v : v);
}
struct DTab { u16 a1[3][64][8]; u16 a1b[3][64][4]; u16 pad[256]; u16 m2[20][2][64][8]; };
constexpr DTab mk_dtab() {
  DTab t{};
  for (int l = 0; l < 64; l++) {
    const int r = l & 15, g = l >> 4;
    for (int T = 0; T < 3; T++) {
      for (int e = 0; e < 8; e++) t.a1[T][l][e] = cx_bf16(a1val(16 * T + r, 16 * (e >> 2) + 4 * g + (e & 3)));
      for (int e = 0; e < 4; e++) t.a1b[T][l][e] = cx_bf16(a1val(16 * T + r, 32 + 4 * g + e));
    }
    for (int G = 0; G < 20; G++) for (int T = 0; T < 2; T++) for (int e = 0; e < 8; e++)
      t.m2[G][T][l][e] = cx_bf16(m2val(G, 8 * (r >> 2) + 4 * T + (r & 3), 8 * g + e));
  }
  return t;
}
static __device__ const __attribute__((aligned(1024))) DTab g_dt = mk_dtab();

struct ITab { u16 m2[20][2][64][8]; u16 a1[3][64][8]; u16 a1b[3][64][4]; u16 pad[1792]; };
constexpr ITab mk_itab() {
  ITab t{};
  for (int l = 0; l < 64; l++) {
    const int r = l & 15, g = l >> 4;
    for (int T = 0; T < 3; T++) {
      for (int e = 0; e < 8; e++) t.a1[T][l][e] = cx_bf16(a1val(16 * (e >> 2) + 4 * g + (e & 3), 16 * T + r));
      for (int e = 0; e < 4; e++) t.a1b[T][l][e] = cx_bf16(a1val(32 + 4 * g + e, 16 * T + r));
    }
    for (int G = 0; G < 20; G++) for (int P = 0; P < 2; P++) for (int e = 0; e < 8; e++) t.m2[G][P][l][e] = cx_bf16(m2val(G, 8 * g + e, 16 * P + r));
  }
  return t;
}
static __device__ const __attribute__((aligned(1024))) ITab g_it = mk_itab();

struct CTab { u16 v[10 * 512]; };
constexpr CTab mk_ctab() {
  CTab c{};
  for (int f = 0; f < 10; f++) for (int l = 0; l < 64; l++) for (int e = 0; e < 8; e++) {
    const int r = l & 15, g = l >> 4;
    const int m = f < 8 ? (8 * g + e) * (16 * (f >> 1) + r) : 2 * r * (16 * (e >> 2) + 4 * g + (e & 3));
    c.v[(f * 64 + l) * 8 + e] = cx_bf16((f < 8 ? S64 : S32) * COS.v[10 * ((m + (f & 1) * 48) & 63)]);
  }
  return c;
}
static __device__ const __attribute__((aligned(1024))) CTab g_ct = mk_ctab();
constexpr float RSK = (float)(2048.0 * S64 * S64 * S32 * S32 * 2048.0 * S64 * S64 * S32 * S32 / 640.0);

constexpr int RP = 2048 + 64, RBP = 2048 + 128;
constexpr unsigned RBSZ = 640 * RBP * 2;
constexpr unsigned WS_FL = 0;
constexpr unsigned WS_AUX = WS_FL + 3 * 32768;
constexpr unsigned WS_F0 = WS_AUX + 2048;
constexpr unsigned WS_GO = WS_F0 + 8 * 2048;
constexpr unsigned WS_EP = WS_GO + 256 * 64;
constexpr unsigned WS_RS1 = WS_EP + 256;
constexpr unsigned WS_RB0 = WS_RS1 + 8192;
constexpr unsigned WS_RB1 = WS_RB0 + RBSZ;
constexpr unsigned WS_WF = WS_RB1 + RBSZ;
constexpr unsigned WS_WQ = WS_WF + 320 * 8192;
constexpr unsigned WS_WOF = WS_WQ + 16 * 8 * 20 * 1024;
constexpr unsigned WS_WOP = WS_WOF + 40 * 20 * 1024;
constexpr int KVC = 11264;
constexpr unsigned WS_KV = WS_WOP + 40 * 20 * 1024;
constexpr unsigned WS_O = WS_KV + 32 * 16 * KVC;
constexpr unsigned LAYER2 = 32 * 16 * KVC + 32 * 64 * 1280;
constexpr unsigned WS_X4 = WS_KV + 2 * LAYER2;
constexpr unsigned WS_R3 = WS_X4 + 2048 * 640 * 2;
constexpr unsigned WS_RB2 = WS_R3 + 640 * RP * 4;
constexpr unsigned WS_SSQ = WS_RB2 + RBSZ;
constexpr unsigned EMPTY = 0xFFFFFFFFu;
constexpr unsigned WS_END = WS_SSQ + 8 * 2048 * 16;

constexpr int XDELAY = 20, K1DELAY = 40, YIDLE = 12, TPOLL = 250, TSLEEP = 1, GSLEEP = 2, TFILL = 400, YSLEEP = 4, XSLEEP = 1, RSLEEP = 2;

#define SB() __builtin_amdgcn_sched_barrier(0)
typedef __amdgpu_buffer_rsrc_t rsrc_t;
DI rsrc_t mk_rsrc(const void* p, unsigned bytes) { return __builtin_amdgcn_make_buffer_rsrc((void*)p, 0, bytes, 0x00020000); }
template <int AUX> DI void st128(rsrc_t r, unsigned off, u32x4 v) { __builtin_amdgcn_raw_buffer_store_b128(v, r, off, 0, AUX); }
template <int AUX> DI void st64(rsrc_t r, unsigned off, u32x2 v) { __builtin_amdgcn_raw_buffer_store_b64(v, r, off, 0, AUX); }
template <int AUX> DI void st32(rsrc_t r, unsigned off, unsigned v) { __builtin_amdgcn_raw_buffer_store_b32(v, r, off, 0, AUX); }
template <int AUX> DI u32x4 gl128(rsrc_t r, unsigned off) { return __builtin_amdgcn_raw_buffer_load_b128(r, off, 0, AUX); }
template <int AUX> DI u32x2 gl64(rsrc_t r, unsigned off) { return __builtin_amdgcn_raw_buffer_load_b64(r, off, 0, AUX); }
template <int AUX> DI unsigned gl32(rsrc_t r, unsigned off) { return __builtin_amdgcn_raw_buffer_load_b32(r, off, 0, AUX); }
DI u32x4 asu(f32x4 v) { return __builtin_bit_cast(u32x4, v); }
DI f32x4 asf(u32x4 v) { return __builtin_bit_cast(f32x4, v); }
DI unsigned pk2(float a, float b) { f32x2 v = {a, b}; return __builtin_bit_cast(unsigned, __builtin_convertvector(v, bf16x2)); }
DI u32x2 pk4(f32x4 a) { return u32x2{pk2(a[0], a[1]), pk2(a[2], a[3])}; }
DI u32x4 pk8(f32x4 a, f32x4 b) { return u32x4{pk2(a[0], a[1]), pk2(a[2], a[3]), pk2(b[0], b[1]), pk2(b[2], b[3])}; }
DI u32x4 clean(u32x4 v) { return __builtin_elementwise_min(v, u32x4{EMPTY - 1, EMPTY - 1, EMPTY - 1, EMPTY - 1}); }
DI unsigned umax4(unsigned m, u32x4 v) { return max(max(max(m, v[0]), v[1]), max(v[2], v[3])); }
DI float bflo(unsigned u) { return __uint_as_float(u << 16); }
DI float bfhi(unsigned u) { return __uint_as_float(u & 0xFFFF0000u); }
DI int rowof(int p) { return 32 * (((p >> 5) & 0x3C) + (p & 3)) + ((p << 2) & 16) + ((p >> 3) & 15); }
DI int rpos(int p) { return ((p << 8) & 1024) + ((p >> 3) << 2) + (p & 3); }
DI int trow(int T, int r) { return 8 * (r >> 2) + 4 * T + (r & 3); }
template <int CTRL> DI float dpp_add(float v) { return v + __int_as_float(__builtin_amdgcn_update_dpp(0, __float_as_int(v), CTRL, 0xF, 0xF, true)); }
DI float row16_sum(float v) { v = dpp_add<0x128>(v); v = dpp_add<0x124>(v); v = dpp_add<0x122>(v); return dpp_add<0x121>(v); }
DI f32x4 mfma16(u32x4 a, u32x4 b, f32x4 c) {
  return __builtin_amdgcn_mfma_f32_16x16x32_bf16(__builtin_bit_cast(bf16x8, a), __builtin_bit_cast(bf16x8, b), c, 0, 0, 0);
}
DI void mfma16v(f32x4& c, u32x4 a, u32x4 b) { asm volatile("v_mfma_f32_16x16x32_bf16 %0, %1, %2, %0" : "+v"(c) : "v"(a), "v"(b)); }
DI u32x2 ldtr(lptr p) { return __builtin_bit_cast(u32x2, __builtin_amdgcn_ds_read_tr16_b64_v4i16((LDS_AS s16x4*)p)); }
DI u32x4 ld128(lptr p) { return *(LDS_AS u32x4*)p; }
DI u32x2 ld64(lptr p) { return *(LDS_AS u32x2*)p; }
#define GL(v, s, o, x) "global_load_lds_dwordx4 " v ", " s " offset:" #o x "\n\t"
#define M0SET "s_mov_b32 %[sv], m0\n\ts_mov_b32 m0, %[l]\n\ts_nop 2\n\t"
#define M0ADD(x) "s_add_u32 m0, m0, " x "\n\ts_nop 0\n\t"
#define M0RST "s_mov_b32 m0, %[sv]"
DI void dma2(unsigned v, const char* sb, unsigned l) { unsigned sv;
  asm volatile(M0SET GL("%[v]", "%[s]", 0, " sc1") GL("%[v]", "%[s]", 1024, " sc1") M0RST : [sv] "=&s"(sv) : [v] "v"(v), [s] "s"(sb), [l] "s"(l) : "memory"); }
DI void dma4(unsigned v, const char* sb, unsigned l) { unsigned sv;
  asm volatile(M0SET GL("%[v]", "%[s]", 0, "") GL("%[v]", "%[s]", 1024, "") GL("%[v]", "%[s]", 2048, "") GL("%[v]", "%[s]", 3072, "") M0RST
               : [sv] "=&s"(sv) : [v] "v"(v), [s] "s"(sb), [l] "s"(l) : "memory"); }
DI void dma6_sc1(unsigned v, const char* sb, unsigned l) { unsigned sv;
  asm volatile(M0SET GL("%[v]", "%[s]", 0, " sc1") GL("%[v]", "%[s]", 1024, " sc1") GL("%[v]", "%[s]", 2048, " sc1") M0ADD("3072")
               GL("%[w]", "%[s]", 0, " sc1") GL("%[w]", "%[s]", 1024, " sc1") GL("%[w]", "%[s]", 2048, " sc1") M0RST
               : [sv] "=&s"(sv) : [v] "v"(v), [w] "v"(v + 3072), [s] "s"(sb), [l] "s"(l) : "memory", "scc"); }
template <int N> DI void vm_bar() { asm volatile("s_waitcnt vmcnt(%0)\n\ts_barrier" ::"i"(N) : "memory"); }
DI void lds_bar() { asm volatile("s_waitcnt lgkmcnt(0)\n\ts_barrier" ::: "memory"); }
DI void raw_bar() { asm volatile("s_barrier" ::: "memory"); }
template <int N> DI void nap() { __builtin_amdgcn_s_sleep(N); asm volatile("" ::: "memory"); }
DI void drain() { asm volatile("s_waitcnt vmcnt(0)" ::: "memory"); }
#define CODE_ADDR(sym) ({ unsigned lo_, hi_; \
    asm volatile("s_getpc_b64 vcc\n\ts_add_u32 %0, vcc_lo, " sym "@rel32@lo+4\n\ts_addc_u32 %1, vcc_hi, " sym "@rel32@hi+12" : "=s"(lo_), "=s"(hi_) :: "vcc", "scc"); \
    (const unsigned*)(((unsigned long long)hi_ << 32) | lo_); })
#define SYM_MAIN "_Z6k_mainPKtS0_S0_S0_S0_PcPt"
constexpr int CODE_LINES = 380;
DI int wave_id() { return __builtin_amdgcn_readfirstlane(threadIdx.x >> 6); }
DI unsigned lds_addr(lptr p) { return (unsigned)(size_t)p; }

DI int kappa_to_i(int kap) {
  int ks = kap >> 5, g = (kap >> 3) & 3, e = kap & 7;
  if (ks < 16) return (2 * ks + (g >> 1)) * 20 + 8 * (e >> 2) + 4 * (g & 1) + (e & 3);
  return (8 * (ks - 16) + 2 * g + (e >> 2)) * 20 + 16 + (e & 3);
}

DI unsigned wo_frag(int nt, int ks) {
  const int ct = nt / 5, c5 = nt - 5 * ct, kh = ks < 16 ? ks >> 3 : (ks - 16) >> 1, i = ks < 16 ? ks & 7 : 8 + (ks & 1);
  const int a = kh ? (c5 >= 3 ? c5 - 3 : c5 + 2) : c5;
  return ((ct * 2 + kh) * 50 + 5 * i + a) * 1024;
}

constexpr int SLOT32 = 32768;
DI void dma_a(const unsigned* vm, const char* mb, unsigned l) { unsigned sv;
  asm volatile(M0SET GL("%[a0]", "%[m]", 0, " sc1") GL("%[a1]", "%[m]", 1024, " sc1") GL("%[a2]", "%[m]", 2048, " sc1") GL("%[a3]", "%[m]", 3072, " sc1") M0RST
               : [sv] "=&s"(sv) : [a0] "v"(vm[0]), [a1] "v"(vm[1]), [a2] "v"(vm[2]), [a3] "v"(vm[3]), [m] "s"(mb), [l] "s"(l) : "memory"); }
DI void dma_s(unsigned v0, unsigned v1, const char* sb, unsigned l) { unsigned sv;
  asm volatile(M0SET GL("%[v0]", "%[s]", 0, " sc1") GL("%[v0]", "%[s]", 1024, " sc1") GL("%[v1]", "%[s]", 2048, " sc1") GL("%[v1]", "%[s]", 3072, " sc1") M0RST
               : [sv] "=&s"(sv) : [v0] "v"(v0), [v1] "v"(v1), [s] "s"(sb), [l] "s"(l) : "memory"); }
DI void dma1_sc1(unsigned v, const char* sb, unsigned l) { unsigned sv;
  asm volatile(M0SET GL("%[v]", "%[s]", 0, " sc1") M0RST : [sv] "=&s"(sv) : [v] "v"(v), [s] "s"(sb), [l] "s"(l) : "memory"); }
DI void dma1(unsigned v, const char* sb, unsigned l) { unsigned sv;
  asm volatile(M0SET GL("%[v]", "%[s]", 0, "") M0RST : [sv] "=&s"(sv) : [v] "v"(v), [s] "s"(sb), [l] "s"(l) : "memory"); }

constexpr int L_I2 = 40 * 1056, LDS_PREP = L_I2 + 20 * 2064 + 1024;
DI void prep(const u16* x, const u16* wr, const u16* wi, const u16* wqkv, const u16* wo, char* ws) {
  extern __shared__ __attribute__((aligned(16))) char smem[];
  const lptr lds = (lptr)smem;
  const int tid = threadIdx.x, lane = tid & 63, w = wave_id(), g = lane >> 4, r16 = lane & 15;
  const int job = blockIdx.x;
  if (__builtin_expect(job < 144, 1)) {
    const bool cols = job >= 64 && job < 124;
    const int cj = job - 64, cb = cj < 48 ? (cj & 48) + 2 * (cj & 7) + ((cj >> 3) & 1) : cj;
    u32x4 a1[3], m2[3][2]; u32x2 a1b[3];
    if (w < 5) dma1(tid * 16, (const char*)&g_dt, lds_addr(lds) + LDS_PREP + w * 1024);
    if (cols) {
#pragma unroll
      for (int i = 0; i < 5; i++) dma1((16 * (5 * w + i) + (lane >> 2)) * 3840 + cb * 64 + (lane & 3) * 16, (const char*)wqkv, lds_addr(lds) + (5 * w + i) * 1056);
    } else {
      const char* src = (const char*)(job < 64 ? x : wo);
      const int sl = 8 * (w & 3) + 4 * (w >> 2);
      src += (job < 64 ? rowof(32 * job + sl) : kappa_to_i(32 * (job - 124) + sl)) * 1280;
      const int st = (job < 64 ? 31 : 0) * 1280;
#pragma unroll
      for (int i = 0; i < 5; i++) dma1(1024 * i + lane * 16 + ((i == 4 || lane < 16 * i) ? i - 1 : i) * st, src, lds_addr(lds) + (5 * w + i) * 1024);
    }
    unsigned so[3][2]; float sc[2] = {1.f, 1.f};
    if (cols) {
#pragma unroll
      for (int m = 0; m < 2; m++) {
        const int c = 32 * cb + 16 * m + r16;
        const int which = c / 640, rem = c - which * 640, head = rem / 20, d = rem - head * 20, grp = d >> 2;
        const int T = grp < 4 ? (which == 2 ? 2 : 0) + (grp & 1) : (which == 1 ? 3 : 2), gs = grp < 4 ? (which == 1 ? 2 : 0) + (grp >> 1) : (which == 2 ? 3 : 2);
        if (which == 0) sc[m] = 0.22360679774997896f * 1.4426950408889634f;
#pragma unroll
        for (int i = 0; i < 3; i++) so[i][m] = WS_WQ + ((head * 4 + T) * 20 + w + 8 * i) * 1024 + (g * 16 + 4 * gs + (d & 3)) * 16;
      }
    } else if (job < 64) {
#pragma unroll
      for (int i = 0; i < 3; i++)
#pragma unroll
        for (int T = 0; T < 2; T++) so[i][T] = WS_RB0 + (job * 641 + (job & 1) + 32 * (w + 8 * i) + trow(T, r16)) * 64 + g * 16;
    } else {
#pragma unroll
      for (int i = 0; i < 3; i++) {
        const unsigned f = wo_frag(2 * (w + 8 * i) + (r16 >> 3), job - 124);
#pragma unroll
        for (int T = 0; T < 2; T++) so[i][T] = WS_WOF + f + (g * 16 + (trow(T, r16) & 15)) * 16;
      }
    }
    if (w >= 4) { so[2][0] = WS_END; so[2][1] = WS_END; }
    asm volatile("" : "+v"(so[0][0]), "+v"(so[0][1]), "+v"(so[1][0]), "+v"(so[1][1]), "+v"(so[2][0]), "+v"(so[2][1]), "+v"(sc[0]), "+v"(sc[1]));
    const f32x4 z4 = {0.f, 0.f, 0.f, 0.f};
    const lptr i2 = lds + L_I2;
    drain();
#pragma unroll
    for (int i = 0; i < 3; i++)
#pragma unroll
      for (int T = 0; T < 2; T++)
        asm volatile("global_load_dwordx4 %0, %1, %2" : "=v"(m2[i][T]) : "v"(((((i < 2 || w < 4) ? w + 8 * i : w) * 2 + T) * 64 + lane) * 16), "s"(&g_dt.m2[0][0][0][0]) : "memory");
    raw_bar();
#pragma unroll
    for (int T = 0; T < 3; T++) { a1[T] = ld128(lds + LDS_PREP + T * 1024 + lane * 16); a1b[T] = ld64(lds + LDS_PREP + 3072 + T * 512 + lane * 8); }
    f32x4 t1[4][3];
    if (cols) {
      const lptr src = lds + (4 * g + (r16 >> 2)) * 1056 + (w >> 1) * 256 + (w & 1) * 32 + (lane & 3) * 8, srb = src + (32 - 8 * (g >> 1)) * 1056;
#pragma unroll
      for (int i = 0; i < 4; i++) {
        const u32x2 f0 = ldtr(src + i * 64), f1 = ldtr(src + 16 * 1056 + i * 64), f2 = ldtr(srb + i * 64);
#pragma unroll
        for (int T = 0; T < 3; T++) t1[i][T] = mfma16(u32x4{a1b[T][0], a1b[T][1], 0u, 0u}, u32x4{f2[0], f2[1], 0u, 0u}, mfma16(a1[T], u32x4{f0[0], f0[1], f1[0], f1[1]}, z4));
      }
      const int xo = ((w >> 1) * 8) ^ (((r16 >> 1) & 3) * 16);
      const lptr dst = i2 + 2 * g * 2064 + (16 * (w & 1) + r16) * 64;
#pragma unroll
      for (int T = 0; T < 3; T++)
#pragma unroll
        for (int e = 0; e < 4; e++)
          if (T < 2 || g < 2) *(LDS_AS u32x2*)(dst + (8 * T + (e >> 1)) * 2064 + (xo ^ ((e & 1) * 32))) = u32x2{pk2(t1[0][T][e], t1[1][T][e]), pk2(t1[2][T][e], t1[3][T][e])};
    } else {
      const lptr src = lds + w * 5120 + lane * 8, srb = lds + w * 5120 + 1024 + (lane & 31) * 8;
#pragma unroll
      for (int i = 0; i < 4; i++) {
        const u32x2 f0 = ldtr(src + i * 1280), f1 = ldtr(src + i * 1280 + 512), f2 = ldtr(srb + i * 1280);
#pragma unroll
        for (int T = 0; T < 3; T++) t1[i][T] = mfma16(u32x4{f2[0], f2[1], 0u, 0u}, u32x4{a1b[T][0], a1b[T][1], 0u, 0u}, mfma16(u32x4{f0[0], f0[1], f1[0], f1[1]}, a1[T], z4));
      }
      const int xo = ((r16 & 1) * 32 + g * 8) ^ ((w & 1) * 32);
      const lptr dst = i2 + (r16 >> 1) * 2064 + w * 256;
#pragma unroll
      for (int i = 0; i < 4; i++)
#pragma unroll
        for (int T = 0; T < 3; T++)
          if (T < 2 || r16 < 8) *(LDS_AS u32x2*)(dst + T * 8 * 2064 + i * 64 + (xo ^ ((i >> 1) * 16))) = pk4(t1[i][T]);
    }
    lds_bar();
    const rsrc_t rw = mk_rsrc(ws, WS_END);
    asm volatile("s_waitcnt vmcnt(0)" : "+v"(m2[0][0]), "+v"(m2[0][1]), "+v"(m2[1][0]), "+v"(m2[1][1]), "+v"(m2[2][0]), "+v"(m2[2][1]));
    f32x4 sq[2] = {z4, z4};
    u32x4 f[3][2];
#pragma unroll
    for (int i = 0; i < 3; i++) {
      const lptr fp = i2 + ((i < 2 || w < 4) ? w + 8 * i : 0) * 2064 + r16 * 64 + ((g ^ ((r16 >> 1) & 3)) * 16);
      f[i][0] = ld128(fp); f[i][1] = ld128(fp + 1024);
    }
    if (cols) {
#pragma unroll
      for (int i = 0; i < 3; i++)
#pragma unroll
        for (int m = 0; m < 2; m++) st128<16>(rw, so[i][m], pk8(mfma16(m2[i][0], f[i][m], z4) * sc[m], mfma16(m2[i][1], f[i][m], z4) * sc[m]));
    } else {
#pragma unroll
      for (int i = 0; i < 3; i++)
#pragma unroll
        for (int T = 0; T < 2; T++) {
          const f32x4 d0 = mfma16(f[i][0], m2[i][T], z4), d1 = mfma16(f[i][1], m2[i][T], z4);
          st128<16>(rw, so[i][T], pk8(d0, d1));
          const float k = ((i == 2 && w >= 4) || (i == 0 && T == 0 && tid < 64 && r16 == 1)) ? 0.f : 1.f;
          sq[0] += d0 * d0 * k; sq[1] += d1 * d1 * k;
        }
    }
    LDS_AS float* sl = (LDS_AS float*)(lds + LDS_PREP - 1024);
    if (job < 64) {
#pragma unroll
      for (int m = 0; m < 2; m++) {
#pragma unroll
        for (int e = 0; e < 4; e++) sq[m][e] = row16_sum(sq[m][e]);
        if (r16 == 0) *(LDS_AS f32x4*)(sl + w * 32 + 8 * g + 4 * m) = sq[m];
      }
    }
    if (job < 64) {
      lds_bar();
      if (tid < 32) {
        float v = 0.f;
#pragma unroll
        for (int i = 0; i < 8; i++) v += sl[i * 32 + tid];
        st32<16>(rw, WS_RS1 + rpos(32 * job + tid) * 4, __float_as_uint(__builtin_amdgcn_rsqf((v + 640.f * 1e-5f) * RSK)));
      }
    }
    return;
  }
  const int mi = job - 144;
  const rsrc_t rw = mk_rsrc(ws, WS_END);
  if (mi < 64) {
    const int m = mi >> 3, oc = mi & 7;
    const char* src = (const char*)((w >> 2) ? wi : wr) + (64 * (4 * m + (w & 3)) + 8 * oc) * 642;
    dma4(lane * 16, src, lds_addr(lds) + w * 5152);
    dma1(4096 + lane * 16, src, lds_addr(lds) + w * 5152 + 4096);
    if (lane == 0) dma1(5120, src, lds_addr(lds) + w * 5152 + 5120);
    vm_bar<0>();
    LDS_AS u16* L = (LDS_AS u16*)lds + (tid & 7) * 321;
#pragma unroll
    for (int it = 0; it < 5; it++) {
      const int c = (tid >> 3) + 64 * it, col = freq_of(c >> 4, c & 15);
      u32x4 o;
#pragma unroll
      for (int e = 0; e < 4; e++) o[e] = (unsigned)L[e * 2576 + col] | ((unsigned)L[(4 + e) * 2576 + col] << 16);
      st128<16>(rw, WS_WF + c * 8192 + (m * 64 + 8 * oc + (tid & 7)) * 16, o);
    }
#pragma unroll
    for (int it = 0; it < 3; it++) st128<16>(rw, WS_RB1 + ((it * 64 + mi) * 512 + tid) * 16, u32x4{EMPTY, EMPTY, EMPTY, EMPTY});
  } else if (mi < 84) {
    unsigned pf0;
    {
      const unsigned* cm = CODE_ADDR(SYM_MAIN);
      const unsigned* p = mi >= 80 ? (const unsigned*)&g_it + min(tid, 359) * 32 : (mi & 8) ? (const unsigned*)&g_dt.m2[0][0][0][0] + min(tid, 319) * 32 : tid < 80 ? (const unsigned*)&g_ct + tid * 32 : cm + min(tid - 80, CODE_LINES - 1) * 32;
      asm volatile("global_load_dword %0, %1, off" : "=v"(pf0) : "v"(p) : "memory");
    }
    const char* src = (const char*)wo + kappa_to_i(32 * (mi - 64) + 8 * (w & 3) + 4 * (w >> 2)) * 1280;
    dma4(lane * 16, src, lds_addr(lds) + w * 5152);
    dma1(4096 + lane * 16, src, lds_addr(lds) + w * 5152 + 4096);
    asm volatile("s_waitcnt vmcnt(0)\n\ts_barrier" : "+v"(pf0) :: "memory");
#pragma unroll
    for (int i = 0; i < 5; i++) {
      const lptr a = lds + g * 5152 + (r16 >> 2) * 1280 + w * 160 + (i == 2 ? 64 + (lane & 3) * 8 : (i > 2 ? 96 : 0) + (lane & 3) * 16 + (i == 1 || i == 4 ? 8 : 0));
      const u32x2 lo = ldtr(a), hi = ldtr(a + 4 * 5152);
      st128<16>(rw, WS_WOP + wo_frag(5 * w + i, mi - 64) + lane * 16, u32x4{lo[0], lo[1], hi[0], hi[1]});
    }
  } else {
    const int i0 = (mi - 84) * 512 + tid;
    nap<K1DELAY>();
    if (i0 < 6272) st128<16>(rw, WS_FL + i0 * 16, u32x4{0u, 0u, 0u, 0u});
    if (i0 < 10240) st128<16>(rw, WS_WQ + (((i0 / 320) * 4 + 3) * 80 + (i0 % 320 >> 2)) * 256 + (12 + (i0 & 3)) * 16, u32x4{0u, 0u, 0u, 0u});
#pragma unroll
    for (int it = 0; it < 6; it++) { const int i = 98304 + i0 + it * 14336; if (i < (int)RBSZ / 16) st128<16>(rw, WS_RB1 + i * 16, u32x4{EMPTY, EMPTY, EMPTY, EMPTY}); }
  }
}

#define PINF(x) asm volatile("" : "+v"(x))
DI void wave_sync(LDS_AS unsigned* c, unsigned n) {
  asm volatile("s_waitcnt lgkmcnt(0)" ::: "memory");
  if ((threadIdx.x & 63) == 0) __hip_atomic_fetch_add(c, 1u, __ATOMIC_RELAXED, __HIP_MEMORY_SCOPE_WORKGROUP);
  while (__builtin_amdgcn_readfirstlane(*(volatile LDS_AS unsigned*)c) < n) {}
  asm volatile("" ::: "memory");
}
DI void put_scale(lptr lds, int q, int tid, const u32x2* sp) {
  f32x4 r = {640.f * 1e-5f, 640.f * 1e-5f, 640.f * 1e-5f, 640.f * 1e-5f};
#pragma unroll
  for (int i = 0; i < 8; i++) r += f32x4{bflo(sp[i][0]), bfhi(sp[i][0]), bflo(sp[i][1]), bfhi(sp[i][1])};
#pragma unroll
  for (int e = 0; e < 4; e++) r[e] = __builtin_amdgcn_rsqf(r[e] * RSK);
  ((LDS_AS f32x4*)(lds + 2 * SLOT32))[q * 256 + tid] = r;
}
struct ColIn { u32x4 c64, s64, c32, s32, wf[2]; };
DI int col_of() { return blockIdx.x < 64 ? 192 + 2 * blockIdx.x + (wave_id() >> 2) : blockIdx.x - 64; }
DI void col_load(ColIn& k, rsrc_t rw, bool tab, bool filt) {
  int lane = threadIdx.x & 63; PINF(lane);
  const int w = wave_id() & 3;
  const u32x4* ct = (const u32x4*)&g_ct + lane;
  if (tab) { k.c64 = ct[(2 * w) * 64]; k.s64 = ct[(2 * w + 1) * 64]; k.c32 = ct[8 * 64]; k.s32 = ct[9 * 64]; }
  if (filt) {
#pragma unroll
    for (int t = 0; t < 2; t++) k.wf[t] = gl128<16>(rw, WS_WF + col_of() * 8192 + ((4 * t + (lane >> 4)) * 64 + 16 * w + (lane & 15)) * 16);
  }
}
template <bool FIRST>
DI void col_phase(rsrc_t rw, lptr lds, ColIn kin) {
  int lane = threadIdx.x & 63; PINF(lane);
  const int wv = wave_id(), w = wv & 3, q = wv >> 2, tid = w * 64 + lane, g = lane >> 4, r16 = lane & 15;
  const int c = col_of();
  const lptr img = lds + q * SLOT32;
  f32x4 xr[2], xi[2], rs[2]; u32x4 xb[2];
  u32x4 c64[2], s64[2], n64[2], c32[2], s32[2], n32[2];
  if (FIRST) col_load(kin, rw, true, false);
  SB();
  f32x4 t1c[2], t1s[2], t2c[2], t2s[2];
  {
    const float f1 = (float)(16 * w + r16) * (1.f / 2048.f), b1 = (float)(4 * g), f2 = (float)(16 * w + 4 * g) * (1.f / 2048.f), b2 = (float)r16;
#pragma unroll
    for (int t = 0; t < 2; t++) {
#pragma unroll
      for (int e = 0; e < 4; e++) {
        const float a1 = (b1 + (float)(16 * t + e)) * f1, a2 = (b2 + (float)(16 * t)) * (f2 + (float)e * (1.f / 2048.f));
        t1c[t][e] = __builtin_amdgcn_cosf(a1); t1s[t][e] = __builtin_amdgcn_sinf(-a1);
        t2c[t][e] = __builtin_amdgcn_cosf(a2); t2s[t][e] = __builtin_amdgcn_sinf(a2);
      }
      PINF(t1c[t]); PINF(t1s[t]); PINF(t2c[t]); PINF(t2s[t]);
    }
  }
  int wo_ = r16 * 128 + (((2 * w + (g >> 1)) ^ (r16 >> 1)) * 16) + (g & 1) * 8, ro_ = r16 * 128 + ((g ^ (r16 >> 1)) * 16), ro2 = ro_ ^ 64;
  PINF(wo_); PINF(ro_); PINF(ro2);
  {
    const unsigned par = (lane & 1) ? 0x80008000u : 0u;
    c64[0] = kin.c64; s64[0] = kin.s64; c32[0] = kin.c32; s32[0] = kin.s32;
    c64[1] = c64[0] ^ par; s64[1] = s64[0] ^ par; n64[0] = s64[0] ^ 0x80008000u; n64[1] = s64[1] ^ 0x80008000u;
    c32[1] = c32[0] ^ 0x80000000u; s32[1] = s32[0] ^ 0x80000000u; n32[0] = s32[0] ^ 0x80008000u; n32[1] = s32[0] ^ 0x00008000u;
    PINF(c64[1]); PINF(s64[1]); PINF(n64[0]); PINF(n64[1]); PINF(c32[1]); PINF(s32[1]); PINF(n32[0]); PINF(n32[1]);
  }
  SB();
  if (FIRST) {
    raw_bar();
#pragma unroll
    for (int t = 0; t < 2; t++) { xb[t] = gl128<16>(rw, WS_RB0 + ((tid >> 2) * 641 + ((tid >> 2) & 1) + 2 * c + t) * 64 + (tid & 3) * 16); rs[t] = asf(gl128<16>(rw, WS_RS1 + (1024 * t + 4 * tid) * 4)); }
    col_load(kin, rw, false, true);
    SB();
#pragma unroll
    for (int t = 0; t < 2; t++) {
      xr[t] = f32x4{bflo(xb[0][2 * t]), bfhi(xb[0][2 * t]), bflo(xb[0][2 * t + 1]), bfhi(xb[0][2 * t + 1])};
      xi[t] = f32x4{bflo(xb[1][2 * t]), bfhi(xb[1][2 * t]), bflo(xb[1][2 * t + 1]), bfhi(xb[1][2 * t + 1])};
    }
  } else {
    drain();
    u32x2 sp[8];
    const unsigned so = WS_SSQ + (blockIdx.x & 7) * 32768 + (1024 * q + 4 * tid) * 2, xo = WS_R3 + ((tid >> 2) * 642 + 2 * c) * 64 + (tid & 3) * 16;
    bool bs = true, bx = true;
#pragma unroll 1
    for (int spins = 0; spins < (1 << 18); spins++) {
      if (bx) {
#pragma unroll
        for (int t = 0; t < 2; t++) { xr[t] = asf(gl128<16>(rw, xo + 64 * 642 * 64 * t)); xi[t] = asf(gl128<16>(rw, xo + 64 * 642 * 64 * t + 64)); }
      }
      if (bs) {
#pragma unroll
        for (int i = 0; i < 8; i++) sp[i] = gl64<16>(rw, so + i * 4096);
      }
      unsigned m = 0;
#pragma unroll
      for (int t = 0; t < 2; t++) m = umax4(umax4(m, asu(xr[t])), asu(xi[t]));
      bx = __builtin_amdgcn_ballot_w64(m == EMPTY) != 0;
      m = 0;
#pragma unroll
      for (int i = 0; i < 8; i++) m = max(max(m, sp[i][0]), sp[i][1]);
      bs = __builtin_amdgcn_ballot_w64(m == EMPTY) != 0;
      if (!bs && !bx) break;
      nap<YSLEEP>();
    }
    put_scale(lds, q, tid, sp);
    LDS_AS f32x4* ex = (LDS_AS f32x4*)(lds + 2 * SLOT32) + tid;
    lds_bar();
    rs[0] = ex[0]; rs[1] = ex[256];
  }
#pragma unroll
  for (int t = 0; t < 2; t++) {
    u32x2 pb = pk4(xi[t] * rs[t]);
    if (c == 0) pb = u32x2{0u, 0u};
    *(LDS_AS u32x2*)(img + wo_ + t * 2048) = pk4(xr[t] * rs[t]);
    *(LDS_AS u32x2*)(img + 4096 + wo_ + t * 2048) = pb;
  }
  lds_bar();
  u32x4 ur[2][2], ui[2][2], br, bi;
  f32x4 ar[2], ai[2];
  const f32x4 z4 = {0.f, 0.f, 0.f, 0.f};
  auto rd_in = [&](int off) {
#pragma unroll
    for (int ks = 0; ks < 2; ks++)
#pragma unroll
      for (int t = 0; t < 2; t++) { const lptr p = img + off + (ks ? ro2 : ro_) + t * 2048; ur[t][ks] = ld128(p); ui[t][ks] = ld128(p + 4096); }
    SB();
  };
  auto cmul = [&](const f32x4* cc, const f32x4* sn) {
    br = pk8(ar[0] * cc[0] - ai[0] * sn[0], ar[1] * cc[1] - ai[1] * sn[1]);
    bi = pk8(ai[0] * cc[0] + ar[0] * sn[0], ai[1] * cc[1] + ar[1] * sn[1]);
  };
  rd_in(0);
#pragma unroll
  for (int ks = 0; ks < 2; ks++) {
#pragma unroll
    for (int t = 0; t < 2; t++) { ar[t] = mfma16(ur[t][ks], c64[ks], ks ? ar[t] : z4); ai[t] = mfma16(ui[t][ks], c64[ks], ks ? ai[t] : z4); }
#pragma unroll
    for (int t = 0; t < 2; t++) { ar[t] = mfma16(ui[t][ks], s64[ks], ar[t]); ai[t] = mfma16(ur[t][ks], n64[ks], ai[t]); }
  }
  const f32x4 fa[2] = {asf(kin.wf[0] << 16), asf(kin.wf[1] << 16)}, fb[2] = {asf(kin.wf[0] & 0xFFFF0000u), asf(kin.wf[1] & 0xFFFF0000u)};
  cmul(t1c, t1s);
#pragma unroll
  for (int t = 0; t < 2; t++) { ar[t] = mfma16(c32[t], br, z4); ai[t] = mfma16(c32[t], bi, z4); }
#pragma unroll
  for (int t = 0; t < 2; t++) { ar[t] = mfma16(s32[t], bi, ar[t]); ai[t] = mfma16(n32[t], br, ai[t]); }
  cmul(fa, fb);
#pragma unroll
  for (int t = 0; t < 2; t++) { ar[t] = mfma16(br, c32[t], z4); ai[t] = mfma16(bi, c32[t], z4); }
#pragma unroll
  for (int t = 0; t < 2; t++) { ar[t] = mfma16(bi, n32[t], ar[t]); ai[t] = mfma16(br, s32[t], ai[t]); }
  cmul(t2c, t2s);
#pragma unroll
  for (int t = 0; t < 2; t++) {
    *(LDS_AS u32x2*)(img + 8192 + wo_ + t * 2048) = u32x2{br[2 * t], br[2 * t + 1]};
    *(LDS_AS u32x2*)(img + 12288 + wo_ + t * 2048) = u32x2{bi[2 * t], bi[2 * t + 1]};
  }
  lds_bar();
  rd_in(8192);
#pragma unroll
  for (int ks = 0; ks < 2; ks++) {
#pragma unroll
    for (int t = 0; t < 2; t++) { ar[t] = mfma16(c64[ks], ur[t][ks], ks ? ar[t] : xr[t]); ai[t] = mfma16(c64[ks], ui[t][ks], ks ? ai[t] : xi[t]); }
#pragma unroll
    for (int t = 0; t < 2; t++) { ar[t] = mfma16(n64[ks], ui[t][ks], ar[t]); ai[t] = mfma16(s64[ks], ur[t][ks], ai[t]); }
  }
  const unsigned ob = (FIRST ? WS_RB1 : WS_RB2) + (2 * c * RBP + 8 * tid) * 2;
  st128<16>(rw, ob, clean(pk8(ar[0], ar[1])));
  st128<16>(rw, ob + RBP * 2, c == 0 ? u32x4{0u, 0u, 0u, 0u} : clean(pk8(ai[0], ai[1])));
}

DI f32x16 mfma32(u32x4 a, u32x4 b, f32x16 c) {
  return __builtin_amdgcn_mfma_f32_32x32x16_bf16(__builtin_bit_cast(bf16x8, a), __builtin_bit_cast(bf16x8, b), c, 0, 0, 0);
}
DI f32x16 mfma32k8(u32x2 a, u32x2 b, f32x16 c) {
  return __builtin_amdgcn_mfma_f32_32x32x8bf16_1k(__builtin_bit_cast(s16x4, a), __builtin_bit_cast(s16x4, b), c, 0, 0, 0);
}
DI f32x16 scores(u32x4 k1, u32x2 k2, u32x4 q1, u32x2 q2) {
  f32x16 z;
#pragma unroll
  for (int i = 0; i < 16; i++) z[i] = 0.f;
  f32x16 s = mfma32(k1, q1, z);
  asm volatile("s_nop 11" : "+v"(s));
  return mfma32k8(k2, q2, s);
}
DI float max16(const f32x16& s) {
  float m = fmaxf(s[0], s[1]);
#pragma unroll
  for (int i = 2; i < 16; i++) m = fmaxf(m, s[i]);
  return m;
}
#define STR_(x) #x
#define STR(x) STR_(x)
#define KOF(t) ((((t)&7)>>1)*11264+((t)&1)*1280)
#define VOF(t) ((((t)&7)>>1)*11264+5120+((t)&1)*1536)
#define EXP(d, s) "v_exp_f32 v" #d ", v" #s "\n\t"
#define CVT(d, s) "v_cvt_pk_bf16_f32 v" #d ", v" #s ", v[" #s "+1]\n\t"
#define STEPA(t) \
  "v_mfma_f32_32x32x16_bf16 v[48:63], v[80:83], v[136:139], v[120:135]\n\t" \
  "ds_read_b128 v[80:83], v100 offset:" STR(KOF(t+1)) "\n\t" \
  EXP(64,32) EXP(65,33) CVT(78,68) CVT(79,70) EXP(66,34) EXP(67,35) \
  "v_mfma_f32_32x32x16_bf16 v[104:119], v[90:93], v[76:79], v[104:119]\n\t" \
  "ds_read_b64_tr_b16 v[90:91], v102 offset:" STR(VOF(t)+768) "\n\t" \
  "ds_read_b64_tr_b16 v[92:93], v102 offset:" STR(VOF(t)+1152) "\n\t" \
  EXP(68,36) EXP(69,37) CVT(72,64) CVT(73,66) EXP(70,38) EXP(71,39) \
  "v_mfma_f32_32x32x8_bf16 v[48:63], v[84:85], v[140:141], v[48:63]\n\t" \
  "ds_read_b64 v[84:85], v101 offset:" STR(KOF(t+1)) "\n\t" \
  EXP(64,40) EXP(65,41) CVT(74,68) CVT(75,70) EXP(66,42) EXP(67,43) \
  "s_waitcnt lgkmcnt(4)\n\t" \
  "v_mfma_f32_32x32x16_bf16 v[0:15], v[86:89], v[72:75], v[0:15]\n\t" \
  EXP(68,44) EXP(69,45) CVT(76,64) CVT(77,66) EXP(70,46) EXP(71,47)
#define STEPB(t) \
  "s_waitcnt lgkmcnt(0)\n\t" \
  "v_mfma_f32_32x32x16_bf16 v[32:47], v[80:83], v[94:97], v[16:31]\n\t" \
  EXP(64,48) EXP(65,49) CVT(78,68) CVT(79,70) EXP(66,50) EXP(67,51) \
  "v_mfma_f32_32x32x16_bf16 v[0:15], v[90:93], v[76:79], v[0:15]\n\t" \
  EXP(68,52) EXP(69,53) CVT(72,64) CVT(73,66) EXP(70,54) EXP(71,55) \
  "v_mfma_f32_32x32x8_bf16 v[32:47], v[84:85], v[98:99], v[32:47]\n\t" \
  EXP(64,56) EXP(65,57) CVT(74,68) CVT(75,70) EXP(66,58) EXP(67,59) \
  "v_mfma_f32_32x32x16_bf16 v[104:119], v[86:89], v[72:75], v[104:119]\n\t" \
  "ds_read_b64_tr_b16 v[86:87], v102 offset:" STR(VOF(t+1)) "\n\t" \
  "ds_read_b64_tr_b16 v[88:89], v102 offset:" STR(VOF(t+1)+384) "\n\t" \
  EXP(68,60) EXP(69,61) CVT(76,64) CVT(77,66) EXP(70,62) EXP(71,63)
#define PR(n) "s_setprio " #n "\n\t"
#define CHUNK(i) PR(1) STEPA(2*(i)) PR(0) STEPB(2*(i)) "s_barrier\n\t" PR(2) STEPA(2*(i)+1) STEPB(2*(i)+1)
#define Z4(a) "v_mov_b64 v[" #a ":" #a "+1], 0\n\tv_mov_b64 v[" #a "+2:" #a "+3], 0\n\t"

#define R4(b) "v[" STR(b) ":" STR(b) "+3]"
#define MM(d, a, b) "v_mfma_f32_16x16x32_bf16 " R4(d) ", " R4(a) ", " R4(b) ", " R4(d) "\n\t"
#define GM(B, m, i) MM(16*m+4*i, B+8+4*i, B+4*m)
#define RT(B, m, hf, k) "ds_read_b64_tr_b16 v[" STR(B+4*m+2*hf) ":" STR(B+4*m+2*hf+1) "], v[84+" #m "] offset:" STR(k*8192+hf*1024) "\n\t"
#define RS(B, i, k) "ds_read_b128 " R4(B+8+4*i) ", v86 offset:" STR(i*2048+k*1024) "\n\t"
#define GSTEP(B, C, k) GM(B,0,0) RT(C,0,0,k) RT(C,0,1,k) GM(B,1,0) RS(C,0,k) RT(C,1,0,k) GM(B,0,1) RT(C,1,1,k) RS(C,1,k) GM(B,1,1) RS(C,2,k) \
  GM(B,0,2) RS(C,3,k) GM(B,1,2) GM(B,0,3) GM(B,1,3) MM(32,B,B)
#define GEMM_ASM(EXTRA) \
  Z4(0) Z4(4) Z4(8) Z4(12) Z4(16) Z4(20) Z4(24) Z4(28) Z4(32) "s_mov_b32 %[c], 0\n\ts_barrier\n\t" \
  RT(36,0,0,0) RT(36,0,1,0) RS(36,0,0) RT(36,1,0,0) RT(36,1,1,0) RS(36,1,0) RS(36,2,0) RS(36,3,0) EXTRA \
  "L_gemm_%=:\n\ts_add_u32 %[c], %[c], 1\n\ts_and_b32 %[t], %[c], 3\n\ts_cselect_b32 %[t], 1, -3\n\ts_lshl_b32 %[t], %[t], 15\n\ts_waitcnt lgkmcnt(0)\n\t" \
  GSTEP(36,60,1) \
  "v_add_u32 v84, %[t], v84\n\tv_add_u32 v85, %[t], v85\n\tv_add_u32 v86, %[t], v86\n\ts_waitcnt lgkmcnt(0)\n\ts_barrier\n\ts_setprio 1\n\t" \
  GSTEP(60,36,0) "s_setprio 0\n\t" \
  "s_cmp_lg_u32 %[c], 9\n\ts_cbranch_scc1 L_gemm_%=\n\ts_waitcnt lgkmcnt(0)\n\t" \
  GSTEP(36,60,1) "s_waitcnt lgkmcnt(0)\n\t" \
  MM(32,60,60) GM(60,0,0) GM(60,1,0) GM(60,0,1) GM(60,1,1) "s_nop 4\n\t" \
  "v_cndmask_b32 v36, v32, v33, %[m1]\n\tv_cndmask_b32 v37, v34, v35, %[m1]\n\tv_mov_b32 v38, 0x3727c5ac\n\tv_cndmask_b32 v36, v36, v37, %[m2]\n\t" \
  "v_fmac_f32 v38, 0x3acccccd, v36\n\tv_cmp_u_f32 vcc, v36, v36\n\tv_rsq_f32 v38, v38\n\ts_cbranch_vccz L_gok_%=\n\tds_write_b32 v90, v90\n\t" \
  "L_gok_%=:\n\tds_write_b32 v87, v38\n\ts_waitcnt lgkmcnt(0)\n\ts_barrier\n\t" \
  "ds_read_b32 v36, v88\n\tds_read_b32 v37, v89\n\tds_read_b32 v38, v90\n\t" \
  GM(60,0,2) GM(60,1,2) GM(60,0,3) GM(60,1,3) "s_waitcnt vmcnt(0) lgkmcnt(0)"
#define GEMM_OUT "={v[0:3]}"(acc[0][0]), "={v[4:7]}"(acc[0][1]), "={v[8:11]}"(acc[0][2]), "={v[12:15]}"(acc[0][3]), \
    "={v[16:19]}"(acc[1][0]), "={v[20:23]}"(acc[1][1]), "={v[24:27]}"(acc[1][2]), "={v[28:31]}"(acc[1][3]), \
    "={v36}"(rs[0]), "={v37}"(rs[1]), "={v38}"(vote), "+{v84}"(am0), "+{v85}"(am1), "+{v86}"(as), [c] "=&s"(c_), [t] "=&s"(t_)
#define GEMM_IN "{v87}"(a_w), "{v88}"(a_r[0]), "{v89}"(a_r[1]), "{v90}"(a_v), [m1] "s"(0xAAAAAAAAAAAAAAAAull), [m2] "s"(0xCCCCCCCCCCCCCCCCull)
#define GEMM_CLOB "memory", "scc", "vcc", "v32", "v33", "v34", "v35", "v39", "v40", "v41", "v42", "v43", "v44", "v45", "v46", "v47", "v48", "v49", "v50", \
    "v51", "v52", "v53", "v54", "v55", "v56", "v57", "v58", "v59", "v60", "v61", "v62", "v63", "v64", "v65", "v66", "v67", "v68", "v69", "v70", "v71", \
    "v72", "v73", "v74", "v75", "v76", "v77", "v78", "v79", "v80", "v81", "v82", "v83"
#define ITL(d, a) "global_load_dwordx4 " R4(d) ", v" #a ", %[it]\n\tglobal_load_dwordx4 " R4(d+4) ", v" #a ", %[it] offset:1024\n\t"

constexpr int HR = 4 * KVC;
constexpr int L_A = 2 * HR;
constexpr int L_EX = 15360;
constexpr int L_T = 4 * SLOT32, L_IA = L_T + 10240, L_U = L_A + 10240;
constexpr int L_MISC = L_A + 70 * 1024;
constexpr int LDS_FUSED = L_MISC + 2048;

DI bool poll16(rsrc_t rw, unsigned off, unsigned need) {
  const int lane = threadIdx.x & 63;
#pragma unroll 1
  for (int spins = 0; spins < (1 << 20); spins++) {
    const unsigned v = gl32<16>(rw, off + (lane & 15) * 4);
    const unsigned got = (unsigned)__builtin_amdgcn_ballot_w64(v != 0) & 0xFFFFu;
    if ((got & need) == need) return true;
    nap<1>();
  }
  return false;
}

DI void poll_aux(rsrc_t rw, unsigned off) {
#pragma unroll 1
  for (int spins = 0; spins < (1 << 20); spins++) {
    const u32x4 v = gl128<16>(rw, off + (threadIdx.x & 63) * 16);
    if (!__builtin_amdgcn_ballot_w64(min(min(v[0], v[1]), min(v[2], v[3])) == 0u)) break;
    nap<4>();
  }
}
DI void poll_tag(rsrc_t rw, u32x2 tag, unsigned long long need) {
  const unsigned o = WS_F0 + (blockIdx.x & 7) * 2048 + (threadIdx.x & 63) * 32;
#pragma unroll 1
  for (int spins = 0; spins < (1 << 20); spins++) {
    const u32x4 a = gl128<16>(rw, o), b = gl128<16>(rw, o + 16);
    const unsigned d = (a[0] ^ tag[0]) | (a[1] ^ tag[1]) | (a[2] ^ tag[0]) | (a[3] ^ tag[1]) | (b[0] ^ tag[0]) | (b[1] ^ tag[1]) | (b[2] ^ tag[0]) | (b[3] ^ tag[1]);
    if (!(__builtin_amdgcn_ballot_w64(d != 0u) & need)) break;
    nap<TSLEEP>();
  }
}
#define LAYER_VARS \
  extern __shared__ __attribute__((aligned(16))) char smem[]; \
  const lptr lds = (lptr)smem; \
  const rsrc_t rw = mk_rsrc(ws, WS_END); \
  int lane = threadIdx.x & 63; asm volatile("" : "+v"(lane)); \
  const int w = wave_id(), g = lane >> 4, r16 = lane & 15; \
  const int xcd = blockIdx.x & 7, bi = blockIdx.x >> 3, u = 8 * (xcd & 1) + (bi & 7), qt = 4 * (xcd >> 1) + (bi >> 3); \
  const int hf = u & 1, ct = u >> 1;     \
  constexpr unsigned FKV = WS_FL + (FINAL ? 32768 : 0), KV = WS_KV + (FINAL ? LAYER2 : 0), OB = WS_O + (FINAL ? LAYER2 : 0); \
  constexpr unsigned AUX = WS_AUX + (FINAL ? 1024 : 0), RB = FINAL ? WS_RB2 : WS_RB1; \
  (void)hf; (void)AUX; (void)RB; (void)FKV; \
  LDS_AS unsigned* misc = (LDS_AS unsigned*)(lds + L_MISC + 1024) + (FINAL ? 8 : 0);

template <bool FINAL>
DI void loader(char* ws, u32x2 tag) {
  LAYER_VARS
  {
    const int lw = w - 8;
    {
      unsigned vm[4];
#pragma unroll
      for (int r = 0; r < 4; r++) vm[r] = (4 * (4 * lw + r) + g) * (RBP * 2) + 256 * qt + (((r16 >> 1) ^ (g | ((r & 2) << 1))) * 32) + (r16 & 1) * 16 + 3072 - 1024 * r;
      const unsigned l0 = lds_addr(lds) + lw * 4096, v0 = 2 * lw * 20480 + lane * 16;
      constexpr int CNT = 8;
      auto lds_w = [&](int s) {
        const char* sb = ws + WS_WQ + u * 163840 + s * 2048;
        dma_s(v0, v0 + 20480 - 2048, sb, l0 + (s & 3) * SLOT32 + 16384);
      };
      auto lds_a = [&](int s) {
        dma_a(vm, ws + RB - 3072 + s * (64 * RBP * 2), l0 + (s & 3) * SLOT32); };
#pragma unroll 1
      for (int att = 0;; att++) {
        if (att == 0) { raw_bar(); if (FINAL) raw_bar(); else poll_tag(rw, tag, 0x7FFF0000ull); }
        lds_w(0); lds_w(1);
        if (att == 0) raw_bar();
        lds_w(2); lds_w(3);
        if (FINAL) dma2(40960 + lw * 2048 + lane * 16, (const char*)&g_it, lds_addr(lds) + L_IA + lw * 2048);
        auto probe = [&](int s0, int n) {
          const int pl = min(lane, n - 1);
          const unsigned po = RB + ((64 * (s0 + (pl >> 3)) + 16 * lw + 2 * (pl & 7) + 1) * RBP + 128 * qt + 126) * 2;
#pragma unroll 1
          for (int spins = 0; spins < (1 << 20); spins++) {
            if (!__builtin_amdgcn_ballot_w64(gl32<16>(rw, po) == EMPTY)) break;
            nap<XSLEEP>();
          }
        };
        if (att == 0) {
          raw_bar();
          if (blockIdx.x >= 64) nap<XDELAY>();
          probe(0, 48);
        }
        drain();
        unsigned nd = 0;
        if (att == 0 && lane == 0) nd = __hip_atomic_fetch_add(misc + 4, 1u, __ATOMIC_RELAXED, __HIP_MEMORY_SCOPE_WORKGROUP);
        if (nd == 11) st32<16>(rw, AUX + blockIdx.x * 4, 1u);
#pragma unroll 1
        for (int s = 0; s < 4; s++) lds_a(s);
        vm_bar<12>(); vm_bar<8>();
        if (att == 0) probe(6, 32);
        lds_a(4); lds_w(4);
        vm_bar<4 + CNT>();
#pragma unroll 1
        for (int s = 5; s < 10; s++) { lds_a(s); lds_w(s); vm_bar<2 * CNT>(); }
        vm_bar<CNT>();
        poll_aux(rw, AUX);
        if (!FINAL) { poll_tag(rw, tag, ~0ull); if (blockIdx.x == 0 && threadIdx.x == 512) st64<16>(rw, WS_EP, tag); }
        vm_bar<0>();
        if (FINAL) {
#pragma unroll
          for (int i = 0; i < 3; i++) { const int pc = min(3 * lw + i, 9); dma1_sc1(RB + ((64 * pc + lane) * RBP + 128 * qt + 8 * u) * 2, ws, lds_addr(lds) + L_T + pc * 1024); }
        }
        raw_bar();
        if (att || !*(volatile LDS_AS unsigned*)(misc + 2)) break;
      }
    }
    const int hh = lw >> 1, hp = lw & 1;
    const unsigned kvv = KV + (2 * u + hh) * 16 * KVC + hp * 5120 + lane * 16;
    const unsigned lkv = lds_addr(lds) + hh * HR + hp * 5120;
    const unsigned wv = (FINAL ? WS_WOP : WS_WOF) + (ct * 2 + hh) * 51200 + lane * 16;
    const unsigned myfl = FKV + (u * 16 + qt) * 128;
    vm_bar<0>();
    {
      u32x4 v[6];
#pragma unroll
      for (int i = 0; i < 6; i++) v[i] = ld128(lds + hh * HR + hp * 5120 + lane * 16 + i * 1024);
#pragma unroll
      for (int i = 0; i < 6; i++) st128<16>(rw, kvv + qt * KVC + i * 1024, clean(v[i]));
    }
    drain();
    unsigned cnt = 0;
    if (lane == 0) cnt = __hip_atomic_fetch_add(misc, 1u, __ATOMIC_RELAXED, __HIP_MEMORY_SCOPE_WORKGROUP);
    if (__builtin_amdgcn_readfirstlane(cnt) == 3 && lane < 16) st32<16>(rw, FKV + (u * 16 + lane) * 128 + qt * 4, 1u);
    auto ldkv = [&](int seq) {
      const int ch = seq == 0 ? qt : seq < 13 ? ((qt & 12) + 3 + seq) & 15 : (qt & 12) | ((qt + seq) & 3);
      dma6_sc1(kvv + ch * KVC, ws, lkv + (seq & 3) * KVC);
    };
    auto fetch = [&](int seq) {
      const unsigned src = kvv + ((((qt & 12) + 3 + seq) & 15)) * KVC;
      u32x4 v[6];
#pragma unroll 1
      for (int spins = 0; spins < (1 << 18); spins++) {
#pragma unroll
        for (int i = 0; i < 6; i++) v[i] = gl128<16>(rw, src + i * 1024);
        unsigned m = 0;
#pragma unroll
        for (int i = 0; i < 6; i++) m = umax4(m, v[i]);
        if (!__builtin_amdgcn_ballot_w64(m == EMPTY)) break;
        nap<2>();
      }
#pragma unroll
      for (int i = 0; i < 6; i++) *(LDS_AS u32x4*)(lds + hh * HR + hp * 5120 + seq * KVC + lane * 16 + i * 1024) = v[i];
    };
#pragma unroll 1
    for (int attempt = 0;; attempt++) {
      int c = -2;
      if (attempt) { ldkv(0); vm_bar<0>(); }
      else {
        fetch(1); lds_bar();
        fetch(2); poll16(rw, myfl, 12u << (((qt & 12) + 4) & 15)); ldkv(3); lds_bar();
        c = 1;
      }
#pragma unroll 1
      for (; c < 13; c++) {
        if (c == 2 && !attempt) poll16(rw, myfl, 0xFFFFu);
        if (!FINAL && !attempt) {
          if (c >= 2 && c < 9) {
            const u32x4 e = {EMPTY, EMPTY, EMPTY, EMPTY};
            const unsigned o = OB + LAYER2 + (((c - 2) * 256 + blockIdx.x) * 4 + lw) * 2048 + lane * 16;
            st128<16>(rw, o, e); st128<16>(rw, o + 1024, e);
            st128<16>(rw, ((qt & 3) < 2 && c < 8) ? kvv + LAYER2 + qt * KVC + (c - 2) * 1024 : WS_END, e);
            st128<16>(rw, c < 5 ? OB + ((4 * qt + lw) * 32 + 2 * u) * 1280 + min((c - 2) * 1024, 1536) + lane * 16 : WS_END, e);
          }
          if (c == 10) {
            drain();
            unsigned n = 0;
            if (lane == 0) n = __hip_atomic_fetch_add(misc + 3, 1u, __ATOMIC_RELAXED, __HIP_MEMORY_SCOPE_WORKGROUP);
            if (__builtin_amdgcn_readfirstlane(n) == 3 && lane < 32) st32<16>(rw, WS_FL + 65536 + (lane < 16 ? (u * 16 + lane) * 128 + qt * 4 : (r16 * 16 + qt) * 128 + 64 + u * 4), 1u);
          }
        }
        if (c >= 2) { const int e = min(4 * (c - 2) + 2 * hp, 33); dma2(wv + e * 1024, ws, lds_addr(lds) + L_A + (hh * 35 + e) * 1024); }
        ldkv(c + 3);
        if (c == 0) vm_bar<12>();
        if (c >= 2) vm_bar<8>(); else if (c >= 0) vm_bar<6>();
      }
      if (!FINAL && !attempt) poll16(rw, myfl + 65536 + hp * 64, 0xFFFFu);
      vm_bar<0>();
      dma_s(wv + (35 + 7 * hp) * 1024, wv + (35 + 7 * hp) * 1024, ws, lds_addr(lds) + hh * HR + 7 * hp * 1024);
      dma_s(wv + (39 + 7 * hp) * 1024, wv + (39 + 7 * hp) * 1024, ws, lds_addr(lds) + hh * HR + (4 + 7 * hp) * 1024);
      if (FINAL && lw == 0 && lane < 9)
        st64<16>(rw, lane < 8 ? WS_F0 + lane * 2048 + blockIdx.x * 8 : WS_GO + blockIdx.x * 64, u32x2{tag[0], ~tag[1]});
      raw_bar();
      vm_bar<0>();
      if (attempt || !*(volatile LDS_AS unsigned*)(misc + 1)) break;
      raw_bar();
    }
    if (!FINAL) { raw_bar(); raw_bar(); }
  }
}

template <bool FINAL>
DI void compute(char* ws, u16* out, ColIn& kin) {
  LAYER_VARS
  const int wm = w & 3, wn = w >> 2, h = lane >> 5;
  if (blockIdx.x < 64 || wn == 0) col_phase<!FINAL>(rw, lds, kin);
  else if (FINAL) {
    drain();
    nap<YIDLE>();
    u32x2 sp[8];
#pragma unroll 1
    for (int spins = 0; spins < (1 << 18); spins++) {
      unsigned m = 0;
#pragma unroll
      for (int i = 0; i < 8; i++) sp[i] = gl64<16>(rw, WS_SSQ + (blockIdx.x & 7) * 32768 + (1024 + 4 * (wm * 64 + lane)) * 2 + i * 4096);
#pragma unroll
      for (int i = 0; i < 8; i++) m = max(max(m, sp[i][0]), sp[i][1]);
      if (!__builtin_amdgcn_ballot_w64(m == EMPTY)) break;
      nap<YSLEEP>();
    }
    put_scale(lds, 1, wm * 64 + lane, sp);
    lds_bar(); raw_bar(); raw_bar();
  } else {
    raw_bar(); raw_bar(); raw_bar();
  }
  lds_bar();
  {
    drain();
    unsigned n = 0;
    if (lane == 0) n = __hip_atomic_fetch_add(misc + 4, 1u, __ATOMIC_RELAXED, __HIP_MEMORY_SCOPE_WORKGROUP);
    if (n == 11) st32<16>(rw, AUX + blockIdx.x * 4, 1u);
  }

  const int head = 2 * u + wn, kh = w & 1;
  const lptr ring = lds + wn * HR;
  const int pblk = 128 * qt + 64 * hf + 32 * (wm >> 1);
  const unsigned r3t = WS_R3 + (64 * (wm & 1) + (pblk >> 5)) * (642 * 64);
  u32x2 x4v[3]; float dc = 0.f;
  unsigned os = OB + ((4 * qt + wm) * 32 + head) * 1280 + (((lane << 2) & 16) + ((lane >> 1) & 12) + (lane & 3)) * 8;
  unsigned o1 = OB + ((pblk >> 5) * 32 + 16 * wn) * 1280 + (g >> 1) * 1280 + (g & 1) * 512 + (wm & 1) * 256 + r16 * 16;
  unsigned o2 = OB + ((pblk >> 5) * 32 + 16 * wn) * 1280 + 2 * g * 1280 + 1024 + (wm & 1) * 128 + r16 * 8;
  u32x4 o3 = {0u, 0u, 0u, 0u};
  if (!FINAL) {
#pragma unroll
    for (int i = 0; i < 3; i++) {
      const int j = 80 * ct + 48 * wn + 16 * (wn ? (i & 1) : i) + r16;
      o3[i] = (j == 1 ? WS_RB0 + ((pblk >> 5) * 641 + ((pblk >> 5) & 1) + 1) * 64 : WS_RB1 + (j * RBP + pblk) * 2) + 16 * g + 8 * (wm & 1);
    }
  } else {
    o3[3] = r3t + 64 + r16 * 4;
#pragma unroll
    for (int i = 0; i < 3; i++) o3[i] = WS_X4 + ((pblk + trow(wm & 1, r16)) * 640 + 80 * ct + (i < 2 ? 48 * wn + 8 * g + 4 * i : 32 + 4 * g)) * 2;
  }
  asm volatile("" : "+v"(os), "+v"(o1), "+v"(o2), "+v"(o3));
  unsigned ka = lds_addr(ring) + kh * 2560 + lane * 16, kb = lds_addr(ring) + kh * 2560 + 1024 + (lane & 31) * 8;
  unsigned va = lds_addr(ring) + kh * 3072 + (4 * h + (r16 >> 2)) * 48 + ((g & 1) ? ((lane & 3) ? 40 : 32) : (lane & 3) * 8);
  asm volatile("" : "+v"(ka), "+v"(kb), "+v"(va));
  {
    f32x4 acc[2][4]; float rs[2]; u32x8 im[3];
    unsigned mo[2], a0[2], a1[2], a2[2], a_r[2];
#pragma unroll
    for (int m = 0; m < 2; m++) {
      const int q = r16 >> 2, pr = 16 * (m ^ wn) + r16;
      mo[m] = lds_addr(lds) + (8 * g + q) * 256 + (((2 * wm + (m ^ wn)) ^ (q | ((g & 1) << 2))) * 32) + (lane & 3) * 8;
      const unsigned qb = L_A + (wn * 4 + wm) * 1280, kb = wn * HR + wm * 1280, vr = wn * HR + 5120 + (32 * wm + pr) * 48 + (g == 3 ? 32 : g * 16);
      a0[m] = ((g >> 1) ? kb : qb) + (g & 1) * 512 + pr * 16;
      a1[m] = g == 2 ? qb + 1024 + pr * 8 : vr; a2[m] = g == 2 ? kb + 1024 + pr * 8 : vr + 8;
      a_r[m] = lds_addr(lds) + L_MISC + (32 * wm + pr) * 4;
      asm volatile("" : "+v"(a0[m]), "+v"(a1[m]), "+v"(a2[m]));
    }
    const unsigned a_w = a_r[0] + (g == (r16 >> 2) ? 0 : 512), a_v = lds_addr((lptr)(misc + 2));
#pragma unroll 1
    for (int att = 0;; att++) {
      unsigned am0 = mo[0], am1 = mo[1], as = lds_addr(lds) + 16384 + wn * 8192 + lane * 16, vote; int c_, t_;
      if (!FINAL) asm volatile(GEMM_ASM("") : GEMM_OUT : GEMM_IN : GEMM_CLOB);
      else asm volatile(GEMM_ASM(ITL(96, 91) ITL(104, 92) ITL(112, 93)) : GEMM_OUT, "={v[96:103]}"(im[0]), "={v[104:111]}"(im[1]), "={v[112:119]}"(im[2])
                        : GEMM_IN, "{v91}"(w * 2048 + lane * 16), "{v92}"((w + 8) * 2048 + lane * 16), "{v93}"((w < 4 ? w + 16 : w) * 2048 + lane * 16), [it] "s"(&g_it) : GEMM_CLOB);
      if (att || !__builtin_amdgcn_readfirstlane(vote)) break;
    }
    SB();
#pragma unroll
    for (int m = 0; m < 2; m++) *(LDS_AS u32x4*)(lds + a0[m]) = pk8(acc[m][0] * rs[m], acc[m][1] * rs[m]);
    asm volatile("s_nop 1" : "+v"(acc[0][2]), "+v"(acc[1][2]), "+v"(acc[0][3]), "+v"(acc[1][3]) :: "memory");
#pragma unroll
    for (int m = 0; m < 2; m++) {
      u32x2 hi = pk4(acc[m][3] * rs[m]);
      if (g == 3) hi = u32x2{0x3F80u, 0u};
      *(LDS_AS u32x2*)(lds + a1[m]) = pk4(acc[m][2] * rs[m]);
      *(LDS_AS u32x2*)(lds + a2[m]) = hi;
    }
    lds_bar();
    if (FINAL) {
      u32x4 ia[3]; u32x2 ib[3];
#pragma unroll
      for (int T = 0; T < 3; T++) { ia[T] = ld128(lds + L_IA + T * 1024 + lane * 16); ib[T] = ld64(lds + L_IA + 3072 + T * 512 + lane * 8); }
      const f32x4 z4 = {0.f, 0.f, 0.f, 0.f};
#pragma unroll
      for (int i = 0; i < 3; i++) {
        const int G = (i < 2 || w < 4) ? w + 8 * i : w;
        const lptr tp = lds + L_T + (32 * G + 8 * g + (r16 >> 2)) * 16 + (lane & 1) * 8;
        const u32x2 f0 = ldtr(tp), f1 = ldtr(tp + 64);
#pragma unroll
        for (int P = 0; P < 2; P++) *(LDS_AS u32x2*)(lds + L_U + (r16 & 7) * 1296 + (2 * G + P) * 32 + g * 8) = pk4(mfma16(u32x4{im[i][4 * P], im[i][4 * P + 1], im[i][4 * P + 2], im[i][4 * P + 3]}, u32x4{f0[0], f0[1], f1[0], f1[1]}, z4));
      }
      wave_sync(misc + 5, 8);
      const lptr up = lds + L_U + w * 1296;
      const u32x2 f0 = ldtr(up + lane * 8), f1 = ldtr(up + 512 + lane * 8), f2 = ldtr(up + 1024 + (lane & 31) * 8);
#pragma unroll
      for (int T = 0; T < 3; T++) {
        const u32x2 v = pk4(mfma16(u32x4{f2[0], f2[1], 0u, 0u}, u32x4{ib[T][0], ib[T][1], 0u, 0u}, mfma16(u32x4{f0[0], f0[1], f1[0], f1[1]}, ia[T], z4)));
        st64<16>(rw, (T < 2 || r16 < 8) ? WS_X4 + ((128 * qt + 8 * u + w) * 640 + 16 * (16 * T + r16) + 4 * g) * 2 : WS_END, u32x2{min(v[0], EMPTY - 1), min(v[1], EMPTY - 1)});
      }
    }
  }
  f32x4 r[3];
  {
    const lptr qpa = lds + L_A + (wn * 4 + wm) * 1280, qpb = lds + L_A + (wn * 4 + (wm ^ 1)) * 1280;
    const u32x4 qa1 = ld128(qpa + lane * 16), qb1 = ld128(qpb + lane * 16);
    u32x2 qa2 = ld64(qpa + 1024 + (lane & 31) * 8), qb2 = ld64(qpb + 1024 + (lane & 31) * 8);
    if (h) { qa2 = u32x2{0u, 0u}; qb2 = qa2; }
    float ma, mb;
    {
      const u32x4 k1 = ld128(ring + lane * 16); const u32x2 k2 = ld64(ring + 1024 + (lane & 31) * 8);
      ma = max16(scores(k1, k2, qa1, qa2)); mb = max16(scores(k1, k2, qb1, qb2));
    }
    for (int attempt = 0;; attempt++) {
      ma = fmaxf(ma, __shfl_xor(ma, 32)); mb = fmaxf(mb, __shfl_xor(mb, 32));
      f32x16 cia, cib, oa, ob;
#pragma unroll
      for (int i = 0; i < 16; i++) { cia[i] = -ma; cib[i] = -mb; }
      int cnt = 4;
      asm volatile(
          "ds_read_b128 v[80:83], v100\n\t"
          "ds_read_b64 v[84:85], v101\n\t"
          "ds_read_b64_tr_b16 v[86:87], v102 offset:5120\n\t"
          "ds_read_b64_tr_b16 v[88:89], v102 offset:5504\n\t"
          Z4(0) Z4(68) Z4(76) Z4(90)
          "s_waitcnt lgkmcnt(0)\n\t"
          "v_mfma_f32_32x32x16_bf16 v[32:47], v[80:83], v[94:97], v[16:31]\n\t"
          Z4(4) Z4(8) Z4(12) Z4(104) Z4(108) Z4(112) Z4(116)
          "v_mfma_f32_32x32x8_bf16 v[32:47], v[84:85], v[98:99], v[32:47]\n\ts_nop 9\n\t"
          "L_attn_%=:\n\t"
          CHUNK(0) CHUNK(1) CHUNK(2) CHUNK(3)
          "s_sub_u32 %[cnt], %[cnt], 1\n\ts_cmp_lg_u32 %[cnt], 0\n\ts_cbranch_scc1 L_attn_%=\n\t"
          "s_setprio 0\n\t" CVT(78,68) CVT(79,70) "s_waitcnt lgkmcnt(0)\n\ts_nop 1\n\t"
          "v_mfma_f32_32x32x16_bf16 v[104:119], v[90:93], v[76:79], v[104:119]\n\ts_nop 15\n\t"
          : "={v[0:15]}"(oa), "={v[104:119]}"(ob), [cnt] "+s"(cnt)
          : "{v[16:31]}"(cia), "{v[120:135]}"(cib), "{v[94:97]}"(qa1), "{v[98:99]}"(qa2), "{v[136:139]}"(qb1), "{v[140:141]}"(qb2),
            "{v100}"(ka), "{v101}"(kb), "{v102}"(va)
          : "memory", "scc", "v32", "v33", "v34", "v35", "v36", "v37", "v38", "v39", "v40", "v41", "v42", "v43", "v44", "v45", "v46", "v47",
            "v48", "v49", "v50", "v51", "v52", "v53", "v54", "v55", "v56", "v57", "v58", "v59", "v60", "v61", "v62", "v63", "v64", "v65",
            "v66", "v67", "v68", "v69", "v70", "v71", "v72", "v73", "v74", "v75", "v76", "v77", "v78", "v79", "v80", "v81", "v82", "v83",
            "v84", "v85", "v86", "v87", "v88", "v89", "v90", "v91", "v92", "v93");
      LDS_AS f32x4* ex = (LDS_AS f32x4*)(ring + L_EX + wm * 3072) + lane;
#pragma unroll
      for (int i = 0; i < 3; i++) ex[i * 64] = f32x4{ob[4 * i], ob[4 * i + 1], ob[4 * i + 2], ob[4 * i + 3]};
      f32x4 cs = {0.f, 0.f, 0.f, 0.f};
#pragma unroll
      for (int i = 0; i < 3; i++) cs += f32x4{oa[4 * i], oa[4 * i + 1], oa[4 * i + 2], oa[4 * i + 3]} + f32x4{ob[4 * i], ob[4 * i + 1], ob[4 * i + 2], ob[4 * i + 3]};
      if (!(fabsf(cs[0] + cs[1] + cs[2] + cs[3]) < INFINITY) || (h && !(oa[8] + ob[8] < 0x1p60f))) misc[1] = 1u;
      lds_bar();
      const unsigned vote = *(volatile LDS_AS unsigned*)(misc + 1);
      LDS_AS f32x4* ey = (LDS_AS f32x4*)(ring + L_EX + (wm ^ 1) * 3072) + lane;
#pragma unroll
      for (int i = 0; i < 3; i++) r[i] = ey[i * 64] + f32x4{oa[4 * i], oa[4 * i + 1], oa[4 * i + 2], oa[4 * i + 3]};
      if (attempt || !vote) break;
      lds_bar();
      ma = -INFINITY; mb = -INFINITY;
      for (int t = 0; t < 64; t++) {
        const unsigned kb_ = KV + (head * 16 + (t >> 2)) * KVC + (t & 3) * 1280;
        const u32x4 k1 = gl128<16>(rw, kb_ + lane * 16); const u32x2 k2 = gl64<16>(rw, kb_ + 1024 + (lane & 31) * 8);
        ma = fmaxf(ma, max16(scores(k1, k2, qa1, qa2))); mb = fmaxf(mb, max16(scores(k1, k2, qb1, qb2)));
      }
      raw_bar();
    }
  }
  {
    const unsigned lb = __float_as_uint(r[2][0]);
    const float il = __builtin_amdgcn_rcpf(__uint_as_float(__builtin_amdgcn_permlane32_swap(lb, lb, false, false)[1]));
    const unsigned sl8 = os & 255;
    st128<16>(rw, os + sl8 + h * 512, clean(pk8(r[0] * il, r[1] * il)));
    const u32x4 t = clean(pk8(r[2] * il, r[2] * il));
    st64<16>(rw, h ? WS_END : os + 1024, u32x2{t[0], t[1]});
  }
  const lptr wa = lds + L_A + wn * 35 * 1024 + lane * 16, wb = ring + lane * 16;
  constexpr int NPRE = 15;
  u32x4 bf[50];
  f32x4 acc[5];
#pragma unroll
  for (int j = 0; j < NPRE; j++) bf[j] = ld128(wa + j * 1024);
  if (FINAL) dc = __uint_as_float(gl32<16>(rw, o3[3]));
#pragma unroll 1
  for (int spins = 0; spins < (1 << 18); spins++) {
#pragma unroll
    for (int i = 0; i < 3; i++) x4v[i] = gl64<16>(rw, o3[i]);
    if (!FINAL || !__builtin_amdgcn_ballot_w64(umax4(max(x4v[0][0], x4v[0][1]), u32x4{x4v[1][0], x4v[1][1], x4v[2][0], x4v[2][1]}) == EMPTY)) break;
    nap<8>();
  }
  drain();
  auto init = [&](int a) {
    f32x4 v = {0.f, 0.f, 0.f, 0.f};
    if (a < 3) { v = f32x4{bflo(x4v[a][0]), bfhi(x4v[a][0]), bflo(x4v[a][1]), bfhi(x4v[a][1])}; if (FINAL) v += dc * 0.039528470752104741f; if (a == 2 && wn) v = f32x4{0.f, 0.f, 0.f, 0.f}; }
    return v;
  };
  unsigned so_ = FINAL ? (rowof(pblk + trow(wm & 1, r16)) * 640 + 80 * ct) * 2 : r3t + (80 * ct + 48 * wn + r16) * 64 + g * 16;
  {
    u32x4 af[10];
    auto ldf = [&](int i) {
      if (i < 8) af[i] = __builtin_amdgcn_raw_buffer_load_b128(rw, o1, i * 2 * 1280, 16);
      else { const u32x2 a = __builtin_amdgcn_raw_buffer_load_b64(rw, o2, (i - 8) * 8 * 1280, 16), b = __builtin_amdgcn_raw_buffer_load_b64(rw, o2, ((i - 8) * 8 + 1) * 1280, 16);
             af[i] = u32x4{a[0], a[1], b[0], b[1]}; }
    };
#pragma unroll
    for (int i = 0; i < 10; i++) ldf(i);
    SB();
#pragma unroll
    for (int a = 0; a < 5; a++) acc[a] = init(a);
    asm volatile("s_nop 1" : "+v"(acc[0]), "+v"(acc[1]), "+v"(acc[2]), "+v"(acc[3]), "+v"(acc[4]), "+v"(so_));
    SB();
    unsigned m0 = 0;
#pragma unroll
    for (int i = 0; i < 10; i++) {
#pragma unroll
      for (int a = 0; a < 5; a++) {
        if (FINAL) mfma16v(acc[a], bf[5 * i + a], af[i]); else mfma16v(acc[a], af[i], bf[5 * i + a]);
        const int j = 5 * i + a + NPRE; if (j < 50) bf[j] = ld128(j < 35 ? wa + j * 1024 : wb + (j - 35) * 1024);
        if (a == 1) m0 = umax4(m0, af[i]);
        SB();
      }
    }
    asm volatile("s_nop 7\n\ts_nop 1" : "+v"(acc[0]), "+v"(acc[1]), "+v"(acc[2]), "+v"(acc[3]), "+v"(acc[4]));
    if (__builtin_amdgcn_ballot_w64(m0 == EMPTY)) {
#pragma unroll 1
      for (int spins = 0; spins < (1 << 18); spins++) {
#pragma unroll
        for (int i = 0; i < 10; i++) if (__builtin_amdgcn_ballot_w64(umax4(0, af[i]) == EMPTY)) ldf(i);
        unsigned m = 0;
#pragma unroll
        for (int i = 0; i < 10; i++) m = umax4(m, af[i]);
        if (!__builtin_amdgcn_ballot_w64(m == EMPTY)) break;
        nap<RSLEEP>();
      }
#pragma unroll
      for (int j = 0; j < NPRE; j++) bf[j] = ld128(wa + j * 1024);
#pragma unroll
      for (int i = 0; i < 10; i++) {
#pragma unroll
        for (int a = 0; a < 5; a++) {
          const f32x4 c = i ? acc[a] : init(a);
          acc[a] = FINAL ? mfma16(bf[5 * i + a], af[i], c) : mfma16(af[i], bf[5 * i + a], c);
          const int j = 5 * i + a + NPRE; if (j < 50) bf[j] = ld128(j < 35 ? wa + j * 1024 : wb + (j - 35) * 1024);
        }
        asm volatile("" : "+v"(acc[0])); SB();
      }
    }
  }
  asm volatile("s_nop 7\n\ts_nop 1" : "+v"(acc[0]), "+v"(acc[1]), "+v"(acc[2]), "+v"(acc[3]), "+v"(acc[4]) :: "memory");
  {
    LDS_AS f32x4* ex = (LDS_AS f32x4*)(ring + L_EX + 12288 + wm * 3072) + lane;
    LDS_AS f32x4* ey = (LDS_AS f32x4*)(lds + (wn ^ 1) * HR + L_EX + 12288 + wm * 3072) + lane + 64 * wn;
    ex[0] = acc[2]; ex[64] = acc[3]; ex[128] = acc[4];
    if (!FINAL) { SB(); col_load(kin, rw, true, true); SB(); }
    lds_bar();
#pragma unroll
    for (int i = 0; i < 3; i++) acc[i] += ey[64 * i];
  }
  if (FINAL) {
    const rsrc_t ro = mk_rsrc(out, 2048 * 640 * 2);
    st128<16>(ro, so_ + 96 * wn + 16 * g, pk8(acc[0], acc[1]));
    if (wn == 0) st64<16>(ro, so_ + 64 + 8 * g, pk4(acc[2]));
  } else {
    f32x4 sq = {0.f, 0.f, 0.f, 0.f};
#pragma unroll
    for (int i = 0; i < 3; i++)
      if (i < 2 || wn == 0) {
        const int j = 80 * ct + 48 * wn + 16 * i + r16;
        st128<16>(rw, so_ + i * 1024, clean(asu(acc[i])));
        if (j != 1) sq += acc[i] * acc[i];
      }
#pragma unroll
    for (int e = 0; e < 4; e++) sq[e] = row16_sum(sq[e]);
    LDS_AS float* sl = (LDS_AS float*)(lds + L_MISC);
    if (r16 == 0) *(LDS_AS f32x4*)(sl + wn * 64 + 32 * (wm & 1) + 16 * (wm >> 1) + 4 * g) = sq;
    lds_bar();
    if (w == 0) {
      LDS_AS f32x4* s4 = (LDS_AS f32x4*)sl + 2 * (lane & 7);
      st128<16>(rw, WS_SSQ + (lane >> 3) * 32768 + (ct * 2048 + 1024 * ((lane >> 2) & 1) + 64 * qt + 32 * hf + 8 * (lane & 3)) * 2, clean(pk8(s4[0] + s4[16], s4[1] + s4[17])));
    }
  }
}

__global__ void __launch_bounds__(768, 3) k_main(const u16* x, const u16* wr, const u16* wi, const u16* wqkv, const u16* wo, char* ws, u16* out) {
  if (wave_id() >= 8) {
    extern __shared__ __attribute__((aligned(16))) char smem[];
    const unsigned long long t0 = __builtin_amdgcn_s_memrealtime();
    const int job = blockIdx.x;
    if (job < 144) raw_bar();
    const rsrc_t rw = mk_rsrc(ws, WS_END);
    __builtin_amdgcn_s_setprio(3);
    if (threadIdx.x < 528) ((LDS_AS unsigned*)((lptr)smem + L_MISC + 1024))[threadIdx.x - 512] = 0u;
    u32x2 tag = gl64<16>(rw, WS_EP);
    tag[0] += 1u;
    if (job < 228) raw_bar();
    if (job < 64) raw_bar();
    asm volatile("s_waitcnt vmcnt(0) lgkmcnt(0)\n\ts_barrier" : "+v"(tag) :: "memory");
    if (wave_id() == 8) {
      if (threadIdx.x < 520) st64<16>(rw, WS_F0 + (threadIdx.x - 512) * 2048 + job * 8, tag);
      if (job >= 248) {
        while (__builtin_amdgcn_s_memrealtime() - t0 < TPOLL) nap<2>();
        poll_tag(rw, tag, 0xFE0FFFF00000FFFFull);
        if (threadIdx.x < 544) st64<16>(rw, WS_GO + ((threadIdx.x - 512) * 8 + (job & 7)) * 64, tag);
      }
#pragma unroll 1
      for (int spins = 0; spins < (1 << 20); spins++) {
        const u32x2 v = gl64<16>(rw, WS_GO + job * 64);
        if (__builtin_amdgcn_readfirstlane((v[0] ^ tag[0]) | (v[1] ^ tag[1])) == 0) break;
        nap<GSLEEP>();
      }
    }
    if (wave_id() > 8) {
    while (__builtin_amdgcn_s_memrealtime() - t0 < TFILL) nap<2>();
    const int i0 = blockIdx.x * 192 + threadIdx.x - 576;
#pragma unroll
    for (int it = 0; it < 4; it++) {
      const int k = it * 49152 + i0, b = k / 1408;
      st128<16>(rw, k < 128 * 1408 ? WS_KV + b * 4 * KVC + (k - b * 1408) * 16 : WS_END, u32x4{EMPTY, EMPTY, EMPTY, EMPTY});
    }
    }
    raw_bar();
    loader<false>(ws, tag);
    loader<true>(ws, tag);
  } else {
    prep(x, wr, wi, wqkv, wo, ws);
    asm volatile("s_waitcnt vmcnt(0) lgkmcnt(0)\n\ts_barrier" ::: "memory");
    ColIn kin = {};
    compute<false>(ws, out, kin);
    compute<true>(ws, out, kin);
  }
}

extern "C" void kernel_launch(void* const* d_in, const int* in_sizes, int n_in, void* d_out, int out_size, void* d_ws, size_t ws_size,
                              hipStream_t stream) {
  const u16* x = (const u16*)d_in[0];
  const u16* wr = (const u16*)d_in[1];
  const u16* wi = (const u16*)d_in[2];
  const u16* wqkv = (const u16*)d_in[4];
  const u16* wo = (const u16*)d_in[5];
  char* ws = (char*)d_ws;
  (void)hipFuncSetAttribute((const void*)k_main, hipFuncAttributeMaxDynamicSharedMemorySize, LDS_FUSED);
  k_main<<<256, 768, LDS_FUSED, stream>>>(x, wr, wi, wqkv, wo, ws, (u16*)d_out);
}
```

```cpp
#include <hip/hip_runtime.h>
#include <hip/hip_bf16.h>

typedef unsigned short u16;
typedef __attribute__((ext_vector_type(16))) float f32x16;
typedef __attribute__((ext_vector_type(4))) float f32x4;
typedef __attribute__((ext_vector_type(2))) float f32x2;
typedef __attribute__((ext_vector_type(8))) __bf16 bf16x8;
typedef __attribute__((ext_vector_type(2))) __bf16 bf16x2;
typedef __attribute__((ext_vector_type(4))) short s16x4;
typedef __attribute__((ext_vector_type(4))) unsigned u32x4;
typedef __attribute__((ext_vector_type(2))) unsigned u32x2;
typedef __attribute__((ext_vector_type(8))) unsigned u32x8;
#define DI __device__ __forceinline__
#define LDS_AS __attribute__((address_space(3)))
typedef LDS_AS char* lptr;

constexpr double cx_cos_small(double x) { double x2 = x * x, t = 1, s = 1; for (int i = 1; i < 12; i++) { t *= -x2 / ((2 * i - 1) * (2 * i)); s += t; } return s; }
constexpr double cx_sin_small(double x) { double x2 = x * x, t = x, s = x; for (int i = 1; i < 12; i++) { t *= -x2 / ((2 * i) * (2 * i + 1)); s += t; } return s; }
constexpr double cx_cos640(int m) {
  m %= 640; if (m > 320) m = 640 - m; double sg = 1; if (m > 160) { m = 320 - m; sg = -1; }
  const double w = 6.283185307179586476925 / 640; return sg * (m <= 80 ? cx_cos_small(w * m) : cx_sin_small(w * (160 - m)));
}
constexpr u16 cx_bf16(double d) { float f = (float)d; unsigned u = __builtin_bit_cast(unsigned, f); u += 0x7FFFu + ((u >> 16) & 1u); return (u16)(u >> 16); }
struct Cos640 { double v[640]; };
constexpr Cos640 mk_cos() { Cos640 c{}; for (int m = 0; m < 640; m++) c.v[m] = cx_cos640(m); return c; }
constexpr Cos640 COS = mk_cos();
__host__ __device__ constexpr int kf_of(int G, int i) { return G ? G + 40 * i : (i == 0 ? 320 : i < 8 ? 40 * i : 40 * i - 300); }
__host__ __device__ constexpr int freq_of(int G, int i) { return kf_of(G, i) > 320 ? 640 - kf_of(G, i) : kf_of(G, i); }
constexpr double SA = 1.91318, SA0 = 1.9140625, S64 = 1.86954, S32 = 1.6245;
constexpr double a1val(int out, int a) {
  if (out >= 40 || a >= 40) return 0;
  const int G = out >> 1, part = out & 1;
  if (G == 0) return (part && (a & 1)) ? -SA0 : SA0;
  return SA * COS.v[(16 * a * G + 160 * part) % 640];
}
constexpr double m2val(int G, int o, int k) {
  const int i = o >> 1, po = o & 1, part = k >> 4, b = k & 15, kf = kf_of(G, i);
  const double c = COS.v[(b * kf) % 640], sn = COS.v[(b * kf + 480) % 640];
  if (G == 0) {
    if (part != (i >= 8)) return 0;
    if (i == 0) return 0.039528470752104741 / SA0 * (po ? 1.0 : c);
    return 0.055901699437494742 / SA0 * (po ? -sn : c);
  }
  const double v = po ? (part ? c : -sn) : (part ? sn : c);
  return 0.055901699437494742 / SA * ((po && kf > 320) ? -v : v);
}
struct DTab { u16 a1[3][64][8]; u16 a1b[3][64][4]; u16 pad[256]; u16 m2[20][2][64][8]; };
constexpr DTab mk_dtab() {
  DTab t{};
  for (int l = 0; l < 64; l++) {
    const int r = l & 15, g = l >> 4;
    for (int T = 0; T < 3; T++) {
      for (int e = 0; e < 8; e++) t.a1[T][l][e] = cx_bf16(a1val(16 * T + r, 16 * (e >> 2) + 4 * g + (e & 3)));
      for (int e = 0; e < 4; e++) t.a1b[T][l][e] = cx_bf16(a1val(16 * T + r, 32 + 4 * g + e));
    }
    for (int G = 0; G < 20; G++) for (int T = 0; T < 2; T++) for (int e = 0; e < 8; e++)
      t.m2[G][T][l][e] = cx_bf16(m2val(G, 8 * (r >> 2) + 4 * T + (r & 3), 8 * g + e));
  }
  return t;
}
static __device__ const __attribute__((aligned(1024))) DTab g_dt = mk_dtab();

struct ITab { u16 m2[20][2][64][8]; u16 a1[3][64][8]; u16 a1b[3][64][4]; u16 pad[1792]; };
constexpr ITab mk_itab() {
  ITab t{};
  for (int l = 0; l < 64; l++) {
    const int r = l & 15, g = l >> 4;
    for (int T = 0; T < 3; T++) {
      for (int e = 0; e < 8; e++) t.a1[T][l][e] = cx_bf16(a1val(16 * (e >> 2) + 4 * g + (e & 3), 16 * T + r));
      for (int e = 0; e < 4; e++) t.a1b[T][l][e] = cx_bf16(a1val(32 + 4 * g + e, 16 * T + r));
    }
    for (int G = 0; G < 20; G++) for (int P = 0; P < 2; P++) for (int e = 0; e < 8; e++) t.m2[G][P][l][e] = cx_bf16(m2val(G, 8 * g + e, 16 * P + r));
  }
  return t;
}
static __device__ const __attribute__((aligned(1024))) ITab g_it = mk_itab();

struct CTab { u16 v[10 * 512]; };
constexpr CTab mk_ctab() {
  CTab c{};
  for (int f = 0; f < 10; f++) for (int l = 0; l < 64; l++) for (int e = 0; e < 8; e++) {
    const int r = l & 15, g = l >> 4;
    const int m = f < 8 ? (8 * g + e) * (16 * (f >> 1) + r) : 2 * r * (16 * (e >> 2) + 4 * g + (e & 3));
    c.v[(f * 64 + l) * 8 + e] = cx_bf16((f < 8 ? S64 : S32) * COS.v[10 * ((m + (f & 1) * 48) & 63)]);
  }
  return c;
}
static __device__ const __attribute__((aligned(1024))) CTab g_ct = mk_ctab();
constexpr float RSK = (float)(2048.0 * S64 * S64 * S32 * S32 * 2048.0 * S64 * S64 * S32 * S32 / 640.0);

constexpr int RP = 2048 + 64, RBP = 2048 + 128;
constexpr unsigned RBSZ = 640 * RBP * 2;
constexpr unsigned WS_FL = 0;
constexpr unsigned WS_AUX = WS_FL + 3 * 32768;
constexpr unsigned WS_F0 = WS_AUX + 2048;
constexpr unsigned WS_GO = WS_F0 + 8 * 2048;
constexpr unsigned WS_EP = WS_GO + 256 * 64;
constexpr unsigned WS_RS1 = WS_EP + 256;
constexpr unsigned WS_RB0 = WS_RS1 + 8192;
constexpr unsigned WS_RB1 = WS_RB0 + RBSZ;
constexpr unsigned WS_WF = WS_RB1 + RBSZ;
constexpr unsigned WS_WQ = WS_WF + 320 * 8192;
constexpr unsigned WS_WOF = WS_WQ + 16 * 8 * 20 * 1024;
constexpr unsigned WS_WOP = WS_WOF + 40 * 20 * 1024;
constexpr int KVC = 11264;
constexpr unsigned WS_KV = WS_WOP + 40 * 20 * 1024;
constexpr unsigned WS_O = WS_KV + 32 * 16 * KVC;
constexpr unsigned LAYER2 = 32 * 16 * KVC + 32 * 64 * 1280;
constexpr unsigned WS_X4 = WS_KV + 2 * LAYER2;
constexpr unsigned WS_R3 = WS_X4 + 2048 * 640 * 2;
constexpr unsigned WS_RB2 = WS_R3 + 640 * RP * 4;
constexpr unsigned WS_SSQ = WS_RB2 + RBSZ;
constexpr unsigned EMPTY = 0xFFFFFFFFu;
constexpr unsigned WS_END = WS_SSQ + 8 * 2048 * 16;

constexpr int XDELAY = 20, K1DELAY = 40, YIDLE = 12, TPOLL = 250, TSLEEP = 1, GSLEEP = 2, TFILL = 400, YSLEEP = 4, XSLEEP = 1, RSLEEP = 2;

#define SB() __builtin_amdgcn_sched_barrier(0)
typedef __amdgpu_buffer_rsrc_t rsrc_t;
DI rsrc_t mk_rsrc(const void* p, unsigned bytes) { return __builtin_amdgcn_make_buffer_rsrc((void*)p, 0, bytes, 0x00020000); }
template <int AUX> DI void st128(rsrc_t r, unsigned off, u32x4 v) { __builtin_amdgcn_raw_buffer_store_b128(v, r, off, 0, AUX); }
template <int AUX> DI void st64(rsrc_t r, unsigned off, u32x2 v) { __builtin_amdgcn_raw_buffer_store_b64(v, r, off, 0, AUX); }
template <int AUX> DI void st32(rsrc_t r, unsigned off, unsigned v) { __builtin_amdgcn_raw_buffer_store_b32(v, r, off, 0, AUX); }
template <int AUX> DI u32x4 gl128(rsrc_t r, unsigned off) { return __builtin_amdgcn_raw_buffer_load_b128(r, off, 0, AUX); }
template <int AUX> DI u32x2 gl64(rsrc_t r, unsigned off) { return __builtin_amdgcn_raw_buffer_load_b64(r, off, 0, AUX); }
template <int AUX> DI unsigned gl32(rsrc_t r, unsigned off) { return __builtin_amdgcn_raw_buffer_load_b32(r, off, 0, AUX); }
DI u32x4 asu(f32x4 v) { return __builtin_bit_cast(u32x4, v); }
DI f32x4 asf(u32x4 v) { return __builtin_bit_cast(f32x4, v); }
DI unsigned pk2(float a, float b) { f32x2 v = {a, b}; return __builtin_bit_cast(unsigned, __builtin_convertvector(v, bf16x2)); }
DI u32x2 pk4(f32x4 a) { return u32x2{pk2(a[0], a[1]), pk2(a[2], a[3])}; }
DI u32x4 pk8(f32x4 a, f32x4 b) { return u32x4{pk2(a[0], a[1]), pk2(a[2], a[3]), pk2(b[0], b[1]), pk2(b[2], b[3])}; }
DI u32x4 clean(u32x4 v) { return __builtin_elementwise_min(v, u32x4{EMPTY - 1, EMPTY - 1, EMPTY - 1, EMPTY - 1}); }
DI unsigned umax4(unsigned m, u32x4 v) { return max(max(max(m, v[0]), v[1]), max(v[2], v[3])); }
DI float bflo(unsigned u) { return __uint_as_float(u << 16); }
DI float bfhi(unsigned u) { return __uint_as_float(u & 0xFFFF0000u); }
DI int rowof(int p) { return 32 * (((p >> 5) & 0x3C) + (p & 3)) + ((p << 2) & 16) + ((p >> 3) & 15); }
DI int rpos(int p) { return ((p << 8) & 1024) + ((p >> 3) << 2) + (p & 3); }
DI int trow(int T, int r) { return 8 * (r >> 2) + 4 * T + (r & 3); }
template <int CTRL> DI float dpp_add(float v) { return v + __int_as_float(__builtin_amdgcn_update_dpp(0, __float_as_int(v), CTRL, 0xF, 0xF, true)); }
DI float row16_sum(float v) { v = dpp_add<0x128>(v); v = dpp_add<0x124>(v); v = dpp_add<0x122>(v); return dpp_add<0x121>(v); }
DI f32x4 mfma16(u32x4 a, u32x4 b, f32x4 c) {
  return __builtin_amdgcn_mfma_f32_16x16x32_bf16(__builtin_bit_cast(bf16x8, a), __builtin_bit_cast(bf16x8, b), c, 0, 0, 0);
}
DI void mfma16v(f32x4& c, u32x4 a, u32x4 b) { asm volatile("v_mfma_f32_16x16x32_bf16 %0, %1, %2, %0" : "+v"(c) : "v"(a), "v"(b)); }
DI u32x2 ldtr(lptr p) { return __builtin_bit_cast(u32x2, __builtin_amdgcn_ds_read_tr16_b64_v4i16((LDS_AS s16x4*)p)); }
DI u32x4 ld128(lptr p) { return *(LDS_AS u32x4*)p; }
DI u32x2 ld64(lptr p) { return *(LDS_AS u32x2*)p; }
#define GL(v, s, o, x) "global_load_lds_dwordx4 " v ", " s " offset:" #o x "\n\t"
#define M0SET "s_mov_b32 %[sv], m0\n\ts_mov_b32 m0, %[l]\n\ts_nop 2\n\t"
#define M0ADD(x) "s_add_u32 m0, m0, " x "\n\ts_nop 0\n\t"
#define M0RST "s_mov_b32 m0, %[sv]"
DI void dma2(unsigned v, const char* sb, unsigned l) { unsigned sv;
  asm volatile(M0SET GL("%[v]", "%[s]", 0, " sc1") GL("%[v]", "%[s]", 1024, " sc1") M0RST : [sv] "=&s"(sv) : [v] "v"(v), [s] "s"(sb), [l] "s"(l) : "memory"); }
DI void dma4(unsigned v, const char* sb, unsigned l) { unsigned sv;
  asm volatile(M0SET GL("%[v]", "%[s]", 0, "") GL("%[v]", "%[s]", 1024, "") GL("%[v]", "%[s]", 2048, "") GL("%[v]", "%[s]", 3072, "") M0RST
               : [sv] "=&s"(sv) : [v] "v"(v), [s] "s"(sb), [l] "s"(l) : "memory"); }
DI void dma6_sc1(unsigned v, const char* sb, unsigned l) { unsigned sv;
  asm volatile(M0SET GL("%[v]", "%[s]", 0, " sc1") GL("%[v]", "%[s]", 1024, " sc1") GL("%[v]", "%[s]", 2048, " sc1") M0ADD("3072")
               GL("%[w]", "%[s]", 0, " sc1") GL("%[w]", "%[s]", 1024, " sc1") GL("%[w]", "%[s]", 2048, " sc1") M0RST
               : [sv] "=&s"(sv) : [v] "v"(v), [w] "v"(v + 3072), [s] "s"(sb), [l] "s"(l) : "memory", "scc"); }
template <int N> DI void vm_bar() { asm volatile("s_waitcnt vmcnt(%0)\n\ts_barrier" ::"i"(N) : "memory"); }
DI void lds_bar() { asm volatile("s_waitcnt lgkmcnt(0)\n\ts_barrier" ::: "memory"); }
DI void raw_bar() { asm volatile("s_barrier" ::: "memory"); }
template <int N> DI void nap() { __builtin_amdgcn_s_sleep(N); asm volatile("" ::: "memory"); }
DI void drain() { asm volatile("s_waitcnt vmcnt(0)" ::: "memory"); }
#define CODE_ADDR(sym) ({ unsigned lo_, hi_; \
    asm volatile("s_getpc_b64 vcc\n\ts_add_u32 %0, vcc_lo, " sym "@rel32@lo+4\n\ts_addc_u32 %1, vcc_hi, " sym "@rel32@hi+12" : "=s"(lo_), "=s"(hi_) :: "vcc", "scc"); \
    (const unsigned*)(((unsigned long long)hi_ << 32) | lo_); })
#define SYM_MAIN "_Z6k_mainPKtS0_S0_S0_S0_PcPt"
constexpr int CODE_LINES = 380;
DI int wave_id() { return __builtin_amdgcn_readfirstlane(threadIdx.x >> 6); }
DI unsigned lds_addr(lptr p) { return (unsigned)(size_t)p; }

DI int kappa_to_i(int kap) {
  int ks = kap >> 5, g = (kap >> 3) & 3, e = kap & 7;
  if (ks < 16) return (2 * ks + (g >> 1)) * 20 + 8 * (e >> 2) + 4 * (g & 1) + (e & 3);
  return (8 * (ks - 16) + 2 * g + (e >> 2)) * 20 + 16 + (e & 3);
}

DI unsigned wo_frag(int nt, int ks) {
  const int ct = nt / 5, c5 = nt - 5 * ct, kh = ks < 16 ? ks >> 3 : (ks - 16) >> 1, i = ks < 16 ? ks & 7 : 8 + (ks & 1);
  const int a = kh ? (c5 >= 3 ? c5 - 3 : c5 + 2) : c5;
  return ((ct * 2 + kh) * 50 + 5 * i + a) * 1024;
}

constexpr int SLOT32 = 32768;
DI void dma_a(const unsigned* vm, const char* mb, unsigned l) { unsigned sv;
  asm volatile(M0SET GL("%[a0]", "%[m]", 0, " sc1") GL("%[a1]", "%[m]", 1024, " sc1") GL("%[a2]", "%[m]", 2048, " sc1") GL("%[a3]", "%[m]", 3072, " sc1") M0RST
               : [sv] "=&s"(sv) : [a0] "v"(vm[0]), [a1] "v"(vm[1]), [a2] "v"(vm[2]), [a3] "v"(vm[3]), [m] "s"(mb), [l] "s"(l) : "memory"); }
DI void dma_s(unsigned v0, unsigned v1, const char* sb, unsigned l) { unsigned sv;
  asm volatile(M0SET GL("%[v0]", "%[s]", 0, " sc1") GL("%[v0]", "%[s]", 1024, " sc1") GL("%[v1]", "%[s]", 2048, " sc1") GL("%[v1]", "%[s]", 3072, " sc1") M0RST
               : [sv] "=&s"(sv) : [v0] "v"(v0), [v1] "v"(v1), [s] "s"(sb), [l] "s"(l) : "memory"); }
DI void dma1_sc1(unsigned v, const char* sb, unsigned l) { unsigned sv;
  asm volatile(M0SET GL("%[v]", "%[s]", 0, " sc1") M0RST : [sv] "=&s"(sv) : [v] "v"(v), [s] "s"(sb), [l] "s"(l) : "memory"); }
DI void dma1(unsigned v, const char* sb, unsigned l) { unsigned sv;
  asm volatile(M0SET GL("%[v]", "%[s]", 0, "") M0RST : [sv] "=&s"(sv) : [v] "v"(v), [s] "s"(sb), [l] "s"(l) : "memory"); }

constexpr int L_I2 = 40 * 1056, LDS_PREP = L_I2 + 20 * 2064 + 1024;
DI void prep(const u16* x, const u16* wr, const u16* wi, const u16* wqkv, const u16* wo, char* ws) {
  extern __shared__ __attribute__((aligned(16))) char smem[];
  const lptr lds = (lptr)smem;
  const int tid = threadIdx.x, lane = tid & 63, w = wave_id(), g = lane >> 4, r16 = lane & 15;
  const int job = blockIdx.x;
  if (__builtin_expect(job < 144, 1)) {
    const bool cols = job >= 64 && job < 124;
    const int cj = job - 64, cb = cj < 48 ? (cj & 48) + 2 * (cj & 7) + ((cj >> 3) & 1) : cj;
    u32x4 a1[3], m2[3][2]; u32x2 a1b[3];
    if (w < 5) dma1(tid * 16, (const char*)&g_dt, lds_addr(lds) + LDS_PREP + w * 1024);
    if (cols) {
#pragma unroll
      for (int i = 0; i < 5; i++) dma1((16 * (5 * w + i) + (lane >> 2)) * 3840 + cb * 64 + (lane & 3) * 16, (const char*)wqkv, lds_addr(lds) + (5 * w + i) * 1056);
    } else {
      const char* src = (const char*)(job < 64 ? x : wo);
      const int sl = 8 * (w & 3) + 4 * (w >> 2);
      src += (job < 64 ? rowof(32 * job + sl) : kappa_to_i(32 * (job - 124) + sl)) * 1280;
      const int st = (job < 64 ? 31 : 0) * 1280;
#pragma unroll
      for (int i = 0; i < 5; i++) dma1(1024 * i + lane * 16 + ((i == 4 || lane < 16 * i) ? i - 1 : i) * st, src, lds_addr(lds) + (5 * w + i) * 1024);
    }
    unsigned so[3][2]; float sc[2] = {1.f, 1.f};
    if (cols) {
#pragma unroll
      for (int m = 0; m < 2; m++) {
        const int c = 32 * cb + 16 * m + r16;
        const int which = c / 640, rem = c - which * 640, head = rem / 20, d = rem - head * 20, grp = d >> 2;
        const int T = grp < 4 ? (which == 2 ? 2 : 0) + (grp & 1) : (which == 1 ? 3 : 2), gs = grp < 4 ? (which == 1 ? 2 : 0) + (grp >> 1) : (which == 2 ? 3 : 2);
        if (which == 0) sc[m] = 0.22360679774997896f * 1.4426950408889634f;
#pragma unroll
        for (int i = 0; i < 3; i++) so[i][m] = WS_WQ + ((head * 4 + T) * 20 + w + 8 * i) * 1024 + (g * 16 + 4 * gs + (d & 3)) * 16;
      }
    } else if (job < 64) {
#pragma unroll
      for (int i = 0; i < 3; i++)
#pragma unroll
        for (int T = 0; T < 2; T++) so[i][T] = WS_RB0 + (job * 641 + (job & 1) + 32 * (w + 8 * i) + trow(T, r16)) * 64 + g * 16;
    } else {
#pragma unroll
      for (int i = 0; i < 3; i++) {
        const unsigned f = wo_frag(2 * (w + 8 * i) + (r16 >> 3), job - 124);
#pragma unroll
        for (int T = 0; T < 2; T++) so[i][T] = WS_WOF + f + (g * 16 + (trow(T, r16) & 15)) * 16;
      }
    }
    if (w >= 4) { so[2][0] = WS_END; so[2][1] = WS_END; }
    asm volatile("" : "+v"(so[0][0]), "+v"(so[0][1]), "+v"(so[1][0]), "+v"(so[1][1]), "+v"(so[2][0]), "+v"(so[2][1]), "+v"(sc[0]), "+v"(sc[1]));
    const f32x4 z4 = {0.f, 0.f, 0.f, 0.f};
    const lptr i2 = lds + L_I2;
    drain();
#pragma unroll
    for (int i = 0; i < 3; i++)
#pragma unroll
      for (int T = 0; T < 2; T++)
        asm volatile("global_load_dwordx4 %0, %1, %2" : "=v"(m2[i][T]) : "v"(((((i < 2 || w < 4) ? w + 8 * i : w) * 2 + T) * 64 + lane) * 16), "s"(&g_dt.m2[0][0][0][0]) : "memory");
    raw_bar();
#pragma unroll
    for (int T = 0; T < 3; T++) { a1[T] = ld128(lds + LDS_PREP + T * 1024 + lane * 16); a1b[T] = ld64(lds + LDS_PREP + 3072 + T * 512 + lane * 8); }
    f32x4 t1[4][3];
    if (cols) {
      const lptr src = lds + (4 * g + (r16 >> 2)) * 1056 + (w >> 1) * 256 + (w & 1) * 32 + (lane & 3) * 8, srb = src + (32 - 8 * (g >> 1)) * 1056;
#pragma unroll
      for (int i = 0; i < 4; i++) {
        const u32x2 f0 = ldtr(src + i * 64), f1 = ldtr(src + 16 * 1056 + i * 64), f2 = ldtr(srb + i * 64);
#pragma unroll
        for (int T = 0; T < 3; T++) t1[i][T] = mfma16(u32x4{a1b[T][0], a1b[T][1], 0u, 0u}, u32x4{f2[0], f2[1], 0u, 0u}, mfma16(a1[T], u32x4{f0[0], f0[1], f1[0], f1[1]}, z4));
      }
      const int xo = ((w >> 1) * 8) ^ (((r16 >> 1) & 3) * 16);
      const lptr dst = i2 + 2 * g * 2064 + (16 * (w & 1) + r16) * 64;
#pragma unroll
      for (int T = 0; T < 3; T++)
#pragma unroll
        for (int e = 0; e < 4; e++)
          if (T < 2 || g < 2) *(LDS_AS u32x2*)(dst + (8 * T + (e >> 1)) * 2064 + (xo ^ ((e & 1) * 32))) = u32x2{pk2(t1[0][T][e], t1[1][T][e]), pk2(t1[2][T][e], t1[3][T][e])};
    } else {
      const lptr src = lds + w * 5120 + lane * 8, srb = lds + w * 5120 + 1024 + (lane & 31) * 8;
#pragma unroll
      for (int i = 0; i < 4; i++) {
        const u32x2 f0 = ldtr(src + i * 1280), f1 = ldtr(src + i * 1280 + 512), f2 = ldtr(srb + i * 1280);
#pragma unroll
        for (int T = 0; T < 3; T++) t1[i][T] = mfma16(u32x4{f2[0], f2[1], 0u, 0u}, u32x4{a1b[T][0], a1b[T][1], 0u, 0u}, mfma16(u32x4{f0[0], f0[1], f1[0], f1[1]}, a1[T], z4));
      }
      const int xo = ((r16 & 1) * 32 + g * 8) ^ ((w & 1) * 32);
      const lptr dst = i2 + (r16 >> 1) * 2064 + w * 256;
#pragma unroll
      for (int i = 0; i < 4; i++)
#pragma unroll
        for (int T = 0; T < 3; T++)
          if (T < 2 || r16 < 8) *(LDS_AS u32x2*)(dst + T * 8 * 2064 + i * 64 + (xo ^ ((i >> 1) * 16))) = pk4(t1[i][T]);
    }
    lds_bar();
    const rsrc_t rw = mk_rsrc(ws, WS_END);
    asm volatile("s_waitcnt vmcnt(0)" : "+v"(m2[0][0]), "+v"(m2[0][1]), "+v"(m2[1][0]), "+v"(m2[1][1]), "+v"(m2[2][0]), "+v"(m2[2][1]));
    f32x4 sq[2] = {z4, z4};
    u32x4 f[3][2];
#pragma unroll
    for (int i = 0; i < 3; i++) {
      const lptr fp = i2 + ((i < 2 || w < 4) ? w + 8 * i : 0) * 2064 + r16 * 64 + ((g ^ ((r16 >> 1) & 3)) * 16);
      f[i][0] = ld128(fp); f[i][1] = ld128(fp + 1024);
    }
    if (cols) {
#pragma unroll
      for (int i = 0; i < 3; i++)
#pragma unroll
        for (int m = 0; m < 2; m++) st128<16>(rw, so[i][m], pk8(mfma16(m2[i][0], f[i][m], z4) * sc[m], mfma16(m2[i][1], f[i][m], z4) * sc[m]));
    } else {
#pragma unroll
      for (int i = 0; i < 3; i++)
#pragma unroll
        for (int T = 0; T < 2; T++) {
          const f32x4 d0 = mfma16(f[i][0], m2[i][T], z4), d1 = mfma16(f[i][1], m2[i][T], z4);
          st128<16>(rw, so[i][T], pk8(d0, d1));
          const float k = ((i == 2 && w >= 4) || (i == 0 && T == 0 && tid < 64 && r16 == 1)) ? 0.f : 1.f;
          sq[0] += d0 * d0 * k; sq[1] += d1 * d1 * k;
        }
    }
    LDS_AS float* sl = (LDS_AS float*)(lds + LDS_PREP - 1024);
    if (job < 64) {
#pragma unroll
      for (int m = 0; m < 2; m++) {
#pragma unroll
        for (int e = 0; e < 4; e++) sq[m][e] = row16_sum(sq[m][e]);
        if (r16 == 0) *(LDS_AS f32x4*)(sl + w * 32 + 8 * g + 4 * m) = sq[m];
      }
    }
    if (job < 64) {
      lds_bar();
      if (tid < 32) {
        float v = 0.f;
#pragma unroll
        for (int i = 0; i < 8; i++) v += sl[i * 32 + tid];
        st32<16>(rw, WS_RS1 + rpos(32 * job + tid) * 4, __float_as_uint(__builtin_amdgcn_rsqf((v + 640.f * 1e-5f) * RSK)));
      }
    }
    return;
  }
  const int mi = job - 144;
  const rsrc_t rw = mk_rsrc(ws, WS_END);
  if (mi < 64) {
    const int m = mi >> 3, oc = mi & 7;
    const char* src = (const char*)((w >> 2) ? wi : wr) + (64 * (4 * m + (w & 3)) + 8 * oc) * 642;
    dma4(lane * 16, src, lds_addr(lds) + w * 5152);
    dma1(4096 + lane * 16, src, lds_addr(lds) + w * 5152 + 4096);
    if (lane == 0) dma1(5120, src, lds_addr(lds) + w * 5152 + 5120);
    vm_bar<0>();
    LDS_AS u16* L = (LDS_AS u16*)lds + (tid & 7) * 321;
#pragma unroll
    for (int it = 0; it < 5; it++) {
      const int c = (tid >> 3) + 64 * it, col = freq_of(c >> 4, c & 15);
      u32x4 o;
#pragma unroll
      for (int e = 0; e < 4; e++) o[e] = (unsigned)L[e * 2576 + col] | ((unsigned)L[(4 + e) * 2576 + col] << 16);
      st128<16>(rw, WS_WF + c * 8192 + (m * 64 + 8 * oc + (tid & 7)) * 16, o);
    }
#pragma unroll
    for (int it = 0; it < 3; it++) st128<16>(rw, WS_RB1 + ((it * 64 + mi) * 512 + tid) * 16, u32x4{EMPTY, EMPTY, EMPTY, EMPTY});
  } else if (mi < 84) {
    unsigned pf0;
    {
      const unsigned* cm = CODE_ADDR(SYM_MAIN);
      const unsigned* p = mi >= 80 ? (const unsigned*)&g_it + min(tid, 359) * 32 : (mi & 8) ? (const unsigned*)&g_dt.m2[0][0][0][0] + min(tid, 319) * 32 : tid < 80 ? (const unsigned*)&g_ct + tid * 32 : cm + min(tid - 80, CODE_LINES - 1) * 32;
      asm volatile("global_load_dword %0, %1, off" : "=v"(pf0) : "v"(p) : "memory");
    }
    const char* src = (const char*)wo + kappa_to_i(32 * (mi - 64) + 8 * (w & 3) + 4 * (w >> 2)) * 1280;
    dma4(lane * 16, src, lds_addr(lds) + w * 5152);
    dma1(4096 + lane * 16, src, lds_addr(lds) + w * 5152 + 4096);
    asm volatile("s_waitcnt vmcnt(0)\n\ts_barrier" : "+v"(pf0) :: "memory");
#pragma unroll
    for (int i = 0; i < 5; i++) {
      const lptr a = lds + g * 5152 + (r16 >> 2) * 1280 + w * 160 + (i == 2 ? 64 + (lane & 3) * 8 : (i > 2 ? 96 : 0) + (lane & 3) * 16 + (i == 1 || i == 4 ? 8 : 0));
      const u32x2 lo = ldtr(a), hi = ldtr(a + 4 * 5152);
      st128<16>(rw, WS_WOP + wo_frag(5 * w + i, mi - 64) + lane * 16, u32x4{lo[0], lo[1], hi[0], hi[1]});
    }
  } else {
    const int i0 = (mi - 84) * 512 + tid;
    nap<K1DELAY>();
    if (i0 < 6272) st128<16>(rw, WS_FL + i0 * 16, u32x4{0u, 0u, 0u, 0u});
    if (i0 < 10240) st128<16>(rw, WS_WQ + (((i0 / 320) * 4 + 3) * 80 + (i0 % 320 >> 2)) * 256 + (12 + (i0 & 3)) * 16, u32x4{0u, 0u, 0u, 0u});
#pragma unroll
    for (int it = 0; it < 6; it++) { const int i = 98304 + i0 + it * 14336; if (i < (int)RBSZ / 16) st128<16>(rw, WS_RB1 + i * 16, u32x4{EMPTY, EMPTY, EMPTY, EMPTY}); }
  }
}

#define PINF(x) asm volatile("" : "+v"(x))
DI void wave_sync(LDS_AS unsigned* c, unsigned n) {
  asm volatile("s_waitcnt lgkmcnt(0)" ::: "memory");
  if ((threadIdx.x & 63) == 0) __hip_atomic_fetch_add(c, 1u, __ATOMIC_RELAXED, __HIP_MEMORY_SCOPE_WORKGROUP);
  while (__builtin_amdgcn_readfirstlane(*(volatile LDS_AS unsigned*)c) < n) {}
  asm volatile("" ::: "memory");
}
DI void put_scale(lptr lds, int q, int tid, const u32x2* sp) {
  f32x4 r = {640.f * 1e-5f, 640.f * 1e-5f, 640.f * 1e-5f, 640.f * 1e-5f};
#pragma unroll
  for (int i = 0; i < 8; i++) r += f32x4{bflo(sp[i][0]), bfhi(sp[i][0]), bflo(sp[i][1]), bfhi(sp[i][1])};
#pragma unroll
  for (int e = 0; e < 4; e++) r[e] = __builtin_amdgcn_rsqf(r[e] * RSK);
  ((LDS_AS f32x4*)(lds + 2 * SLOT32))[q * 256 + tid] = r;
}
struct ColIn { u32x4 c64, s64, c32, s32, wf[2]; };
DI int col_of() { return blockIdx.x < 64 ? 192 + 2 * blockIdx.x + (wave_id() >> 2) : blockIdx.x - 64; }
DI void col_load(ColIn& k, rsrc_t rw, bool tab, bool filt) {
  int lane = threadIdx.x & 63; PINF(lane);
  const int w = wave_id() & 3;
  const u32x4* ct = (const u32x4*)&g_ct + lane;
  if (tab) { k.c64 = ct[(2 * w) * 64]; k.s64 = ct[(2 * w + 1) * 64]; k.c32 = ct[8 * 64]; k.s32 = ct[9 * 64]; }
  if (filt) {
#pragma unroll
    for (int t = 0; t < 2; t++) k.wf[t] = gl128<16>(rw, WS_WF + col_of() * 8192 + ((4 * t + (lane >> 4)) * 64 + 16 * w + (lane & 15)) * 16);
  }
}
template <bool FIRST>
DI void col_phase(rsrc_t rw, lptr lds, ColIn kin) {
  int lane = threadIdx.x & 63; PINF(lane);
  const int wv = wave_id(), w = wv & 3, q = wv >> 2, tid = w * 64 + lane, g = lane >> 4, r16 = lane & 15;
  const int c = col_of();
  const lptr img = lds + q * SLOT32;
  f32x4 xr[2], xi[2], rs[2]; u32x4 xb[2];
  u32x4 c64[2], s64[2], n64[2], c32[2], s32[2], n32[2];
  if (FIRST) col_load(kin, rw, true, false);
  SB();
  f32x4 t1c[2], t1s[2], t2c[2], t2s[2];
  {
    const float f1 = (float)(16 * w + r16) * (1.f / 2048.f), b1 = (float)(4 * g), f2 = (float)(16 * w + 4 * g) * (1.f / 2048.f), b2 = (float)r16;
#pragma unroll
    for (int t = 0; t < 2; t++) {
#pragma unroll
      for (int e = 0; e < 4; e++) {
        const float a1 = (b1 + (float)(16 * t + e)) * f1, a2 = (b2 + (float)(16 * t)) * (f2 + (float)e * (1.f / 2048.f));
        t1c[t][e] = __builtin_amdgcn_cosf(a1); t1s[t][e] = __builtin_amdgcn_sinf(-a1);
        t2c[t][e] = __builtin_amdgcn_cosf(a2); t2s[t][e] = __builtin_amdgcn_sinf(a2);
      }
      PINF(t1c[t]); PINF(t1s[t]); PINF(t2c[t]); PINF(t2s[t]);
    }
  }
  int wo_ = r16 * 128 + (((2 * w + (g >> 1)) ^ (r16 >> 1)) * 16) + (g & 1) * 8, ro_ = r16 * 128 + ((g ^ (r16 >> 1)) * 16), ro2 = ro_ ^ 64;
  PINF(wo_); PINF(ro_); PINF(ro2);
  {
    const unsigned par = (lane & 1) ? 0x80008000u : 0u;
    c64[0] = kin.c64; s64[0] = kin.s64; c32[0] = kin.c32; s32[0] = kin.s32;
    c64[1] = c64[0] ^ par; s64[1] = s64[0] ^ par; n64[0] = s64[0] ^ 0x80008000u; n64[1] = s64[1] ^ 0x80008000u;
    c32[1] = c32[0] ^ 0x80000000u; s32[1] = s32[0] ^ 0x80000000u; n32[0] = s32[0] ^ 0x80008000u; n32[1] = s32[0] ^ 0x00008000u;
    PINF(c64[1]); PINF(s64[1]); PINF(n64[0]); PINF(n64[1]); PINF(c32[1]); PINF(s32[1]); PINF(n32[0]); PINF(n32[1]);
  }
  SB();
  if (FIRST) {
    raw_bar();
#pragma unroll
    for (int t = 0; t < 2; t++) { xb[t] = gl128<16>(rw, WS_RB0 + ((tid >> 2) * 641 + ((tid >> 2) & 1) + 2 * c + t) * 64 + (tid & 3) * 16); rs[t] = asf(gl128<16>(rw, WS_RS1 + (1024 * t + 4 * tid) * 4)); }
    col_load(kin, rw, false, true);
    SB();
#pragma unroll
    for (int t = 0; t < 2; t++) {
      xr[t] = f32x4{bflo(xb[0][2 * t]), bfhi(xb[0][2 * t]), bflo(xb[0][2 * t + 1]), bfhi(xb[0][2 * t + 1])};
      xi[t] = f32x4{bflo(xb[1][2 * t]), bfhi(xb[1][2 * t]), bflo(xb[1][2 * t + 1]), bfhi(xb[1][2 * t + 1])};
    }
  } else {
    drain();
    u32x2 sp[8];
    const unsigned so = WS_SSQ + (blockIdx.x & 7) * 32768 + (1024 * q + 4 * tid) * 2, xo = WS_R3 + ((tid >> 2) * 642 + 2 * c) * 64 + (tid & 3) * 16;
    bool bs = true, bx = true;
#pragma unroll 1
    for (int spins = 0; spins < (1 << 18); spins++) {
      if (bx) {
#pragma unroll
        for (int t = 0; t < 2; t++) { xr[t] = asf(gl128<16>(rw, xo + 64 * 642 * 64 * t)); xi[t] = asf(gl128<16>(rw, xo + 64 * 642 * 64 * t + 64)); }
      }
      if (bs) {
#pragma unroll
        for (int i = 0; i < 8; i++) sp[i] = gl64<16>(rw, so + i * 4096);
      }
      unsigned m = 0;
#pragma unroll
      for (int t = 0; t < 2; t++) m = umax4(umax4(m, asu(xr[t])), asu(xi[t]));
      bx = __builtin_amdgcn_ballot_w64(m == EMPTY) != 0;
      m = 0;
#pragma unroll
      for (int i = 0; i < 8; i++) m = max(max(m, sp[i][0]), sp[i][1]);
      bs = __builtin_amdgcn_ballot_w64(m == EMPTY) != 0;
      if (!bs && !bx) break;
      nap<YSLEEP>();
    }
    put_scale(lds, q, tid, sp);
    LDS_AS f32x4* ex = (LDS_AS f32x4*)(lds + 2 * SLOT32) + tid;
    lds_bar();
    rs[0] = ex[0]; rs[1] = ex[256];
  }
#pragma unroll
  for (int t = 0; t < 2; t++) {
    u32x2 pb = pk4(xi[t] * rs[t]);
    if (c == 0) pb = u32x2{0u, 0u};
    *(LDS_AS u32x2*)(img + wo_ + t * 2048) = pk4(xr[t] * rs[t]);
    *(LDS_AS u32x2*)(img + 4096 + wo_ + t * 2048) = pb;
  }
  lds_bar();
  u32x4 ur[2][2], ui[2][2], br, bi;
  f32x4 ar[2], ai[2];
  const f32x4 z4 = {0.f, 0.f, 0.f, 0.f};
  auto rd_in = [&](int off) {
#pragma unroll
    for (int ks = 0; ks < 2; ks++)
#pragma unroll
      for (int t = 0; t < 2; t++) { const lptr p = img + off + (ks ? ro2 : ro_) + t * 2048; ur[t][ks] = ld128(p); ui[t][ks] = ld128(p + 4096); }
    SB();
  };
  auto cmul = [&](const f32x4* cc, const f32x4* sn) {
    br = pk8(ar[0] * cc[0] - ai[0] * sn[0], ar[1] * cc[1] - ai[1] * sn[1]);
    bi = pk8(ai[0] * cc[0] + ar[0] * sn[0], ai[1] * cc[1] + ar[1] * sn[1]);
  };
  rd_in(0);
#pragma unroll
  for (int ks = 0; ks < 2; ks++) {
#pragma unroll
    for (int t = 0; t < 2; t++) { ar[t] = mfma16(ur[t][ks], c64[ks], ks ? ar[t] : z4); ai[t] = mfma16(ui[t][ks], c64[ks], ks ? ai[t] : z4); }
#pragma unroll
    for (int t = 0; t < 2; t++) { ar[t] = mfma16(ui[t][ks], s64[ks], ar[t]); ai[t] = mfma16(ur[t][ks], n64[ks], ai[t]); }
  }
  const f32x4 fa[2] = {asf(kin.wf[0] << 16), asf(kin.wf[1] << 16)}, fb[2] = {asf(kin.wf[0] & 0xFFFF0000u), asf(kin.wf[1] & 0xFFFF0000u)};
  cmul(t1c, t1s);
#pragma unroll
  for (int t = 0; t < 2; t++) { ar[t] = mfma16(c32[t], br, z4); ai[t] = mfma16(c32[t], bi, z4); }
#pragma unroll
  for (int t = 0; t < 2; t++) { ar[t] = mfma16(s32[t], bi, ar[t]); ai[t] = mfma16(n32[t], br, ai[t]); }
  cmul(fa, fb);
#pragma unroll
  for (int t = 0; t < 2; t++) { ar[t] = mfma16(br, c32[t], z4); ai[t] = mfma16(bi, c32[t], z4); }
#pragma unroll
  for (int t = 0; t < 2; t++) { ar[t] = mfma16(bi, n32[t], ar[t]); ai[t] = mfma16(br, s32[t], ai[t]); }
  cmul(t2c, t2s);
#pragma unroll
  for (int t = 0; t < 2; t++) {
    *(LDS_AS u32x2*)(img + 8192 + wo_ + t * 2048) = u32x2{br[2 * t], br[2 * t + 1]};
    *(LDS_AS u32x2*)(img + 12288 + wo_ + t * 2048) = u32x2{bi[2 * t], bi[2 * t + 1]};
  }
  lds_bar();
  rd_in(8192);
#pragma unroll
  for (int ks = 0; ks < 2; ks++) {
#pragma unroll
    for (int t = 0; t < 2; t++) { ar[t] = mfma16(c64[ks], ur[t][ks], ks ? ar[t] : xr[t]); ai[t] = mfma16(c64[ks], ui[t][ks], ks ? ai[t] : xi[t]); }
#pragma unroll
    for (int t = 0; t < 2; t++) { ar[t] = mfma16(n64[ks], ui[t][ks], ar[t]); ai[t] = mfma16(s64[ks], ur[t][ks], ai[t]); }
  }
  const unsigned ob = (FIRST ? WS_RB1 : WS_RB2) + (2 * c * RBP + 8 * tid) * 2;
  st128<16>(rw, ob, clean(pk8(ar[0], ar[1])));
  st128<16>(rw, ob + RBP * 2, c == 0 ? u32x4{0u, 0u, 0u, 0u} : clean(pk8(ai[0], ai[1])));
}

DI f32x16 mfma32(u32x4 a, u32x4 b, f32x16 c) {
  return __builtin_amdgcn_mfma_f32_32x32x16_bf16(__builtin_bit_cast(bf16x8, a), __builtin_bit_cast(bf16x8, b), c, 0, 0, 0);
}
DI f32x16 mfma32k8(u32x2 a, u32x2 b, f32x16 c) {
  return __builtin_amdgcn_mfma_f32_32x32x8bf16_1k(__builtin_bit_cast(s16x4, a), __builtin_bit_cast(s16x4, b), c, 0, 0, 0);
}
DI f32x16 scores(u32x4 k1, u32x2 k2, u32x4 q1, u32x2 q2) {
  f32x16 z;
#pragma unroll
  for (int i = 0; i < 16; i++) z[i] = 0.f;
  f32x16 s = mfma32(k1, q1, z);
  asm volatile("s_nop 11" : "+v"(s));
  return mfma32k8(k2, q2, s);
}
DI float max16(const f32x16& s) {
  float m = fmaxf(s[0], s[1]);
#pragma unroll
  for (int i = 2; i < 16; i++) m = fmaxf(m, s[i]);
  return m;
}
#define STR_(x) #x
#define STR(x) STR_(x)
#define KOF(t) ((((t)&7)>>1)*11264+((t)&1)*1280)
#define VOF(t) ((((t)&7)>>1)*11264+5120+((t)&1)*1536)
#define EXP(d, s) "v_exp_f32 v" #d ", v" #s "\n\t"
#define CVT(d, s) "v_cvt_pk_bf16_f32 v" #d ", v" #s ", v[" #s "+1]\n\t"
#define STEPA(t) \
  "v_mfma_f32_32x32x16_bf16 v[48:63], v[80:83], v[136:139], v[120:135]\n\t" \
  "ds_read_b128 v[80:83], v100 offset:" STR(KOF(t+1)) "\n\t" \
  EXP(64,32) EXP(65,33) CVT(78,68) CVT(79,70) EXP(66,34) EXP(67,35) \
  "v_mfma_f32_32x32x16_bf16 v[104:119], v[90:93], v[76:79], v[104:119]\n\t" \
  "ds_read_b64_tr_b16 v[90:91], v102 offset:" STR(VOF(t)+768) "\n\t" \
  "ds_read_b64_tr_b16 v[92:93], v102 offset:" STR(VOF(t)+1152) "\n\t" \
  EXP(68,36) EXP(69,37) CVT(72,64) CVT(73,66) EXP(70,38) EXP(71,39) \
  "v_mfma_f32_32x32x8_bf16 v[48:63], v[84:85], v[140:141], v[48:63]\n\t" \
  "ds_read_b64 v[84:85], v101 offset:" STR(KOF(t+1)) "\n\t" \
  EXP(64,40) EXP(65,41) CVT(74,68) CVT(75,70) EXP(66,42) EXP(67,43) \
  "s_waitcnt lgkmcnt(4)\n\t" \
  "v_mfma_f32_32x32x16_bf16 v[0:15], v[86:89], v[72:75], v[0:15]\n\t" \
  EXP(68,44) EXP(69,45) CVT(76,64) CVT(77,66) EXP(70,46) EXP(71,47)
#define STEPB(t) \
  "s_waitcnt lgkmcnt(0)\n\t" \
  "v_mfma_f32_32x32x16_bf16 v[32:47], v[80:83], v[94:97], v[16:31]\n\t" \
  EXP(64,48) EXP(65,49) CVT(78,68) CVT(79,70) EXP(66,50) EXP(67,51) \
  "v_mfma_f32_32x32x16_bf16 v[0:15], v[90:93], v[76:79], v[0:15]\n\t" \
  EXP(68,52) EXP(69,53) CVT(72,64) CVT(73,66) EXP(70,54) EXP(71,55) \
  "v_mfma_f32_32x32x8_bf16 v[32:47], v[84:85], v[98:99], v[32:47]\n\t" \
  EXP(64,56) EXP(65,57) CVT(74,68) CVT(75,70) EXP(66,58) EXP(67,59) \
  "v_mfma_f32_32x32x16_bf16 v[104:119], v[86:89], v[72:75], v[104:119]\n\t" \
  "ds_read_b64_tr_b16 v[86:87], v102 offset:" STR(VOF(t+1)) "\n\t" \
  "ds_read_b64_tr_b16 v[88:89], v102 offset:" STR(VOF(t+1)+384) "\n\t" \
  EXP(68,60) EXP(69,61) CVT(76,64) CVT(77,66) EXP(70,62) EXP(71,63)
#define PR(n) "s_setprio " #n "\n\t"
#define CHUNK(i) PR(1) STEPA(2*(i)) PR(0) STEPB(2*(i)) "s_barrier\n\t" PR(2) STEPA(2*(i)+1) STEPB(2*(i)+1)
#define Z4(a) "v_mov_b64 v[" #a ":" #a "+1], 0\n\tv_mov_b64 v[" #a "+2:" #a "+3], 0\n\t"

#define R4(b) "v[" STR(b) ":" STR(b) "+3]"
#define MM(d, a, b) "v_mfma_f32_16x16x32_bf16 " R4(d) ", " R4(a) ", " R4(b) ", " R4(d) "\n\t"
#define GM(B, m, i) MM(16*m+4*i, B+8+4*i, B+4*m)
#define RT(B, m, hf, k) "ds_read_b64_tr_b16 v[" STR(B+4*m+2*hf) ":" STR(B+4*m+2*hf+1) "], v[84+" #m "] offset:" STR(k*8192+hf*1024) "\n\t"
#define RS(B, i, k) "ds_read_b128 " R4(B+8+4*i) ", v86 offset:" STR(i*2048+k*1024) "\n\t"
#define GSTEP(B, C, k) GM(B,0,0) RT(C,0,0,k) RT(C,0,1,k) GM(B,1,0) RS(C,0,k) RT(C,1,0,k) GM(B,0,1) RT(C,1,1,k) RS(C,1,k) GM(B,1,1) RS(C,2,k) \
  GM(B,0,2) RS(C,3,k) GM(B,1,2) GM(B,0,3) GM(B,1,3) MM(32,B,B)
#define GEMM_ASM(EXTRA) \
  Z4(0) Z4(4) Z4(8) Z4(12) Z4(16) Z4(20) Z4(24) Z4(28) Z4(32) "s_mov_b32 %[c], 0\n\ts_barrier\n\t" \
  RT(36,0,0,0) RT(36,0,1,0) RS(36,0,0) RT(36,1,0,0) RT(36,1,1,0) RS(36,1,0) RS(36,2,0) RS(36,3,0) EXTRA \
  "L_gemm_%=:\n\ts_add_u32 %[c], %[c], 1\n\ts_and_b32 %[t], %[c], 3\n\ts_cselect_b32 %[t], 1, -3\n\ts_lshl_b32 %[t], %[t], 15\n\ts_waitcnt lgkmcnt(0)\n\t" \
  GSTEP(36,60,1) \
  "v_add_u32 v84, %[t], v84\n\tv_add_u32 v85, %[t], v85\n\tv_add_u32 v86, %[t], v86\n\ts_waitcnt lgkmcnt(0)\n\ts_barrier\n\ts_setprio 1\n\t" \
  GSTEP(60,36,0) "s_setprio 0\n\t" \
  "s_cmp_lg_u32 %[c], 9\n\ts_cbranch_scc1 L_gemm_%=\n\ts_waitcnt lgkmcnt(0)\n\t" \
  GSTEP(36,60,1) "s_waitcnt lgkmcnt(0)\n\t" \
  MM(32,60,60) GM(60,0,0) GM(60,1,0) GM(60,0,1) GM(60,1,1) "s_nop 4\n\t" \
  "v_cndmask_b32 v36, v32, v33, %[m1]\n\tv_cndmask_b32 v37, v34, v35, %[m1]\n\tv_mov_b32 v38, 0x3727c5ac\n\tv_cndmask_b32 v36, v36, v37, %[m2]\n\t" \
  "v_fmac_f32 v38, 0x3acccccd, v36\n\tv_cmp_u_f32 vcc, v36, v36\n\tv_rsq_f32 v38, v38\n\ts_cbranch_vccz L_gok_%=\n\tds_write_b32 v90, v90\n\t" \
  "L_gok_%=:\n\tds_write_b32 v87, v38\n\ts_waitcnt lgkmcnt(0)\n\ts_barrier\n\t" \
  "ds_read_b32 v36, v88\n\tds_read_b32 v37, v89\n\tds_read_b32 v38, v90\n\t" \
  GM(60,0,2) GM(60,1,2) GM(60,0,3) GM(60,1,3) "s_waitcnt vmcnt(0) lgkmcnt(0)"
#define GEMM_OUT "={v[0:3]}"(acc[0][0]), "={v[4:7]}"(acc[0][1]), "={v[8:11]}"(acc[0][2]), "={v[12:15]}"(acc[0][3]), \
    "={v[16:19]}"(acc[1][0]), "={v[20:23]}"(acc[1][1]), "={v[24:27]}"(acc[1][2]), "={v[28:31]}"(acc[1][3]), \
    "={v36}"(rs[0]), "={v37}"(rs[1]), "={v38}"(vote), "+{v84}"(am0), "+{v85}"(am1), "+{v86}"(as), [c] "=&s"(c_), [t] "=&s"(t_)
#define GEMM_IN "{v87}"(a_w), "{v88}"(a_r[0]), "{v89}"(a_r[1]), "{v90}"(a_v), [m1] "s"(0xAAAAAAAAAAAAAAAAull), [m2] "s"(0xCCCCCCCCCCCCCCCCull)
#define GEMM_CLOB "memory", "scc", "vcc", "v32", "v33", "v34", "v35", "v39", "v40", "v41", "v42", "v43", "v44", "v45", "v46", "v47", "v48", "v49", "v50", \
    "v51", "v52", "v53", "v54", "v55", "v56", "v57", "v58", "v59", "v60", "v61", "v62", "v63", "v64", "v65", "v66", "v67", "v68", "v69", "v70", "v71", \
    "v72", "v73", "v74", "v75", "v76", "v77", "v78", "v79", "v80", "v81", "v82", "v83"
#define ITL(d, a) "global_load_dwordx4 " R4(d) ", v" #a ", %[it]\n\tglobal_load_dwordx4 " R4(d+4) ", v" #a ", %[it] offset:1024\n\t"

constexpr int HR = 4 * KVC;
constexpr int L_A = 2 * HR;
constexpr int L_EX = 15360;
constexpr int L_T = 4 * SLOT32, L_IA = L_T + 10240, L_U = L_A + 10240;
constexpr int L_MISC = L_A + 70 * 1024;
constexpr int LDS_FUSED = L_MISC + 2048;

DI bool poll16(rsrc_t rw, unsigned off, unsigned need) {
  const int lane = threadIdx.x & 63;
#pragma unroll 1
  for (int spins = 0; spins < (1 << 20); spins++) {
    const unsigned v = gl32<16>(rw, off + (lane & 15) * 4);
    const unsigned got = (unsigned)__builtin_amdgcn_ballot_w64(v != 0) & 0xFFFFu;
    if ((got & need) == need) return true;
    nap<1>();
  }
  return false;
}

DI void poll_aux(rsrc_t rw, unsigned off) {
#pragma unroll 1
  for (int spins = 0; spins < (1 << 20); spins++) {
    const u32x4 v = gl128<16>(rw, off + (threadIdx.x & 63) * 16);
    if (!__builtin_amdgcn_ballot_w64(min(min(v[0], v[1]), min(v[2], v[3])) == 0u)) break;
    nap<4>();
  }
}
DI void poll_tag(rsrc_t rw, u32x2 tag, unsigned long long need) {
  const unsigned o = WS_F0 + (blockIdx.x & 7) * 2048 + (threadIdx.x & 63) * 32;
#pragma unroll 1
  for (int spins = 0; spins < (1 << 20); spins++) {
    const u32x4 a = gl128<16>(rw, o), b = gl128<16>(rw, o + 16);
    const unsigned d = (a[0] ^ tag[0]) | (a[1] ^ tag[1]) | (a[2] ^ tag[0]) | (a[3] ^ tag[1]) | (b[0] ^ tag[0]) | (b[1] ^ tag[1]) | (b[2] ^ tag[0]) | (b[3] ^ tag[1]);
    if (!(__builtin_amdgcn_ballot_w64(d != 0u) & need)) break;
    nap<TSLEEP>();
  }
}
#define LAYER_VARS \
  extern __shared__ __attribute__((aligned(16))) char smem[]; \
  const lptr lds = (lptr)smem; \
  const rsrc_t rw = mk_rsrc(ws, WS_END); \
  int lane = threadIdx.x & 63; asm volatile("" : "+v"(lane)); \
  const int w = wave_id(), g = lane >> 4, r16 = lane & 15; \
  const int xcd = blockIdx.x & 7, bi = blockIdx.x >> 3, u = 8 * (xcd & 1) + (bi & 7), qt = 4 * (xcd >> 1) + (bi >> 3); \
  const int hf = u & 1, ct = u >> 1;     \
  constexpr unsigned FKV = WS_FL + (FINAL ? 32768 : 0), KV = WS_KV + (FINAL ? LAYER2 : 0), OB = WS_O + (FINAL ? LAYER2 : 0); \
  constexpr unsigned AUX = WS_AUX + (FINAL ? 1024 : 0), RB = FINAL ? WS_RB2 : WS_RB1; \
  (void)hf; (void)AUX; (void)RB; (void)FKV; \
  LDS_AS unsigned* misc = (LDS_AS unsigned*)(lds + L_MISC + 1024) + (FINAL ? 8 : 0);

template <bool FINAL>
DI void loader(char* ws, u32x2 tag) {
  LAYER_VARS
  {
    const int lw = w - 8;
    {
      unsigned vm[4];
#pragma unroll
      for (int r = 0; r < 4; r++) vm[r] = (4 * (4 * lw + r) + g) * (RBP * 2) + 256 * qt + (((r16 >> 1) ^ (g | ((r & 2) << 1))) * 32) + (r16 & 1) * 16 + 3072 - 1024 * r;
      const unsigned l0 = lds_addr(lds) + lw * 4096, v0 = 2 * lw * 20480 + lane * 16;
      constexpr int CNT = 8;
      auto lds_w = [&](int s) {
        const char* sb = ws + WS_WQ + u * 163840 + s * 2048;
        dma_s(v0, v0 + 20480 - 2048, sb, l0 + (s & 3) * SLOT32 + 16384);
      };
      auto lds_a = [&](int s) {
        dma_a(vm, ws + RB - 3072 + s * (64 * RBP * 2), l0 + (s & 3) * SLOT32); };
#pragma unroll 1
      for (int att = 0;; att++) {
        if (att == 0) { raw_bar(); if (FINAL) raw_bar(); else poll_tag(rw, tag, 0x7FFF0000ull); }
        lds_w(0); lds_w(1);
        if (att == 0) raw_bar();
        lds_w(2); lds_w(3);
        if (FINAL) dma2(40960 + lw * 2048 + lane * 16, (const char*)&g_it, lds_addr(lds) + L_IA + lw * 2048);
        auto probe = [&](int s0, int n) {
          const int pl = min(lane, n - 1);
          const unsigned po = RB + ((64 * (s0 + (pl >> 3)) + 16 * lw + 2 * (pl & 7) + 1) * RBP + 128 * qt + 126) * 2;
#pragma unroll 1
          for (int spins = 0; spins < (1 << 20); spins++) {
            if (!__builtin_amdgcn_ballot_w64(gl32<16>(rw, po) == EMPTY)) break;
            nap<XSLEEP>();
          }
        };
        if (att == 0) {
          raw_bar();
          if (blockIdx.x >= 64) nap<XDELAY>();
          probe(0, 48);
        }
        drain();
        unsigned nd = 0;
        if (att == 0 && lane == 0) nd = __hip_atomic_fetch_add(misc + 4, 1u, __ATOMIC_RELAXED, __HIP_MEMORY_SCOPE_WORKGROUP);
        if (nd == 11) st32<16>(rw, AUX + blockIdx.x * 4, 1u);
#pragma unroll 1
        for (int s = 0; s < 4; s++) lds_a(s);
        vm_bar<12>(); vm_bar<8>();
        if (att == 0) probe(6, 32);
        lds_a(4); lds_w(4);
        vm_bar<4 + CNT>();
#pragma unroll 1
        for (int s = 5; s < 10; s++) { lds_a(s); lds_w(s); vm_bar<2 * CNT>(); }
        vm_bar<CNT>();
        poll_aux(rw, AUX);
        if (!FINAL) { poll_tag(rw, tag, ~0ull); if (blockIdx.x == 0 && threadIdx.x == 512) st64<16>(rw, WS_EP, tag); }
        vm_bar<0>();
        if (FINAL) {
#pragma unroll
          for (int i = 0; i < 3; i++) { const int pc = min(3 * lw + i, 9); dma1_sc1(RB + ((64 * pc + lane) * RBP + 128 * qt + 8 * u) * 2, ws, lds_addr(lds) + L_T + pc * 1024); }
        }
        raw_bar();
        if (att || !*(volatile LDS_AS unsigned*)(misc + 2)) break;
      }
    }
    const int hh = lw >> 1, hp = lw & 1;
    const unsigned kvv = KV + (2 * u + hh) * 16 * KVC + hp * 5120 + lane * 16;
    const unsigned lkv = lds_addr(lds) + hh * HR + hp * 5120;
    const unsigned wv = (FINAL ? WS_WOP : WS_WOF) + (ct * 2 + hh) * 51200 + lane * 16;
    const unsigned myfl = FKV + (u * 16 + qt) * 128;
    vm_bar<0>();
    {
      u32x4 v[6];
#pragma unroll
      for (int i = 0; i < 6; i++) v[i] = ld128(lds + hh * HR + hp * 5120 + lane * 16 + i * 1024);
#pragma unroll
      for (int i = 0; i < 6; i++) st128<16>(rw, kvv + qt * KVC + i * 1024, clean(v[i]));
    }
    drain();
    unsigned cnt = 0;
    if (lane == 0) cnt = __hip_atomic_fetch_add(misc, 1u, __ATOMIC_RELAXED, __HIP_MEMORY_SCOPE_WORKGROUP);
    if (__builtin_amdgcn_readfirstlane(cnt) == 3 && lane < 16) st32<16>(rw, FKV + (u * 16 + lane) * 128 + qt * 4, 1u);
    auto ldkv = [&](int seq) {
      const int ch = seq == 0 ? qt : seq < 13 ? ((qt & 12) + 3 + seq) & 15 : (qt & 12) | ((qt + seq) & 3);
      dma6_sc1(kvv + ch * KVC, ws, lkv + (seq & 3) * KVC);
    };
    auto fetch = [&](int seq) {
      const unsigned src = kvv + ((((qt & 12) + 3 + seq) & 15)) * KVC;
      u32x4 v[6];
#pragma unroll 1
      for (int spins = 0; spins < (1 << 18); spins++) {
#pragma unroll
        for (int i = 0; i < 6; i++) v[i] = gl128<16>(rw, src + i * 1024);
        unsigned m = 0;
#pragma unroll
        for (int i = 0; i < 6; i++) m = umax4(m, v[i]);
        if (!__builtin_amdgcn_ballot_w64(m == EMPTY)) break;
        nap<2>();
      }
#pragma unroll
      for (int i = 0; i < 6; i++) *(LDS_AS u32x4*)(lds + hh * HR + hp * 5120 + seq * KVC + lane * 16 + i * 1024) = v[i];
    };
#pragma unroll 1
    for (int attempt = 0;; attempt++) {
      int c = -2;
      if (attempt) { ldkv(0); vm_bar<0>(); }
      else {
        fetch(1); lds_bar();
        fetch(2); poll16(rw, myfl, 12u << (((qt & 12) + 4) & 15)); ldkv(3); lds_bar();
        c = 1;
      }
#pragma unroll 1
      for (; c < 13; c++) {
        if (c == 2 && !attempt) poll16(rw, myfl, 0xFFFFu);
        if (!FINAL && !attempt) {
          if (c >= 2 && c < 9) {
            const u32x4 e = {EMPTY, EMPTY, EMPTY, EMPTY};
            const unsigned o = OB + LAYER2 + (((c - 2) * 256 + blockIdx.x) * 4 + lw) * 2048 + lane * 16;
            st128<16>(rw, o, e); st128<16>(rw, o + 1024, e);
            st128<16>(rw, ((qt & 3) < 2 && c < 8) ? kvv + LAYER2 + qt * KVC + (c - 2) * 1024 : WS_END, e);
            st128<16>(rw, c < 5 ? OB + ((4 * qt + lw) * 32 + 2 * u) * 1280 + min((c - 2) * 1024, 1536) + lane * 16 : WS_END, e);
          }
          if (c == 10) {
            drain();
            unsigned n = 0;
            if (lane == 0) n = __hip_atomic_fetch_add(misc + 3, 1u, __ATOMIC_RELAXED, __HIP_MEMORY_SCOPE_WORKGROUP);
            if (__builtin_amdgcn_readfirstlane(n) == 3 && lane < 32) st32<16>(rw, WS_FL + 65536 + (lane < 16 ? (u * 16 + lane) * 128 + qt * 4 : (r16 * 16 + qt) * 128 + 64 + u * 4), 1u);
          }
        }
        if (c >= 2) { const int e = min(4 * (c - 2) + 2 * hp, 33); dma2(wv + e * 1024, ws, lds_addr(lds) + L_A + (hh * 35 + e) * 1024); }
        ldkv(c + 3);
        if (c == 0) vm_bar<12>();
        if (c >= 2) vm_bar<8>(); else if (c >= 0) vm_bar<6>();
      }
      if (!FINAL && !attempt) poll16(rw, myfl + 65536 + hp * 64, 0xFFFFu);
      vm_bar<0>();
      dma_s(wv + (35 + 7 * hp) * 1024, wv + (35 + 7 * hp) * 1024, ws, lds_addr(lds) + hh * HR + 7 * hp * 1024);
      dma_s(wv + (39 + 7 * hp) * 1024, wv + (39 + 7 * hp) * 1024, ws, lds_addr(lds) + hh * HR + (4 + 7 * hp) * 1024);
      if (FINAL && lw == 0 && lane < 9)
        st64<16>(rw, lane < 8 ? WS_F0 + lane * 2048 + blockIdx.x * 8 : WS_GO + blockIdx.x * 64, u32x2{tag[0], ~tag[1]});
      raw_bar();
      vm_bar<0>();
      if (attempt || !*(volatile LDS_AS unsigned*)(misc + 1)) break;
      raw_bar();
    }
    if (!FINAL) { raw_bar(); raw_bar(); }
  }
}

template <bool FINAL>
DI void compute(char* ws, u16* out, ColIn& kin) {
  LAYER_VARS
  const int wm = w & 3, wn = w >> 2, h = lane >> 5;
  if (blockIdx.x < 64 || wn == 0) col_phase<!FINAL>(rw, lds, kin);
  else if (FINAL) {
    drain();
    nap<YIDLE>();
    u32x2 sp[8];
#pragma unroll 1
    for (int spins = 0; spins < (1 << 18); spins++) {
      unsigned m = 0;
#pragma unroll
      for (int i = 0; i < 8; i++) sp[i] = gl64<16>(rw, WS_SSQ + (blockIdx.x & 7) * 32768 + (1024 + 4 * (wm * 64 + lane)) * 2 + i * 4096);
#pragma unroll
      for (int i = 0; i < 8; i++) m = max(max(m, sp[i][0]), sp[i][1]);
      if (!__builtin_amdgcn_ballot_w64(m == EMPTY)) break;
      nap<YSLEEP>();
    }
    put_scale(lds, 1, wm * 64 + lane, sp);
    lds_bar(); raw_bar(); raw_bar();
  } else {
    raw_bar(); raw_bar(); raw_bar();
  }
  lds_bar();
  {
    drain();
    unsigned n = 0;
    if (lane == 0) n = __hip_atomic_fetch_add(misc + 4, 1u, __ATOMIC_RELAXED, __HIP_MEMORY_SCOPE_WORKGROUP);
    if (n == 11) st32<16>(rw, AUX + blockIdx.x * 4, 1u);
  }

  const int head = 2 * u + wn, kh = w & 1;
  const lptr ring = lds + wn * HR;
  const int pblk = 128 * qt + 64 * hf + 32 * (wm >> 1);
  const unsigned r3t = WS_R3 + (64 * (wm & 1) + (pblk >> 5)) * (642 * 64);
  u32x2 x4v[3]; float dc = 0.f;
  unsigned os = OB + ((4 * qt + wm) * 32 + head) * 1280 + (((lane << 2) & 16) + ((lane >> 1) & 12) + (lane & 3)) * 8;
  unsigned o1 = OB + ((pblk >> 5) * 32 + 16 * wn) * 1280 + (g >> 1) * 1280 + (g & 1) * 512 + (wm & 1) * 256 + r16 * 16;
  unsigned o2 = OB + ((pblk >> 5) * 32 + 16 * wn) * 1280 + 2 * g * 1280 + 1024 + (wm & 1) * 128 + r16 * 8;
  u32x4 o3 = {0u, 0u, 0u, 0u};
  if (!FINAL) {
#pragma unroll
    for (int i = 0; i < 3; i++) {
      const int j = 80 * ct + 48 * wn + 16 * (wn ? (i & 1) : i) + r16;
      o3[i] = (j == 1 ? WS_RB0 + ((pblk >> 5) * 641 + ((pblk >> 5) & 1) + 1) * 64 : WS_RB1 + (j * RBP + pblk) * 2) + 16 * g + 8 * (wm & 1);
    }
  } else {
    o3[3] = r3t + 64 + r16 * 4;
#pragma unroll
    for (int i = 0; i < 3; i++) o3[i] = WS_X4 + ((pblk + trow(wm & 1, r16)) * 640 + 80 * ct + (i < 2 ? 48 * wn + 8 * g + 4 * i : 32 + 4 * g)) * 2;
  }
  asm volatile("" : "+v"(os), "+v"(o1), "+v"(o2), "+v"(o3));
  unsigned ka = lds_addr(ring) + kh * 2560 + lane * 16, kb = lds_addr(ring) + kh * 2560 + 1024 + (lane & 31) * 8;
  unsigned va = lds_addr(ring) + kh * 3072 + (4 * h + (r16 >> 2)) * 48 + ((g & 1) ? ((lane & 3) ? 40 : 32) : (lane & 3) * 8);
  asm volatile("" : "+v"(ka), "+v"(kb), "+v"(va));
  {
    f32x4 acc[2][4]; float rs[2]; u32x8 im[3];
    unsigned mo[2], a0[2], a1[2], a2[2], a_r[2];
#pragma unroll
    for (int m = 0; m < 2; m++) {
      const int q = r16 >> 2, pr = 16 * (m ^ wn) + r16;
      mo[m] = lds_addr(lds) + (8 * g + q) * 256 + (((2 * wm + (m ^ wn)) ^ (q | ((g & 1) << 2))) * 32) + (lane & 3) * 8;
      const unsigned qb = L_A + (wn * 4 + wm) * 1280, kb = wn * HR + wm * 1280, vr = wn * HR + 5120 + (32 * wm + pr) * 48 + (g == 3 ? 32 : g * 16);
      a0[m] = ((g >> 1) ? kb : qb) + (g & 1) * 512 + pr * 16;
      a1[m] = g == 2 ? qb + 1024 + pr * 8 : vr; a2[m] = g == 2 ? kb + 1024 + pr * 8 : vr + 8;
      a_r[m] = lds_addr(lds) + L_MISC + (32 * wm + pr) * 4;
      asm volatile("" : "+v"(a0[m]), "+v"(a1[m]), "+v"(a2[m]));
    }
    const unsigned a_w = a_r[0] + (g == (r16 >> 2) ? 0 : 512), a_v = lds_addr((lptr)(misc + 2));
#pragma unroll 1
    for (int att = 0;; att++) {
      unsigned am0 = mo[0], am1 = mo[1], as = lds_addr(lds) + 16384 + wn * 8192 + lane * 16, vote; int c_, t_;
      if (!FINAL) asm volatile(GEMM_ASM("") : GEMM_OUT : GEMM_IN : GEMM_CLOB);
      else asm volatile(GEMM_ASM(ITL(96, 91) ITL(104, 92) ITL(112, 93)) : GEMM_OUT, "={v[96:103]}"(im[0]), "={v[104:111]}"(im[1]), "={v[112:119]}"(im[2])
                        : GEMM_IN, "{v91}"(w * 2048 + lane * 16), "{v92}"((w + 8) * 2048 + lane * 16), "{v93}"((w < 4 ? w + 16 : w) * 2048 + lane * 16), [it] "s"(&g_it) : GEMM_CLOB);
      if (att || !__builtin_amdgcn_readfirstlane(vote)) break;
    }
    SB();
#pragma unroll
    for (int m = 0; m < 2; m++) *(LDS_AS u32x4*)(lds + a0[m]) = pk8(acc[m][0] * rs[m], acc[m][1] * rs[m]);
    asm volatile("s_nop 1" : "+v"(acc[0][2]), "+v"(acc[1][2]), "+v"(acc[0][3]), "+v"(acc[1][3]) :: "memory");
#pragma unroll
    for (int m = 0; m < 2; m++) {
      u32x2 hi = pk4(acc[m][3] * rs[m]);
      if (g == 3) hi = u32x2{0x3F80u, 0u};
      *(LDS_AS u32x2*)(lds + a1[m]) = pk4(acc[m][2] * rs[m]);
      *(LDS_AS u32x2*)(lds + a2[m]) = hi;
    }
    lds_bar();
    if (FINAL) {
      u32x4 ia[3]; u32x2 ib[3];
#pragma unroll
      for (int T = 0; T < 3; T++) { ia[T] = ld128(lds + L_IA + T * 1024 + lane * 16); ib[T] = ld64(lds + L_IA + 3072 + T * 512 + lane * 8); }
      const f32x4 z4 = {0.f, 0.f, 0.f, 0.f};
#pragma unroll
      for (int i = 0; i < 3; i++) {
        const int G = (i < 2 || w < 4) ? w + 8 * i : w;
        const lptr tp = lds + L_T + (32 * G + 8 * g + (r16 >> 2)) * 16 + (lane & 1) * 8;
        const u32x2 f0 = ldtr(tp), f1 = ldtr(tp + 64);
#pragma unroll
        for (int P = 0; P < 2; P++) *(LDS_AS u32x2*)(lds + L_U + (r16 & 7) * 1296 + (2 * G + P) * 32 + g * 8) = pk4(mfma16(u32x4{im[i][4 * P], im[i][4 * P + 1], im[i][4 * P + 2], im[i][4 * P + 3]}, u32x4{f0[0], f0[1], f1[0], f1[1]}, z4));
      }
      wave_sync(misc + 5, 8);
      const lptr up = lds + L_U + w * 1296;
      const u32x2 f0 = ldtr(up + lane * 8), f1 = ldtr(up + 512 + lane * 8), f2 = ldtr(up + 1024 + (lane & 31) * 8);
#pragma unroll
      for (int T = 0; T < 3; T++) {
        const u32x2 v = pk4(mfma16(u32x4{f2[0], f2[1], 0u, 0u}, u32x4{ib[T][0], ib[T][1], 0u, 0u}, mfma16(u32x4{f0[0], f0[1], f1[0], f1[1]}, ia[T], z4)));
        st64<16>(rw, (T < 2 || r16 < 8) ? WS_X4 + ((128 * qt + 8 * u + w) * 640 + 16 * (16 * T + r16) + 4 * g) * 2 : WS_END, u32x2{min(v[0], EMPTY - 1), min(v[1], EMPTY - 1)});
      }
    }
  }
  f32x4 r[3];
  {
    const lptr qpa = lds + L_A + (wn * 4 + wm) * 1280, qpb = lds + L_A + (wn * 4 + (wm ^ 1)) * 1280;
    const u32x4 qa1 = ld128(qpa + lane * 16), qb1 = ld128(qpb + lane * 16);
    u32x2 qa2 = ld64(qpa + 1024 + (lane & 31) * 8), qb2 = ld64(qpb + 1024 + (lane & 31) * 8);
    if (h) { qa2 = u32x2{0u, 0u}; qb2 = qa2; }
    float ma, mb;
    {
      const u32x4 k1 = ld128(ring + lane * 16); const u32x2 k2 = ld64(ring + 1024 + (lane & 31) * 8);
      ma = max16(scores(k1, k2, qa1, qa2)); mb = max16(scores(k1, k2, qb1, qb2));
    }
    for (int attempt = 0;; attempt++) {
      ma = fmaxf(ma, __shfl_xor(ma, 32)); mb = fmaxf(mb, __shfl_xor(mb, 32));
      f32x16 cia, cib, oa, ob;
#pragma unroll
      for (int i = 0; i < 16; i++) { cia[i] = -ma; cib[i] = -mb; }
      int cnt = 4;
      asm volatile(
          "ds_read_b128 v[80:83], v100\n\t"
          "ds_read_b64 v[84:85], v101\n\t"
          "ds_read_b64_tr_b16 v[86:87], v102 offset:5120\n\t"
          "ds_read_b64_tr_b16 v[88:89], v102 offset:5504\n\t"
          Z4(0) Z4(68) Z4(76) Z4(90)
          "s_waitcnt lgkmcnt(0)\n\t"
          "v_mfma_f32_32x32x16_bf16 v[32:47], v[80:83], v[94:97], v[16:31]\n\t"
          Z4(4) Z4(8) Z4(12) Z4(104) Z4(108) Z4(112) Z4(116)
          "v_mfma_f32_32x32x8_bf16 v[32:47], v[84:85], v[98:99], v[32:47]\n\ts_nop 9\n\t"
          "L_attn_%=:\n\t"
          CHUNK(0) CHUNK(1) CHUNK(2) CHUNK(3)
          "s_sub_u32 %[cnt], %[cnt], 1\n\ts_cmp_lg_u32 %[cnt], 0\n\ts_cbranch_scc1 L_attn_%=\n\t"
          "s_setprio 0\n\t" CVT(78,68) CVT(79,70) "s_waitcnt lgkmcnt(0)\n\ts_nop 1\n\t"
          "v_mfma_f32_32x32x16_bf16 v[104:119], v[90:93], v[76:79], v[104:119]\n\ts_nop 15\n\t"
          : "={v[0:15]}"(oa), "={v[104:119]}"(ob), [cnt] "+s"(cnt)
          : "{v[16:31]}"(cia), "{v[120:135]}"(cib), "{v[94:97]}"(qa1), "{v[98:99]}"(qa2), "{v[136:139]}"(qb1), "{v[140:141]}"(qb2),
            "{v100}"(ka), "{v101}"(kb), "{v102}"(va)
          : "memory", "scc", "v32", "v33", "v34", "v35", "v36", "v37", "v38", "v39", "v40", "v41", "v42", "v43", "v44", "v45", "v46", "v47",
            "v48", "v49", "v50", "v51", "v52", "v53", "v54", "v55", "v56", "v57", "v58", "v59", "v60", "v61", "v62", "v63", "v64", "v65",
            "v66", "v67", "v68", "v69", "v70", "v71", "v72", "v73", "v74", "v75", "v76", "v77", "v78", "v79", "v80", "v81", "v82", "v83",
            "v84", "v85", "v86", "v87", "v88", "v89", "v90", "v91", "v92", "v93");
      LDS_AS f32x4* ex = (LDS_AS f32x4*)(ring + L_EX + wm * 3072) + lane;
#pragma unroll
      for (int i = 0; i < 3; i++) ex[i * 64] = f32x4{ob[4 * i], ob[4 * i + 1], ob[4 * i + 2], ob[4 * i + 3]};
      f32x4 cs = {0.f, 0.f, 0.f, 0.f};
#pragma unroll
      for (int i = 0; i < 3; i++) cs += f32x4{oa[4 * i], oa[4 * i + 1], oa[4 * i + 2], oa[4 * i + 3]} + f32x4{ob[4 * i], ob[4 * i + 1], ob[4 * i + 2], ob[4 * i + 3]};
      if (!(fabsf(cs[0] + cs[1] + cs[2] + cs[3]) < INFINITY) || (h && !(oa[8] + ob[8] < 0x1p60f))) misc[1] = 1u;
      lds_bar();
      const unsigned vote = *(volatile LDS_AS unsigned*)(misc + 1);
      LDS_AS f32x4* ey = (LDS_AS f32x4*)(ring + L_EX + (wm ^ 1) * 3072) + lane;
#pragma unroll
      for (int i = 0; i < 3; i++) r[i] = ey[i * 64] + f32x4{oa[4 * i], oa[4 * i + 1], oa[4 * i + 2], oa[4 * i + 3]};
      if (attempt || !vote) break;
      lds_bar();
      ma = -INFINITY; mb = -INFINITY;
      for (int t = 0; t < 64; t++) {
        const unsigned kb_ = KV + (head * 16 + (t >> 2)) * KVC + (t & 3) * 1280;
        const u32x4 k1 = gl128<16>(rw, kb_ + lane * 16); const u32x2 k2 = gl64<16>(rw, kb_ + 1024 + (lane & 31) * 8);
        ma = fmaxf(ma, max16(scores(k1, k2, qa1, qa2))); mb = fmaxf(mb, max16(scores(k1, k2, qb1, qb2)));
      }
      raw_bar();
    }
  }
  {
    const unsigned lb = __float_as_uint(r[2][0]);
    const float il = __builtin_amdgcn_rcpf(__uint_as_float(__builtin_amdgcn_permlane32_swap(lb, lb, false, false)[1]));
    const unsigned sl8 = os & 255;
    st128<16>(rw, os + sl8 + h * 512, clean(pk8(r[0] * il, r[1] * il)));
    const u32x4 t = clean(pk8(r[2] * il, r[2] * il));
    st64<16>(rw, h ? WS_END : os + 1024, u32x2{t[0], t[1]});
  }
  const lptr wa = lds + L_A + wn * 35 * 1024 + lane * 16, wb = ring + lane * 16;
  constexpr int NPRE = 15;
  u32x4 bf[50];
  f32x4 acc[5];
#pragma unroll
  for (int j = 0; j < NPRE; j++) bf[j] = ld128(wa + j * 1024);
  if (FINAL) dc = __uint_as_float(gl32<16>(rw, o3[3]));
#pragma unroll 1
  for (int spins = 0; spins < (1 << 18); spins++) {
#pragma unroll
    for (int i = 0; i < 3; i++) x4v[i] = gl64<16>(rw, o3[i]);
    if (!FINAL || !__builtin_amdgcn_ballot_w64(umax4(max(x4v[0][0], x4v[0][1]), u32x4{x4v[1][0], x4v[1][1], x4v[2][0], x4v[2][1]}) == EMPTY)) break;
    nap<8>();
  }
  drain();
  auto init = [&](int a) {
    f32x4 v = {0.f, 0.f, 0.f, 0.f};
    if (a < 3) { v = f32x4{bflo(x4v[a][0]), bfhi(x4v[a][0]), bflo(x4v[a][1]), bfhi(x4v[a][1])}; if (FINAL) v += dc * 0.039528470752104741f; if (a == 2 && wn) v = f32x4{0.f, 0.f, 0.f, 0.f}; }
    return v;
  };
  unsigned so_ = FINAL ? (rowof(pblk + trow(wm & 1, r16)) * 640 + 80 * ct) * 2 : r3t + (80 * ct + 48 * wn + r16) * 64 + g * 16;
  {
    u32x4 af[10];
    auto ldf = [&](int i) {
      if (i < 8) af[i] = __builtin_amdgcn_raw_buffer_load_b128(rw, o1, i * 2 * 1280, 16);
      else { const u32x2 a = __builtin_amdgcn_raw_buffer_load_b64(rw, o2, (i - 8) * 8 * 1280, 16), b = __builtin_amdgcn_raw_buffer_load_b64(rw, o2, ((i - 8) * 8 + 1) * 1280, 16);
             af[i] = u32x4{a[0], a[1], b[0], b[1]}; }
    };
#pragma unroll
    for (int i = 0; i < 10; i++) ldf(i);
    SB();
#pragma unroll
    for (int a = 0; a < 5; a++) acc[a] = init(a);
    asm volatile("s_nop 1" : "+v"(acc[0]), "+v"(acc[1]), "+v"(acc[2]), "+v"(acc[3]), "+v"(acc[4]), "+v"(so_));
    SB();
    unsigned m0 = 0;
#pragma unroll
    for (int i = 0; i < 10; i++) {
#pragma unroll
      for (int a = 0; a < 5; a++) {
        if (FINAL) mfma16v(acc[a], bf[5 * i + a], af[i]); else mfma16v(acc[a], af[i], bf[5 * i + a]);
        const int j = 5 * i + a + NPRE; if (j < 50) bf[j] = ld128(j < 35 ? wa + j * 1024 : wb + (j - 35) * 1024);
        if (a == 1) m0 = umax4(m0, af[i]);
        SB();
      }
    }
    asm volatile("s_nop 7\n\ts_nop 1" : "+v"(acc[0]), "+v"(acc[1]), "+v"(acc[2]), "+v"(acc[3]), "+v"(acc[4]) :: "memory");
    if (__builtin_amdgcn_ballot_w64(m0 == EMPTY)) {
#pragma unroll 1
      for (int spins = 0; spins < (1 << 18); spins++) {
#pragma unroll
        for (int i = 0; i < 10; i++) if (__builtin_amdgcn_ballot_w64(umax4(0, af[i]) == EMPTY)) ldf(i);
        unsigned m = 0;
#pragma unroll
        for (int i = 0; i < 10; i++) m = umax4(m, af[i]);
        if (!__builtin_amdgcn_ballot_w64(m == EMPTY)) break;
        nap<RSLEEP>();
      }
#pragma unroll
      for (int j = 0; j < NPRE; j++) bf[j] = ld128(wa + j * 1024);
#pragma unroll
      for (int i = 0; i < 10; i++) {
#pragma unroll
        for (int a = 0; a < 5; a++) {
          const f32x4 c = i ? acc[a] : init(a);
          acc[a] = FINAL ? mfma16(bf[5 * i + a], af[i], c) : mfma16(af[i], bf[5 * i + a], c);
          const int j = 5 * i + a + NPRE; if (j < 50) bf[j] = ld128(j < 35 ? wa + j * 1024 : wb + (j - 35) * 1024);
        }
        asm volatile("" : "+v"(acc[0])); SB();
      }
    }
  }
  {
    LDS_AS f32x4* ex = (LDS_AS f32x4*)(ring + L_EX + 12288 + wm * 3072) + lane;
    LDS_AS f32x4* ey = (LDS_AS f32x4*)(lds + (wn ^ 1) * HR + L_EX + 12288 + wm * 3072) + lane + 64 * wn;
    ex[0] = acc[2]; ex[64] = acc[3]; ex[128] = acc[4];
    if (!FINAL) { SB(); col_load(kin, rw, true, true); SB(); }
    lds_bar();
#pragma unroll
    for (int i = 0; i < 3; i++) acc[i] += ey[64 * i];
  }
  if (FINAL) {
    const rsrc_t ro = mk_rsrc(out, 2048 * 640 * 2);
    st128<16>(ro, so_ + 96 * wn + 16 * g, pk8(acc[0], acc[1]));
    if (wn == 0) st64<16>(ro, so_ + 64 + 8 * g, pk4(acc[2]));
  } else {
    f32x4 sq = {0.f, 0.f, 0.f, 0.f};
#pragma unroll
    for (int i = 0; i < 3; i++)
      if (i < 2 || wn == 0) {
        const int j = 80 * ct + 48 * wn + 16 * i + r16;
        st128<16>(rw, so_ + i * 1024, clean(asu(acc[i])));
        if (j != 1) sq += acc[i] * acc[i];
      }
#pragma unroll
    for (int e = 0; e < 4; e++) sq[e] = row16_sum(sq[e]);
    LDS_AS float* sl = (LDS_AS float*)(lds + L_MISC);
    if (r16 == 0) *(LDS_AS f32x4*)(sl + wn * 64 + 32 * (wm & 1) + 16 * (wm >> 1) + 4 * g) = sq;
    lds_bar();
    if (w == 0) {
      LDS_AS f32x4* s4 = (LDS_AS f32x4*)sl + 2 * (lane & 7);
      st128<16>(rw, WS_SSQ + (lane >> 3) * 32768 + (ct * 2048 + 1024 * ((lane >> 2) & 1) + 64 * qt + 32 * hf + 8 * (lane & 3)) * 2, clean(pk8(s4[0] + s4[16], s4[1] + s4[17])));
    }
  }
}

__global__ void __launch_bounds__(768, 3) k_main(const u16* x, const u16* wr, const u16* wi, const u16* wqkv, const u16* wo, char* ws, u16* out) {
  if (wave_id() >= 8) {
    extern __shared__ __attribute__((aligned(16))) char smem[];
    const unsigned long long t0 = __builtin_amdgcn_s_memrealtime();
    const int job = blockIdx.x;
    if (job < 144) raw_bar();
    const rsrc_t rw = mk_rsrc(ws, WS_END);
    __builtin_amdgcn_s_setprio(3);
    if (threadIdx.x < 528) ((LDS_AS unsigned*)((lptr)smem + L_MISC + 1024))[threadIdx.x - 512] = 0u;
    u32x2 tag = gl64<16>(rw, WS_EP);
    tag[0] += 1u;
    if (job < 228) raw_bar();
    if (job < 64) raw_bar();
    asm volatile("s_waitcnt vmcnt(0) lgkmcnt(0)\n\ts_barrier" : "+v"(tag) :: "memory");
    if (wave_id() == 8) {
      if (threadIdx.x < 520) st64<16>(rw, WS_F0 + (threadIdx.x - 512) * 2048 + job * 8, tag);
      if (job >= 248) {
        while (__builtin_amdgcn_s_memrealtime() - t0 < TPOLL) nap<2>();
        poll_tag(rw, tag, 0xFE0FFFF00000FFFFull);
        if (threadIdx.x < 544) st64<16>(rw, WS_GO + ((threadIdx.x - 512) * 8 + (job & 7)) * 64, tag);
      }
#pragma unroll 1
      for (int spins = 0; spins < (1 << 20); spins++) {
        const u32x2 v = gl64<16>(rw, WS_GO + job * 64);
        if (__builtin_amdgcn_readfirstlane((v[0] ^ tag[0]) | (v[1] ^ tag[1])) == 0) break;
        nap<GSLEEP>();
      }
    }
    if (wave_id() > 8) {
      while (__builtin_amdgcn_s_memrealtime() - t0 < TFILL) nap<2>();
      const int i0 = blockIdx.x * 192 + threadIdx.x - 576;
#pragma unroll
      for (int it = 0; it < 4; it++) {
        const int k = it * 49152 + i0, b = k / 1408;
        st128<16>(rw, k < 128 * 1408 ? WS_KV + b * 4 * KVC + (k - b * 1408) * 16 : WS_END, u32x4{EMPTY, EMPTY, EMPTY, EMPTY});
      }
    }
    raw_bar();
    loader<false>(ws, tag);
    loader<true>(ws, tag);
  } else {
    prep(x, wr, wi, wqkv, wo, ws);
    asm volatile("s_waitcnt vmcnt(0) lgkmcnt(0)\n\ts_barrier" ::: "memory");
    ColIn kin = {};
    compute<false>(ws, out, kin);
    compute<true>(ws, out, kin);
  }
}

extern "C" void kernel_launch(void* const* d_in, const int* in_sizes, int n_in, void* d_out, int out_size, void* d_ws, size_t ws_size,
                              hipStream_t stream) {
  const u16* x = (const u16*)d_in[0];
  const u16* wr = (const u16*)d_in[1];
  const u16* wi = (const u16*)d_in[2];
  const u16* wqkv = (const u16*)d_in[4];
  const u16* wo = (const u16*)d_in[5];
  char* ws = (char*)d_ws;
  (void)hipFuncSetAttribute((const void*)k_main, hipFuncAttributeMaxDynamicSharedMemorySize, LDS_FUSED);
  k_main<<<256, 768, LDS_FUSED, stream>>>(x, wr, wi, wqkv, wo, ws, (u16*)d_out);
}
```
